```python
import torch

def _softmax(x, axis=-1):
    return torch.softmax(x, dim=axis)
def _gelu(x):
    return torch.nn.functional.gelu(x, approximate="tanh")
def _silu(x):
    return torch.nn.functional.silu(x)
def _sigmoid(x):
    return torch.sigmoid(x)
def _layernorm(x, axis=-1, eps=1e-5):
    m = x.mean(dim=axis, keepdim=True)
    v = x.var(dim=axis, keepdim=True, unbiased=False)
    return (x - m) / torch.sqrt(v + eps)
def _scatter_add(tgt, idx, src, axis=0):
    return tgt.clone().index_add_(axis, idx, src)
def _segment_sum(src, seg, n):
    out = torch.zeros((n,) + src.shape[1:], dtype=src.dtype)
    return out.index_add_(0, seg, src)
def _pad(x, pw):
    flat = tuple(v for pair in reversed(pw) for v in pair)
    return torch.nn.functional.pad(x, flat)

kernel_name = 'recurrent_scan__mha_block_cached__geglu_ffn__prompt'

def setup_inputs():

    _w = (lambda a: (lambda t: t.to(torch.bfloat16) if t.is_floating_point() else t)(torch.as_tensor(a)))
    def _A_setup():
        B, Sq, D = 1, 2048, 1024
        H, Sk, Dh = 32, 2048, 128
        return {
            "x": _w((lambda *s: torch.randn(*s, dtype=torch.bfloat16) * 0.05)(B, Sq, D)),
            "k_cache": _w((lambda *s: torch.randn(*s, dtype=torch.bfloat16) * 0.8)(B, H, Sk, Dh)),
            "v_cache": _w((lambda *s: torch.randn(*s, dtype=torch.bfloat16) * 0.2)(B, H, Sk, Dh)),
            "wq": _w((lambda *s: torch.randn(*s, dtype=torch.bfloat16) * 0.8)(D, H * Dh) * (D ** -0.5)),
            "wo": _w((lambda *s: torch.randn(*s, dtype=torch.bfloat16) * 0.2)(H * Dh, D) * ((H * Dh) ** -0.5)),
        }
    _A_in = _A_setup()

    def _B_setup():
        B, S, D, F = 1, 2048, 1024, 3584
        return {
            "x": _w((lambda *s: torch.randn(*s, dtype=torch.bfloat16) * 0.05)(B, S, D)),
            "w_gate": _w((lambda *s: torch.randn(*s, dtype=torch.bfloat16) * 0.1)(D, F)),
            "w_up": _w((lambda *s: torch.randn(*s, dtype=torch.bfloat16) * 0.1)(D, F)),
            "w_down": _w((lambda *s: torch.randn(*s, dtype=torch.bfloat16) * 0.1)(F, D)),
        }
    _B_in = _B_setup()

    return {
        "A_x": _A_in["x"],
        "A_k_cache": _A_in["k_cache"],
        "A_v_cache": _A_in["v_cache"],
        "A_wq": _A_in["wq"],
        "A_wo": _A_in["wo"],
        "B_x": _B_in["x"],
        "B_w_gate": _B_in["w_gate"],
        "B_w_up": _B_in["w_up"],
        "B_w_down": _B_in["w_down"],
    }

def reference(A_x, A_k_cache, A_v_cache, A_wq, A_wo, B_x, B_w_gate, B_w_up, B_w_down):
    def _A_ref(x, k_cache, v_cache, wq, wo):
        B, Sq, D = x.shape
        H, Dh = 32, 128
        q = (lambda x, *d: x.permute(*d))((x @ wq).reshape(B, Sq, H, Dh), 0, 2, 1, 3)
        scores = torch.einsum("bhqd,bhkd->bhqk", q, k_cache) * (Dh ** -0.5)
        p = _softmax(scores, axis=-1)
        o = torch.einsum("bhqk,bhkd->bhqd", p, v_cache)
        return (lambda x, *d: x.permute(*d))(o, 0, 2, 1, 3).reshape(B, Sq, H * Dh) @ wo

    def _B_ref(x, w_gate, w_up, w_down):
        return (_gelu(x @ w_gate) * (x @ w_up)) @ w_down

    x = A_x
    xn = _layernorm(x, axis=-1)
    s = _A_ref(xn, A_k_cache, A_v_cache, A_wq, A_wo)
    g = _silu(_B_ref(xn, B_w_gate, B_w_up, B_w_down))
    return x + s * g
```

```cpp
#include <hip/hip_runtime.h>
#include <hip/hip_bf16.h>
#include <stdint.h>

typedef unsigned short u16;
typedef unsigned int u32;
typedef unsigned long long u64;

constexpr size_t WS_XN = 0;
constexpr size_t WS_O  = 4ull << 20;
constexpr size_t WS_H  = 20ull << 20;
constexpr size_t WS_G  = 36ull << 20;
constexpr size_t WS_F  = 40ull << 20;
constexpr size_t WS_DBG = 48ull << 20;

#define NARX 0u

__global__ __launch_bounds__(256, 1) void mid_kernel(const u16* __restrict__ kc, const u16* __restrict__ vc, const u16* __restrict__ wq,
                                                     const u16* __restrict__ wg, const u16* __restrict__ wu, char* __restrict__ ws,
                                                     const u16* __restrict__ wo, const u16* __restrict__ wd,
                                                     const u16* __restrict__ x, u16* __restrict__ out) {
  __shared__ __attribute__((aligned(16))) char lds[163840];
  asm volatile("" :: "v"(lds));
  const u32 b = blockIdx.x, xc = b & 7u, j = b >> 3;
  const u32 hd = xc * 4u + (j & 3u), qt = j >> 2;
  const u32 nar = (xc & 1u) ^ NARX, mt = ((xc >> 1) & 1u) * 4u + (j & 3u), fi = (xc >> 2) * 8u + (j >> 2);
  const u32 blk0 = (fi >> 1) * 28u + (nar ? 8u + 6u * (fi & 1u) : 20u * (fi & 1u));
  const u32 X1o = (u32)WS_XN + mt * 524288u;
  const char* WQ = (const char*)wq + hd * 256u;
  const char* Kp = (const char*)kc + (size_t)hd * 524288u;
  const char* Vp = (const char*)vc + (size_t)hd * 524288u;
  const u32 Oo = (u32)WS_O + (2u * qt * 128u + hd * 4u) * 8192u;
  const char* WG = (const char*)wg + blk0 * 32u;
  const char* WU = (const char*)wu + blk0 * 32u;
  const u32 Ho = (u32)WS_H + (2u * mt * 112u + (blk0 >> 1)) * 8192u;
  const u32 HoS = Ho + (nar ? 2u : 3u) * 8192u;
  const u32 NWQ = 1024u * 8192u - hd * 256u;
  const u32 NWG = 1024u * 7168u - blk0 * 32u;
  const u32 wave = __builtin_amdgcn_readfirstlane(threadIdx.x >> 6);
  asm volatile(
"MID_BEGIN:\n"
"s_nop 4\n"
"s_mov_b32 s55, %16\n"
"v_and_b32 v1, 63, %18\n"
"v_lshlrev_b32 v2, 4, v1\n"
"v_lshrrev_b32 v3, 4, v1\n"
"v_and_b32 v4, 15, v1\n"
"v_and_b32 v108, 7, v1\n"
"v_bfe_u32 v109, v1, 3, 2\n"
"v_lshrrev_b32 v110, 5, v1\n"
"s_lshl_b32 s61, %17, 14\n"
"s_lshl_b32 s98, s55, 9\n"
"s_add_u32 s61, s61, s98\n"
"v_lshlrev_b32 v5, 11, v108\n"
"v_lshl_add_u32 v5, v110, 6, v5\n"
"v_lshl_add_u32 v5, v109, 3, v5\n"
"v_add_u32 v5, s61, v5\n"
"global_load_dwordx2 v[128:129], v5, %8 offset:0\n"
"global_load_dwordx2 v[130:131], v5, %8 offset:32\n"
"global_load_dwordx2 v[132:133], v5, %8 offset:128\n"
"global_load_dwordx2 v[134:135], v5, %8 offset:160\n"
"global_load_dwordx2 v[136:137], v5, %8 offset:256\n"
"global_load_dwordx2 v[138:139], v5, %8 offset:288\n"
"global_load_dwordx2 v[140:141], v5, %8 offset:384\n"
"global_load_dwordx2 v[142:143], v5, %8 offset:416\n"
"s_mov_b64 s[40:41], %1\n"
"s_mov_b32 s42, %14\n"
"s_mov_b32 s43, 0x20000\n"
"s_lshl_b32 s59, s55, 12\n"
"s_lshl_b32 s61, s55, 17\n"
"s_mov_b32 s99, 8192\n"
"v_mul_lo_u32 v6, s99, v3\n"
"v_add_u32 v6, s61, v6\n"
"v_add_u32 v5, 0, v3\n"
"v_lshlrev_b32 v5, 1, v5\n"
"v_xor_b32 v5, v5, v4\n"
"v_lshl_add_u32 v5, v5, 4, v6\n"
"v_add_u32 v80, 0, v5\n"
"v_add_u32 v5, 0, v3\n"
"v_lshlrev_b32 v5, 1, v5\n"
"v_xor_b32 v5, v5, v4\n"
"v_lshl_add_u32 v5, v5, 4, v6\n"
"v_add_u32 v81, 31744, v5\n"
"v_add_u32 v5, 4, v3\n"
"v_lshlrev_b32 v5, 1, v5\n"
"v_xor_b32 v5, v5, v4\n"
"v_lshl_add_u32 v5, v5, 4, v6\n"
"v_add_u32 v82, 63488, v5\n"
"v_add_u32 v5, 4, v3\n"
"v_lshlrev_b32 v5, 1, v5\n"
"v_xor_b32 v5, v5, v4\n"
"v_lshl_add_u32 v5, v5, 4, v6\n"
"v_add_u32 v83, 95232, v5\n"
"s_lshr_b32 s56, %17, 5\n"
"s_lshl_b32 s58, s56, 20\n"
"s_lshl_b32 s56, s56, 8\n"
"s_add_u32 m0, s59, 98304\n"
"s_nop 0\n"
"buffer_load_dwordx4 v80, s[40:43], s58 offen offset:0 lds\n"
"buffer_load_dwordx4 v81, s[40:43], s58 offen offset:1024 lds\n"
"buffer_load_dwordx4 v82, s[40:43], s58 offen offset:2048 lds\n"
"buffer_load_dwordx4 v83, s[40:43], s58 offen offset:3072 lds\n"
"s_add_u32 s58, s58, 524288\n"
"s_and_b32 s58, s58, 0x7fffff\n"
"s_lshr_b32 s98, %17, 5\n"
"s_lshl_b32 s98, s98, 19\n"
"s_mov_b64 s[36:37], %8\n"
"s_add_u32 s36, s36, s98\n"
"s_addc_u32 s37, s37, 0\n"
"s_mov_b32 s38, 524288\n"
"s_mov_b32 s39, 0x20000\n"
"v_lshrrev_b32 v6, 3, v1\n"
"v_lshlrev_b32 v6, 11, v6\n"
"v_add_u32 v6, s61, v6\n"
"v_add_u32 v5, 0, v3\n"
"v_xor_b32 v5, v5, v108\n"
"v_lshl_add_u32 v5, v5, 4, v6\n"
"v_add_u32 v84, 0, v5\n"
"v_add_u32 v5, 4, v3\n"
"v_xor_b32 v5, v5, v108\n"
"v_lshl_add_u32 v5, v5, 4, v6\n"
"v_add_u32 v85, 15360, v5\n"
"v_add_u32 v5, 0, v3\n"
"v_xor_b32 v5, v5, v108\n"
"v_lshl_add_u32 v5, v5, 4, v6\n"
"v_add_u32 v90, 30720, v5\n"
"v_add_u32 v5, 4, v3\n"
"v_xor_b32 v5, v5, v108\n"
"v_lshl_add_u32 v5, v5, 4, v6\n"
"v_add_u32 v91, 46080, v5\n"
"s_add_u32 s57, s56, 65536\n"
"s_lshl_b32 s34, s55, 14\n"
"s_add_u32 m0, s34, 0\n"
"s_nop 0\n"
"buffer_load_dwordx4 v84, s[36:39], s56 offen offset:0 lds\n"
"buffer_load_dwordx4 v85, s[36:39], s56 offen offset:1024 lds\n"
"buffer_load_dwordx4 v90, s[36:39], s56 offen offset:2048 lds\n"
"buffer_load_dwordx4 v91, s[36:39], s56 offen offset:3072 lds\n"
"s_add_u32 s56, s56, 128\n"
"s_and_b32 s56, s56, 0x107ff\n"
"s_add_u32 m0, s34, 4096\n"
"s_nop 0\n"
"buffer_load_dwordx4 v84, s[36:39], s57 offen offset:0 lds\n"
"buffer_load_dwordx4 v85, s[36:39], s57 offen offset:1024 lds\n"
"buffer_load_dwordx4 v90, s[36:39], s57 offen offset:2048 lds\n"
"buffer_load_dwordx4 v91, s[36:39], s57 offen offset:3072 lds\n"
"s_add_u32 s57, s57, 128\n"
"s_and_b32 s57, s57, 0x107ff\n"
"v_mov_b32 v94, 42500096\n"
"global_load_dwordx2 v[92:93], v94, %0 sc1\n"
"s_lshr_b32 s68, %17, 5\n"
"s_lshl_b32 s68, s68, 20\n"
"s_add_u32 s53, s68, 524288\n"
"s_getpc_b64 s[62:63]\n"
"MID_PC:\n"
"s_sub_u32 s62, s62, MID_PC-MID_BEGIN\n"
"s_subb_u32 s63, s63, 0\n"
"s_and_b32 s62, s62, -16\n"
"s_lshr_b32 s61, %17, 3\n"
"s_lshl_b32 s61, s61, 12\n"
"s_lshl_b32 s98, s55, 10\n"
"s_add_u32 s61, s61, s98\n"
"v_add_u32 v95, s61, v2\n"
"v_min_u32 v95, MID_END-MID_BEGIN-16, v95\n"
"v_and_b32 v95, -16, v95\n"
"s_lshr_b32 s61, %17, 4\n"
"s_lshl_b32 s61, s61, 18\n"
"s_bfe_u32 s98, %17, 0x30001\n"
"s_lshl_b32 s98, s98, 10\n"
"s_add_u32 s61, s61, s98\n"
"s_and_b32 s98, %17, 1\n"
"s_lshl_b32 s98, s98, 7\n"
"s_add_u32 s61, s61, s98\n"
"s_lshl_b32 s98, s55, 16\n"
"s_add_u32 s61, s61, s98\n"
"v_lshlrev_b32 v112, 13, v110\n"
"v_lshl_add_u32 v112, v109, 8, v112\n"
"v_lshl_add_u32 v112, v108, 4, v112\n"
"v_add_u32 v112, s61, v112\n"
"v_add_u32 v113, 16384, v112\n"
"v_add_u32 v114, 32768, v112\n"
"v_add_u32 v115, 49152, v112\n"
"v_lshlrev_b32 v117, 4, v108\n"
"v_add_u32 v117, 65536, v117\n"
"s_lshl_b32 s98, s55, 2\n"
"v_add_u32 v116, s98, v117\n"
"s_lshl_b32 s61, %17, 6\n"
"s_add_u32 s61, s61, 42598400\n"
"v_lshlrev_b32 v118, 3, v108\n"
"v_add_u32 v118, s61, v118\n"
"s_lshl_b32 s61, %17, 12\n"
"s_lshl_b32 s52, s55, 10\n"
"s_add_u32 s61, s61, s52\n"
"v_add_u32 v38, s61, v2\n"
"s_mov_b32 s52, 0\n"
"s_mov_b64 s[96:97], %0\n"
"s_add_u32 s96, s96, 41943040\n"
"s_addc_u32 s97, s97, 0\n"
"s_mov_b32 s98, 73728\n"
"s_mov_b32 s99, 0x20000\n"
"v_mov_b32 v100, 0\n"
"v_mov_b32 v101, 0\n"
"v_mov_b32 v102, 0\n"
"v_mov_b32 v103, 0\n"
"buffer_store_dwordx4 v[100:103], v38, s[96:99], 0 offen sc1\n"
"s_lshr_b32 s98, s55, 1\n"
"s_mul_i32 s98, s98, 262144\n"
"s_and_b32 s99, s55, 1\n"
"s_lshl_b32 s99, s99, 12\n"
"s_add_u32 s98, s98, s99\n"
"v_add_u32 v39, s98, v2\n"
"s_mov_b64 s[44:45], %2\n"
"s_mov_b32 s46, 524288\n"
"s_mov_b32 s47, 0x20000\n"
"s_mov_b64 s[48:49], %3\n"
"s_mov_b32 s50, 524288\n"
"s_mov_b32 s51, 0x20000\n"
"v_lshrrev_b32 v5, 2, v1\n"
"v_and_b32 v6, 7, v5\n"
"v_and_b32 v7, 3, v1\n"
"v_lshlrev_b32 v7, 3, v7\n"
"v_and_b32 v5, 3, v5\n"
"v_lshl_add_u32 v5, v3, 3, v5\n"
"v_lshl_add_u32 v7, v5, 8, v7\n"
"v_add_u32 v7, 98304, v7\n"
"v_xor_b32 v5, 0, v6\n"
"v_lshl_add_u32 v64, v5, 5, v7\n"
"v_xor_b32 v5, 1, v6\n"
"v_lshl_add_u32 v65, v5, 5, v7\n"
"v_xor_b32 v5, 2, v6\n"
"v_lshl_add_u32 v66, v5, 5, v7\n"
"v_xor_b32 v5, 3, v6\n"
"v_lshl_add_u32 v67, v5, 5, v7\n"
"v_xor_b32 v5, 4, v6\n"
"v_lshl_add_u32 v68, v5, 5, v7\n"
"s_mov_b32 s98, 0xffff0000\n"
"s_waitcnt vmcnt(21)\n"
"v_lshlrev_b32 v144, 16, v128\n"
"v_and_b32 v145, s98, v128\n"
"v_lshlrev_b32 v146, 16, v129\n"
"v_and_b32 v147, s98, v129\n"
"s_waitcnt vmcnt(20)\n"
"v_lshlrev_b32 v148, 16, v130\n"
"v_and_b32 v149, s98, v130\n"
"v_lshlrev_b32 v150, 16, v131\n"
"v_and_b32 v151, s98, v131\n"
"s_waitcnt vmcnt(19)\n"
"v_lshlrev_b32 v152, 16, v132\n"
"v_and_b32 v153, s98, v132\n"
"v_lshlrev_b32 v154, 16, v133\n"
"v_and_b32 v155, s98, v133\n"
"s_waitcnt vmcnt(18)\n"
"v_lshlrev_b32 v156, 16, v134\n"
"v_and_b32 v157, s98, v134\n"
"s_add_u32 m0, s59, 114688\n"
"s_nop 0\n"
"buffer_load_dwordx4 v80, s[40:43], s58 offen offset:0 lds\n"
"v_lshlrev_b32 v158, 16, v135\n"
"v_and_b32 v159, s98, v135\n"
"s_waitcnt vmcnt(18)\n"
"v_lshlrev_b32 v160, 16, v136\n"
"v_and_b32 v161, s98, v136\n"
"v_lshlrev_b32 v162, 16, v137\n"
"v_and_b32 v163, s98, v137\n"
"s_waitcnt vmcnt(17)\n"
"v_lshlrev_b32 v164, 16, v138\n"
"v_and_b32 v165, s98, v138\n"
"v_lshlrev_b32 v166, 16, v139\n"
"v_and_b32 v167, s98, v139\n"
"s_waitcnt vmcnt(16)\n"
"v_lshlrev_b32 v168, 16, v140\n"
"v_and_b32 v169, s98, v140\n"
"v_lshlrev_b32 v170, 16, v141\n"
"v_and_b32 v171, s98, v141\n"
"buffer_load_dwordx4 v81, s[40:43], s58 offen offset:1024 lds\n"
"s_waitcnt vmcnt(16)\n"
"v_lshlrev_b32 v172, 16, v142\n"
"v_and_b32 v173, s98, v142\n"
"v_lshlrev_b32 v174, 16, v143\n"
"v_and_b32 v175, s98, v143\n"
"v_pk_add_f32 v[100:101], v[144:145], v[146:147]\n"
"v_pk_add_f32 v[102:103], v[148:149], v[150:151]\n"
"v_pk_add_f32 v[100:101], v[100:101], v[152:153]\n"
"v_pk_add_f32 v[102:103], v[102:103], v[154:155]\n"
"v_pk_add_f32 v[100:101], v[100:101], v[156:157]\n"
"v_pk_add_f32 v[102:103], v[102:103], v[158:159]\n"
"v_pk_add_f32 v[100:101], v[100:101], v[160:161]\n"
"v_pk_add_f32 v[102:103], v[102:103], v[162:163]\n"
"v_pk_add_f32 v[100:101], v[100:101], v[164:165]\n"
"v_pk_add_f32 v[102:103], v[102:103], v[166:167]\n"
"buffer_load_dwordx4 v82, s[40:43], s58 offen offset:2048 lds\n"
"v_pk_add_f32 v[100:101], v[100:101], v[168:169]\n"
"v_pk_add_f32 v[102:103], v[102:103], v[170:171]\n"
"v_pk_add_f32 v[100:101], v[100:101], v[172:173]\n"
"v_pk_add_f32 v[102:103], v[102:103], v[174:175]\n"
"v_pk_add_f32 v[100:101], v[100:101], v[102:103]\n"
"v_add_f32 v100, v100, v101\n"
"s_nop 1\n"
"v_add_f32_dpp v100, v100, v100 row_ror:8 row_mask:0xf bank_mask:0xf\n"
"v_mov_b32 v101, v100\n"
"s_nop 1\n"
"v_permlane16_swap_b32 v100, v101\n"
"v_add_f32 v100, v100, v101\n"
"v_mov_b32 v101, v100\n"
"s_nop 1\n"
"v_permlane32_swap_b32 v100, v101\n"
"v_add_f32 v100, v100, v101\n"
"ds_write_b32 v116, v100 offset:0\n"
"s_waitcnt lgkmcnt(0)\n"
"s_barrier\n"
"ds_read_b128 v[100:103], v117 offset:0\n"
"buffer_load_dwordx4 v83, s[40:43], s58 offen offset:3072 lds\n"
"s_add_u32 s58, s58, 524288\n"
"s_and_b32 s58, s58, 0x7fffff\n"
"s_waitcnt lgkmcnt(0)\n"
"v_pk_add_f32 v[100:101], v[100:101], v[102:103]\n"
"v_add_f32 v100, v100, v101\n"
"v_mul_f32 v100, 0x3a800000, v100\n"
"v_sub_f32 v104, 0, v100\n"
"v_mov_b32 v105, v104\n"
"v_cvt_pk_bf16_f32 v106, v104, v104\n"
"v_lshlrev_b32 v106, 16, v106\n"
"v_pk_add_f32 v[212:213], v[144:145], v[104:105]\n"
"v_pk_add_f32 v[214:215], v[146:147], v[104:105]\n"
"v_pk_add_f32 v[216:217], v[148:149], v[104:105]\n"
"v_pk_add_f32 v[218:219], v[150:151], v[104:105]\n"
"v_pk_add_f32 v[220:221], v[152:153], v[104:105]\n"
"v_pk_add_f32 v[222:223], v[154:155], v[104:105]\n"
"v_pk_add_f32 v[224:225], v[156:157], v[104:105]\n"
"s_add_u32 m0, s34, 8192\n"
"s_nop 0\n"
"buffer_load_dwordx4 v84, s[36:39], s56 offen offset:0 lds\n"
"v_pk_add_f32 v[226:227], v[158:159], v[104:105]\n"
"v_pk_add_f32 v[228:229], v[160:161], v[104:105]\n"
"v_pk_add_f32 v[230:231], v[162:163], v[104:105]\n"
"v_pk_add_f32 v[232:233], v[164:165], v[104:105]\n"
"v_pk_add_f32 v[234:235], v[166:167], v[104:105]\n"
"v_pk_add_f32 v[236:237], v[168:169], v[104:105]\n"
"v_pk_add_f32 v[238:239], v[170:171], v[104:105]\n"
"v_pk_add_f32 v[240:241], v[172:173], v[104:105]\n"
"v_pk_add_f32 v[242:243], v[174:175], v[104:105]\n"
"v_pk_mul_f32 v[212:213], v[212:213], v[212:213]\n"
"v_pk_mul_f32 v[214:215], v[214:215], v[214:215]\n"
"v_pk_mul_f32 v[216:217], v[216:217], v[216:217]\n"
"v_pk_mul_f32 v[218:219], v[218:219], v[218:219]\n"
"v_pk_mul_f32 v[220:221], v[220:221], v[220:221]\n"
"buffer_load_dwordx4 v85, s[36:39], s56 offen offset:1024 lds\n"
"v_pk_mul_f32 v[222:223], v[222:223], v[222:223]\n"
"v_pk_mul_f32 v[224:225], v[224:225], v[224:225]\n"
"v_pk_mul_f32 v[226:227], v[226:227], v[226:227]\n"
"v_pk_mul_f32 v[228:229], v[228:229], v[228:229]\n"
"v_pk_mul_f32 v[230:231], v[230:231], v[230:231]\n"
"v_pk_mul_f32 v[232:233], v[232:233], v[232:233]\n"
"v_pk_mul_f32 v[234:235], v[234:235], v[234:235]\n"
"v_pk_mul_f32 v[236:237], v[236:237], v[236:237]\n"
"v_pk_mul_f32 v[238:239], v[238:239], v[238:239]\n"
"v_pk_mul_f32 v[240:241], v[240:241], v[240:241]\n"
"v_pk_mul_f32 v[242:243], v[242:243], v[242:243]\n"
"v_pk_add_f32 v[100:101], v[212:213], v[214:215]\n"
"v_pk_add_f32 v[102:103], v[216:217], v[218:219]\n"
"v_pk_add_f32 v[100:101], v[100:101], v[220:221]\n"
"buffer_load_dwordx4 v90, s[36:39], s56 offen offset:2048 lds\n"
"v_pk_add_f32 v[102:103], v[102:103], v[222:223]\n"
"v_pk_add_f32 v[100:101], v[100:101], v[224:225]\n"
"v_pk_add_f32 v[102:103], v[102:103], v[226:227]\n"
"v_pk_add_f32 v[100:101], v[100:101], v[228:229]\n"
"v_pk_add_f32 v[102:103], v[102:103], v[230:231]\n"
"v_pk_add_f32 v[100:101], v[100:101], v[232:233]\n"
"v_pk_add_f32 v[102:103], v[102:103], v[234:235]\n"
"v_pk_add_f32 v[100:101], v[100:101], v[236:237]\n"
"v_pk_add_f32 v[102:103], v[102:103], v[238:239]\n"
"v_pk_add_f32 v[100:101], v[100:101], v[240:241]\n"
"v_pk_add_f32 v[102:103], v[102:103], v[242:243]\n"
"v_pk_add_f32 v[100:101], v[100:101], v[102:103]\n"
"v_add_f32 v100, v100, v101\n"
"s_nop 1\n"
"v_add_f32_dpp v100, v100, v100 row_ror:8 row_mask:0xf bank_mask:0xf\n"
"buffer_load_dwordx4 v91, s[36:39], s56 offen offset:3072 lds\n"
"s_add_u32 s56, s56, 128\n"
"s_and_b32 s56, s56, 0x107ff\n"
"v_mov_b32 v101, v100\n"
"s_nop 1\n"
"v_permlane16_swap_b32 v100, v101\n"
"v_add_f32 v100, v100, v101\n"
"v_mov_b32 v101, v100\n"
"s_nop 1\n"
"v_permlane32_swap_b32 v100, v101\n"
"v_add_f32 v100, v100, v101\n"
"ds_write_b32 v116, v100 offset:128\n"
"s_waitcnt lgkmcnt(0)\n"
"s_barrier\n"
"ds_read_b128 v[100:103], v117 offset:128\n"
"s_waitcnt lgkmcnt(0)\n"
"v_pk_add_f32 v[100:101], v[100:101], v[102:103]\n"
"v_add_f32 v100, v100, v101\n"
"v_mul_f32 v100, 0x3a800000, v100\n"
"v_cvt_pk_bf16_f32 v100, v100, v100\n"
"s_add_u32 m0, s34, 12288\n"
"s_nop 0\n"
"buffer_load_dwordx4 v84, s[36:39], s57 offen offset:0 lds\n"
"v_lshlrev_b32 v100, 16, v100\n"
"v_add_f32 v100, 0x3727c5ac, v100\n"
"v_cvt_pk_bf16_f32 v100, v100, v100\n"
"v_lshlrev_b32 v100, 16, v100\n"
"v_sqrt_f32 v100, v100\n"
"s_nop 0\n"
"v_cvt_pk_bf16_f32 v100, v100, v100\n"
"v_lshlrev_b32 v100, 16, v100\n"
"v_rcp_f32 v107, v100\n"
"s_nop 0\n"
"v_pk_add_f32 v[144:145], v[144:145], v[106:107] op_sel_hi:[1,0]\n"
"v_pk_add_f32 v[146:147], v[146:147], v[106:107] op_sel_hi:[1,0]\n"
"v_pk_add_f32 v[148:149], v[148:149], v[106:107] op_sel_hi:[1,0]\n"
"v_pk_add_f32 v[150:151], v[150:151], v[106:107] op_sel_hi:[1,0]\n"
"v_pk_add_f32 v[152:153], v[152:153], v[106:107] op_sel_hi:[1,0]\n"
"v_pk_add_f32 v[154:155], v[154:155], v[106:107] op_sel_hi:[1,0]\n"
"buffer_load_dwordx4 v85, s[36:39], s57 offen offset:1024 lds\n"
"v_pk_add_f32 v[156:157], v[156:157], v[106:107] op_sel_hi:[1,0]\n"
"v_pk_add_f32 v[158:159], v[158:159], v[106:107] op_sel_hi:[1,0]\n"
"v_pk_add_f32 v[160:161], v[160:161], v[106:107] op_sel_hi:[1,0]\n"
"v_pk_add_f32 v[162:163], v[162:163], v[106:107] op_sel_hi:[1,0]\n"
"v_pk_add_f32 v[164:165], v[164:165], v[106:107] op_sel_hi:[1,0]\n"
"v_pk_add_f32 v[166:167], v[166:167], v[106:107] op_sel_hi:[1,0]\n"
"v_pk_add_f32 v[168:169], v[168:169], v[106:107] op_sel_hi:[1,0]\n"
"v_pk_add_f32 v[170:171], v[170:171], v[106:107] op_sel_hi:[1,0]\n"
"v_pk_add_f32 v[172:173], v[172:173], v[106:107] op_sel_hi:[1,0]\n"
"v_pk_add_f32 v[174:175], v[174:175], v[106:107] op_sel_hi:[1,0]\n"
"s_mov_b64 exec, 0xff\n"
"global_store_dwordx2 v118, v[106:107], %0 sc1\n"
"s_mov_b64 exec, -1\n"
"v_cvt_pk_bf16_f32 v128, v144, v145\n"
"buffer_load_dwordx4 v90, s[36:39], s57 offen offset:2048 lds\n"
"v_cvt_pk_bf16_f32 v129, v146, v147\n"
"v_cvt_pk_bf16_f32 v130, v148, v149\n"
"v_cvt_pk_bf16_f32 v131, v150, v151\n"
"v_cvt_pk_bf16_f32 v132, v152, v153\n"
"v_cvt_pk_bf16_f32 v133, v154, v155\n"
"v_cvt_pk_bf16_f32 v134, v156, v157\n"
"v_cvt_pk_bf16_f32 v135, v158, v159\n"
"v_cvt_pk_bf16_f32 v136, v160, v161\n"
"v_cvt_pk_bf16_f32 v137, v162, v163\n"
"v_cvt_pk_bf16_f32 v138, v164, v165\n"
"v_cvt_pk_bf16_f32 v139, v166, v167\n"
"v_cvt_pk_bf16_f32 v140, v168, v169\n"
"v_cvt_pk_bf16_f32 v141, v170, v171\n"
"v_cvt_pk_bf16_f32 v142, v172, v173\n"
"buffer_load_dwordx4 v91, s[36:39], s57 offen offset:3072 lds\n"
"s_add_u32 s57, s57, 128\n"
"s_and_b32 s57, s57, 0x107ff\n"
"v_cvt_pk_bf16_f32 v143, v174, v175\n"
"v_lshlrev_b32 v144, 16, v128\n"
"v_and_b32 v145, s98, v128\n"
"v_lshlrev_b32 v146, 16, v129\n"
"v_and_b32 v147, s98, v129\n"
"v_lshlrev_b32 v148, 16, v130\n"
"v_and_b32 v149, s98, v130\n"
"v_lshlrev_b32 v150, 16, v131\n"
"v_and_b32 v151, s98, v131\n"
"v_pk_mul_f32 v[144:145], v[144:145], v[106:107] op_sel:[0,1]\n"
"v_pk_mul_f32 v[146:147], v[146:147], v[106:107] op_sel:[0,1]\n"
"v_pk_mul_f32 v[148:149], v[148:149], v[106:107] op_sel:[0,1]\n"
"v_pk_mul_f32 v[150:151], v[150:151], v[106:107] op_sel:[0,1]\n"
"v_cvt_pk_bf16_f32 v176, v144, v145\n"
"s_add_u32 m0, s59, 81920\n"
"s_nop 0\n"
"global_load_lds_dwordx4 v95, s[62:63]\n"
"v_cvt_pk_bf16_f32 v177, v146, v147\n"
"v_cvt_pk_bf16_f32 v178, v148, v149\n"
"v_cvt_pk_bf16_f32 v179, v150, v151\n"
"global_store_dwordx4 v112, v[176:179], %0 sc1\n"
"v_lshlrev_b32 v152, 16, v132\n"
"v_and_b32 v153, s98, v132\n"
"v_lshlrev_b32 v154, 16, v133\n"
"v_and_b32 v155, s98, v133\n"
"v_lshlrev_b32 v156, 16, v134\n"
"v_and_b32 v157, s98, v134\n"
"v_lshlrev_b32 v158, 16, v135\n"
"v_and_b32 v159, s98, v135\n"
"v_pk_mul_f32 v[152:153], v[152:153], v[106:107] op_sel:[0,1]\n"
"v_pk_mul_f32 v[154:155], v[154:155], v[106:107] op_sel:[0,1]\n"
"v_pk_mul_f32 v[156:157], v[156:157], v[106:107] op_sel:[0,1]\n"
"v_pk_mul_f32 v[158:159], v[158:159], v[106:107] op_sel:[0,1]\n"
"v_cvt_pk_bf16_f32 v180, v152, v153\n"
"v_cvt_pk_bf16_f32 v181, v154, v155\n"
"v_cvt_pk_bf16_f32 v182, v156, v157\n"
"v_cvt_pk_bf16_f32 v183, v158, v159\n"
"global_store_dwordx4 v113, v[180:183], %0 sc1\n"
"v_lshlrev_b32 v160, 16, v136\n"
"v_and_b32 v161, s98, v136\n"
"v_lshlrev_b32 v162, 16, v137\n"
"v_and_b32 v163, s98, v137\n"
"v_lshlrev_b32 v164, 16, v138\n"
"v_and_b32 v165, s98, v138\n"
"v_lshlrev_b32 v166, 16, v139\n"
"v_and_b32 v167, s98, v139\n"
"v_pk_mul_f32 v[160:161], v[160:161], v[106:107] op_sel:[0,1]\n"
"v_pk_mul_f32 v[162:163], v[162:163], v[106:107] op_sel:[0,1]\n"
"v_pk_mul_f32 v[164:165], v[164:165], v[106:107] op_sel:[0,1]\n"
"v_pk_mul_f32 v[166:167], v[166:167], v[106:107] op_sel:[0,1]\n"
"v_cvt_pk_bf16_f32 v184, v160, v161\n"
"v_cvt_pk_bf16_f32 v185, v162, v163\n"
"v_cvt_pk_bf16_f32 v186, v164, v165\n"
"v_cvt_pk_bf16_f32 v187, v166, v167\n"
"global_store_dwordx4 v114, v[184:187], %0 sc1\n"
"v_lshlrev_b32 v168, 16, v140\n"
"v_and_b32 v169, s98, v140\n"
"v_lshlrev_b32 v170, 16, v141\n"
"v_and_b32 v171, s98, v141\n"
"v_lshlrev_b32 v172, 16, v142\n"
"v_and_b32 v173, s98, v142\n"
"v_lshlrev_b32 v174, 16, v143\n"
"v_and_b32 v175, s98, v143\n"
"v_pk_mul_f32 v[168:169], v[168:169], v[106:107] op_sel:[0,1]\n"
"v_pk_mul_f32 v[170:171], v[170:171], v[106:107] op_sel:[0,1]\n"
"v_pk_mul_f32 v[172:173], v[172:173], v[106:107] op_sel:[0,1]\n"
"v_pk_mul_f32 v[174:175], v[174:175], v[106:107] op_sel:[0,1]\n"
"v_cvt_pk_bf16_f32 v188, v168, v169\n"
"v_cvt_pk_bf16_f32 v189, v170, v171\n"
"v_cvt_pk_bf16_f32 v190, v172, v173\n"
"v_cvt_pk_bf16_f32 v191, v174, v175\n"
"global_store_dwordx4 v115, v[188:191], %0 sc1\n"
"v_xor_b32 v5, 5, v6\n"
"v_lshl_add_u32 v69, v5, 5, v7\n"
"v_xor_b32 v5, 6, v6\n"
"v_lshl_add_u32 v70, v5, 5, v7\n"
"v_xor_b32 v5, 7, v6\n"
"v_lshl_add_u32 v71, v5, 5, v7\n"
"s_lshl_b32 s98, s55, 1\n"
"v_xor_b32 v5, s98, v6\n"
"v_lshl_add_u32 v62, v5, 5, v7\n"
"v_xor_b32 v5, 1, v5\n"
"v_lshl_add_u32 v63, v5, 5, v7\n"
"v_lshrrev_b32 v5, 2, v1\n"
"v_and_b32 v6, 7, v5\n"
"v_and_b32 v7, 3, v1\n"
"v_lshlrev_b32 v7, 3, v7\n"
"v_lshl_add_u32 v7, v5, 8, v7\n"
"v_add_u32 v7, 65536, v7\n"
"v_xor_b32 v5, 0, v6\n"
"v_lshl_add_u32 v12, v5, 5, v7\n"
"v_xor_b32 v5, 1, v6\n"
"v_lshl_add_u32 v13, v5, 5, v7\n"
"v_xor_b32 v5, 2, v6\n"
"v_lshl_add_u32 v14, v5, 5, v7\n"
"v_xor_b32 v5, 3, v6\n"
"v_lshl_add_u32 v15, v5, 5, v7\n"
"v_xor_b32 v5, 4, v6\n"
"v_lshl_add_u32 v16, v5, 5, v7\n"
"v_xor_b32 v5, 5, v6\n"
"v_lshl_add_u32 v17, v5, 5, v7\n"
"v_xor_b32 v5, 6, v6\n"
"v_lshl_add_u32 v18, v5, 5, v7\n"
"v_xor_b32 v5, 7, v6\n"
"v_lshl_add_u32 v19, v5, 5, v7\n"
"v_add_u32 v5, 0, v3\n"
"v_xor_b32 v5, v5, v4\n"
"v_lshlrev_b32 v5, 4, v5\n"
"v_lshl_add_u32 v8, v4, 8, v5\n"
"v_add_u32 v5, 4, v3\n"
"v_xor_b32 v5, v5, v4\n"
"v_lshlrev_b32 v5, 4, v5\n"
"v_lshl_add_u32 v9, v4, 8, v5\n"
"v_add_u32 v5, 8, v3\n"
"v_xor_b32 v5, v5, v4\n"
"v_lshlrev_b32 v5, 4, v5\n"
"v_lshl_add_u32 v10, v4, 8, v5\n"
"v_add_u32 v5, 12, v3\n"
"v_xor_b32 v5, v5, v4\n"
"v_lshlrev_b32 v5, 4, v5\n"
"v_lshl_add_u32 v11, v4, 8, v5\n"
"v_lshlrev_b32 v6, 8, v3\n"
"v_add_u32 v6, s59, v6\n"
"v_add_u32 v5, 0, v3\n"
"v_xor_b32 v5, v5, v4\n"
"v_lshl_add_u32 v20, v5, 4, v6\n"
"v_add_u32 v5, 4, v3\n"
"v_xor_b32 v5, v5, v4\n"
"v_lshl_add_u32 v21, v5, 4, v6\n"
"v_add_u32 v5, 8, v3\n"
"v_xor_b32 v5, v5, v4\n"
"v_lshl_add_u32 v22, v5, 4, v6\n"
"v_add_u32 v5, 12, v3\n"
"v_xor_b32 v5, v5, v4\n"
"v_lshl_add_u32 v23, v5, 4, v6\n"
"v_add_u32 v5, 0, v3\n"
"v_lshlrev_b32 v5, 1, v5\n"
"v_xor_b32 v5, v5, v4\n"
"v_lshl_add_u32 v24, v5, 4, v6\n"
"v_add_u32 v5, 4, v3\n"
"v_lshlrev_b32 v5, 1, v5\n"
"v_xor_b32 v5, v5, v4\n"
"v_lshl_add_u32 v25, v5, 4, v6\n"
"v_add_u32 v5, 0, v3\n"
"v_lshlrev_b32 v5, 1, v5\n"
"v_xor_b32 v5, v5, v4\n"
"v_lshl_add_u32 v26, v5, 4, v6\n"
"v_add_u32 v5, 4, v3\n"
"v_lshlrev_b32 v5, 1, v5\n"
"v_xor_b32 v5, v5, v4\n"
"v_lshl_add_u32 v27, v5, 4, v6\n"
"s_lshl_b32 s65, s55, 11\n"
"v_lshlrev_b32 v6, 8, v3\n"
"v_add_u32 v6, s65, v6\n"
"v_add_u32 v5, 0, v3\n"
"v_lshlrev_b32 v5, 1, v5\n"
"v_xor_b32 v5, v5, v4\n"
"v_lshl_add_u32 v88, v5, 4, v6\n"
"v_add_u32 v5, 4, v3\n"
"v_lshlrev_b32 v5, 1, v5\n"
"v_xor_b32 v5, v5, v4\n"
"v_lshl_add_u32 v89, v5, 4, v6\n"
"s_mov_b32 s69, 0\n"
"s_add_u32 s35, s65, 65536\n"
"v_lshrrev_b32 v6, 1, v4\n"
"v_lshlrev_b32 v7, 7, v4\n"
"v_add_u32 v7, s34, v7\n"
"v_add_u32 v5, 0, v3\n"
"v_xor_b32 v5, v5, v6\n"
"v_lshl_add_u32 v86, v5, 4, v7\n"
"v_add_u32 v5, 4, v3\n"
"v_xor_b32 v5, v5, v6\n"
"v_lshl_add_u32 v87, v5, 4, v7\n"
"v_mov_b32 v56, 0x3f803f80\n"
"v_mov_b32 v57, 0x3f803f80\n"
"v_mov_b32 v58, 0x3f803f80\n"
"v_mov_b32 v59, 0x3f803f80\n"
"s_mov_b32 s64, 0x45800000\n"
"s_mov_b32 s54, 16384\n"
"s_sub_u32 s86, 7340032, %15\n"
"s_mov_b64 s[84:85], %5\n"
"s_sub_u32 s84, s84, s86\n"
"s_subb_u32 s85, s85, 0\n"
"s_mov_b32 s86, 7340032\n"
"s_mov_b32 s87, 0x20000\n"
"s_mov_b64 s[88:89], %6\n"
"s_mov_b32 s90, 8388608\n"
"s_mov_b32 s91, 0x20000\n"
"s_mov_b64 s[92:93], %7\n"
"s_mov_b32 s94, 7340032\n"
"s_mov_b32 s95, 0x20000\n"
"v_add_u32 v37, 42467328, v2\n"
"s_waitcnt vmcnt(19)\n"
"v_readfirstlane_b32 s96, v92\n"
"v_readfirstlane_b32 s97, v93\n"
"s_add_u32 s96, s96, 0x7f4a7c15\n"
"s_addc_u32 s97, s97, 0x9e3779b9\n"
"v_mov_b32 v92, s96\n"
"v_mov_b32 v93, s97\n"
"s_lshl_b32 s61, %17, 3\n"
"s_add_u32 s61, s61, 42467328\n"
"v_mov_b32 v94, s61\n"
"s_mov_b32 s60, 2\n"
"ds_read_b128 a[0:3], v86 offset:0\n"
"ds_read_b128 a[4:7], v87 offset:0\n"
"ds_read_b128 a[8:11], v86 offset:2048\n"
"ds_read_b128 a[12:15], v87 offset:2048\n"
"ds_read_b128 a[16:19], v86 offset:4096\n"
"ds_read_b128 a[20:23], v87 offset:4096\n"
"ds_read_b128 a[24:27], v86 offset:6144\n"
"ds_read_b128 a[28:31], v87 offset:6144\n"
"s_barrier\n"
"ds_read_b64_tr_b16 v[96:97], v64 offset:0\n"
"ds_read_b64_tr_b16 v[98:99], v64 offset:1024\n"
"ds_read_b64_tr_b16 v[100:101], v64 offset:8192\n"
"ds_read_b64_tr_b16 v[102:103], v64 offset:9216\n"
"ds_read_b64_tr_b16 v[104:105], v65 offset:0\n"
"ds_read_b64_tr_b16 v[106:107], v65 offset:1024\n"
"ds_read_b64_tr_b16 v[108:109], v65 offset:8192\n"
"ds_read_b64_tr_b16 v[110:111], v65 offset:9216\n"
"s_waitcnt lgkmcnt(8)\n"
"s_nop 0\n"
"s_add_u32 m0, s34, 0\n"
"s_nop 0\n"
"buffer_load_dwordx4 v84, s[36:39], s56 offen offset:0 lds\n"
"buffer_load_dwordx4 v85, s[36:39], s56 offen offset:1024 lds\n"
"buffer_load_dwordx4 v90, s[36:39], s56 offen offset:2048 lds\n"
"buffer_load_dwordx4 v91, s[36:39], s56 offen offset:3072 lds\n"
"s_add_u32 s56, s56, 128\n"
"s_and_b32 s56, s56, 0x107ff\n"
"s_add_u32 m0, s34, 4096\n"
"s_nop 0\n"
"buffer_load_dwordx4 v84, s[36:39], s57 offen offset:0 lds\n"
"buffer_load_dwordx4 v85, s[36:39], s57 offen offset:1024 lds\n"
"buffer_load_dwordx4 v90, s[36:39], s57 offen offset:2048 lds\n"
"buffer_load_dwordx4 v91, s[36:39], s57 offen offset:3072 lds\n"
"s_add_u32 s57, s57, 128\n"
"s_and_b32 s57, s57, 0x107ff\n"
"s_mov_b32 s63, 0\n"
"s_waitcnt lgkmcnt(4)\n"
"v_mfma_f32_16x16x32_bf16 v[128:131], v[96:99], a[0:3], 0\n"
"ds_read_b64_tr_b16 v[112:113], v66 offset:0\n"
"v_mfma_f32_16x16x32_bf16 v[128:131], v[100:103], a[4:7], v[128:131]\n"
"s_waitcnt vmcnt(13)\n"
"ds_read_b128 a[32:35], v86 offset:8192\n"
"ds_read_b128 a[36:39], v87 offset:8192\n"
"v_mfma_f32_16x16x32_bf16 v[160:163], v[96:99], a[8:11], 0\n"
"ds_read_b64_tr_b16 v[114:115], v66 offset:1024\n"
"v_mfma_f32_16x16x32_bf16 v[160:163], v[100:103], a[12:15], v[160:163]\n"
"ds_read_b128 a[40:43], v86 offset:10240\n"
"ds_read_b128 a[44:47], v87 offset:10240\n"
"v_mfma_f32_16x16x32_bf16 v[192:195], v[96:99], a[16:19], 0\n"
"ds_read_b64_tr_b16 v[116:117], v66 offset:8192\n"
"v_mfma_f32_16x16x32_bf16 v[192:195], v[100:103], a[20:23], v[192:195]\n"
"ds_read_b128 a[48:51], v86 offset:12288\n"
"ds_read_b128 a[52:55], v87 offset:12288\n"
"v_mfma_f32_16x16x32_bf16 v[224:227], v[96:99], a[24:27], 0\n"
"ds_read_b64_tr_b16 v[118:119], v66 offset:9216\n"
"v_mfma_f32_16x16x32_bf16 v[224:227], v[100:103], a[28:31], v[224:227]\n"
"ds_read_b128 a[56:59], v86 offset:14336\n"
"ds_read_b128 a[60:63], v87 offset:14336\n"
"s_waitcnt lgkmcnt(12)\n"
"v_mfma_f32_16x16x32_bf16 v[132:135], v[104:107], a[0:3], 0\n"
"ds_read_b64_tr_b16 v[120:121], v67 offset:0\n"
"s_add_u32 m0, s59, 131072\n"
"v_mfma_f32_16x16x32_bf16 v[132:135], v[108:111], a[4:7], v[132:135]\n"
"buffer_load_dwordx4 v80, s[40:43], s58 offen offset:0 lds\n"
"v_mfma_f32_16x16x32_bf16 v[164:167], v[104:107], a[8:11], 0\n"
"ds_read_b64_tr_b16 v[122:123], v67 offset:1024\n"
"v_mfma_f32_16x16x32_bf16 v[164:167], v[108:111], a[12:15], v[164:167]\n"
"ds_read_b64_tr_b16 v[40:41], v62 offset:0\n"
"ds_read_b64_tr_b16 v[42:43], v62 offset:1024\n"
"v_mfma_f32_16x16x32_bf16 v[196:199], v[104:107], a[16:19], 0\n"
"ds_read_b64_tr_b16 v[124:125], v67 offset:8192\n"
"v_mfma_f32_16x16x32_bf16 v[196:199], v[108:111], a[20:23], v[196:199]\n"
"buffer_load_dwordx4 v81, s[40:43], s58 offen offset:1024 lds\n"
"v_mfma_f32_16x16x32_bf16 v[228:231], v[104:107], a[24:27], 0\n"
"ds_read_b64_tr_b16 v[126:127], v67 offset:9216\n"
"v_mfma_f32_16x16x32_bf16 v[228:231], v[108:111], a[28:31], v[228:231]\n"
"ds_read_b64_tr_b16 v[44:45], v62 offset:8192\n"
"ds_read_b64_tr_b16 v[46:47], v62 offset:9216\n"
"s_waitcnt lgkmcnt(10)\n"
"v_mfma_f32_16x16x32_bf16 v[136:139], v[112:115], a[0:3], 0\n"
"ds_read_b64_tr_b16 v[96:97], v68 offset:0\n"
"v_mfma_f32_16x16x32_bf16 v[136:139], v[116:119], a[4:7], v[136:139]\n"
"buffer_load_dwordx4 v82, s[40:43], s58 offen offset:2048 lds\n"
"v_mfma_f32_16x16x32_bf16 v[168:171], v[112:115], a[8:11], 0\n"
"ds_read_b64_tr_b16 v[98:99], v68 offset:1024\n"
"v_mfma_f32_16x16x32_bf16 v[168:171], v[116:119], a[12:15], v[168:171]\n"
"v_mfma_f32_16x16x32_bf16 v[200:203], v[112:115], a[16:19], 0\n"
"ds_read_b64_tr_b16 v[100:101], v68 offset:8192\n"
"v_mfma_f32_16x16x32_bf16 v[200:203], v[116:119], a[20:23], v[200:203]\n"
"buffer_load_dwordx4 v83, s[40:43], s58 offen offset:3072 lds\n"
"s_add_u32 s58, s58, 524288\n"
"s_and_b32 s58, s58, 0x7fffff\n"
"v_mfma_f32_16x16x32_bf16 v[232:235], v[112:115], a[24:27], 0\n"
"ds_read_b64_tr_b16 v[102:103], v68 offset:9216\n"
"v_mfma_f32_16x16x32_bf16 v[232:235], v[116:119], a[28:31], v[232:235]\n"
"s_waitcnt lgkmcnt(4)\n"
"v_mfma_f32_16x16x32_bf16 v[72:75], v[40:43], v[56:59], 0\n"
"v_mfma_f32_16x16x32_bf16 v[72:75], v[44:47], v[56:59], v[72:75]\n"
"v_mfma_f32_16x16x32_bf16 v[140:143], v[120:123], a[0:3], 0\n"
"ds_read_b64_tr_b16 v[104:105], v69 offset:0\n"
"s_nop 0\n"
"s_add_u32 m0, s34, 8192\n"
"v_mfma_f32_16x16x32_bf16 v[140:143], v[124:127], a[4:7], v[140:143]\n"
"buffer_load_dwordx4 v84, s[36:39], s56 offen offset:0 lds\n"
"v_mfma_f32_16x16x32_bf16 v[172:175], v[120:123], a[8:11], 0\n"
"ds_read_b64_tr_b16 v[106:107], v69 offset:1024\n"
"v_mfma_f32_16x16x32_bf16 v[172:175], v[124:127], a[12:15], v[172:175]\n"
"ds_read_b64_tr_b16 v[48:49], v63 offset:0\n"
"ds_read_b64_tr_b16 v[50:51], v63 offset:1024\n"
"v_mfma_f32_16x16x32_bf16 v[204:207], v[120:123], a[16:19], 0\n"
"ds_read_b64_tr_b16 v[108:109], v69 offset:8192\n"
"v_mfma_f32_16x16x32_bf16 v[204:207], v[124:127], a[20:23], v[204:207]\n"
"buffer_load_dwordx4 v85, s[36:39], s56 offen offset:1024 lds\n"
"v_mfma_f32_16x16x32_bf16 v[236:239], v[120:123], a[24:27], 0\n"
"ds_read_b64_tr_b16 v[110:111], v69 offset:9216\n"
"v_mfma_f32_16x16x32_bf16 v[236:239], v[124:127], a[28:31], v[236:239]\n"
"ds_read_b64_tr_b16 v[52:53], v63 offset:8192\n"
"ds_read_b64_tr_b16 v[54:55], v63 offset:9216\n"
"s_waitcnt lgkmcnt(8)\n"
"v_mfma_f32_16x16x32_bf16 v[144:147], v[96:99], a[0:3], 0\n"
"ds_read_b64_tr_b16 v[112:113], v70 offset:0\n"
"v_mfma_f32_16x16x32_bf16 v[144:147], v[100:103], a[4:7], v[144:147]\n"
"v_mfma_f32_16x16x32_bf16 v[176:179], v[96:99], a[8:11], 0\n"
"ds_read_b64_tr_b16 v[114:115], v70 offset:1024\n"
"v_mfma_f32_16x16x32_bf16 v[176:179], v[100:103], a[12:15], v[176:179]\n"
"buffer_load_dwordx4 v90, s[36:39], s56 offen offset:2048 lds\n"
"v_mfma_f32_16x16x32_bf16 v[208:211], v[96:99], a[16:19], 0\n"
"ds_read_b64_tr_b16 v[116:117], v70 offset:8192\n"
"v_mfma_f32_16x16x32_bf16 v[208:211], v[100:103], a[20:23], v[208:211]\n"
"buffer_load_dwordx4 v91, s[36:39], s56 offen offset:3072 lds\n"
"s_add_u32 s56, s56, 128\n"
"s_and_b32 s56, s56, 0x107ff\n"
"v_mfma_f32_16x16x32_bf16 v[240:243], v[96:99], a[24:27], 0\n"
"ds_read_b64_tr_b16 v[118:119], v70 offset:9216\n"
"v_mfma_f32_16x16x32_bf16 v[240:243], v[100:103], a[28:31], v[240:243]\n"
"s_waitcnt lgkmcnt(4)\n"
"v_mfma_f32_16x16x32_bf16 v[76:79], v[48:51], v[56:59], 0\n"
"v_mfma_f32_16x16x32_bf16 v[76:79], v[52:55], v[56:59], v[76:79]\n"
"v_mfma_f32_16x16x32_bf16 v[148:151], v[104:107], a[0:3], 0\n"
"ds_read_b64_tr_b16 v[120:121], v71 offset:0\n"
"s_add_u32 m0, s34, 12288\n"
"v_mfma_f32_16x16x32_bf16 v[148:151], v[108:111], a[4:7], v[148:151]\n"
"buffer_load_dwordx4 v84, s[36:39], s57 offen offset:0 lds\n"
"v_mfma_f32_16x16x32_bf16 v[180:183], v[104:107], a[8:11], 0\n"
"ds_read_b64_tr_b16 v[122:123], v71 offset:1024\n"
"v_mfma_f32_16x16x32_bf16 v[180:183], v[108:111], a[12:15], v[180:183]\n"
"v_mfma_f32_16x16x32_bf16 v[212:215], v[104:107], a[16:19], 0\n"
"ds_read_b64_tr_b16 v[124:125], v71 offset:8192\n"
"v_mfma_f32_16x16x32_bf16 v[212:215], v[108:111], a[20:23], v[212:215]\n"
"buffer_load_dwordx4 v85, s[36:39], s57 offen offset:1024 lds\n"
"v_mfma_f32_16x16x32_bf16 v[244:247], v[104:107], a[24:27], 0\n"
"ds_read_b64_tr_b16 v[126:127], v71 offset:9216\n"
"v_mfma_f32_16x16x32_bf16 v[244:247], v[108:111], a[28:31], v[244:247]\n"
"s_barrier\n"
"s_waitcnt lgkmcnt(4)\n"
"v_mfma_f32_16x16x32_bf16 v[152:155], v[112:115], a[0:3], 0\n"
"ds_read_b64_tr_b16 v[96:97], v64 offset:16384\n"
"v_mfma_f32_16x16x32_bf16 v[152:155], v[116:119], a[4:7], v[152:155]\n"
"buffer_load_dwordx4 v90, s[36:39], s57 offen offset:2048 lds\n"
"v_mfma_f32_16x16x32_bf16 v[184:187], v[112:115], a[8:11], 0\n"
"ds_read_b64_tr_b16 v[98:99], v64 offset:17408\n"
"v_mfma_f32_16x16x32_bf16 v[184:187], v[116:119], a[12:15], v[184:187]\n"
"buffer_load_dwordx4 v91, s[36:39], s57 offen offset:3072 lds\n"
"s_add_u32 s57, s57, 128\n"
"s_and_b32 s57, s57, 0x107ff\n"
"v_mfma_f32_16x16x32_bf16 v[216:219], v[112:115], a[16:19], 0\n"
"ds_read_b64_tr_b16 v[100:101], v64 offset:24576\n"
"v_mfma_f32_16x16x32_bf16 v[216:219], v[116:119], a[20:23], v[216:219]\n"
"v_mfma_f32_16x16x32_bf16 v[248:251], v[112:115], a[24:27], 0\n"
"ds_read_b64_tr_b16 v[102:103], v64 offset:25600\n"
"v_mfma_f32_16x16x32_bf16 v[248:251], v[116:119], a[28:31], v[248:251]\n"
"s_waitcnt lgkmcnt(4)\n"
"v_mfma_f32_16x16x32_bf16 v[156:159], v[120:123], a[0:3], 0\n"
"ds_read_b64_tr_b16 v[104:105], v65 offset:16384\n"
"v_mfma_f32_16x16x32_bf16 v[156:159], v[124:127], a[4:7], v[156:159]\n"
"v_mfma_f32_16x16x32_bf16 v[188:191], v[120:123], a[8:11], 0\n"
"ds_read_b64_tr_b16 v[106:107], v65 offset:17408\n"
"v_mfma_f32_16x16x32_bf16 v[188:191], v[124:127], a[12:15], v[188:191]\n"
"v_mfma_f32_16x16x32_bf16 v[220:223], v[120:123], a[16:19], 0\n"
"ds_read_b64_tr_b16 v[108:109], v65 offset:24576\n"
"v_mfma_f32_16x16x32_bf16 v[220:223], v[124:127], a[20:23], v[220:223]\n"
"v_mfma_f32_16x16x32_bf16 v[252:255], v[120:123], a[24:27], 0\n"
"ds_read_b64_tr_b16 v[110:111], v65 offset:25600\n"
"v_mfma_f32_16x16x32_bf16 v[252:255], v[124:127], a[28:31], v[252:255]\n"
"s_waitcnt lgkmcnt(4)\n"
"v_mfma_f32_16x16x32_bf16 v[128:131], v[96:99], a[32:35], v[128:131]\n"
"ds_read_b64_tr_b16 v[112:113], v66 offset:16384\n"
"v_mfma_f32_16x16x32_bf16 v[128:131], v[100:103], a[36:39], v[128:131]\n"
"s_waitcnt vmcnt(12)\n"
"ds_read_b128 a[0:3], v86 offset:0\n"
"ds_read_b128 a[4:7], v87 offset:0\n"
"v_mfma_f32_16x16x32_bf16 v[160:163], v[96:99], a[40:43], v[160:163]\n"
"ds_read_b64_tr_b16 v[114:115], v66 offset:17408\n"
"v_mfma_f32_16x16x32_bf16 v[160:163], v[100:103], a[44:47], v[160:163]\n"
"ds_read_b128 a[8:11], v86 offset:2048\n"
"ds_read_b128 a[12:15], v87 offset:2048\n"
"v_mfma_f32_16x16x32_bf16 v[192:195], v[96:99], a[48:51], v[192:195]\n"
"ds_read_b64_tr_b16 v[116:117], v66 offset:24576\n"
"v_mfma_f32_16x16x32_bf16 v[192:195], v[100:103], a[52:55], v[192:195]\n"
"ds_read_b128 a[16:19], v86 offset:4096\n"
"ds_read_b128 a[20:23], v87 offset:4096\n"
"v_mfma_f32_16x16x32_bf16 v[224:227], v[96:99], a[56:59], v[224:227]\n"
"ds_read_b64_tr_b16 v[118:119], v66 offset:25600\n"
"v_mfma_f32_16x16x32_bf16 v[224:227], v[100:103], a[60:63], v[224:227]\n"
"ds_read_b128 a[24:27], v86 offset:6144\n"
"ds_read_b128 a[28:31], v87 offset:6144\n"
"s_waitcnt lgkmcnt(12)\n"
"v_mfma_f32_16x16x32_bf16 v[132:135], v[104:107], a[32:35], v[132:135]\n"
"ds_read_b64_tr_b16 v[120:121], v67 offset:16384\n"
"s_add_u32 m0, s59, 147456\n"
"v_mfma_f32_16x16x32_bf16 v[132:135], v[108:111], a[36:39], v[132:135]\n"
"buffer_load_dwordx4 v80, s[40:43], s58 offen offset:0 lds\n"
"v_mfma_f32_16x16x32_bf16 v[164:167], v[104:107], a[40:43], v[164:167]\n"
"ds_read_b64_tr_b16 v[122:123], v67 offset:17408\n"
"v_mfma_f32_16x16x32_bf16 v[164:167], v[108:111], a[44:47], v[164:167]\n"
"ds_read_b64_tr_b16 v[40:41], v62 offset:16384\n"
"ds_read_b64_tr_b16 v[42:43], v62 offset:17408\n"
"v_mfma_f32_16x16x32_bf16 v[196:199], v[104:107], a[48:51], v[196:199]\n"
"ds_read_b64_tr_b16 v[124:125], v67 offset:24576\n"
"v_mfma_f32_16x16x32_bf16 v[196:199], v[108:111], a[52:55], v[196:199]\n"
"buffer_load_dwordx4 v81, s[40:43], s58 offen offset:1024 lds\n"
"v_mfma_f32_16x16x32_bf16 v[228:231], v[104:107], a[56:59], v[228:231]\n"
"ds_read_b64_tr_b16 v[126:127], v67 offset:25600\n"
"v_mfma_f32_16x16x32_bf16 v[228:231], v[108:111], a[60:63], v[228:231]\n"
"ds_read_b64_tr_b16 v[44:45], v62 offset:24576\n"
"ds_read_b64_tr_b16 v[46:47], v62 offset:25600\n"
"s_waitcnt lgkmcnt(10)\n"
"v_mfma_f32_16x16x32_bf16 v[136:139], v[112:115], a[32:35], v[136:139]\n"
"ds_read_b64_tr_b16 v[96:97], v68 offset:16384\n"
"v_mfma_f32_16x16x32_bf16 v[136:139], v[116:119], a[36:39], v[136:139]\n"
"buffer_load_dwordx4 v82, s[40:43], s58 offen offset:2048 lds\n"
"v_mfma_f32_16x16x32_bf16 v[168:171], v[112:115], a[40:43], v[168:171]\n"
"ds_read_b64_tr_b16 v[98:99], v68 offset:17408\n"
"v_mfma_f32_16x16x32_bf16 v[168:171], v[116:119], a[44:47], v[168:171]\n"
"buffer_load_dwordx4 v83, s[40:43], s58 offen offset:3072 lds\n"
"s_add_u32 s58, s58, 524288\n"
"s_and_b32 s58, s58, 0x7fffff\n"
"v_mfma_f32_16x16x32_bf16 v[200:203], v[112:115], a[48:51], v[200:203]\n"
"ds_read_b64_tr_b16 v[100:101], v68 offset:24576\n"
"s_waitcnt lgkmcnt(11)\n"
"s_nop 0\n"
"s_add_u32 m0, s34, 0\n"
"v_mfma_f32_16x16x32_bf16 v[200:203], v[116:119], a[52:55], v[200:203]\n"
"buffer_load_dwordx4 v84, s[36:39], s56 offen offset:0 lds\n"
"v_mfma_f32_16x16x32_bf16 v[232:235], v[112:115], a[56:59], v[232:235]\n"
"ds_read_b64_tr_b16 v[102:103], v68 offset:25600\n"
"v_mfma_f32_16x16x32_bf16 v[232:235], v[116:119], a[60:63], v[232:235]\n"
"s_waitcnt lgkmcnt(4)\n"
"v_mfma_f32_16x16x32_bf16 v[72:75], v[40:43], v[56:59], v[72:75]\n"
"v_mfma_f32_16x16x32_bf16 v[72:75], v[44:47], v[56:59], v[72:75]\n"
"v_mfma_f32_16x16x32_bf16 v[140:143], v[120:123], a[32:35], v[140:143]\n"
"ds_read_b64_tr_b16 v[104:105], v69 offset:16384\n"
"v_mfma_f32_16x16x32_bf16 v[140:143], v[124:127], a[36:39], v[140:143]\n"
"buffer_load_dwordx4 v85, s[36:39], s56 offen offset:1024 lds\n"
"v_mfma_f32_16x16x32_bf16 v[172:175], v[120:123], a[40:43], v[172:175]\n"
"ds_read_b64_tr_b16 v[106:107], v69 offset:17408\n"
"v_mfma_f32_16x16x32_bf16 v[172:175], v[124:127], a[44:47], v[172:175]\n"
"ds_read_b64_tr_b16 v[48:49], v63 offset:16384\n"
"ds_read_b64_tr_b16 v[50:51], v63 offset:17408\n"
"v_mfma_f32_16x16x32_bf16 v[204:207], v[120:123], a[48:51], v[204:207]\n"
"ds_read_b64_tr_b16 v[108:109], v69 offset:24576\n"
"v_mfma_f32_16x16x32_bf16 v[204:207], v[124:127], a[52:55], v[204:207]\n"
"buffer_load_dwordx4 v90, s[36:39], s56 offen offset:2048 lds\n"
"v_mfma_f32_16x16x32_bf16 v[236:239], v[120:123], a[56:59], v[236:239]\n"
"ds_read_b64_tr_b16 v[110:111], v69 offset:25600\n"
"v_mfma_f32_16x16x32_bf16 v[236:239], v[124:127], a[60:63], v[236:239]\n"
"ds_read_b64_tr_b16 v[52:53], v63 offset:24576\n"
"ds_read_b64_tr_b16 v[54:55], v63 offset:25600\n"
"s_waitcnt lgkmcnt(8)\n"
"v_mfma_f32_16x16x32_bf16 v[144:147], v[96:99], a[32:35], v[144:147]\n"
"ds_read_b64_tr_b16 v[112:113], v70 offset:16384\n"
"v_mfma_f32_16x16x32_bf16 v[144:147], v[100:103], a[36:39], v[144:147]\n"
"buffer_load_dwordx4 v91, s[36:39], s56 offen offset:3072 lds\n"
"s_add_u32 s56, s56, 128\n"
"s_and_b32 s56, s56, 0x107ff\n"
"v_mfma_f32_16x16x32_bf16 v[176:179], v[96:99], a[40:43], v[176:179]\n"
"ds_read_b64_tr_b16 v[114:115], v70 offset:17408\n"
"s_add_u32 m0, s34, 4096\n"
"v_mfma_f32_16x16x32_bf16 v[176:179], v[100:103], a[44:47], v[176:179]\n"
"buffer_load_dwordx4 v84, s[36:39], s57 offen offset:0 lds\n"
"v_mfma_f32_16x16x32_bf16 v[208:211], v[96:99], a[48:51], v[208:211]\n"
"ds_read_b64_tr_b16 v[116:117], v70 offset:24576\n"
"v_mfma_f32_16x16x32_bf16 v[208:211], v[100:103], a[52:55], v[208:211]\n"
"buffer_load_dwordx4 v85, s[36:39], s57 offen offset:1024 lds\n"
"v_mfma_f32_16x16x32_bf16 v[240:243], v[96:99], a[56:59], v[240:243]\n"
"ds_read_b64_tr_b16 v[118:119], v70 offset:25600\n"
"v_mfma_f32_16x16x32_bf16 v[240:243], v[100:103], a[60:63], v[240:243]\n"
"s_waitcnt lgkmcnt(4)\n"
"v_mfma_f32_16x16x32_bf16 v[76:79], v[48:51], v[56:59], v[76:79]\n"
"v_mfma_f32_16x16x32_bf16 v[76:79], v[52:55], v[56:59], v[76:79]\n"
"v_mfma_f32_16x16x32_bf16 v[148:151], v[104:107], a[32:35], v[148:151]\n"
"ds_read_b64_tr_b16 v[120:121], v71 offset:16384\n"
"v_mfma_f32_16x16x32_bf16 v[148:151], v[108:111], a[36:39], v[148:151]\n"
"buffer_load_dwordx4 v90, s[36:39], s57 offen offset:2048 lds\n"
"v_mfma_f32_16x16x32_bf16 v[180:183], v[104:107], a[40:43], v[180:183]\n"
"ds_read_b64_tr_b16 v[122:123], v71 offset:17408\n"
"v_mfma_f32_16x16x32_bf16 v[180:183], v[108:111], a[44:47], v[180:183]\n"
"buffer_load_dwordx4 v91, s[36:39], s57 offen offset:3072 lds\n"
"s_add_u32 s57, s57, 128\n"
"s_and_b32 s57, s57, 0x107ff\n"
"v_mfma_f32_16x16x32_bf16 v[212:215], v[104:107], a[48:51], v[212:215]\n"
"ds_read_b64_tr_b16 v[124:125], v71 offset:24576\n"
"v_mfma_f32_16x16x32_bf16 v[212:215], v[108:111], a[52:55], v[212:215]\n"
"v_mfma_f32_16x16x32_bf16 v[244:247], v[104:107], a[56:59], v[244:247]\n"
"ds_read_b64_tr_b16 v[126:127], v71 offset:25600\n"
"v_mfma_f32_16x16x32_bf16 v[244:247], v[108:111], a[60:63], v[244:247]\n"
"s_waitcnt vmcnt(20)\n"
"s_barrier\n"
"s_waitcnt lgkmcnt(4)\n"
"v_mfma_f32_16x16x32_bf16 v[152:155], v[112:115], a[32:35], v[152:155]\n"
"ds_read_b64_tr_b16 v[96:97], v64 offset:32768\n"
"v_mfma_f32_16x16x32_bf16 v[152:155], v[116:119], a[36:39], v[152:155]\n"
"s_mov_b64 exec, 1\n"
"global_store_dwordx2 v94, v[92:93], %0 sc1\n"
"s_mov_b64 exec, -1\n"
"v_mfma_f32_16x16x32_bf16 v[184:187], v[112:115], a[40:43], v[184:187]\n"
"ds_read_b64_tr_b16 v[98:99], v64 offset:33792\n"
"v_mfma_f32_16x16x32_bf16 v[184:187], v[116:119], a[44:47], v[184:187]\n"
"v_mfma_f32_16x16x32_bf16 v[216:219], v[112:115], a[48:51], v[216:219]\n"
"ds_read_b64_tr_b16 v[100:101], v64 offset:40960\n"
"v_mfma_f32_16x16x32_bf16 v[216:219], v[116:119], a[52:55], v[216:219]\n"
"v_mfma_f32_16x16x32_bf16 v[248:251], v[112:115], a[56:59], v[248:251]\n"
"ds_read_b64_tr_b16 v[102:103], v64 offset:41984\n"
"v_mfma_f32_16x16x32_bf16 v[248:251], v[116:119], a[60:63], v[248:251]\n"
"s_waitcnt lgkmcnt(4)\n"
"v_mfma_f32_16x16x32_bf16 v[156:159], v[120:123], a[32:35], v[156:159]\n"
"ds_read_b64_tr_b16 v[104:105], v65 offset:32768\n"
"v_mfma_f32_16x16x32_bf16 v[156:159], v[124:127], a[36:39], v[156:159]\n"
"v_mfma_f32_16x16x32_bf16 v[188:191], v[120:123], a[40:43], v[188:191]\n"
"ds_read_b64_tr_b16 v[106:107], v65 offset:33792\n"
"v_mfma_f32_16x16x32_bf16 v[188:191], v[124:127], a[44:47], v[188:191]\n"
"v_mfma_f32_16x16x32_bf16 v[220:223], v[120:123], a[48:51], v[220:223]\n"
"ds_read_b64_tr_b16 v[108:109], v65 offset:40960\n"
"v_mfma_f32_16x16x32_bf16 v[220:223], v[124:127], a[52:55], v[220:223]\n"
"v_mfma_f32_16x16x32_bf16 v[252:255], v[120:123], a[56:59], v[252:255]\n"
"ds_read_b64_tr_b16 v[110:111], v65 offset:41984\n"
"v_mfma_f32_16x16x32_bf16 v[252:255], v[124:127], a[60:63], v[252:255]\n"
"s_waitcnt lgkmcnt(4)\n"
"v_mfma_f32_16x16x32_bf16 v[128:131], v[96:99], a[0:3], v[128:131]\n"
"ds_read_b64_tr_b16 v[112:113], v66 offset:32768\n"
"v_accvgpr_write_b32 a64, 0\n"
"v_mfma_f32_16x16x32_bf16 v[128:131], v[100:103], a[4:7], v[128:131]\n"
"s_waitcnt vmcnt(13)\n"
"ds_read_b128 a[32:35], v86 offset:8192\n"
"ds_read_b128 a[36:39], v87 offset:8192\n"
"v_mfma_f32_16x16x32_bf16 v[160:163], v[96:99], a[8:11], v[160:163]\n"
"ds_read_b64_tr_b16 v[114:115], v66 offset:33792\n"
"v_accvgpr_write_b32 a65, 0\n"
"v_mfma_f32_16x16x32_bf16 v[160:163], v[100:103], a[12:15], v[160:163]\n"
"ds_read_b128 a[40:43], v86 offset:10240\n"
"ds_read_b128 a[44:47], v87 offset:10240\n"
"v_mfma_f32_16x16x32_bf16 v[192:195], v[96:99], a[16:19], v[192:195]\n"
"ds_read_b64_tr_b16 v[116:117], v66 offset:40960\n"
"v_accvgpr_write_b32 a66, 0\n"
"v_mfma_f32_16x16x32_bf16 v[192:195], v[100:103], a[20:23], v[192:195]\n"
"ds_read_b128 a[48:51], v86 offset:12288\n"
"ds_read_b128 a[52:55], v87 offset:12288\n"
"v_mfma_f32_16x16x32_bf16 v[224:227], v[96:99], a[24:27], v[224:227]\n"
"ds_read_b64_tr_b16 v[118:119], v66 offset:41984\n"
"v_accvgpr_write_b32 a67, 0\n"
"v_mfma_f32_16x16x32_bf16 v[224:227], v[100:103], a[28:31], v[224:227]\n"
"ds_read_b128 a[56:59], v86 offset:14336\n"
"ds_read_b128 a[60:63], v87 offset:14336\n"
"s_waitcnt lgkmcnt(12)\n"
"v_mfma_f32_16x16x32_bf16 v[132:135], v[104:107], a[0:3], v[132:135]\n"
"ds_read_b64_tr_b16 v[120:121], v67 offset:32768\n"
"s_add_u32 m0, s59, 98304\n"
"v_mfma_f32_16x16x32_bf16 v[132:135], v[108:111], a[4:7], v[132:135]\n"
"buffer_load_dwordx4 v80, s[40:43], s58 offen offset:0 lds\n"
"v_mfma_f32_16x16x32_bf16 v[164:167], v[104:107], a[8:11], v[164:167]\n"
"ds_read_b64_tr_b16 v[122:123], v67 offset:33792\n"
"v_accvgpr_write_b32 a68, 0\n"
"v_mfma_f32_16x16x32_bf16 v[164:167], v[108:111], a[12:15], v[164:167]\n"
"ds_read_b64_tr_b16 v[40:41], v62 offset:32768\n"
"ds_read_b64_tr_b16 v[42:43], v62 offset:33792\n"
"v_mfma_f32_16x16x32_bf16 v[196:199], v[104:107], a[16:19], v[196:199]\n"
"ds_read_b64_tr_b16 v[124:125], v67 offset:40960\n"
"v_accvgpr_write_b32 a69, 0\n"
"v_mfma_f32_16x16x32_bf16 v[196:199], v[108:111], a[20:23], v[196:199]\n"
"buffer_load_dwordx4 v81, s[40:43], s58 offen offset:1024 lds\n"
"v_mfma_f32_16x16x32_bf16 v[228:231], v[104:107], a[24:27], v[228:231]\n"
"ds_read_b64_tr_b16 v[126:127], v67 offset:41984\n"
"v_accvgpr_write_b32 a70, 0\n"
"v_mfma_f32_16x16x32_bf16 v[228:231], v[108:111], a[28:31], v[228:231]\n"
"ds_read_b64_tr_b16 v[44:45], v62 offset:40960\n"
"ds_read_b64_tr_b16 v[46:47], v62 offset:41984\n"
"s_waitcnt lgkmcnt(10)\n"
"v_mfma_f32_16x16x32_bf16 v[136:139], v[112:115], a[0:3], v[136:139]\n"
"ds_read_b64_tr_b16 v[96:97], v68 offset:32768\n"
"v_accvgpr_write_b32 a71, 0\n"
"v_mfma_f32_16x16x32_bf16 v[136:139], v[116:119], a[4:7], v[136:139]\n"
"buffer_load_dwordx4 v82, s[40:43], s58 offen offset:2048 lds\n"
"v_mfma_f32_16x16x32_bf16 v[168:171], v[112:115], a[8:11], v[168:171]\n"
"ds_read_b64_tr_b16 v[98:99], v68 offset:33792\n"
"v_accvgpr_write_b32 a72, 0\n"
"v_mfma_f32_16x16x32_bf16 v[168:171], v[116:119], a[12:15], v[168:171]\n"
"buffer_load_dwordx4 v83, s[40:43], s58 offen offset:3072 lds\n"
"s_add_u32 s58, s58, 524288\n"
"s_and_b32 s58, s58, 0x7fffff\n"
"v_mfma_f32_16x16x32_bf16 v[200:203], v[112:115], a[16:19], v[200:203]\n"
"ds_read_b64_tr_b16 v[100:101], v68 offset:40960\n"
"s_add_u32 m0, s35, s69\n"
"v_mfma_f32_16x16x32_bf16 v[200:203], v[116:119], a[20:23], v[200:203]\n"
"buffer_load_dwordx4 v88, s[48:51], s69 offen offset:0 lds\n"
"v_mfma_f32_16x16x32_bf16 v[232:235], v[112:115], a[24:27], v[232:235]\n"
"ds_read_b64_tr_b16 v[102:103], v68 offset:41984\n"
"v_accvgpr_write_b32 a73, 0\n"
"v_mfma_f32_16x16x32_bf16 v[232:235], v[116:119], a[28:31], v[232:235]\n"
"s_waitcnt lgkmcnt(4)\n"
"v_mfma_f32_16x16x32_bf16 v[72:75], v[40:43], v[56:59], v[72:75]\n"
"v_mfma_f32_16x16x32_bf16 v[72:75], v[44:47], v[56:59], v[72:75]\n"
"v_mfma_f32_16x16x32_bf16 v[140:143], v[120:123], a[0:3], v[140:143]\n"
"ds_read_b64_tr_b16 v[104:105], v69 offset:32768\n"
"v_accvgpr_write_b32 a74, 0\n"
"v_mfma_f32_16x16x32_bf16 v[140:143], v[124:127], a[4:7], v[140:143]\n"
"buffer_load_dwordx4 v89, s[48:51], s69 offen offset:1024 lds\n"
"s_add_u32 s69, s69, 8192\n"
"v_mfma_f32_16x16x32_bf16 v[172:175], v[120:123], a[8:11], v[172:175]\n"
"ds_read_b64_tr_b16 v[106:107], v69 offset:33792\n"
"v_accvgpr_write_b32 a75, 0\n"
"v_mfma_f32_16x16x32_bf16 v[172:175], v[124:127], a[12:15], v[172:175]\n"
"ds_read_b64_tr_b16 v[48:49], v63 offset:32768\n"
"ds_read_b64_tr_b16 v[50:51], v63 offset:33792\n"
"v_mfma_f32_16x16x32_bf16 v[204:207], v[120:123], a[16:19], v[204:207]\n"
"ds_read_b64_tr_b16 v[108:109], v69 offset:40960\n"
"s_nop 0\n"
"s_add_u32 m0, s34, 8192\n"
"v_mfma_f32_16x16x32_bf16 v[204:207], v[124:127], a[20:23], v[204:207]\n"
"buffer_load_dwordx4 v84, s[36:39], s56 offen offset:0 lds\n"
"v_mfma_f32_16x16x32_bf16 v[236:239], v[120:123], a[24:27], v[236:239]\n"
"ds_read_b64_tr_b16 v[110:111], v69 offset:41984\n"
"v_accvgpr_write_b32 a76, 0\n"
"v_mfma_f32_16x16x32_bf16 v[236:239], v[124:127], a[28:31], v[236:239]\n"
"ds_read_b64_tr_b16 v[52:53], v63 offset:40960\n"
"ds_read_b64_tr_b16 v[54:55], v63 offset:41984\n"
"s_waitcnt lgkmcnt(8)\n"
"v_mfma_f32_16x16x32_bf16 v[144:147], v[96:99], a[0:3], v[144:147]\n"
"ds_read_b64_tr_b16 v[112:113], v70 offset:32768\n"
"v_accvgpr_write_b32 a77, 0\n"
"v_mfma_f32_16x16x32_bf16 v[144:147], v[100:103], a[4:7], v[144:147]\n"
"v_accvgpr_write_b32 a78, 0\n"
"v_accvgpr_write_b32 a79, 0\n"
"v_mfma_f32_16x16x32_bf16 v[176:179], v[96:99], a[8:11], v[176:179]\n"
"ds_read_b64_tr_b16 v[114:115], v70 offset:33792\n"
"v_accvgpr_write_b32 a80, 0\n"
"v_mfma_f32_16x16x32_bf16 v[176:179], v[100:103], a[12:15], v[176:179]\n"
"buffer_load_dwordx4 v85, s[36:39], s56 offen offset:1024 lds\n"
"v_mfma_f32_16x16x32_bf16 v[208:211], v[96:99], a[16:19], v[208:211]\n"
"ds_read_b64_tr_b16 v[116:117], v70 offset:40960\n"
"v_accvgpr_write_b32 a81, 0\n"
"v_mfma_f32_16x16x32_bf16 v[208:211], v[100:103], a[20:23], v[208:211]\n"
"buffer_load_dwordx4 v90, s[36:39], s56 offen offset:2048 lds\n"
"v_mfma_f32_16x16x32_bf16 v[240:243], v[96:99], a[24:27], v[240:243]\n"
"ds_read_b64_tr_b16 v[118:119], v70 offset:41984\n"
"v_accvgpr_write_b32 a82, 0\n"
"v_mfma_f32_16x16x32_bf16 v[240:243], v[100:103], a[28:31], v[240:243]\n"
"s_waitcnt lgkmcnt(4)\n"
"v_mfma_f32_16x16x32_bf16 v[76:79], v[48:51], v[56:59], v[76:79]\n"
"v_mfma_f32_16x16x32_bf16 v[76:79], v[52:55], v[56:59], v[76:79]\n"
"v_mfma_f32_16x16x32_bf16 v[148:151], v[104:107], a[0:3], v[148:151]\n"
"ds_read_b64_tr_b16 v[120:121], v71 offset:32768\n"
"v_accvgpr_write_b32 a83, 0\n"
"v_mfma_f32_16x16x32_bf16 v[148:151], v[108:111], a[4:7], v[148:151]\n"
"buffer_load_dwordx4 v91, s[36:39], s56 offen offset:3072 lds\n"
"s_add_u32 s56, s56, 128\n"
"s_and_b32 s56, s56, 0x107ff\n"
"v_mfma_f32_16x16x32_bf16 v[180:183], v[104:107], a[8:11], v[180:183]\n"
"ds_read_b64_tr_b16 v[122:123], v71 offset:33792\n"
"s_add_u32 m0, s34, 12288\n"
"v_mfma_f32_16x16x32_bf16 v[180:183], v[108:111], a[12:15], v[180:183]\n"
"buffer_load_dwordx4 v84, s[36:39], s57 offen offset:0 lds\n"
"v_mfma_f32_16x16x32_bf16 v[212:215], v[104:107], a[16:19], v[212:215]\n"
"ds_read_b64_tr_b16 v[124:125], v71 offset:40960\n"
"v_accvgpr_write_b32 a84, 0\n"
"v_mfma_f32_16x16x32_bf16 v[212:215], v[108:111], a[20:23], v[212:215]\n"
"buffer_load_dwordx4 v85, s[36:39], s57 offen offset:1024 lds\n"
"v_mfma_f32_16x16x32_bf16 v[244:247], v[104:107], a[24:27], v[244:247]\n"
"ds_read_b64_tr_b16 v[126:127], v71 offset:41984\n"
"v_accvgpr_write_b32 a85, 0\n"
"v_mfma_f32_16x16x32_bf16 v[244:247], v[108:111], a[28:31], v[244:247]\n"
"s_waitcnt vmcnt(21)\n"
"s_barrier\n"
"s_waitcnt lgkmcnt(4)\n"
"v_mfma_f32_16x16x32_bf16 v[152:155], v[112:115], a[0:3], v[152:155]\n"
"ds_read_b64_tr_b16 v[96:97], v64 offset:49152\n"
"v_accvgpr_write_b32 a86, 0\n"
"v_mfma_f32_16x16x32_bf16 v[152:155], v[116:119], a[4:7], v[152:155]\n"
"buffer_load_dwordx4 v90, s[36:39], s57 offen offset:2048 lds\n"
"v_mfma_f32_16x16x32_bf16 v[184:187], v[112:115], a[8:11], v[184:187]\n"
"ds_read_b64_tr_b16 v[98:99], v64 offset:50176\n"
"v_accvgpr_write_b32 a87, 0\n"
"v_mfma_f32_16x16x32_bf16 v[184:187], v[116:119], a[12:15], v[184:187]\n"
"buffer_load_dwordx4 v91, s[36:39], s57 offen offset:3072 lds\n"
"s_add_u32 s57, s57, 128\n"
"s_and_b32 s57, s57, 0x107ff\n"
"v_mfma_f32_16x16x32_bf16 v[216:219], v[112:115], a[16:19], v[216:219]\n"
"ds_read_b64_tr_b16 v[100:101], v64 offset:57344\n"
"v_accvgpr_write_b32 a88, 0\n"
"v_mfma_f32_16x16x32_bf16 v[216:219], v[116:119], a[20:23], v[216:219]\n"
"v_accvgpr_write_b32 a89, 0\n"
"v_accvgpr_write_b32 a90, 0\n"
"v_mfma_f32_16x16x32_bf16 v[248:251], v[112:115], a[24:27], v[248:251]\n"
"ds_read_b64_tr_b16 v[102:103], v64 offset:58368\n"
"v_accvgpr_write_b32 a91, 0\n"
"v_mfma_f32_16x16x32_bf16 v[248:251], v[116:119], a[28:31], v[248:251]\n"
"v_accvgpr_write_b32 a92, 0\n"
"v_accvgpr_write_b32 a93, 0\n"
"s_waitcnt lgkmcnt(4)\n"
"v_mfma_f32_16x16x32_bf16 v[156:159], v[120:123], a[0:3], v[156:159]\n"
"ds_read_b64_tr_b16 v[104:105], v65 offset:49152\n"
"v_accvgpr_write_b32 a94, 0\n"
"v_mfma_f32_16x16x32_bf16 v[156:159], v[124:127], a[4:7], v[156:159]\n"
"v_accvgpr_write_b32 a95, 0\n"
"v_mfma_f32_16x16x32_bf16 v[188:191], v[120:123], a[8:11], v[188:191]\n"
"ds_read_b64_tr_b16 v[106:107], v65 offset:50176\n"
"v_mfma_f32_16x16x32_bf16 v[188:191], v[124:127], a[12:15], v[188:191]\n"
"v_mfma_f32_16x16x32_bf16 v[220:223], v[120:123], a[16:19], v[220:223]\n"
"ds_read_b64_tr_b16 v[108:109], v65 offset:57344\n"
"v_mfma_f32_16x16x32_bf16 v[220:223], v[124:127], a[20:23], v[220:223]\n"
"v_mfma_f32_16x16x32_bf16 v[252:255], v[120:123], a[24:27], v[252:255]\n"
"ds_read_b64_tr_b16 v[110:111], v65 offset:58368\n"
"v_mfma_f32_16x16x32_bf16 v[252:255], v[124:127], a[28:31], v[252:255]\n"
"s_waitcnt lgkmcnt(4)\n"
"v_mfma_f32_16x16x32_bf16 v[128:131], v[96:99], a[32:35], v[128:131]\n"
"ds_read_b64_tr_b16 v[112:113], v66 offset:49152\n"
"v_accvgpr_write_b32 a96, 0\n"
"v_mfma_f32_16x16x32_bf16 v[128:131], v[100:103], a[36:39], v[128:131]\n"
"s_waitcnt vmcnt(15)\n"
"ds_read_b128 a[0:3], v86 offset:0\n"
"ds_read_b128 a[4:7], v87 offset:0\n"
"v_mfma_f32_16x16x32_bf16 v[160:163], v[96:99], a[40:43], v[160:163]\n"
"ds_read_b64_tr_b16 v[114:115], v66 offset:50176\n"
"v_accvgpr_write_b32 a97, 0\n"
"v_mfma_f32_16x16x32_bf16 v[160:163], v[100:103], a[44:47], v[160:163]\n"
"ds_read_b128 a[8:11], v86 offset:2048\n"
"ds_read_b128 a[12:15], v87 offset:2048\n"
"v_mfma_f32_16x16x32_bf16 v[192:195], v[96:99], a[48:51], v[192:195]\n"
"ds_read_b64_tr_b16 v[116:117], v66 offset:57344\n"
"v_accvgpr_write_b32 a98, 0\n"
"v_mfma_f32_16x16x32_bf16 v[192:195], v[100:103], a[52:55], v[192:195]\n"
"ds_read_b128 a[16:19], v86 offset:4096\n"
"ds_read_b128 a[20:23], v87 offset:4096\n"
"v_mfma_f32_16x16x32_bf16 v[224:227], v[96:99], a[56:59], v[224:227]\n"
"ds_read_b64_tr_b16 v[118:119], v66 offset:58368\n"
"v_accvgpr_write_b32 a99, 0\n"
"v_mfma_f32_16x16x32_bf16 v[224:227], v[100:103], a[60:63], v[224:227]\n"
"ds_read_b128 a[24:27], v86 offset:6144\n"
"ds_read_b128 a[28:31], v87 offset:6144\n"
"s_waitcnt lgkmcnt(12)\n"
"v_mfma_f32_16x16x32_bf16 v[132:135], v[104:107], a[32:35], v[132:135]\n"
"ds_read_b64_tr_b16 v[120:121], v67 offset:49152\n"
"s_add_u32 m0, s59, 114688\n"
"v_mfma_f32_16x16x32_bf16 v[132:135], v[108:111], a[36:39], v[132:135]\n"
"buffer_load_dwordx4 v80, s[40:43], s58 offen offset:0 lds\n"
"v_mfma_f32_16x16x32_bf16 v[164:167], v[104:107], a[40:43], v[164:167]\n"
"ds_read_b64_tr_b16 v[122:123], v67 offset:50176\n"
"v_accvgpr_write_b32 a100, 0\n"
"v_mfma_f32_16x16x32_bf16 v[164:167], v[108:111], a[44:47], v[164:167]\n"
"ds_read_b64_tr_b16 v[40:41], v62 offset:49152\n"
"ds_read_b64_tr_b16 v[42:43], v62 offset:50176\n"
"v_mfma_f32_16x16x32_bf16 v[196:199], v[104:107], a[48:51], v[196:199]\n"
"ds_read_b64_tr_b16 v[124:125], v67 offset:57344\n"
"v_accvgpr_write_b32 a101, 0\n"
"v_mfma_f32_16x16x32_bf16 v[196:199], v[108:111], a[52:55], v[196:199]\n"
"buffer_load_dwordx4 v81, s[40:43], s58 offen offset:1024 lds\n"
"v_mfma_f32_16x16x32_bf16 v[228:231], v[104:107], a[56:59], v[228:231]\n"
"ds_read_b64_tr_b16 v[126:127], v67 offset:58368\n"
"v_accvgpr_write_b32 a102, 0\n"
"v_mfma_f32_16x16x32_bf16 v[228:231], v[108:111], a[60:63], v[228:231]\n"
"ds_read_b64_tr_b16 v[44:45], v62 offset:57344\n"
"ds_read_b64_tr_b16 v[46:47], v62 offset:58368\n"
"s_waitcnt lgkmcnt(10)\n"
"v_mfma_f32_16x16x32_bf16 v[136:139], v[112:115], a[32:35], v[136:139]\n"
"ds_read_b64_tr_b16 v[96:97], v68 offset:49152\n"
"v_accvgpr_write_b32 a103, 0\n"
"v_mfma_f32_16x16x32_bf16 v[136:139], v[116:119], a[36:39], v[136:139]\n"
"buffer_load_dwordx4 v82, s[40:43], s58 offen offset:2048 lds\n"
"v_mfma_f32_16x16x32_bf16 v[168:171], v[112:115], a[40:43], v[168:171]\n"
"ds_read_b64_tr_b16 v[98:99], v68 offset:50176\n"
"v_accvgpr_write_b32 a104, 0\n"
"v_mfma_f32_16x16x32_bf16 v[168:171], v[116:119], a[44:47], v[168:171]\n"
"v_accvgpr_write_b32 a105, 0\n"
"v_accvgpr_write_b32 a106, 0\n"
"v_mfma_f32_16x16x32_bf16 v[200:203], v[112:115], a[48:51], v[200:203]\n"
"ds_read_b64_tr_b16 v[100:101], v68 offset:57344\n"
"v_accvgpr_write_b32 a107, 0\n"
"v_mfma_f32_16x16x32_bf16 v[200:203], v[116:119], a[52:55], v[200:203]\n"
"buffer_load_dwordx4 v83, s[40:43], s58 offen offset:3072 lds\n"
"s_add_u32 s58, s58, 524288\n"
"s_and_b32 s58, s58, 0x7fffff\n"
"v_mfma_f32_16x16x32_bf16 v[232:235], v[112:115], a[56:59], v[232:235]\n"
"ds_read_b64_tr_b16 v[102:103], v68 offset:58368\n"
"v_accvgpr_write_b32 a108, 0\n"
"v_mfma_f32_16x16x32_bf16 v[232:235], v[116:119], a[60:63], v[232:235]\n"
"s_waitcnt lgkmcnt(4)\n"
"v_mfma_f32_16x16x32_bf16 v[72:75], v[40:43], v[56:59], v[72:75]\n"
"v_mfma_f32_16x16x32_bf16 v[72:75], v[44:47], v[56:59], v[72:75]\n"
"v_mfma_f32_16x16x32_bf16 v[140:143], v[120:123], a[32:35], v[140:143]\n"
"ds_read_b64_tr_b16 v[104:105], v69 offset:49152\n"
"s_nop 0\n"
"s_add_u32 m0, s34, 0\n"
"v_mfma_f32_16x16x32_bf16 v[140:143], v[124:127], a[36:39], v[140:143]\n"
"buffer_load_dwordx4 v84, s[36:39], s56 offen offset:0 lds\n"
"v_mfma_f32_16x16x32_bf16 v[172:175], v[120:123], a[40:43], v[172:175]\n"
"ds_read_b64_tr_b16 v[106:107], v69 offset:50176\n"
"v_accvgpr_write_b32 a109, 0\n"
"v_mfma_f32_16x16x32_bf16 v[172:175], v[124:127], a[44:47], v[172:175]\n"
"ds_read_b64_tr_b16 v[48:49], v63 offset:49152\n"
"ds_read_b64_tr_b16 v[50:51], v63 offset:50176\n"
"v_mfma_f32_16x16x32_bf16 v[204:207], v[120:123], a[48:51], v[204:207]\n"
"ds_read_b64_tr_b16 v[108:109], v69 offset:57344\n"
"v_accvgpr_write_b32 a110, 0\n"
"v_mfma_f32_16x16x32_bf16 v[204:207], v[124:127], a[52:55], v[204:207]\n"
"buffer_load_dwordx4 v85, s[36:39], s56 offen offset:1024 lds\n"
"v_mfma_f32_16x16x32_bf16 v[236:239], v[120:123], a[56:59], v[236:239]\n"
"ds_read_b64_tr_b16 v[110:111], v69 offset:58368\n"
"v_accvgpr_write_b32 a111, 0\n"
"v_mfma_f32_16x16x32_bf16 v[236:239], v[124:127], a[60:63], v[236:239]\n"
"ds_read_b64_tr_b16 v[52:53], v63 offset:57344\n"
"ds_read_b64_tr_b16 v[54:55], v63 offset:58368\n"
"s_waitcnt lgkmcnt(8)\n"
"v_mfma_f32_16x16x32_bf16 v[144:147], v[96:99], a[32:35], v[144:147]\n"
"ds_read_b64_tr_b16 v[112:113], v70 offset:49152\n"
"v_accvgpr_write_b32 a112, 0\n"
"v_mfma_f32_16x16x32_bf16 v[144:147], v[100:103], a[36:39], v[144:147]\n"
"v_accvgpr_write_b32 a113, 0\n"
"v_accvgpr_write_b32 a114, 0\n"
"v_mfma_f32_16x16x32_bf16 v[176:179], v[96:99], a[40:43], v[176:179]\n"
"ds_read_b64_tr_b16 v[114:115], v70 offset:50176\n"
"v_accvgpr_write_b32 a115, 0\n"
"v_mfma_f32_16x16x32_bf16 v[176:179], v[100:103], a[44:47], v[176:179]\n"
"buffer_load_dwordx4 v90, s[36:39], s56 offen offset:2048 lds\n"
"v_mfma_f32_16x16x32_bf16 v[208:211], v[96:99], a[48:51], v[208:211]\n"
"ds_read_b64_tr_b16 v[116:117], v70 offset:57344\n"
"v_accvgpr_write_b32 a116, 0\n"
"v_mfma_f32_16x16x32_bf16 v[208:211], v[100:103], a[52:55], v[208:211]\n"
"buffer_load_dwordx4 v91, s[36:39], s56 offen offset:3072 lds\n"
"s_add_u32 s56, s56, 128\n"
"s_and_b32 s56, s56, 0x107ff\n"
"v_mfma_f32_16x16x32_bf16 v[240:243], v[96:99], a[56:59], v[240:243]\n"
"ds_read_b64_tr_b16 v[118:119], v70 offset:58368\n"
"v_accvgpr_write_b32 a117, 0\n"
"v_mfma_f32_16x16x32_bf16 v[240:243], v[100:103], a[60:63], v[240:243]\n"
"s_waitcnt lgkmcnt(4)\n"
"v_mfma_f32_16x16x32_bf16 v[76:79], v[48:51], v[56:59], v[76:79]\n"
"v_mfma_f32_16x16x32_bf16 v[76:79], v[52:55], v[56:59], v[76:79]\n"
"v_mfma_f32_16x16x32_bf16 v[148:151], v[104:107], a[32:35], v[148:151]\n"
"ds_read_b64_tr_b16 v[120:121], v71 offset:49152\n"
"s_add_u32 m0, s34, 4096\n"
"v_mfma_f32_16x16x32_bf16 v[148:151], v[108:111], a[36:39], v[148:151]\n"
"buffer_load_dwordx4 v84, s[36:39], s57 offen offset:0 lds\n"
"v_mfma_f32_16x16x32_bf16 v[180:183], v[104:107], a[40:43], v[180:183]\n"
"ds_read_b64_tr_b16 v[122:123], v71 offset:50176\n"
"v_accvgpr_write_b32 a118, 0\n"
"v_mfma_f32_16x16x32_bf16 v[180:183], v[108:111], a[44:47], v[180:183]\n"
"v_accvgpr_write_b32 a119, 0\n"
"v_accvgpr_write_b32 a120, 0\n"
"v_mfma_f32_16x16x32_bf16 v[212:215], v[104:107], a[48:51], v[212:215]\n"
"ds_read_b64_tr_b16 v[124:125], v71 offset:57344\n"
"v_accvgpr_write_b32 a121, 0\n"
"v_mfma_f32_16x16x32_bf16 v[212:215], v[108:111], a[52:55], v[212:215]\n"
"buffer_load_dwordx4 v85, s[36:39], s57 offen offset:1024 lds\n"
"v_mfma_f32_16x16x32_bf16 v[244:247], v[104:107], a[56:59], v[244:247]\n"
"ds_read_b64_tr_b16 v[126:127], v71 offset:58368\n"
"v_accvgpr_write_b32 a122, 0\n"
"v_mfma_f32_16x16x32_bf16 v[244:247], v[108:111], a[60:63], v[244:247]\n"
"s_waitcnt vmcnt(20)\n"
"s_barrier\n"
"s_waitcnt lgkmcnt(4)\n"
"v_mfma_f32_16x16x32_bf16 v[152:155], v[112:115], a[32:35], v[152:155]\n"
"ds_read_b64_tr_b16 v[96:97], v64 offset:0\n"
"v_accvgpr_write_b32 a123, 0\n"
"v_mfma_f32_16x16x32_bf16 v[152:155], v[116:119], a[36:39], v[152:155]\n"
"buffer_load_dwordx4 v90, s[36:39], s57 offen offset:2048 lds\n"
"v_mfma_f32_16x16x32_bf16 v[184:187], v[112:115], a[40:43], v[184:187]\n"
"ds_read_b64_tr_b16 v[98:99], v64 offset:1024\n"
"v_accvgpr_write_b32 a124, 0\n"
"v_mfma_f32_16x16x32_bf16 v[184:187], v[116:119], a[44:47], v[184:187]\n"
"buffer_load_dwordx4 v91, s[36:39], s57 offen offset:3072 lds\n"
"s_add_u32 s57, s57, 128\n"
"s_and_b32 s57, s57, 0x107ff\n"
"v_mfma_f32_16x16x32_bf16 v[216:219], v[112:115], a[48:51], v[216:219]\n"
"ds_read_b64_tr_b16 v[100:101], v64 offset:8192\n"
"v_accvgpr_write_b32 a125, 0\n"
"v_mfma_f32_16x16x32_bf16 v[216:219], v[116:119], a[52:55], v[216:219]\n"
"v_accvgpr_write_b32 a126, 0\n"
"v_accvgpr_write_b32 a127, 0\n"
"v_mfma_f32_16x16x32_bf16 v[248:251], v[112:115], a[56:59], v[248:251]\n"
"ds_read_b64_tr_b16 v[102:103], v64 offset:9216\n"
"v_mfma_f32_16x16x32_bf16 v[248:251], v[116:119], a[60:63], v[248:251]\n"
"s_waitcnt lgkmcnt(4)\n"
"v_mfma_f32_16x16x32_bf16 v[156:159], v[120:123], a[32:35], v[156:159]\n"
"ds_read_b64_tr_b16 v[104:105], v65 offset:0\n"
"v_mfma_f32_16x16x32_bf16 v[156:159], v[124:127], a[36:39], v[156:159]\n"
"v_mfma_f32_16x16x32_bf16 v[188:191], v[120:123], a[40:43], v[188:191]\n"
"ds_read_b64_tr_b16 v[106:107], v65 offset:1024\n"
"v_mfma_f32_16x16x32_bf16 v[188:191], v[124:127], a[44:47], v[188:191]\n"
"v_mfma_f32_16x16x32_bf16 v[220:223], v[120:123], a[48:51], v[220:223]\n"
"ds_read_b64_tr_b16 v[108:109], v65 offset:8192\n"
"v_mfma_f32_16x16x32_bf16 v[220:223], v[124:127], a[52:55], v[220:223]\n"
"v_mfma_f32_16x16x32_bf16 v[252:255], v[120:123], a[56:59], v[252:255]\n"
"ds_read_b64_tr_b16 v[110:111], v65 offset:9216\n"
"v_mfma_f32_16x16x32_bf16 v[252:255], v[124:127], a[60:63], v[252:255]\n"
"AQ_LOOP:\n"
"s_waitcnt lgkmcnt(4)\n"
"v_mfma_f32_16x16x32_bf16 v[128:131], v[96:99], a[0:3], v[128:131]\n"
"ds_read_b64_tr_b16 v[112:113], v66 offset:0\n"
"v_mfma_f32_16x16x32_bf16 v[128:131], v[100:103], a[4:7], v[128:131]\n"
"s_waitcnt vmcnt(12)\n"
"ds_read_b128 a[32:35], v86 offset:8192\n"
"ds_read_b128 a[36:39], v87 offset:8192\n"
"v_mfma_f32_16x16x32_bf16 v[160:163], v[96:99], a[8:11], v[160:163]\n"
"ds_read_b64_tr_b16 v[114:115], v66 offset:1024\n"
"v_mfma_f32_16x16x32_bf16 v[160:163], v[100:103], a[12:15], v[160:163]\n"
"ds_read_b128 a[40:43], v86 offset:10240\n"
"ds_read_b128 a[44:47], v87 offset:10240\n"
"v_mfma_f32_16x16x32_bf16 v[192:195], v[96:99], a[16:19], v[192:195]\n"
"ds_read_b64_tr_b16 v[116:117], v66 offset:8192\n"
"v_mfma_f32_16x16x32_bf16 v[192:195], v[100:103], a[20:23], v[192:195]\n"
"ds_read_b128 a[48:51], v86 offset:12288\n"
"ds_read_b128 a[52:55], v87 offset:12288\n"
"v_mfma_f32_16x16x32_bf16 v[224:227], v[96:99], a[24:27], v[224:227]\n"
"ds_read_b64_tr_b16 v[118:119], v66 offset:9216\n"
"v_mfma_f32_16x16x32_bf16 v[224:227], v[100:103], a[28:31], v[224:227]\n"
"ds_read_b128 a[56:59], v86 offset:14336\n"
"ds_read_b128 a[60:63], v87 offset:14336\n"
"s_waitcnt lgkmcnt(12)\n"
"v_mfma_f32_16x16x32_bf16 v[132:135], v[104:107], a[0:3], v[132:135]\n"
"ds_read_b64_tr_b16 v[120:121], v67 offset:0\n"
"s_add_u32 m0, s59, 131072\n"
"v_mfma_f32_16x16x32_bf16 v[132:135], v[108:111], a[4:7], v[132:135]\n"
"buffer_load_dwordx4 v80, s[40:43], s58 offen offset:0 lds\n"
"v_mfma_f32_16x16x32_bf16 v[164:167], v[104:107], a[8:11], v[164:167]\n"
"ds_read_b64_tr_b16 v[122:123], v67 offset:1024\n"
"v_mfma_f32_16x16x32_bf16 v[164:167], v[108:111], a[12:15], v[164:167]\n"
"ds_read_b64_tr_b16 v[40:41], v62 offset:0\n"
"ds_read_b64_tr_b16 v[42:43], v62 offset:1024\n"
"v_mfma_f32_16x16x32_bf16 v[196:199], v[104:107], a[16:19], v[196:199]\n"
"ds_read_b64_tr_b16 v[124:125], v67 offset:8192\n"
"v_mfma_f32_16x16x32_bf16 v[196:199], v[108:111], a[20:23], v[196:199]\n"
"buffer_load_dwordx4 v81, s[40:43], s58 offen offset:1024 lds\n"
"v_mfma_f32_16x16x32_bf16 v[228:231], v[104:107], a[24:27], v[228:231]\n"
"ds_read_b64_tr_b16 v[126:127], v67 offset:9216\n"
"v_mfma_f32_16x16x32_bf16 v[228:231], v[108:111], a[28:31], v[228:231]\n"
"ds_read_b64_tr_b16 v[44:45], v62 offset:8192\n"
"ds_read_b64_tr_b16 v[46:47], v62 offset:9216\n"
"s_waitcnt lgkmcnt(10)\n"
"v_mfma_f32_16x16x32_bf16 v[136:139], v[112:115], a[0:3], v[136:139]\n"
"ds_read_b64_tr_b16 v[96:97], v68 offset:0\n"
"v_mfma_f32_16x16x32_bf16 v[136:139], v[116:119], a[4:7], v[136:139]\n"
"buffer_load_dwordx4 v82, s[40:43], s58 offen offset:2048 lds\n"
"v_mfma_f32_16x16x32_bf16 v[168:171], v[112:115], a[8:11], v[168:171]\n"
"ds_read_b64_tr_b16 v[98:99], v68 offset:1024\n"
"v_mfma_f32_16x16x32_bf16 v[168:171], v[116:119], a[12:15], v[168:171]\n"
"v_mfma_f32_16x16x32_bf16 v[200:203], v[112:115], a[16:19], v[200:203]\n"
"ds_read_b64_tr_b16 v[100:101], v68 offset:8192\n"
"v_mfma_f32_16x16x32_bf16 v[200:203], v[116:119], a[20:23], v[200:203]\n"
"buffer_load_dwordx4 v83, s[40:43], s58 offen offset:3072 lds\n"
"s_add_u32 s58, s58, 524288\n"
"s_and_b32 s58, s58, 0x7fffff\n"
"v_mfma_f32_16x16x32_bf16 v[232:235], v[112:115], a[24:27], v[232:235]\n"
"ds_read_b64_tr_b16 v[102:103], v68 offset:9216\n"
"v_mfma_f32_16x16x32_bf16 v[232:235], v[116:119], a[28:31], v[232:235]\n"
"s_waitcnt lgkmcnt(4)\n"
"v_mfma_f32_16x16x32_bf16 v[72:75], v[40:43], v[56:59], v[72:75]\n"
"v_mfma_f32_16x16x32_bf16 v[72:75], v[44:47], v[56:59], v[72:75]\n"
"v_mfma_f32_16x16x32_bf16 v[140:143], v[120:123], a[0:3], v[140:143]\n"
"ds_read_b64_tr_b16 v[104:105], v69 offset:0\n"
"s_nop 0\n"
"s_add_u32 m0, s34, 8192\n"
"v_mfma_f32_16x16x32_bf16 v[140:143], v[124:127], a[4:7], v[140:143]\n"
"buffer_load_dwordx4 v84, s[36:39], s56 offen offset:0 lds\n"
"v_mfma_f32_16x16x32_bf16 v[172:175], v[120:123], a[8:11], v[172:175]\n"
"ds_read_b64_tr_b16 v[106:107], v69 offset:1024\n"
"v_mfma_f32_16x16x32_bf16 v[172:175], v[124:127], a[12:15], v[172:175]\n"
"ds_read_b64_tr_b16 v[48:49], v63 offset:0\n"
"ds_read_b64_tr_b16 v[50:51], v63 offset:1024\n"
"v_mfma_f32_16x16x32_bf16 v[204:207], v[120:123], a[16:19], v[204:207]\n"
"ds_read_b64_tr_b16 v[108:109], v69 offset:8192\n"
"v_mfma_f32_16x16x32_bf16 v[204:207], v[124:127], a[20:23], v[204:207]\n"
"buffer_load_dwordx4 v85, s[36:39], s56 offen offset:1024 lds\n"
"v_mfma_f32_16x16x32_bf16 v[236:239], v[120:123], a[24:27], v[236:239]\n"
"ds_read_b64_tr_b16 v[110:111], v69 offset:9216\n"
"v_mfma_f32_16x16x32_bf16 v[236:239], v[124:127], a[28:31], v[236:239]\n"
"ds_read_b64_tr_b16 v[52:53], v63 offset:8192\n"
"ds_read_b64_tr_b16 v[54:55], v63 offset:9216\n"
"s_waitcnt lgkmcnt(8)\n"
"v_mfma_f32_16x16x32_bf16 v[144:147], v[96:99], a[0:3], v[144:147]\n"
"ds_read_b64_tr_b16 v[112:113], v70 offset:0\n"
"v_mfma_f32_16x16x32_bf16 v[144:147], v[100:103], a[4:7], v[144:147]\n"
"v_mfma_f32_16x16x32_bf16 v[176:179], v[96:99], a[8:11], v[176:179]\n"
"ds_read_b64_tr_b16 v[114:115], v70 offset:1024\n"
"v_mfma_f32_16x16x32_bf16 v[176:179], v[100:103], a[12:15], v[176:179]\n"
"buffer_load_dwordx4 v90, s[36:39], s56 offen offset:2048 lds\n"
"v_mfma_f32_16x16x32_bf16 v[208:211], v[96:99], a[16:19], v[208:211]\n"
"ds_read_b64_tr_b16 v[116:117], v70 offset:8192\n"
"v_mfma_f32_16x16x32_bf16 v[208:211], v[100:103], a[20:23], v[208:211]\n"
"buffer_load_dwordx4 v91, s[36:39], s56 offen offset:3072 lds\n"
"s_add_u32 s56, s56, 128\n"
"s_and_b32 s56, s56, 0x107ff\n"
"v_mfma_f32_16x16x32_bf16 v[240:243], v[96:99], a[24:27], v[240:243]\n"
"ds_read_b64_tr_b16 v[118:119], v70 offset:9216\n"
"v_mfma_f32_16x16x32_bf16 v[240:243], v[100:103], a[28:31], v[240:243]\n"
"s_waitcnt lgkmcnt(4)\n"
"v_mfma_f32_16x16x32_bf16 v[76:79], v[48:51], v[56:59], v[76:79]\n"
"v_mfma_f32_16x16x32_bf16 v[76:79], v[52:55], v[56:59], v[76:79]\n"
"v_mfma_f32_16x16x32_bf16 v[148:151], v[104:107], a[0:3], v[148:151]\n"
"ds_read_b64_tr_b16 v[120:121], v71 offset:0\n"
"s_add_u32 m0, s34, 12288\n"
"v_mfma_f32_16x16x32_bf16 v[148:151], v[108:111], a[4:7], v[148:151]\n"
"buffer_load_dwordx4 v84, s[36:39], s57 offen offset:0 lds\n"
"v_mfma_f32_16x16x32_bf16 v[180:183], v[104:107], a[8:11], v[180:183]\n"
"ds_read_b64_tr_b16 v[122:123], v71 offset:1024\n"
"v_mfma_f32_16x16x32_bf16 v[180:183], v[108:111], a[12:15], v[180:183]\n"
"v_mfma_f32_16x16x32_bf16 v[212:215], v[104:107], a[16:19], v[212:215]\n"
"ds_read_b64_tr_b16 v[124:125], v71 offset:8192\n"
"v_mfma_f32_16x16x32_bf16 v[212:215], v[108:111], a[20:23], v[212:215]\n"
"buffer_load_dwordx4 v85, s[36:39], s57 offen offset:1024 lds\n"
"v_mfma_f32_16x16x32_bf16 v[244:247], v[104:107], a[24:27], v[244:247]\n"
"ds_read_b64_tr_b16 v[126:127], v71 offset:9216\n"
"v_mfma_f32_16x16x32_bf16 v[244:247], v[108:111], a[28:31], v[244:247]\n"
"s_waitcnt vmcnt(18)\n"
"s_barrier\n"
"s_waitcnt lgkmcnt(4)\n"
"v_mfma_f32_16x16x32_bf16 v[152:155], v[112:115], a[0:3], v[152:155]\n"
"ds_read_b64_tr_b16 v[96:97], v64 offset:16384\n"
"v_mfma_f32_16x16x32_bf16 v[152:155], v[116:119], a[4:7], v[152:155]\n"
"buffer_load_dwordx4 v90, s[36:39], s57 offen offset:2048 lds\n"
"v_mfma_f32_16x16x32_bf16 v[184:187], v[112:115], a[8:11], v[184:187]\n"
"ds_read_b64_tr_b16 v[98:99], v64 offset:17408\n"
"v_mfma_f32_16x16x32_bf16 v[184:187], v[116:119], a[12:15], v[184:187]\n"
"buffer_load_dwordx4 v91, s[36:39], s57 offen offset:3072 lds\n"
"s_add_u32 s57, s57, 128\n"
"s_and_b32 s57, s57, 0x107ff\n"
"v_mfma_f32_16x16x32_bf16 v[216:219], v[112:115], a[16:19], v[216:219]\n"
"ds_read_b64_tr_b16 v[100:101], v64 offset:24576\n"
"v_mfma_f32_16x16x32_bf16 v[216:219], v[116:119], a[20:23], v[216:219]\n"
"v_mfma_f32_16x16x32_bf16 v[248:251], v[112:115], a[24:27], v[248:251]\n"
"ds_read_b64_tr_b16 v[102:103], v64 offset:25600\n"
"v_mfma_f32_16x16x32_bf16 v[248:251], v[116:119], a[28:31], v[248:251]\n"
"s_waitcnt lgkmcnt(4)\n"
"v_mfma_f32_16x16x32_bf16 v[156:159], v[120:123], a[0:3], v[156:159]\n"
"ds_read_b64_tr_b16 v[104:105], v65 offset:16384\n"
"v_mfma_f32_16x16x32_bf16 v[156:159], v[124:127], a[4:7], v[156:159]\n"
"v_mfma_f32_16x16x32_bf16 v[188:191], v[120:123], a[8:11], v[188:191]\n"
"ds_read_b64_tr_b16 v[106:107], v65 offset:17408\n"
"v_mfma_f32_16x16x32_bf16 v[188:191], v[124:127], a[12:15], v[188:191]\n"
"v_mfma_f32_16x16x32_bf16 v[220:223], v[120:123], a[16:19], v[220:223]\n"
"ds_read_b64_tr_b16 v[108:109], v65 offset:24576\n"
"v_mfma_f32_16x16x32_bf16 v[220:223], v[124:127], a[20:23], v[220:223]\n"
"v_mfma_f32_16x16x32_bf16 v[252:255], v[120:123], a[24:27], v[252:255]\n"
"ds_read_b64_tr_b16 v[110:111], v65 offset:25600\n"
"v_mfma_f32_16x16x32_bf16 v[252:255], v[124:127], a[28:31], v[252:255]\n"
"s_waitcnt lgkmcnt(4)\n"
"v_mfma_f32_16x16x32_bf16 v[128:131], v[96:99], a[32:35], v[128:131]\n"
"ds_read_b64_tr_b16 v[112:113], v66 offset:16384\n"
"v_mfma_f32_16x16x32_bf16 v[128:131], v[100:103], a[36:39], v[128:131]\n"
"s_waitcnt vmcnt(12)\n"
"ds_read_b128 a[0:3], v86 offset:0\n"
"ds_read_b128 a[4:7], v87 offset:0\n"
"v_mfma_f32_16x16x32_bf16 v[160:163], v[96:99], a[40:43], v[160:163]\n"
"ds_read_b64_tr_b16 v[114:115], v66 offset:17408\n"
"v_mfma_f32_16x16x32_bf16 v[160:163], v[100:103], a[44:47], v[160:163]\n"
"ds_read_b128 a[8:11], v86 offset:2048\n"
"ds_read_b128 a[12:15], v87 offset:2048\n"
"v_mfma_f32_16x16x32_bf16 v[192:195], v[96:99], a[48:51], v[192:195]\n"
"ds_read_b64_tr_b16 v[116:117], v66 offset:24576\n"
"v_mfma_f32_16x16x32_bf16 v[192:195], v[100:103], a[52:55], v[192:195]\n"
"ds_read_b128 a[16:19], v86 offset:4096\n"
"ds_read_b128 a[20:23], v87 offset:4096\n"
"v_mfma_f32_16x16x32_bf16 v[224:227], v[96:99], a[56:59], v[224:227]\n"
"ds_read_b64_tr_b16 v[118:119], v66 offset:25600\n"
"v_mfma_f32_16x16x32_bf16 v[224:227], v[100:103], a[60:63], v[224:227]\n"
"ds_read_b128 a[24:27], v86 offset:6144\n"
"ds_read_b128 a[28:31], v87 offset:6144\n"
"s_waitcnt lgkmcnt(12)\n"
"v_mfma_f32_16x16x32_bf16 v[132:135], v[104:107], a[32:35], v[132:135]\n"
"ds_read_b64_tr_b16 v[120:121], v67 offset:16384\n"
"s_add_u32 m0, s59, 147456\n"
"v_mfma_f32_16x16x32_bf16 v[132:135], v[108:111], a[36:39], v[132:135]\n"
"buffer_load_dwordx4 v80, s[40:43], s58 offen offset:0 lds\n"
"v_mfma_f32_16x16x32_bf16 v[164:167], v[104:107], a[40:43], v[164:167]\n"
"ds_read_b64_tr_b16 v[122:123], v67 offset:17408\n"
"v_mfma_f32_16x16x32_bf16 v[164:167], v[108:111], a[44:47], v[164:167]\n"
"ds_read_b64_tr_b16 v[40:41], v62 offset:16384\n"
"ds_read_b64_tr_b16 v[42:43], v62 offset:17408\n"
"v_mfma_f32_16x16x32_bf16 v[196:199], v[104:107], a[48:51], v[196:199]\n"
"ds_read_b64_tr_b16 v[124:125], v67 offset:24576\n"
"v_mfma_f32_16x16x32_bf16 v[196:199], v[108:111], a[52:55], v[196:199]\n"
"buffer_load_dwordx4 v81, s[40:43], s58 offen offset:1024 lds\n"
"v_mfma_f32_16x16x32_bf16 v[228:231], v[104:107], a[56:59], v[228:231]\n"
"ds_read_b64_tr_b16 v[126:127], v67 offset:25600\n"
"v_mfma_f32_16x16x32_bf16 v[228:231], v[108:111], a[60:63], v[228:231]\n"
"ds_read_b64_tr_b16 v[44:45], v62 offset:24576\n"
"ds_read_b64_tr_b16 v[46:47], v62 offset:25600\n"
"s_waitcnt lgkmcnt(10)\n"
"v_mfma_f32_16x16x32_bf16 v[136:139], v[112:115], a[32:35], v[136:139]\n"
"ds_read_b64_tr_b16 v[96:97], v68 offset:16384\n"
"v_mfma_f32_16x16x32_bf16 v[136:139], v[116:119], a[36:39], v[136:139]\n"
"buffer_load_dwordx4 v82, s[40:43], s58 offen offset:2048 lds\n"
"v_mfma_f32_16x16x32_bf16 v[168:171], v[112:115], a[40:43], v[168:171]\n"
"ds_read_b64_tr_b16 v[98:99], v68 offset:17408\n"
"v_mfma_f32_16x16x32_bf16 v[168:171], v[116:119], a[44:47], v[168:171]\n"
"v_mfma_f32_16x16x32_bf16 v[200:203], v[112:115], a[48:51], v[200:203]\n"
"ds_read_b64_tr_b16 v[100:101], v68 offset:24576\n"
"v_mfma_f32_16x16x32_bf16 v[200:203], v[116:119], a[52:55], v[200:203]\n"
"buffer_load_dwordx4 v83, s[40:43], s58 offen offset:3072 lds\n"
"s_add_u32 s58, s58, 524288\n"
"s_and_b32 s58, s58, 0x7fffff\n"
"v_mfma_f32_16x16x32_bf16 v[232:235], v[112:115], a[56:59], v[232:235]\n"
"ds_read_b64_tr_b16 v[102:103], v68 offset:25600\n"
"v_mfma_f32_16x16x32_bf16 v[232:235], v[116:119], a[60:63], v[232:235]\n"
"s_waitcnt lgkmcnt(4)\n"
"v_mfma_f32_16x16x32_bf16 v[72:75], v[40:43], v[56:59], v[72:75]\n"
"v_mfma_f32_16x16x32_bf16 v[72:75], v[44:47], v[56:59], v[72:75]\n"
"v_mfma_f32_16x16x32_bf16 v[140:143], v[120:123], a[32:35], v[140:143]\n"
"ds_read_b64_tr_b16 v[104:105], v69 offset:16384\n"
"s_nop 0\n"
"s_add_u32 m0, s34, 0\n"
"v_mfma_f32_16x16x32_bf16 v[140:143], v[124:127], a[36:39], v[140:143]\n"
"buffer_load_dwordx4 v84, s[36:39], s56 offen offset:0 lds\n"
"v_mfma_f32_16x16x32_bf16 v[172:175], v[120:123], a[40:43], v[172:175]\n"
"ds_read_b64_tr_b16 v[106:107], v69 offset:17408\n"
"v_mfma_f32_16x16x32_bf16 v[172:175], v[124:127], a[44:47], v[172:175]\n"
"ds_read_b64_tr_b16 v[48:49], v63 offset:16384\n"
"ds_read_b64_tr_b16 v[50:51], v63 offset:17408\n"
"v_mfma_f32_16x16x32_bf16 v[204:207], v[120:123], a[48:51], v[204:207]\n"
"ds_read_b64_tr_b16 v[108:109], v69 offset:24576\n"
"v_mfma_f32_16x16x32_bf16 v[204:207], v[124:127], a[52:55], v[204:207]\n"
"buffer_load_dwordx4 v85, s[36:39], s56 offen offset:1024 lds\n"
"v_mfma_f32_16x16x32_bf16 v[236:239], v[120:123], a[56:59], v[236:239]\n"
"ds_read_b64_tr_b16 v[110:111], v69 offset:25600\n"
"v_mfma_f32_16x16x32_bf16 v[236:239], v[124:127], a[60:63], v[236:239]\n"
"ds_read_b64_tr_b16 v[52:53], v63 offset:24576\n"
"ds_read_b64_tr_b16 v[54:55], v63 offset:25600\n"
"s_waitcnt lgkmcnt(8)\n"
"v_mfma_f32_16x16x32_bf16 v[144:147], v[96:99], a[32:35], v[144:147]\n"
"ds_read_b64_tr_b16 v[112:113], v70 offset:16384\n"
"v_mfma_f32_16x16x32_bf16 v[144:147], v[100:103], a[36:39], v[144:147]\n"
"v_mfma_f32_16x16x32_bf16 v[176:179], v[96:99], a[40:43], v[176:179]\n"
"ds_read_b64_tr_b16 v[114:115], v70 offset:17408\n"
"v_mfma_f32_16x16x32_bf16 v[176:179], v[100:103], a[44:47], v[176:179]\n"
"buffer_load_dwordx4 v90, s[36:39], s56 offen offset:2048 lds\n"
"v_mfma_f32_16x16x32_bf16 v[208:211], v[96:99], a[48:51], v[208:211]\n"
"ds_read_b64_tr_b16 v[116:117], v70 offset:24576\n"
"v_mfma_f32_16x16x32_bf16 v[208:211], v[100:103], a[52:55], v[208:211]\n"
"buffer_load_dwordx4 v91, s[36:39], s56 offen offset:3072 lds\n"
"s_add_u32 s56, s56, 128\n"
"s_and_b32 s56, s56, 0x107ff\n"
"v_mfma_f32_16x16x32_bf16 v[240:243], v[96:99], a[56:59], v[240:243]\n"
"ds_read_b64_tr_b16 v[118:119], v70 offset:25600\n"
"v_mfma_f32_16x16x32_bf16 v[240:243], v[100:103], a[60:63], v[240:243]\n"
"s_waitcnt lgkmcnt(4)\n"
"v_mfma_f32_16x16x32_bf16 v[76:79], v[48:51], v[56:59], v[76:79]\n"
"v_mfma_f32_16x16x32_bf16 v[76:79], v[52:55], v[56:59], v[76:79]\n"
"v_mfma_f32_16x16x32_bf16 v[148:151], v[104:107], a[32:35], v[148:151]\n"
"ds_read_b64_tr_b16 v[120:121], v71 offset:16384\n"
"s_add_u32 m0, s34, 4096\n"
"v_mfma_f32_16x16x32_bf16 v[148:151], v[108:111], a[36:39], v[148:151]\n"
"buffer_load_dwordx4 v84, s[36:39], s57 offen offset:0 lds\n"
"v_mfma_f32_16x16x32_bf16 v[180:183], v[104:107], a[40:43], v[180:183]\n"
"ds_read_b64_tr_b16 v[122:123], v71 offset:17408\n"
"v_mfma_f32_16x16x32_bf16 v[180:183], v[108:111], a[44:47], v[180:183]\n"
"v_mfma_f32_16x16x32_bf16 v[212:215], v[104:107], a[48:51], v[212:215]\n"
"ds_read_b64_tr_b16 v[124:125], v71 offset:24576\n"
"v_mfma_f32_16x16x32_bf16 v[212:215], v[108:111], a[52:55], v[212:215]\n"
"buffer_load_dwordx4 v85, s[36:39], s57 offen offset:1024 lds\n"
"v_mfma_f32_16x16x32_bf16 v[244:247], v[104:107], a[56:59], v[244:247]\n"
"ds_read_b64_tr_b16 v[126:127], v71 offset:25600\n"
"v_mfma_f32_16x16x32_bf16 v[244:247], v[108:111], a[60:63], v[244:247]\n"
"s_waitcnt vmcnt(18)\n"
"s_barrier\n"
"s_waitcnt lgkmcnt(4)\n"
"v_mfma_f32_16x16x32_bf16 v[152:155], v[112:115], a[32:35], v[152:155]\n"
"ds_read_b64_tr_b16 v[96:97], v64 offset:32768\n"
"v_mfma_f32_16x16x32_bf16 v[152:155], v[116:119], a[36:39], v[152:155]\n"
"buffer_load_dwordx4 v90, s[36:39], s57 offen offset:2048 lds\n"
"v_mfma_f32_16x16x32_bf16 v[184:187], v[112:115], a[40:43], v[184:187]\n"
"ds_read_b64_tr_b16 v[98:99], v64 offset:33792\n"
"v_mfma_f32_16x16x32_bf16 v[184:187], v[116:119], a[44:47], v[184:187]\n"
"buffer_load_dwordx4 v91, s[36:39], s57 offen offset:3072 lds\n"
"s_add_u32 s57, s57, 128\n"
"s_and_b32 s57, s57, 0x107ff\n"
"v_mfma_f32_16x16x32_bf16 v[216:219], v[112:115], a[48:51], v[216:219]\n"
"ds_read_b64_tr_b16 v[100:101], v64 offset:40960\n"
"v_mfma_f32_16x16x32_bf16 v[216:219], v[116:119], a[52:55], v[216:219]\n"
"v_mfma_f32_16x16x32_bf16 v[248:251], v[112:115], a[56:59], v[248:251]\n"
"ds_read_b64_tr_b16 v[102:103], v64 offset:41984\n"
"v_mfma_f32_16x16x32_bf16 v[248:251], v[116:119], a[60:63], v[248:251]\n"
"s_waitcnt lgkmcnt(4)\n"
"v_mfma_f32_16x16x32_bf16 v[156:159], v[120:123], a[32:35], v[156:159]\n"
"ds_read_b64_tr_b16 v[104:105], v65 offset:32768\n"
"v_mfma_f32_16x16x32_bf16 v[156:159], v[124:127], a[36:39], v[156:159]\n"
"v_mfma_f32_16x16x32_bf16 v[188:191], v[120:123], a[40:43], v[188:191]\n"
"ds_read_b64_tr_b16 v[106:107], v65 offset:33792\n"
"v_mfma_f32_16x16x32_bf16 v[188:191], v[124:127], a[44:47], v[188:191]\n"
"v_mfma_f32_16x16x32_bf16 v[220:223], v[120:123], a[48:51], v[220:223]\n"
"ds_read_b64_tr_b16 v[108:109], v65 offset:40960\n"
"v_mfma_f32_16x16x32_bf16 v[220:223], v[124:127], a[52:55], v[220:223]\n"
"v_mfma_f32_16x16x32_bf16 v[252:255], v[120:123], a[56:59], v[252:255]\n"
"ds_read_b64_tr_b16 v[110:111], v65 offset:41984\n"
"v_mfma_f32_16x16x32_bf16 v[252:255], v[124:127], a[60:63], v[252:255]\n"
"s_waitcnt lgkmcnt(4)\n"
"v_mfma_f32_16x16x32_bf16 v[128:131], v[96:99], a[0:3], v[128:131]\n"
"ds_read_b64_tr_b16 v[112:113], v66 offset:32768\n"
"v_mfma_f32_16x16x32_bf16 v[128:131], v[100:103], a[4:7], v[128:131]\n"
"s_waitcnt vmcnt(12)\n"
"ds_read_b128 a[32:35], v86 offset:8192\n"
"ds_read_b128 a[36:39], v87 offset:8192\n"
"v_mfma_f32_16x16x32_bf16 v[160:163], v[96:99], a[8:11], v[160:163]\n"
"ds_read_b64_tr_b16 v[114:115], v66 offset:33792\n"
"v_mfma_f32_16x16x32_bf16 v[160:163], v[100:103], a[12:15], v[160:163]\n"
"ds_read_b128 a[40:43], v86 offset:10240\n"
"ds_read_b128 a[44:47], v87 offset:10240\n"
"v_mfma_f32_16x16x32_bf16 v[192:195], v[96:99], a[16:19], v[192:195]\n"
"ds_read_b64_tr_b16 v[116:117], v66 offset:40960\n"
"v_mfma_f32_16x16x32_bf16 v[192:195], v[100:103], a[20:23], v[192:195]\n"
"ds_read_b128 a[48:51], v86 offset:12288\n"
"ds_read_b128 a[52:55], v87 offset:12288\n"
"v_mfma_f32_16x16x32_bf16 v[224:227], v[96:99], a[24:27], v[224:227]\n"
"ds_read_b64_tr_b16 v[118:119], v66 offset:41984\n"
"v_mfma_f32_16x16x32_bf16 v[224:227], v[100:103], a[28:31], v[224:227]\n"
"ds_read_b128 a[56:59], v86 offset:14336\n"
"ds_read_b128 a[60:63], v87 offset:14336\n"
"s_waitcnt lgkmcnt(12)\n"
"v_mfma_f32_16x16x32_bf16 v[132:135], v[104:107], a[0:3], v[132:135]\n"
"ds_read_b64_tr_b16 v[120:121], v67 offset:32768\n"
"s_add_u32 m0, s59, 98304\n"
"v_mfma_f32_16x16x32_bf16 v[132:135], v[108:111], a[4:7], v[132:135]\n"
"buffer_load_dwordx4 v80, s[40:43], s58 offen offset:0 lds\n"
"v_mfma_f32_16x16x32_bf16 v[164:167], v[104:107], a[8:11], v[164:167]\n"
"ds_read_b64_tr_b16 v[122:123], v67 offset:33792\n"
"v_mfma_f32_16x16x32_bf16 v[164:167], v[108:111], a[12:15], v[164:167]\n"
"ds_read_b64_tr_b16 v[40:41], v62 offset:32768\n"
"ds_read_b64_tr_b16 v[42:43], v62 offset:33792\n"
"v_mfma_f32_16x16x32_bf16 v[196:199], v[104:107], a[16:19], v[196:199]\n"
"ds_read_b64_tr_b16 v[124:125], v67 offset:40960\n"
"v_mfma_f32_16x16x32_bf16 v[196:199], v[108:111], a[20:23], v[196:199]\n"
"buffer_load_dwordx4 v81, s[40:43], s58 offen offset:1024 lds\n"
"v_mfma_f32_16x16x32_bf16 v[228:231], v[104:107], a[24:27], v[228:231]\n"
"ds_read_b64_tr_b16 v[126:127], v67 offset:41984\n"
"v_mfma_f32_16x16x32_bf16 v[228:231], v[108:111], a[28:31], v[228:231]\n"
"ds_read_b64_tr_b16 v[44:45], v62 offset:40960\n"
"ds_read_b64_tr_b16 v[46:47], v62 offset:41984\n"
"s_waitcnt lgkmcnt(10)\n"
"v_mfma_f32_16x16x32_bf16 v[136:139], v[112:115], a[0:3], v[136:139]\n"
"ds_read_b64_tr_b16 v[96:97], v68 offset:32768\n"
"v_mfma_f32_16x16x32_bf16 v[136:139], v[116:119], a[4:7], v[136:139]\n"
"buffer_load_dwordx4 v82, s[40:43], s58 offen offset:2048 lds\n"
"v_mfma_f32_16x16x32_bf16 v[168:171], v[112:115], a[8:11], v[168:171]\n"
"ds_read_b64_tr_b16 v[98:99], v68 offset:33792\n"
"v_mfma_f32_16x16x32_bf16 v[168:171], v[116:119], a[12:15], v[168:171]\n"
"buffer_load_dwordx4 v83, s[40:43], s58 offen offset:3072 lds\n"
"s_add_u32 s58, s58, 524288\n"
"s_and_b32 s58, s58, 0x7fffff\n"
"v_mfma_f32_16x16x32_bf16 v[200:203], v[112:115], a[16:19], v[200:203]\n"
"ds_read_b64_tr_b16 v[100:101], v68 offset:40960\n"
"s_add_u32 m0, s35, s69\n"
"v_mfma_f32_16x16x32_bf16 v[200:203], v[116:119], a[20:23], v[200:203]\n"
"buffer_load_dwordx4 v88, s[48:51], s69 offen offset:0 lds\n"
"v_mfma_f32_16x16x32_bf16 v[232:235], v[112:115], a[24:27], v[232:235]\n"
"ds_read_b64_tr_b16 v[102:103], v68 offset:41984\n"
"v_mfma_f32_16x16x32_bf16 v[232:235], v[116:119], a[28:31], v[232:235]\n"
"s_waitcnt lgkmcnt(4)\n"
"v_mfma_f32_16x16x32_bf16 v[72:75], v[40:43], v[56:59], v[72:75]\n"
"v_mfma_f32_16x16x32_bf16 v[72:75], v[44:47], v[56:59], v[72:75]\n"
"v_mfma_f32_16x16x32_bf16 v[140:143], v[120:123], a[0:3], v[140:143]\n"
"ds_read_b64_tr_b16 v[104:105], v69 offset:32768\n"
"v_mfma_f32_16x16x32_bf16 v[140:143], v[124:127], a[4:7], v[140:143]\n"
"buffer_load_dwordx4 v89, s[48:51], s69 offen offset:1024 lds\n"
"s_add_u32 s69, s69, 8192\n"
"v_mfma_f32_16x16x32_bf16 v[172:175], v[120:123], a[8:11], v[172:175]\n"
"ds_read_b64_tr_b16 v[106:107], v69 offset:33792\n"
"v_mfma_f32_16x16x32_bf16 v[172:175], v[124:127], a[12:15], v[172:175]\n"
"ds_read_b64_tr_b16 v[48:49], v63 offset:32768\n"
"ds_read_b64_tr_b16 v[50:51], v63 offset:33792\n"
"v_mfma_f32_16x16x32_bf16 v[204:207], v[120:123], a[16:19], v[204:207]\n"
"ds_read_b64_tr_b16 v[108:109], v69 offset:40960\n"
"s_nop 0\n"
"s_add_u32 m0, s34, 8192\n"
"v_mfma_f32_16x16x32_bf16 v[204:207], v[124:127], a[20:23], v[204:207]\n"
"buffer_load_dwordx4 v84, s[36:39], s56 offen offset:0 lds\n"
"v_mfma_f32_16x16x32_bf16 v[236:239], v[120:123], a[24:27], v[236:239]\n"
"ds_read_b64_tr_b16 v[110:111], v69 offset:41984\n"
"v_mfma_f32_16x16x32_bf16 v[236:239], v[124:127], a[28:31], v[236:239]\n"
"ds_read_b64_tr_b16 v[52:53], v63 offset:40960\n"
"ds_read_b64_tr_b16 v[54:55], v63 offset:41984\n"
"s_waitcnt lgkmcnt(8)\n"
"v_mfma_f32_16x16x32_bf16 v[144:147], v[96:99], a[0:3], v[144:147]\n"
"ds_read_b64_tr_b16 v[112:113], v70 offset:32768\n"
"v_mfma_f32_16x16x32_bf16 v[144:147], v[100:103], a[4:7], v[144:147]\n"
"v_mfma_f32_16x16x32_bf16 v[176:179], v[96:99], a[8:11], v[176:179]\n"
"ds_read_b64_tr_b16 v[114:115], v70 offset:33792\n"
"v_mfma_f32_16x16x32_bf16 v[176:179], v[100:103], a[12:15], v[176:179]\n"
"buffer_load_dwordx4 v85, s[36:39], s56 offen offset:1024 lds\n"
"v_mfma_f32_16x16x32_bf16 v[208:211], v[96:99], a[16:19], v[208:211]\n"
"ds_read_b64_tr_b16 v[116:117], v70 offset:40960\n"
"v_mfma_f32_16x16x32_bf16 v[208:211], v[100:103], a[20:23], v[208:211]\n"
"buffer_load_dwordx4 v90, s[36:39], s56 offen offset:2048 lds\n"
"v_mfma_f32_16x16x32_bf16 v[240:243], v[96:99], a[24:27], v[240:243]\n"
"ds_read_b64_tr_b16 v[118:119], v70 offset:41984\n"
"v_mfma_f32_16x16x32_bf16 v[240:243], v[100:103], a[28:31], v[240:243]\n"
"s_waitcnt lgkmcnt(4)\n"
"v_mfma_f32_16x16x32_bf16 v[76:79], v[48:51], v[56:59], v[76:79]\n"
"v_mfma_f32_16x16x32_bf16 v[76:79], v[52:55], v[56:59], v[76:79]\n"
"v_mfma_f32_16x16x32_bf16 v[148:151], v[104:107], a[0:3], v[148:151]\n"
"ds_read_b64_tr_b16 v[120:121], v71 offset:32768\n"
"v_mfma_f32_16x16x32_bf16 v[148:151], v[108:111], a[4:7], v[148:151]\n"
"buffer_load_dwordx4 v91, s[36:39], s56 offen offset:3072 lds\n"
"s_add_u32 s56, s56, 128\n"
"s_and_b32 s56, s56, 0x107ff\n"
"v_mfma_f32_16x16x32_bf16 v[180:183], v[104:107], a[8:11], v[180:183]\n"
"ds_read_b64_tr_b16 v[122:123], v71 offset:33792\n"
"s_add_u32 m0, s34, 12288\n"
"v_mfma_f32_16x16x32_bf16 v[180:183], v[108:111], a[12:15], v[180:183]\n"
"buffer_load_dwordx4 v84, s[36:39], s57 offen offset:0 lds\n"
"v_mfma_f32_16x16x32_bf16 v[212:215], v[104:107], a[16:19], v[212:215]\n"
"ds_read_b64_tr_b16 v[124:125], v71 offset:40960\n"
"v_mfma_f32_16x16x32_bf16 v[212:215], v[108:111], a[20:23], v[212:215]\n"
"buffer_load_dwordx4 v85, s[36:39], s57 offen offset:1024 lds\n"
"v_mfma_f32_16x16x32_bf16 v[244:247], v[104:107], a[24:27], v[244:247]\n"
"ds_read_b64_tr_b16 v[126:127], v71 offset:41984\n"
"v_mfma_f32_16x16x32_bf16 v[244:247], v[108:111], a[28:31], v[244:247]\n"
"s_waitcnt vmcnt(20)\n"
"s_barrier\n"
"s_waitcnt lgkmcnt(4)\n"
"v_mfma_f32_16x16x32_bf16 v[152:155], v[112:115], a[0:3], v[152:155]\n"
"ds_read_b64_tr_b16 v[96:97], v64 offset:49152\n"
"v_mfma_f32_16x16x32_bf16 v[152:155], v[116:119], a[4:7], v[152:155]\n"
"buffer_load_dwordx4 v90, s[36:39], s57 offen offset:2048 lds\n"
"v_mfma_f32_16x16x32_bf16 v[184:187], v[112:115], a[8:11], v[184:187]\n"
"ds_read_b64_tr_b16 v[98:99], v64 offset:50176\n"
"v_mfma_f32_16x16x32_bf16 v[184:187], v[116:119], a[12:15], v[184:187]\n"
"buffer_load_dwordx4 v91, s[36:39], s57 offen offset:3072 lds\n"
"s_add_u32 s57, s57, 128\n"
"s_and_b32 s57, s57, 0x107ff\n"
"v_mfma_f32_16x16x32_bf16 v[216:219], v[112:115], a[16:19], v[216:219]\n"
"ds_read_b64_tr_b16 v[100:101], v64 offset:57344\n"
"v_mfma_f32_16x16x32_bf16 v[216:219], v[116:119], a[20:23], v[216:219]\n"
"v_mfma_f32_16x16x32_bf16 v[248:251], v[112:115], a[24:27], v[248:251]\n"
"ds_read_b64_tr_b16 v[102:103], v64 offset:58368\n"
"v_mfma_f32_16x16x32_bf16 v[248:251], v[116:119], a[28:31], v[248:251]\n"
"s_waitcnt lgkmcnt(4)\n"
"v_mfma_f32_16x16x32_bf16 v[156:159], v[120:123], a[0:3], v[156:159]\n"
"ds_read_b64_tr_b16 v[104:105], v65 offset:49152\n"
"v_mfma_f32_16x16x32_bf16 v[156:159], v[124:127], a[4:7], v[156:159]\n"
"v_mfma_f32_16x16x32_bf16 v[188:191], v[120:123], a[8:11], v[188:191]\n"
"ds_read_b64_tr_b16 v[106:107], v65 offset:50176\n"
"v_mfma_f32_16x16x32_bf16 v[188:191], v[124:127], a[12:15], v[188:191]\n"
"v_mfma_f32_16x16x32_bf16 v[220:223], v[120:123], a[16:19], v[220:223]\n"
"ds_read_b64_tr_b16 v[108:109], v65 offset:57344\n"
"v_mfma_f32_16x16x32_bf16 v[220:223], v[124:127], a[20:23], v[220:223]\n"
"v_mfma_f32_16x16x32_bf16 v[252:255], v[120:123], a[24:27], v[252:255]\n"
"ds_read_b64_tr_b16 v[110:111], v65 offset:58368\n"
"v_mfma_f32_16x16x32_bf16 v[252:255], v[124:127], a[28:31], v[252:255]\n"
"s_waitcnt lgkmcnt(4)\n"
"v_mfma_f32_16x16x32_bf16 v[128:131], v[96:99], a[32:35], v[128:131]\n"
"ds_read_b64_tr_b16 v[112:113], v66 offset:49152\n"
"v_mfma_f32_16x16x32_bf16 v[128:131], v[100:103], a[36:39], v[128:131]\n"
"s_waitcnt vmcnt(14)\n"
"ds_read_b128 a[0:3], v86 offset:0\n"
"ds_read_b128 a[4:7], v87 offset:0\n"
"v_mfma_f32_16x16x32_bf16 v[160:163], v[96:99], a[40:43], v[160:163]\n"
"ds_read_b64_tr_b16 v[114:115], v66 offset:50176\n"
"v_mfma_f32_16x16x32_bf16 v[160:163], v[100:103], a[44:47], v[160:163]\n"
"ds_read_b128 a[8:11], v86 offset:2048\n"
"ds_read_b128 a[12:15], v87 offset:2048\n"
"v_mfma_f32_16x16x32_bf16 v[192:195], v[96:99], a[48:51], v[192:195]\n"
"ds_read_b64_tr_b16 v[116:117], v66 offset:57344\n"
"v_mfma_f32_16x16x32_bf16 v[192:195], v[100:103], a[52:55], v[192:195]\n"
"ds_read_b128 a[16:19], v86 offset:4096\n"
"ds_read_b128 a[20:23], v87 offset:4096\n"
"v_mfma_f32_16x16x32_bf16 v[224:227], v[96:99], a[56:59], v[224:227]\n"
"ds_read_b64_tr_b16 v[118:119], v66 offset:58368\n"
"v_mfma_f32_16x16x32_bf16 v[224:227], v[100:103], a[60:63], v[224:227]\n"
"ds_read_b128 a[24:27], v86 offset:6144\n"
"ds_read_b128 a[28:31], v87 offset:6144\n"
"s_waitcnt lgkmcnt(12)\n"
"v_mfma_f32_16x16x32_bf16 v[132:135], v[104:107], a[32:35], v[132:135]\n"
"ds_read_b64_tr_b16 v[120:121], v67 offset:49152\n"
"s_add_u32 m0, s59, 114688\n"
"v_mfma_f32_16x16x32_bf16 v[132:135], v[108:111], a[36:39], v[132:135]\n"
"buffer_load_dwordx4 v80, s[40:43], s58 offen offset:0 lds\n"
"v_mfma_f32_16x16x32_bf16 v[164:167], v[104:107], a[40:43], v[164:167]\n"
"ds_read_b64_tr_b16 v[122:123], v67 offset:50176\n"
"v_mfma_f32_16x16x32_bf16 v[164:167], v[108:111], a[44:47], v[164:167]\n"
"ds_read_b64_tr_b16 v[40:41], v62 offset:49152\n"
"ds_read_b64_tr_b16 v[42:43], v62 offset:50176\n"
"v_mfma_f32_16x16x32_bf16 v[196:199], v[104:107], a[48:51], v[196:199]\n"
"ds_read_b64_tr_b16 v[124:125], v67 offset:57344\n"
"v_mfma_f32_16x16x32_bf16 v[196:199], v[108:111], a[52:55], v[196:199]\n"
"buffer_load_dwordx4 v81, s[40:43], s58 offen offset:1024 lds\n"
"v_mfma_f32_16x16x32_bf16 v[228:231], v[104:107], a[56:59], v[228:231]\n"
"ds_read_b64_tr_b16 v[126:127], v67 offset:58368\n"
"v_mfma_f32_16x16x32_bf16 v[228:231], v[108:111], a[60:63], v[228:231]\n"
"ds_read_b64_tr_b16 v[44:45], v62 offset:57344\n"
"ds_read_b64_tr_b16 v[46:47], v62 offset:58368\n"
"s_waitcnt lgkmcnt(10)\n"
"v_mfma_f32_16x16x32_bf16 v[136:139], v[112:115], a[32:35], v[136:139]\n"
"ds_read_b64_tr_b16 v[96:97], v68 offset:49152\n"
"v_mfma_f32_16x16x32_bf16 v[136:139], v[116:119], a[36:39], v[136:139]\n"
"buffer_load_dwordx4 v82, s[40:43], s58 offen offset:2048 lds\n"
"v_mfma_f32_16x16x32_bf16 v[168:171], v[112:115], a[40:43], v[168:171]\n"
"ds_read_b64_tr_b16 v[98:99], v68 offset:50176\n"
"v_mfma_f32_16x16x32_bf16 v[168:171], v[116:119], a[44:47], v[168:171]\n"
"v_mfma_f32_16x16x32_bf16 v[200:203], v[112:115], a[48:51], v[200:203]\n"
"ds_read_b64_tr_b16 v[100:101], v68 offset:57344\n"
"v_mfma_f32_16x16x32_bf16 v[200:203], v[116:119], a[52:55], v[200:203]\n"
"buffer_load_dwordx4 v83, s[40:43], s58 offen offset:3072 lds\n"
"s_add_u32 s58, s58, 524288\n"
"s_and_b32 s58, s58, 0x7fffff\n"
"v_mfma_f32_16x16x32_bf16 v[232:235], v[112:115], a[56:59], v[232:235]\n"
"ds_read_b64_tr_b16 v[102:103], v68 offset:58368\n"
"v_mfma_f32_16x16x32_bf16 v[232:235], v[116:119], a[60:63], v[232:235]\n"
"s_waitcnt lgkmcnt(4)\n"
"v_mfma_f32_16x16x32_bf16 v[72:75], v[40:43], v[56:59], v[72:75]\n"
"v_mfma_f32_16x16x32_bf16 v[72:75], v[44:47], v[56:59], v[72:75]\n"
"v_mfma_f32_16x16x32_bf16 v[140:143], v[120:123], a[32:35], v[140:143]\n"
"ds_read_b64_tr_b16 v[104:105], v69 offset:49152\n"
"s_nop 0\n"
"s_add_u32 m0, s34, 0\n"
"v_mfma_f32_16x16x32_bf16 v[140:143], v[124:127], a[36:39], v[140:143]\n"
"buffer_load_dwordx4 v84, s[36:39], s56 offen offset:0 lds\n"
"v_mfma_f32_16x16x32_bf16 v[172:175], v[120:123], a[40:43], v[172:175]\n"
"ds_read_b64_tr_b16 v[106:107], v69 offset:50176\n"
"v_mfma_f32_16x16x32_bf16 v[172:175], v[124:127], a[44:47], v[172:175]\n"
"ds_read_b64_tr_b16 v[48:49], v63 offset:49152\n"
"ds_read_b64_tr_b16 v[50:51], v63 offset:50176\n"
"v_mfma_f32_16x16x32_bf16 v[204:207], v[120:123], a[48:51], v[204:207]\n"
"ds_read_b64_tr_b16 v[108:109], v69 offset:57344\n"
"v_mfma_f32_16x16x32_bf16 v[204:207], v[124:127], a[52:55], v[204:207]\n"
"buffer_load_dwordx4 v85, s[36:39], s56 offen offset:1024 lds\n"
"v_mfma_f32_16x16x32_bf16 v[236:239], v[120:123], a[56:59], v[236:239]\n"
"ds_read_b64_tr_b16 v[110:111], v69 offset:58368\n"
"v_mfma_f32_16x16x32_bf16 v[236:239], v[124:127], a[60:63], v[236:239]\n"
"ds_read_b64_tr_b16 v[52:53], v63 offset:57344\n"
"ds_read_b64_tr_b16 v[54:55], v63 offset:58368\n"
"s_waitcnt lgkmcnt(8)\n"
"v_mfma_f32_16x16x32_bf16 v[144:147], v[96:99], a[32:35], v[144:147]\n"
"ds_read_b64_tr_b16 v[112:113], v70 offset:49152\n"
"v_mfma_f32_16x16x32_bf16 v[144:147], v[100:103], a[36:39], v[144:147]\n"
"v_mfma_f32_16x16x32_bf16 v[176:179], v[96:99], a[40:43], v[176:179]\n"
"ds_read_b64_tr_b16 v[114:115], v70 offset:50176\n"
"v_mfma_f32_16x16x32_bf16 v[176:179], v[100:103], a[44:47], v[176:179]\n"
"buffer_load_dwordx4 v90, s[36:39], s56 offen offset:2048 lds\n"
"v_mfma_f32_16x16x32_bf16 v[208:211], v[96:99], a[48:51], v[208:211]\n"
"ds_read_b64_tr_b16 v[116:117], v70 offset:57344\n"
"v_mfma_f32_16x16x32_bf16 v[208:211], v[100:103], a[52:55], v[208:211]\n"
"buffer_load_dwordx4 v91, s[36:39], s56 offen offset:3072 lds\n"
"s_add_u32 s56, s56, 128\n"
"s_and_b32 s56, s56, 0x107ff\n"
"v_mfma_f32_16x16x32_bf16 v[240:243], v[96:99], a[56:59], v[240:243]\n"
"ds_read_b64_tr_b16 v[118:119], v70 offset:58368\n"
"v_mfma_f32_16x16x32_bf16 v[240:243], v[100:103], a[60:63], v[240:243]\n"
"s_waitcnt lgkmcnt(4)\n"
"v_mfma_f32_16x16x32_bf16 v[76:79], v[48:51], v[56:59], v[76:79]\n"
"v_mfma_f32_16x16x32_bf16 v[76:79], v[52:55], v[56:59], v[76:79]\n"
"v_mfma_f32_16x16x32_bf16 v[148:151], v[104:107], a[32:35], v[148:151]\n"
"ds_read_b64_tr_b16 v[120:121], v71 offset:49152\n"
"s_add_u32 m0, s34, 4096\n"
"v_mfma_f32_16x16x32_bf16 v[148:151], v[108:111], a[36:39], v[148:151]\n"
"buffer_load_dwordx4 v84, s[36:39], s57 offen offset:0 lds\n"
"v_mfma_f32_16x16x32_bf16 v[180:183], v[104:107], a[40:43], v[180:183]\n"
"ds_read_b64_tr_b16 v[122:123], v71 offset:50176\n"
"v_mfma_f32_16x16x32_bf16 v[180:183], v[108:111], a[44:47], v[180:183]\n"
"v_mfma_f32_16x16x32_bf16 v[212:215], v[104:107], a[48:51], v[212:215]\n"
"ds_read_b64_tr_b16 v[124:125], v71 offset:57344\n"
"v_mfma_f32_16x16x32_bf16 v[212:215], v[108:111], a[52:55], v[212:215]\n"
"buffer_load_dwordx4 v85, s[36:39], s57 offen offset:1024 lds\n"
"v_mfma_f32_16x16x32_bf16 v[244:247], v[104:107], a[56:59], v[244:247]\n"
"ds_read_b64_tr_b16 v[126:127], v71 offset:58368\n"
"v_mfma_f32_16x16x32_bf16 v[244:247], v[108:111], a[60:63], v[244:247]\n"
"s_waitcnt vmcnt(20)\n"
"s_barrier\n"
"s_waitcnt lgkmcnt(4)\n"
"v_mfma_f32_16x16x32_bf16 v[152:155], v[112:115], a[32:35], v[152:155]\n"
"ds_read_b64_tr_b16 v[96:97], v64 offset:0\n"
"v_mfma_f32_16x16x32_bf16 v[152:155], v[116:119], a[36:39], v[152:155]\n"
"buffer_load_dwordx4 v90, s[36:39], s57 offen offset:2048 lds\n"
"v_mfma_f32_16x16x32_bf16 v[184:187], v[112:115], a[40:43], v[184:187]\n"
"ds_read_b64_tr_b16 v[98:99], v64 offset:1024\n"
"v_mfma_f32_16x16x32_bf16 v[184:187], v[116:119], a[44:47], v[184:187]\n"
"buffer_load_dwordx4 v91, s[36:39], s57 offen offset:3072 lds\n"
"s_add_u32 s57, s57, 128\n"
"s_and_b32 s57, s57, 0x107ff\n"
"v_mfma_f32_16x16x32_bf16 v[216:219], v[112:115], a[48:51], v[216:219]\n"
"ds_read_b64_tr_b16 v[100:101], v64 offset:8192\n"
"v_mfma_f32_16x16x32_bf16 v[216:219], v[116:119], a[52:55], v[216:219]\n"
"v_mfma_f32_16x16x32_bf16 v[248:251], v[112:115], a[56:59], v[248:251]\n"
"ds_read_b64_tr_b16 v[102:103], v64 offset:9216\n"
"v_mfma_f32_16x16x32_bf16 v[248:251], v[116:119], a[60:63], v[248:251]\n"
"s_waitcnt lgkmcnt(4)\n"
"v_mfma_f32_16x16x32_bf16 v[156:159], v[120:123], a[32:35], v[156:159]\n"
"ds_read_b64_tr_b16 v[104:105], v65 offset:0\n"
"v_mfma_f32_16x16x32_bf16 v[156:159], v[124:127], a[36:39], v[156:159]\n"
"v_mfma_f32_16x16x32_bf16 v[188:191], v[120:123], a[40:43], v[188:191]\n"
"ds_read_b64_tr_b16 v[106:107], v65 offset:1024\n"
"v_mfma_f32_16x16x32_bf16 v[188:191], v[124:127], a[44:47], v[188:191]\n"
"v_mfma_f32_16x16x32_bf16 v[220:223], v[120:123], a[48:51], v[220:223]\n"
"ds_read_b64_tr_b16 v[108:109], v65 offset:8192\n"
"v_mfma_f32_16x16x32_bf16 v[220:223], v[124:127], a[52:55], v[220:223]\n"
"v_mfma_f32_16x16x32_bf16 v[252:255], v[120:123], a[56:59], v[252:255]\n"
"ds_read_b64_tr_b16 v[110:111], v65 offset:9216\n"
"v_mfma_f32_16x16x32_bf16 v[252:255], v[124:127], a[60:63], v[252:255]\n"
"s_sub_u32 s60, s60, 1\n"
"s_cmp_lg_u32 s60, 0\n"
"s_cbranch_scc1 AQ_LOOP\n"
"s_waitcnt lgkmcnt(4)\n"
"v_mfma_f32_16x16x32_bf16 v[128:131], v[96:99], a[0:3], v[128:131]\n"
"ds_read_b64_tr_b16 v[112:113], v66 offset:0\n"
"v_mfma_f32_16x16x32_bf16 v[128:131], v[100:103], a[4:7], v[128:131]\n"
"s_waitcnt vmcnt(12)\n"
"ds_read_b128 a[32:35], v86 offset:8192\n"
"ds_read_b128 a[36:39], v87 offset:8192\n"
"v_mfma_f32_16x16x32_bf16 v[160:163], v[96:99], a[8:11], v[160:163]\n"
"ds_read_b64_tr_b16 v[114:115], v66 offset:1024\n"
"v_mfma_f32_16x16x32_bf16 v[160:163], v[100:103], a[12:15], v[160:163]\n"
"ds_read_b128 a[40:43], v86 offset:10240\n"
"ds_read_b128 a[44:47], v87 offset:10240\n"
"v_mfma_f32_16x16x32_bf16 v[192:195], v[96:99], a[16:19], v[192:195]\n"
"ds_read_b64_tr_b16 v[116:117], v66 offset:8192\n"
"v_mfma_f32_16x16x32_bf16 v[192:195], v[100:103], a[20:23], v[192:195]\n"
"ds_read_b128 a[48:51], v86 offset:12288\n"
"ds_read_b128 a[52:55], v87 offset:12288\n"
"v_mfma_f32_16x16x32_bf16 v[224:227], v[96:99], a[24:27], v[224:227]\n"
"ds_read_b64_tr_b16 v[118:119], v66 offset:9216\n"
"v_mfma_f32_16x16x32_bf16 v[224:227], v[100:103], a[28:31], v[224:227]\n"
"ds_read_b128 a[56:59], v86 offset:14336\n"
"ds_read_b128 a[60:63], v87 offset:14336\n"
"s_waitcnt lgkmcnt(12)\n"
"v_mfma_f32_16x16x32_bf16 v[132:135], v[104:107], a[0:3], v[132:135]\n"
"ds_read_b64_tr_b16 v[120:121], v67 offset:0\n"
"s_add_u32 m0, s59, 131072\n"
"v_mfma_f32_16x16x32_bf16 v[132:135], v[108:111], a[4:7], v[132:135]\n"
"buffer_load_dwordx4 v80, s[40:43], s58 offen offset:0 lds\n"
"v_mfma_f32_16x16x32_bf16 v[164:167], v[104:107], a[8:11], v[164:167]\n"
"ds_read_b64_tr_b16 v[122:123], v67 offset:1024\n"
"v_mfma_f32_16x16x32_bf16 v[164:167], v[108:111], a[12:15], v[164:167]\n"
"ds_read_b64_tr_b16 v[40:41], v62 offset:0\n"
"ds_read_b64_tr_b16 v[42:43], v62 offset:1024\n"
"v_mfma_f32_16x16x32_bf16 v[196:199], v[104:107], a[16:19], v[196:199]\n"
"ds_read_b64_tr_b16 v[124:125], v67 offset:8192\n"
"v_mfma_f32_16x16x32_bf16 v[196:199], v[108:111], a[20:23], v[196:199]\n"
"buffer_load_dwordx4 v81, s[40:43], s58 offen offset:1024 lds\n"
"v_mfma_f32_16x16x32_bf16 v[228:231], v[104:107], a[24:27], v[228:231]\n"
"ds_read_b64_tr_b16 v[126:127], v67 offset:9216\n"
"v_mfma_f32_16x16x32_bf16 v[228:231], v[108:111], a[28:31], v[228:231]\n"
"ds_read_b64_tr_b16 v[44:45], v62 offset:8192\n"
"ds_read_b64_tr_b16 v[46:47], v62 offset:9216\n"
"s_waitcnt lgkmcnt(10)\n"
"v_mfma_f32_16x16x32_bf16 v[136:139], v[112:115], a[0:3], v[136:139]\n"
"ds_read_b64_tr_b16 v[96:97], v68 offset:0\n"
"v_mfma_f32_16x16x32_bf16 v[136:139], v[116:119], a[4:7], v[136:139]\n"
"buffer_load_dwordx4 v82, s[40:43], s58 offen offset:2048 lds\n"
"v_mfma_f32_16x16x32_bf16 v[168:171], v[112:115], a[8:11], v[168:171]\n"
"ds_read_b64_tr_b16 v[98:99], v68 offset:1024\n"
"v_mfma_f32_16x16x32_bf16 v[168:171], v[116:119], a[12:15], v[168:171]\n"
"buffer_load_dwordx4 v83, s[40:43], s58 offen offset:3072 lds\n"
"s_add_u32 s58, s58, 524288\n"
"s_and_b32 s58, s58, 0x7fffff\n"
"v_mfma_f32_16x16x32_bf16 v[200:203], v[112:115], a[16:19], v[200:203]\n"
"ds_read_b64_tr_b16 v[100:101], v68 offset:8192\n"
"v_mfma_f32_16x16x32_bf16 v[200:203], v[116:119], a[20:23], v[200:203]\n"
"global_load_dwordx4 v[28:31], v37, %0 offset:0 sc1\n"
"v_mfma_f32_16x16x32_bf16 v[232:235], v[112:115], a[24:27], v[232:235]\n"
"ds_read_b64_tr_b16 v[102:103], v68 offset:9216\n"
"v_mfma_f32_16x16x32_bf16 v[232:235], v[116:119], a[28:31], v[232:235]\n"
"s_waitcnt lgkmcnt(4)\n"
"v_mfma_f32_16x16x32_bf16 v[72:75], v[40:43], v[56:59], v[72:75]\n"
"v_mfma_f32_16x16x32_bf16 v[72:75], v[44:47], v[56:59], v[72:75]\n"
"v_mfma_f32_16x16x32_bf16 v[140:143], v[120:123], a[0:3], v[140:143]\n"
"ds_read_b64_tr_b16 v[104:105], v69 offset:0\n"
"v_mfma_f32_16x16x32_bf16 v[140:143], v[124:127], a[4:7], v[140:143]\n"
"global_load_dwordx4 v[32:35], v37, %0 offset:1024 sc1\n"
"v_mfma_f32_16x16x32_bf16 v[172:175], v[120:123], a[8:11], v[172:175]\n"
"ds_read_b64_tr_b16 v[106:107], v69 offset:1024\n"
"v_mfma_f32_16x16x32_bf16 v[172:175], v[124:127], a[12:15], v[172:175]\n"
"ds_read_b64_tr_b16 v[48:49], v63 offset:0\n"
"ds_read_b64_tr_b16 v[50:51], v63 offset:1024\n"
"v_mfma_f32_16x16x32_bf16 v[204:207], v[120:123], a[16:19], v[204:207]\n"
"ds_read_b64_tr_b16 v[108:109], v69 offset:8192\n"
"s_add_u32 m0, s35, s69\n"
"v_mfma_f32_16x16x32_bf16 v[204:207], v[124:127], a[20:23], v[204:207]\n"
"buffer_load_dwordx4 v88, s[48:51], s69 offen offset:0 lds\n"
"v_mfma_f32_16x16x32_bf16 v[236:239], v[120:123], a[24:27], v[236:239]\n"
"ds_read_b64_tr_b16 v[110:111], v69 offset:9216\n"
"v_mfma_f32_16x16x32_bf16 v[236:239], v[124:127], a[28:31], v[236:239]\n"
"ds_read_b64_tr_b16 v[52:53], v63 offset:8192\n"
"ds_read_b64_tr_b16 v[54:55], v63 offset:9216\n"
"s_waitcnt lgkmcnt(8)\n"
"v_mfma_f32_16x16x32_bf16 v[144:147], v[96:99], a[0:3], v[144:147]\n"
"ds_read_b64_tr_b16 v[112:113], v70 offset:0\n"
"v_mfma_f32_16x16x32_bf16 v[144:147], v[100:103], a[4:7], v[144:147]\n"
"buffer_load_dwordx4 v89, s[48:51], s69 offen offset:1024 lds\n"
"s_add_u32 s69, s69, 8192\n"
"v_mfma_f32_16x16x32_bf16 v[176:179], v[96:99], a[8:11], v[176:179]\n"
"ds_read_b64_tr_b16 v[114:115], v70 offset:1024\n"
"s_nop 0\n"
"s_add_u32 m0, s34, 8192\n"
"v_mfma_f32_16x16x32_bf16 v[176:179], v[100:103], a[12:15], v[176:179]\n"
"buffer_load_dwordx4 v84, s[36:39], s56 offen offset:0 lds\n"
"v_mfma_f32_16x16x32_bf16 v[208:211], v[96:99], a[16:19], v[208:211]\n"
"ds_read_b64_tr_b16 v[116:117], v70 offset:8192\n"
"v_mfma_f32_16x16x32_bf16 v[208:211], v[100:103], a[20:23], v[208:211]\n"
"buffer_load_dwordx4 v85, s[36:39], s56 offen offset:1024 lds\n"
"v_mfma_f32_16x16x32_bf16 v[240:243], v[96:99], a[24:27], v[240:243]\n"
"ds_read_b64_tr_b16 v[118:119], v70 offset:9216\n"
"v_mfma_f32_16x16x32_bf16 v[240:243], v[100:103], a[28:31], v[240:243]\n"
"s_waitcnt lgkmcnt(4)\n"
"v_mfma_f32_16x16x32_bf16 v[76:79], v[48:51], v[56:59], v[76:79]\n"
"v_mfma_f32_16x16x32_bf16 v[76:79], v[52:55], v[56:59], v[76:79]\n"
"v_mfma_f32_16x16x32_bf16 v[148:151], v[104:107], a[0:3], v[148:151]\n"
"ds_read_b64_tr_b16 v[120:121], v71 offset:0\n"
"v_mfma_f32_16x16x32_bf16 v[148:151], v[108:111], a[4:7], v[148:151]\n"
"buffer_load_dwordx4 v90, s[36:39], s56 offen offset:2048 lds\n"
"v_mfma_f32_16x16x32_bf16 v[180:183], v[104:107], a[8:11], v[180:183]\n"
"ds_read_b64_tr_b16 v[122:123], v71 offset:1024\n"
"v_mfma_f32_16x16x32_bf16 v[180:183], v[108:111], a[12:15], v[180:183]\n"
"buffer_load_dwordx4 v91, s[36:39], s56 offen offset:3072 lds\n"
"s_add_u32 s56, s56, 128\n"
"s_and_b32 s56, s56, 0x107ff\n"
"v_mfma_f32_16x16x32_bf16 v[212:215], v[104:107], a[16:19], v[212:215]\n"
"ds_read_b64_tr_b16 v[124:125], v71 offset:8192\n"
"s_add_u32 m0, s34, 12288\n"
"v_mfma_f32_16x16x32_bf16 v[212:215], v[108:111], a[20:23], v[212:215]\n"
"buffer_load_dwordx4 v84, s[36:39], s57 offen offset:0 lds\n"
"v_mfma_f32_16x16x32_bf16 v[244:247], v[104:107], a[24:27], v[244:247]\n"
"ds_read_b64_tr_b16 v[126:127], v71 offset:9216\n"
"v_mfma_f32_16x16x32_bf16 v[244:247], v[108:111], a[28:31], v[244:247]\n"
"s_waitcnt vmcnt(21)\n"
"s_barrier\n"
"s_waitcnt lgkmcnt(4)\n"
"v_mfma_f32_16x16x32_bf16 v[152:155], v[112:115], a[0:3], v[152:155]\n"
"ds_read_b64_tr_b16 v[96:97], v64 offset:16384\n"
"v_mfma_f32_16x16x32_bf16 v[152:155], v[116:119], a[4:7], v[152:155]\n"
"buffer_load_dwordx4 v85, s[36:39], s57 offen offset:1024 lds\n"
"v_mfma_f32_16x16x32_bf16 v[184:187], v[112:115], a[8:11], v[184:187]\n"
"ds_read_b64_tr_b16 v[98:99], v64 offset:17408\n"
"v_mfma_f32_16x16x32_bf16 v[184:187], v[116:119], a[12:15], v[184:187]\n"
"buffer_load_dwordx4 v90, s[36:39], s57 offen offset:2048 lds\n"
"v_mfma_f32_16x16x32_bf16 v[216:219], v[112:115], a[16:19], v[216:219]\n"
"ds_read_b64_tr_b16 v[100:101], v64 offset:24576\n"
"v_mfma_f32_16x16x32_bf16 v[216:219], v[116:119], a[20:23], v[216:219]\n"
"buffer_load_dwordx4 v91, s[36:39], s57 offen offset:3072 lds\n"
"s_add_u32 s57, s57, 128\n"
"s_and_b32 s57, s57, 0x107ff\n"
"v_mfma_f32_16x16x32_bf16 v[248:251], v[112:115], a[24:27], v[248:251]\n"
"ds_read_b64_tr_b16 v[102:103], v64 offset:25600\n"
"v_mfma_f32_16x16x32_bf16 v[248:251], v[116:119], a[28:31], v[248:251]\n"
"s_waitcnt lgkmcnt(4)\n"
"v_mfma_f32_16x16x32_bf16 v[156:159], v[120:123], a[0:3], v[156:159]\n"
"ds_read_b64_tr_b16 v[104:105], v65 offset:16384\n"
"v_mfma_f32_16x16x32_bf16 v[156:159], v[124:127], a[4:7], v[156:159]\n"
"v_mfma_f32_16x16x32_bf16 v[188:191], v[120:123], a[8:11], v[188:191]\n"
"ds_read_b64_tr_b16 v[106:107], v65 offset:17408\n"
"v_mfma_f32_16x16x32_bf16 v[188:191], v[124:127], a[12:15], v[188:191]\n"
"v_mfma_f32_16x16x32_bf16 v[220:223], v[120:123], a[16:19], v[220:223]\n"
"ds_read_b64_tr_b16 v[108:109], v65 offset:24576\n"
"v_mfma_f32_16x16x32_bf16 v[220:223], v[124:127], a[20:23], v[220:223]\n"
"v_mfma_f32_16x16x32_bf16 v[252:255], v[120:123], a[24:27], v[252:255]\n"
"ds_read_b64_tr_b16 v[110:111], v65 offset:25600\n"
"v_mfma_f32_16x16x32_bf16 v[252:255], v[124:127], a[28:31], v[252:255]\n"
"s_waitcnt lgkmcnt(4)\n"
"v_mfma_f32_16x16x32_bf16 v[128:131], v[96:99], a[32:35], v[128:131]\n"
"ds_read_b64_tr_b16 v[112:113], v66 offset:16384\n"
"v_mfma_f32_16x16x32_bf16 v[128:131], v[100:103], a[36:39], v[128:131]\n"
"s_waitcnt vmcnt(16)\n"
"ds_read_b128 a[0:3], v86 offset:0\n"
"ds_read_b128 a[4:7], v87 offset:0\n"
"v_mfma_f32_16x16x32_bf16 v[160:163], v[96:99], a[40:43], v[160:163]\n"
"ds_read_b64_tr_b16 v[114:115], v66 offset:17408\n"
"v_mfma_f32_16x16x32_bf16 v[160:163], v[100:103], a[44:47], v[160:163]\n"
"ds_read_b128 a[8:11], v86 offset:2048\n"
"ds_read_b128 a[12:15], v87 offset:2048\n"
"v_mfma_f32_16x16x32_bf16 v[192:195], v[96:99], a[48:51], v[192:195]\n"
"ds_read_b64_tr_b16 v[116:117], v66 offset:24576\n"
"v_mfma_f32_16x16x32_bf16 v[192:195], v[100:103], a[52:55], v[192:195]\n"
"ds_read_b128 a[16:19], v86 offset:4096\n"
"ds_read_b128 a[20:23], v87 offset:4096\n"
"v_mfma_f32_16x16x32_bf16 v[224:227], v[96:99], a[56:59], v[224:227]\n"
"ds_read_b64_tr_b16 v[118:119], v66 offset:25600\n"
"v_mfma_f32_16x16x32_bf16 v[224:227], v[100:103], a[60:63], v[224:227]\n"
"ds_read_b128 a[24:27], v86 offset:6144\n"
"ds_read_b128 a[28:31], v87 offset:6144\n"
"s_waitcnt lgkmcnt(12)\n"
"v_mfma_f32_16x16x32_bf16 v[132:135], v[104:107], a[32:35], v[132:135]\n"
"ds_read_b64_tr_b16 v[120:121], v67 offset:16384\n"
"s_add_u32 m0, s59, 147456\n"
"v_mfma_f32_16x16x32_bf16 v[132:135], v[108:111], a[36:39], v[132:135]\n"
"buffer_load_dwordx4 v80, s[40:43], s58 offen offset:0 lds\n"
"v_mfma_f32_16x16x32_bf16 v[164:167], v[104:107], a[40:43], v[164:167]\n"
"ds_read_b64_tr_b16 v[122:123], v67 offset:17408\n"
"v_mfma_f32_16x16x32_bf16 v[164:167], v[108:111], a[44:47], v[164:167]\n"
"ds_read_b64_tr_b16 v[40:41], v62 offset:16384\n"
"ds_read_b64_tr_b16 v[42:43], v62 offset:17408\n"
"v_mfma_f32_16x16x32_bf16 v[196:199], v[104:107], a[48:51], v[196:199]\n"
"ds_read_b64_tr_b16 v[124:125], v67 offset:24576\n"
"v_mfma_f32_16x16x32_bf16 v[196:199], v[108:111], a[52:55], v[196:199]\n"
"v_mfma_f32_16x16x32_bf16 v[228:231], v[104:107], a[56:59], v[228:231]\n"
"ds_read_b64_tr_b16 v[126:127], v67 offset:25600\n"
"v_mfma_f32_16x16x32_bf16 v[228:231], v[108:111], a[60:63], v[228:231]\n"
"ds_read_b64_tr_b16 v[44:45], v62 offset:24576\n"
"ds_read_b64_tr_b16 v[46:47], v62 offset:25600\n"
"s_waitcnt lgkmcnt(10)\n"
"v_mfma_f32_16x16x32_bf16 v[136:139], v[112:115], a[32:35], v[136:139]\n"
"ds_read_b64_tr_b16 v[96:97], v68 offset:16384\n"
"v_mfma_f32_16x16x32_bf16 v[136:139], v[116:119], a[36:39], v[136:139]\n"
"v_mfma_f32_16x16x32_bf16 v[168:171], v[112:115], a[40:43], v[168:171]\n"
"ds_read_b64_tr_b16 v[98:99], v68 offset:17408\n"
"v_mfma_f32_16x16x32_bf16 v[168:171], v[116:119], a[44:47], v[168:171]\n"
"v_mfma_f32_16x16x32_bf16 v[200:203], v[112:115], a[48:51], v[200:203]\n"
"ds_read_b64_tr_b16 v[100:101], v68 offset:24576\n"
"v_mfma_f32_16x16x32_bf16 v[200:203], v[116:119], a[52:55], v[200:203]\n"
"buffer_load_dwordx4 v81, s[40:43], s58 offen offset:1024 lds\n"
"v_mfma_f32_16x16x32_bf16 v[232:235], v[112:115], a[56:59], v[232:235]\n"
"ds_read_b64_tr_b16 v[102:103], v68 offset:25600\n"
"v_mfma_f32_16x16x32_bf16 v[232:235], v[116:119], a[60:63], v[232:235]\n"
"s_waitcnt lgkmcnt(4)\n"
"v_mfma_f32_16x16x32_bf16 v[72:75], v[40:43], v[56:59], v[72:75]\n"
"v_mfma_f32_16x16x32_bf16 v[72:75], v[44:47], v[56:59], v[72:75]\n"
"v_mfma_f32_16x16x32_bf16 v[140:143], v[120:123], a[32:35], v[140:143]\n"
"ds_read_b64_tr_b16 v[104:105], v69 offset:16384\n"
"v_mfma_f32_16x16x32_bf16 v[140:143], v[124:127], a[36:39], v[140:143]\n"
"v_mfma_f32_16x16x32_bf16 v[172:175], v[120:123], a[40:43], v[172:175]\n"
"ds_read_b64_tr_b16 v[106:107], v69 offset:17408\n"
"v_mfma_f32_16x16x32_bf16 v[172:175], v[124:127], a[44:47], v[172:175]\n"
"ds_read_b64_tr_b16 v[48:49], v63 offset:16384\n"
"ds_read_b64_tr_b16 v[50:51], v63 offset:17408\n"
"v_mfma_f32_16x16x32_bf16 v[204:207], v[120:123], a[48:51], v[204:207]\n"
"ds_read_b64_tr_b16 v[108:109], v69 offset:24576\n"
"v_mfma_f32_16x16x32_bf16 v[204:207], v[124:127], a[52:55], v[204:207]\n"
"v_mfma_f32_16x16x32_bf16 v[236:239], v[120:123], a[56:59], v[236:239]\n"
"ds_read_b64_tr_b16 v[110:111], v69 offset:25600\n"
"v_mfma_f32_16x16x32_bf16 v[236:239], v[124:127], a[60:63], v[236:239]\n"
"ds_read_b64_tr_b16 v[52:53], v63 offset:24576\n"
"ds_read_b64_tr_b16 v[54:55], v63 offset:25600\n"
"s_waitcnt lgkmcnt(8)\n"
"v_mfma_f32_16x16x32_bf16 v[144:147], v[96:99], a[32:35], v[144:147]\n"
"ds_read_b64_tr_b16 v[112:113], v70 offset:16384\n"
"v_mfma_f32_16x16x32_bf16 v[144:147], v[100:103], a[36:39], v[144:147]\n"
"v_mfma_f32_16x16x32_bf16 v[176:179], v[96:99], a[40:43], v[176:179]\n"
"ds_read_b64_tr_b16 v[114:115], v70 offset:17408\n"
"v_mfma_f32_16x16x32_bf16 v[176:179], v[100:103], a[44:47], v[176:179]\n"
"buffer_load_dwordx4 v82, s[40:43], s58 offen offset:2048 lds\n"
"v_mfma_f32_16x16x32_bf16 v[208:211], v[96:99], a[48:51], v[208:211]\n"
"ds_read_b64_tr_b16 v[116:117], v70 offset:24576\n"
"v_mfma_f32_16x16x32_bf16 v[208:211], v[100:103], a[52:55], v[208:211]\n"
"v_mfma_f32_16x16x32_bf16 v[240:243], v[96:99], a[56:59], v[240:243]\n"
"ds_read_b64_tr_b16 v[118:119], v70 offset:25600\n"
"v_mfma_f32_16x16x32_bf16 v[240:243], v[100:103], a[60:63], v[240:243]\n"
"s_waitcnt lgkmcnt(4)\n"
"v_mfma_f32_16x16x32_bf16 v[76:79], v[48:51], v[56:59], v[76:79]\n"
"v_mfma_f32_16x16x32_bf16 v[76:79], v[52:55], v[56:59], v[76:79]\n"
"v_mfma_f32_16x16x32_bf16 v[148:151], v[104:107], a[32:35], v[148:151]\n"
"ds_read_b64_tr_b16 v[120:121], v71 offset:16384\n"
"v_mfma_f32_16x16x32_bf16 v[148:151], v[108:111], a[36:39], v[148:151]\n"
"v_mfma_f32_16x16x32_bf16 v[180:183], v[104:107], a[40:43], v[180:183]\n"
"ds_read_b64_tr_b16 v[122:123], v71 offset:17408\n"
"v_mfma_f32_16x16x32_bf16 v[180:183], v[108:111], a[44:47], v[180:183]\n"
"v_mfma_f32_16x16x32_bf16 v[212:215], v[104:107], a[48:51], v[212:215]\n"
"ds_read_b64_tr_b16 v[124:125], v71 offset:24576\n"
"v_mfma_f32_16x16x32_bf16 v[212:215], v[108:111], a[52:55], v[212:215]\n"
"buffer_load_dwordx4 v83, s[40:43], s58 offen offset:3072 lds\n"
"s_add_u32 s58, s58, 524288\n"
"s_and_b32 s58, s58, 0x7fffff\n"
"v_mfma_f32_16x16x32_bf16 v[244:247], v[104:107], a[56:59], v[244:247]\n"
"ds_read_b64_tr_b16 v[126:127], v71 offset:25600\n"
"v_mfma_f32_16x16x32_bf16 v[244:247], v[108:111], a[60:63], v[244:247]\n"
"s_waitcnt vmcnt(16)\n"
"s_barrier\n"
"s_waitcnt lgkmcnt(4)\n"
"v_mfma_f32_16x16x32_bf16 v[152:155], v[112:115], a[32:35], v[152:155]\n"
"ds_read_b64_tr_b16 v[96:97], v64 offset:32768\n"
"v_mfma_f32_16x16x32_bf16 v[152:155], v[116:119], a[36:39], v[152:155]\n"
"v_mfma_f32_16x16x32_bf16 v[184:187], v[112:115], a[40:43], v[184:187]\n"
"ds_read_b64_tr_b16 v[98:99], v64 offset:33792\n"
"v_mfma_f32_16x16x32_bf16 v[184:187], v[116:119], a[44:47], v[184:187]\n"
"v_mfma_f32_16x16x32_bf16 v[216:219], v[112:115], a[48:51], v[216:219]\n"
"ds_read_b64_tr_b16 v[100:101], v64 offset:40960\n"
"v_mfma_f32_16x16x32_bf16 v[216:219], v[116:119], a[52:55], v[216:219]\n"
"v_mfma_f32_16x16x32_bf16 v[248:251], v[112:115], a[56:59], v[248:251]\n"
"ds_read_b64_tr_b16 v[102:103], v64 offset:41984\n"
"v_mfma_f32_16x16x32_bf16 v[248:251], v[116:119], a[60:63], v[248:251]\n"
"s_waitcnt lgkmcnt(4)\n"
"v_mfma_f32_16x16x32_bf16 v[156:159], v[120:123], a[32:35], v[156:159]\n"
"ds_read_b64_tr_b16 v[104:105], v65 offset:32768\n"
"v_mfma_f32_16x16x32_bf16 v[156:159], v[124:127], a[36:39], v[156:159]\n"
"v_mfma_f32_16x16x32_bf16 v[188:191], v[120:123], a[40:43], v[188:191]\n"
"ds_read_b64_tr_b16 v[106:107], v65 offset:33792\n"
"v_mfma_f32_16x16x32_bf16 v[188:191], v[124:127], a[44:47], v[188:191]\n"
"v_mfma_f32_16x16x32_bf16 v[220:223], v[120:123], a[48:51], v[220:223]\n"
"ds_read_b64_tr_b16 v[108:109], v65 offset:40960\n"
"v_mfma_f32_16x16x32_bf16 v[220:223], v[124:127], a[52:55], v[220:223]\n"
"v_mfma_f32_16x16x32_bf16 v[252:255], v[120:123], a[56:59], v[252:255]\n"
"ds_read_b64_tr_b16 v[110:111], v65 offset:41984\n"
"v_mfma_f32_16x16x32_bf16 v[252:255], v[124:127], a[60:63], v[252:255]\n"
"AL_CHK:\n"
"s_waitcnt vmcnt(15)\n"
"v_cmp_ne_u64 s[66:67], v[28:29], s[96:97]\n"
"v_cmp_ne_u64 vcc, v[30:31], s[96:97]\n"
"s_nop 0\n"
"s_or_b64 s[66:67], s[66:67], vcc\n"
"s_waitcnt vmcnt(14)\n"
"v_cmp_ne_u64 s[98:99], v[32:33], s[96:97]\n"
"v_cmp_ne_u64 vcc, v[34:35], s[96:97]\n"
"s_nop 0\n"
"s_or_b64 s[98:99], s[98:99], vcc\n"
"s_or_b64 s[98:99], s[98:99], s[66:67]\n"
"s_cbranch_scc1 AL_SPIN\n"
"s_or_b32 s98, %17, s55\n"
"s_cmp_eq_u32 s98, 0\n"
"s_cbranch_scc1 AL_EPOCH\n"
"AL_OK:\n"
"s_lshr_b32 s98, %17, 5\n"
"s_lshl_b32 s98, s98, 11\n"
"s_lshl_b32 s99, s55, 9\n"
"s_add_u32 s98, s98, s99\n"
"s_add_u32 s98, s98, 42598400\n"
"v_lshlrev_b32 v36, 3, v4\n"
"v_add_u32 v36, s98, v36\n"
"s_waitcnt lgkmcnt(4)\n"
"v_mfma_f32_16x16x32_bf16 v[128:131], v[96:99], a[0:3], v[128:131]\n"
"ds_read_b64_tr_b16 v[112:113], v66 offset:32768\n"
"v_mfma_f32_16x16x32_bf16 v[128:131], v[100:103], a[4:7], v[128:131]\n"
"s_waitcnt vmcnt(4)\n"
"ds_read_b128 a[32:35], v86 offset:8192\n"
"ds_read_b128 a[36:39], v87 offset:8192\n"
"v_mfma_f32_16x16x32_bf16 v[160:163], v[96:99], a[8:11], v[160:163]\n"
"ds_read_b64_tr_b16 v[114:115], v66 offset:33792\n"
"v_mfma_f32_16x16x32_bf16 v[160:163], v[100:103], a[12:15], v[160:163]\n"
"ds_read_b128 a[40:43], v86 offset:10240\n"
"ds_read_b128 a[44:47], v87 offset:10240\n"
"v_mfma_f32_16x16x32_bf16 v[192:195], v[96:99], a[16:19], v[192:195]\n"
"ds_read_b64_tr_b16 v[116:117], v66 offset:40960\n"
"v_mfma_f32_16x16x32_bf16 v[192:195], v[100:103], a[20:23], v[192:195]\n"
"ds_read_b128 a[48:51], v86 offset:12288\n"
"ds_read_b128 a[52:55], v87 offset:12288\n"
"v_mfma_f32_16x16x32_bf16 v[224:227], v[96:99], a[24:27], v[224:227]\n"
"ds_read_b64_tr_b16 v[118:119], v66 offset:41984\n"
"v_mfma_f32_16x16x32_bf16 v[224:227], v[100:103], a[28:31], v[224:227]\n"
"ds_read_b128 a[56:59], v86 offset:14336\n"
"ds_read_b128 a[60:63], v87 offset:14336\n"
"s_waitcnt lgkmcnt(12)\n"
"v_mfma_f32_16x16x32_bf16 v[132:135], v[104:107], a[0:3], v[132:135]\n"
"ds_read_b64_tr_b16 v[120:121], v67 offset:32768\n"
"v_mfma_f32_16x16x32_bf16 v[132:135], v[108:111], a[4:7], v[132:135]\n"
"global_load_dwordx2 v[28:29], v36, %0 offset:0 sc1\n"
"v_mfma_f32_16x16x32_bf16 v[164:167], v[104:107], a[8:11], v[164:167]\n"
"ds_read_b64_tr_b16 v[122:123], v67 offset:33792\n"
"v_mfma_f32_16x16x32_bf16 v[164:167], v[108:111], a[12:15], v[164:167]\n"
"ds_read_b64_tr_b16 v[40:41], v62 offset:32768\n"
"ds_read_b64_tr_b16 v[42:43], v62 offset:33792\n"
"v_mfma_f32_16x16x32_bf16 v[196:199], v[104:107], a[16:19], v[196:199]\n"
"ds_read_b64_tr_b16 v[124:125], v67 offset:40960\n"
"v_mfma_f32_16x16x32_bf16 v[196:199], v[108:111], a[20:23], v[196:199]\n"
"v_mfma_f32_16x16x32_bf16 v[228:231], v[104:107], a[24:27], v[228:231]\n"
"ds_read_b64_tr_b16 v[126:127], v67 offset:41984\n"
"v_mfma_f32_16x16x32_bf16 v[228:231], v[108:111], a[28:31], v[228:231]\n"
"ds_read_b64_tr_b16 v[44:45], v62 offset:40960\n"
"ds_read_b64_tr_b16 v[46:47], v62 offset:41984\n"
"s_waitcnt lgkmcnt(10)\n"
"v_mfma_f32_16x16x32_bf16 v[136:139], v[112:115], a[0:3], v[136:139]\n"
"ds_read_b64_tr_b16 v[96:97], v68 offset:32768\n"
"v_mfma_f32_16x16x32_bf16 v[136:139], v[116:119], a[4:7], v[136:139]\n"
"global_load_dwordx2 v[30:31], v36, %0 offset:128 sc1\n"
"v_mfma_f32_16x16x32_bf16 v[168:171], v[112:115], a[8:11], v[168:171]\n"
"ds_read_b64_tr_b16 v[98:99], v68 offset:33792\n"
"v_mfma_f32_16x16x32_bf16 v[168:171], v[116:119], a[12:15], v[168:171]\n"
"v_mfma_f32_16x16x32_bf16 v[200:203], v[112:115], a[16:19], v[200:203]\n"
"ds_read_b64_tr_b16 v[100:101], v68 offset:40960\n"
"v_mfma_f32_16x16x32_bf16 v[200:203], v[116:119], a[20:23], v[200:203]\n"
"global_load_dwordx2 v[32:33], v36, %0 offset:256 sc1\n"
"v_mfma_f32_16x16x32_bf16 v[232:235], v[112:115], a[24:27], v[232:235]\n"
"ds_read_b64_tr_b16 v[102:103], v68 offset:41984\n"
"v_mfma_f32_16x16x32_bf16 v[232:235], v[116:119], a[28:31], v[232:235]\n"
"s_waitcnt lgkmcnt(4)\n"
"v_mfma_f32_16x16x32_bf16 v[72:75], v[40:43], v[56:59], v[72:75]\n"
"v_mfma_f32_16x16x32_bf16 v[72:75], v[44:47], v[56:59], v[72:75]\n"
"v_mfma_f32_16x16x32_bf16 v[140:143], v[120:123], a[0:3], v[140:143]\n"
"ds_read_b64_tr_b16 v[104:105], v69 offset:32768\n"
"v_mfma_f32_16x16x32_bf16 v[140:143], v[124:127], a[4:7], v[140:143]\n"
"v_mfma_f32_16x16x32_bf16 v[172:175], v[120:123], a[8:11], v[172:175]\n"
"ds_read_b64_tr_b16 v[106:107], v69 offset:33792\n"
"v_mfma_f32_16x16x32_bf16 v[172:175], v[124:127], a[12:15], v[172:175]\n"
"ds_read_b64_tr_b16 v[48:49], v63 offset:32768\n"
"ds_read_b64_tr_b16 v[50:51], v63 offset:33792\n"
"v_mfma_f32_16x16x32_bf16 v[204:207], v[120:123], a[16:19], v[204:207]\n"
"ds_read_b64_tr_b16 v[108:109], v69 offset:40960\n"
"v_mfma_f32_16x16x32_bf16 v[204:207], v[124:127], a[20:23], v[204:207]\n"
"global_load_dwordx2 v[34:35], v36, %0 offset:384 sc1\n"
"v_mfma_f32_16x16x32_bf16 v[236:239], v[120:123], a[24:27], v[236:239]\n"
"ds_read_b64_tr_b16 v[110:111], v69 offset:41984\n"
"v_mfma_f32_16x16x32_bf16 v[236:239], v[124:127], a[28:31], v[236:239]\n"
"ds_read_b64_tr_b16 v[52:53], v63 offset:40960\n"
"ds_read_b64_tr_b16 v[54:55], v63 offset:41984\n"
"s_waitcnt lgkmcnt(8)\n"
"v_mfma_f32_16x16x32_bf16 v[144:147], v[96:99], a[0:3], v[144:147]\n"
"ds_read_b64_tr_b16 v[112:113], v70 offset:32768\n"
"v_mfma_f32_16x16x32_bf16 v[144:147], v[100:103], a[4:7], v[144:147]\n"
"v_mfma_f32_16x16x32_bf16 v[176:179], v[96:99], a[8:11], v[176:179]\n"
"ds_read_b64_tr_b16 v[114:115], v70 offset:33792\n"
"s_mov_b32 s62, 32768\n"
"s_add_u32 m0, s59, 98304\n"
"v_mfma_f32_16x16x32_bf16 v[176:179], v[100:103], a[12:15], v[176:179]\n"
"buffer_load_dwordx4 v24, s[48:51], s62 offen offset:0 lds\n"
"v_mfma_f32_16x16x32_bf16 v[208:211], v[96:99], a[16:19], v[208:211]\n"
"ds_read_b64_tr_b16 v[116:117], v70 offset:40960\n"
"v_mfma_f32_16x16x32_bf16 v[208:211], v[100:103], a[20:23], v[208:211]\n"
"v_mfma_f32_16x16x32_bf16 v[240:243], v[96:99], a[24:27], v[240:243]\n"
"ds_read_b64_tr_b16 v[118:119], v70 offset:41984\n"
"v_mfma_f32_16x16x32_bf16 v[240:243], v[100:103], a[28:31], v[240:243]\n"
"s_waitcnt lgkmcnt(4)\n"
"v_mfma_f32_16x16x32_bf16 v[76:79], v[48:51], v[56:59], v[76:79]\n"
"v_mfma_f32_16x16x32_bf16 v[76:79], v[52:55], v[56:59], v[76:79]\n"
"v_mfma_f32_16x16x32_bf16 v[148:151], v[104:107], a[0:3], v[148:151]\n"
"ds_read_b64_tr_b16 v[120:121], v71 offset:32768\n"
"v_mfma_f32_16x16x32_bf16 v[148:151], v[108:111], a[4:7], v[148:151]\n"
"buffer_load_dwordx4 v25, s[48:51], s62 offen offset:1024 lds\n"
"v_mfma_f32_16x16x32_bf16 v[180:183], v[104:107], a[8:11], v[180:183]\n"
"ds_read_b64_tr_b16 v[122:123], v71 offset:33792\n"
"v_mfma_f32_16x16x32_bf16 v[180:183], v[108:111], a[12:15], v[180:183]\n"
"v_mfma_f32_16x16x32_bf16 v[212:215], v[104:107], a[16:19], v[212:215]\n"
"ds_read_b64_tr_b16 v[124:125], v71 offset:40960\n"
"v_mfma_f32_16x16x32_bf16 v[212:215], v[108:111], a[20:23], v[212:215]\n"
"buffer_load_dwordx4 v26, s[48:51], s62 offen offset:2048 lds\n"
"v_mfma_f32_16x16x32_bf16 v[244:247], v[104:107], a[24:27], v[244:247]\n"
"ds_read_b64_tr_b16 v[126:127], v71 offset:41984\n"
"v_mfma_f32_16x16x32_bf16 v[244:247], v[108:111], a[28:31], v[244:247]\n"
"s_waitcnt vmcnt(7)\n"
"s_barrier\n"
"s_waitcnt lgkmcnt(4)\n"
"v_mfma_f32_16x16x32_bf16 v[152:155], v[112:115], a[0:3], v[152:155]\n"
"ds_read_b64_tr_b16 v[96:97], v64 offset:49152\n"
"v_mfma_f32_16x16x32_bf16 v[152:155], v[116:119], a[4:7], v[152:155]\n"
"v_mfma_f32_16x16x32_bf16 v[184:187], v[112:115], a[8:11], v[184:187]\n"
"ds_read_b64_tr_b16 v[98:99], v64 offset:50176\n"
"v_mfma_f32_16x16x32_bf16 v[184:187], v[116:119], a[12:15], v[184:187]\n"
"buffer_load_dwordx4 v27, s[48:51], s62 offen offset:3072 lds\n"
"v_mfma_f32_16x16x32_bf16 v[216:219], v[112:115], a[16:19], v[216:219]\n"
"ds_read_b64_tr_b16 v[100:101], v64 offset:57344\n"
"v_mfma_f32_16x16x32_bf16 v[216:219], v[116:119], a[20:23], v[216:219]\n"
"v_mfma_f32_16x16x32_bf16 v[248:251], v[112:115], a[24:27], v[248:251]\n"
"ds_read_b64_tr_b16 v[102:103], v64 offset:58368\n"
"v_mfma_f32_16x16x32_bf16 v[248:251], v[116:119], a[28:31], v[248:251]\n"
"s_waitcnt lgkmcnt(4)\n"
"v_mfma_f32_16x16x32_bf16 v[156:159], v[120:123], a[0:3], v[156:159]\n"
"ds_read_b64_tr_b16 v[104:105], v65 offset:49152\n"
"v_mfma_f32_16x16x32_bf16 v[156:159], v[124:127], a[4:7], v[156:159]\n"
"v_mfma_f32_16x16x32_bf16 v[188:191], v[120:123], a[8:11], v[188:191]\n"
"ds_read_b64_tr_b16 v[106:107], v65 offset:50176\n"
"v_mfma_f32_16x16x32_bf16 v[188:191], v[124:127], a[12:15], v[188:191]\n"
"v_mfma_f32_16x16x32_bf16 v[220:223], v[120:123], a[16:19], v[220:223]\n"
"ds_read_b64_tr_b16 v[108:109], v65 offset:57344\n"
"v_mfma_f32_16x16x32_bf16 v[220:223], v[124:127], a[20:23], v[220:223]\n"
"v_mfma_f32_16x16x32_bf16 v[252:255], v[120:123], a[24:27], v[252:255]\n"
"ds_read_b64_tr_b16 v[110:111], v65 offset:58368\n"
"v_mfma_f32_16x16x32_bf16 v[252:255], v[124:127], a[28:31], v[252:255]\n"
"s_waitcnt lgkmcnt(4)\n"
"v_mfma_f32_16x16x32_bf16 v[128:131], v[96:99], a[32:35], v[128:131]\n"
"ds_read_b64_tr_b16 v[112:113], v66 offset:49152\n"
"s_mov_b32 s62, 0\n"
"s_add_u32 m0, s59, 0\n"
"v_mfma_f32_16x16x32_bf16 v[128:131], v[100:103], a[36:39], v[128:131]\n"
"buffer_load_dwordx4 v20, s[44:47], s62 offen offset:0 lds\n"
"v_mfma_f32_16x16x32_bf16 v[160:163], v[96:99], a[40:43], v[160:163]\n"
"ds_read_b64_tr_b16 v[114:115], v66 offset:50176\n"
"v_accvgpr_write_b32 a0, 0\n"
"v_mfma_f32_16x16x32_bf16 v[160:163], v[100:103], a[44:47], v[160:163]\n"
"buffer_load_dwordx4 v21, s[44:47], s62 offen offset:1024 lds\n"
"v_mfma_f32_16x16x32_bf16 v[192:195], v[96:99], a[48:51], v[192:195]\n"
"ds_read_b64_tr_b16 v[116:117], v66 offset:57344\n"
"v_accvgpr_write_b32 a1, 0\n"
"v_mfma_f32_16x16x32_bf16 v[192:195], v[100:103], a[52:55], v[192:195]\n"
"v_accvgpr_write_b32 a2, 0\n"
"v_accvgpr_write_b32 a3, 0\n"
"v_mfma_f32_16x16x32_bf16 v[224:227], v[96:99], a[56:59], v[224:227]\n"
"ds_read_b64_tr_b16 v[118:119], v66 offset:58368\n"
"v_accvgpr_write_b32 a4, 0\n"
"v_mfma_f32_16x16x32_bf16 v[224:227], v[100:103], a[60:63], v[224:227]\n"
"buffer_load_dwordx4 v22, s[44:47], s62 offen offset:2048 lds\n"
"s_waitcnt lgkmcnt(4)\n"
"v_mfma_f32_16x16x32_bf16 v[132:135], v[104:107], a[32:35], v[132:135]\n"
"ds_read_b64_tr_b16 v[120:121], v67 offset:49152\n"
"v_accvgpr_write_b32 a5, 0\n"
"v_mfma_f32_16x16x32_bf16 v[132:135], v[108:111], a[36:39], v[132:135]\n"
"buffer_load_dwordx4 v23, s[44:47], s62 offen offset:3072 lds\n"
"v_mfma_f32_16x16x32_bf16 v[164:167], v[104:107], a[40:43], v[164:167]\n"
"ds_read_b64_tr_b16 v[122:123], v67 offset:50176\n"
"v_accvgpr_write_b32 a6, 0\n"
"v_mfma_f32_16x16x32_bf16 v[164:167], v[108:111], a[44:47], v[164:167]\n"
"ds_read_b64_tr_b16 v[40:41], v62 offset:49152\n"
"ds_read_b64_tr_b16 v[42:43], v62 offset:50176\n"
"v_mfma_f32_16x16x32_bf16 v[196:199], v[104:107], a[48:51], v[196:199]\n"
"ds_read_b64_tr_b16 v[124:125], v67 offset:57344\n"
"v_accvgpr_write_b32 a7, 0\n"
"v_mfma_f32_16x16x32_bf16 v[196:199], v[108:111], a[52:55], v[196:199]\n"
"v_accvgpr_write_b32 a8, 0\n"
"v_accvgpr_write_b32 a9, 0\n"
"v_mfma_f32_16x16x32_bf16 v[228:231], v[104:107], a[56:59], v[228:231]\n"
"ds_read_b64_tr_b16 v[126:127], v67 offset:58368\n"
"v_accvgpr_write_b32 a10, 0\n"
"v_mfma_f32_16x16x32_bf16 v[228:231], v[108:111], a[60:63], v[228:231]\n"
"ds_read_b64_tr_b16 v[44:45], v62 offset:57344\n"
"ds_read_b64_tr_b16 v[46:47], v62 offset:58368\n"
"s_waitcnt lgkmcnt(8)\n"
"v_mfma_f32_16x16x32_bf16 v[136:139], v[112:115], a[32:35], v[136:139]\n"
"ds_read_b64_tr_b16 v[96:97], v68 offset:49152\n"
"s_mov_b32 s62, 16384\n"
"s_add_u32 m0, s59, 16384\n"
"v_mfma_f32_16x16x32_bf16 v[136:139], v[116:119], a[36:39], v[136:139]\n"
"buffer_load_dwordx4 v20, s[44:47], s62 offen offset:0 lds\n"
"v_mfma_f32_16x16x32_bf16 v[168:171], v[112:115], a[40:43], v[168:171]\n"
"ds_read_b64_tr_b16 v[98:99], v68 offset:50176\n"
"v_accvgpr_write_b32 a11, 0\n"
"v_mfma_f32_16x16x32_bf16 v[168:171], v[116:119], a[44:47], v[168:171]\n"
"buffer_load_dwordx4 v21, s[44:47], s62 offen offset:1024 lds\n"
"v_mfma_f32_16x16x32_bf16 v[200:203], v[112:115], a[48:51], v[200:203]\n"
"ds_read_b64_tr_b16 v[100:101], v68 offset:57344\n"
"v_accvgpr_write_b32 a12, 0\n"
"v_mfma_f32_16x16x32_bf16 v[200:203], v[116:119], a[52:55], v[200:203]\n"
"v_accvgpr_write_b32 a13, 0\n"
"v_accvgpr_write_b32 a14, 0\n"
"v_mfma_f32_16x16x32_bf16 v[232:235], v[112:115], a[56:59], v[232:235]\n"
"ds_read_b64_tr_b16 v[102:103], v68 offset:58368\n"
"v_accvgpr_write_b32 a15, 0\n"
"v_mfma_f32_16x16x32_bf16 v[232:235], v[116:119], a[60:63], v[232:235]\n"
"s_waitcnt lgkmcnt(4)\n"
"v_mfma_f32_16x16x32_bf16 v[72:75], v[40:43], v[56:59], v[72:75]\n"
"v_mfma_f32_16x16x32_bf16 v[72:75], v[44:47], v[56:59], v[72:75]\n"
"v_mfma_f32_16x16x32_bf16 v[140:143], v[120:123], a[32:35], v[140:143]\n"
"ds_read_b64_tr_b16 v[104:105], v69 offset:49152\n"
"v_accvgpr_write_b32 a16, 0\n"
"v_mfma_f32_16x16x32_bf16 v[140:143], v[124:127], a[36:39], v[140:143]\n"
"buffer_load_dwordx4 v22, s[44:47], s62 offen offset:2048 lds\n"
"v_mfma_f32_16x16x32_bf16 v[172:175], v[120:123], a[40:43], v[172:175]\n"
"ds_read_b64_tr_b16 v[106:107], v69 offset:50176\n"
"v_accvgpr_write_b32 a17, 0\n"
"v_mfma_f32_16x16x32_bf16 v[172:175], v[124:127], a[44:47], v[172:175]\n"
"ds_read_b64_tr_b16 v[48:49], v63 offset:49152\n"
"ds_read_b64_tr_b16 v[50:51], v63 offset:50176\n"
"v_mfma_f32_16x16x32_bf16 v[204:207], v[120:123], a[48:51], v[204:207]\n"
"ds_read_b64_tr_b16 v[108:109], v69 offset:57344\n"
"v_accvgpr_write_b32 a18, 0\n"
"v_mfma_f32_16x16x32_bf16 v[204:207], v[124:127], a[52:55], v[204:207]\n"
"v_accvgpr_write_b32 a19, 0\n"
"v_accvgpr_write_b32 a20, 0\n"
"v_mfma_f32_16x16x32_bf16 v[236:239], v[120:123], a[56:59], v[236:239]\n"
"ds_read_b64_tr_b16 v[110:111], v69 offset:58368\n"
"v_accvgpr_write_b32 a21, 0\n"
"v_mfma_f32_16x16x32_bf16 v[236:239], v[124:127], a[60:63], v[236:239]\n"
"ds_read_b64_tr_b16 v[52:53], v63 offset:57344\n"
"ds_read_b64_tr_b16 v[54:55], v63 offset:58368\n"
"s_waitcnt lgkmcnt(8)\n"
"v_mfma_f32_16x16x32_bf16 v[144:147], v[96:99], a[32:35], v[144:147]\n"
"ds_read_b64_tr_b16 v[112:113], v70 offset:49152\n"
"v_accvgpr_write_b32 a22, 0\n"
"v_mfma_f32_16x16x32_bf16 v[144:147], v[100:103], a[36:39], v[144:147]\n"
"buffer_load_dwordx4 v23, s[44:47], s62 offen offset:3072 lds\n"
"v_mfma_f32_16x16x32_bf16 v[176:179], v[96:99], a[40:43], v[176:179]\n"
"ds_read_b64_tr_b16 v[114:115], v70 offset:50176\n"
"s_mov_b32 s62, 32768\n"
"s_add_u32 m0, s59, 32768\n"
"v_mfma_f32_16x16x32_bf16 v[176:179], v[100:103], a[44:47], v[176:179]\n"
"buffer_load_dwordx4 v20, s[44:47], s62 offen offset:0 lds\n"
"v_mfma_f32_16x16x32_bf16 v[208:211], v[96:99], a[48:51], v[208:211]\n"
"ds_read_b64_tr_b16 v[116:117], v70 offset:57344\n"
"v_accvgpr_write_b32 a23, 0\n"
"v_mfma_f32_16x16x32_bf16 v[208:211], v[100:103], a[52:55], v[208:211]\n"
"v_accvgpr_write_b32 a24, 0\n"
"v_accvgpr_write_b32 a25, 0\n"
"v_mfma_f32_16x16x32_bf16 v[240:243], v[96:99], a[56:59], v[240:243]\n"
"ds_read_b64_tr_b16 v[118:119], v70 offset:58368\n"
"v_accvgpr_write_b32 a26, 0\n"
"v_mfma_f32_16x16x32_bf16 v[240:243], v[100:103], a[60:63], v[240:243]\n"
"s_waitcnt lgkmcnt(4)\n"
"v_mfma_f32_16x16x32_bf16 v[76:79], v[48:51], v[56:59], v[76:79]\n"
"v_mfma_f32_16x16x32_bf16 v[76:79], v[52:55], v[56:59], v[76:79]\n"
"v_mfma_f32_16x16x32_bf16 v[148:151], v[104:107], a[32:35], v[148:151]\n"
"ds_read_b64_tr_b16 v[120:121], v71 offset:49152\n"
"v_accvgpr_write_b32 a27, 0\n"
"v_mfma_f32_16x16x32_bf16 v[148:151], v[108:111], a[36:39], v[148:151]\n"
"buffer_load_dwordx4 v21, s[44:47], s62 offen offset:1024 lds\n"
"v_mfma_f32_16x16x32_bf16 v[180:183], v[104:107], a[40:43], v[180:183]\n"
"ds_read_b64_tr_b16 v[122:123], v71 offset:50176\n"
"v_accvgpr_write_b32 a28, 0\n"
"v_mfma_f32_16x16x32_bf16 v[180:183], v[108:111], a[44:47], v[180:183]\n"
"buffer_load_dwordx4 v22, s[44:47], s62 offen offset:2048 lds\n"
"v_mfma_f32_16x16x32_bf16 v[212:215], v[104:107], a[48:51], v[212:215]\n"
"ds_read_b64_tr_b16 v[124:125], v71 offset:57344\n"
"v_accvgpr_write_b32 a29, 0\n"
"v_mfma_f32_16x16x32_bf16 v[212:215], v[108:111], a[52:55], v[212:215]\n"
"v_accvgpr_write_b32 a30, 0\n"
"v_accvgpr_write_b32 a31, 0\n"
"v_mfma_f32_16x16x32_bf16 v[244:247], v[104:107], a[56:59], v[244:247]\n"
"ds_read_b64_tr_b16 v[126:127], v71 offset:58368\n"
"v_lshlrev_b32 v37, 4, v3\n"
"v_lshl_add_u32 v60, s55, 7, v37\n"
"v_mfma_f32_16x16x32_bf16 v[244:247], v[108:111], a[60:63], v[244:247]\n"
"s_barrier\n"
"s_waitcnt lgkmcnt(4)\n"
"v_mfma_f32_16x16x32_bf16 v[152:155], v[112:115], a[32:35], v[152:155]\n"
"v_mfma_f32_16x16x32_bf16 v[152:155], v[116:119], a[36:39], v[152:155]\n"
"buffer_load_dwordx4 v23, s[44:47], s62 offen offset:3072 lds\n"
"v_mfma_f32_16x16x32_bf16 v[184:187], v[112:115], a[40:43], v[184:187]\n"
"v_mfma_f32_16x16x32_bf16 v[184:187], v[116:119], a[44:47], v[184:187]\n"
"v_mfma_f32_16x16x32_bf16 v[216:219], v[112:115], a[48:51], v[216:219]\n"
"v_mfma_f32_16x16x32_bf16 v[216:219], v[116:119], a[52:55], v[216:219]\n"
"v_mfma_f32_16x16x32_bf16 v[248:251], v[112:115], a[56:59], v[248:251]\n"
"v_mfma_f32_16x16x32_bf16 v[248:251], v[116:119], a[60:63], v[248:251]\n"
"s_waitcnt lgkmcnt(0)\n"
"v_mfma_f32_16x16x32_bf16 v[156:159], v[120:123], a[32:35], v[156:159]\n"
"v_mfma_f32_16x16x32_bf16 v[156:159], v[124:127], a[36:39], v[156:159]\n"
"v_mfma_f32_16x16x32_bf16 v[188:191], v[120:123], a[40:43], v[188:191]\n"
"v_mfma_f32_16x16x32_bf16 v[188:191], v[124:127], a[44:47], v[188:191]\n"
"v_mfma_f32_16x16x32_bf16 v[220:223], v[120:123], a[48:51], v[220:223]\n"
"v_mfma_f32_16x16x32_bf16 v[220:223], v[124:127], a[52:55], v[220:223]\n"
"v_mfma_f32_16x16x32_bf16 v[252:255], v[120:123], a[56:59], v[252:255]\n"
"v_mfma_f32_16x16x32_bf16 v[252:255], v[124:127], a[60:63], v[252:255]\n"
"v_add_u32 v60, 131072, v60\n"
"v_add_u32 v37, 131072, v37\n"
"ds_write_b128 v60, v[72:75]\n"
"ds_write_b128 v60, v[76:79] offset:64\n"
"s_sub_u32 s42, 7340032, %15\n"
"s_mov_b64 s[40:41], %4\n"
"s_sub_u32 s40, s40, s42\n"
"s_subb_u32 s41, s41, 0\n"
"s_mov_b32 s42, 7340032\n"
"s_mov_b32 s43, 0x20000\n"
"v_accvgpr_write_b32 a32, 0\n"
"v_accvgpr_write_b32 a33, 0\n"
"v_accvgpr_write_b32 a34, 0\n"
"v_accvgpr_write_b32 a35, 0\n"
"v_accvgpr_write_b32 a36, 0\n"
"v_accvgpr_write_b32 a37, 0\n"
"v_accvgpr_write_b32 a38, 0\n"
"v_accvgpr_write_b32 a39, 0\n"
"v_accvgpr_write_b32 a40, 0\n"
"v_accvgpr_write_b32 a41, 0\n"
"v_accvgpr_write_b32 a42, 0\n"
"v_accvgpr_write_b32 a43, 0\n"
"v_accvgpr_write_b32 a44, 0\n"
"v_accvgpr_write_b32 a45, 0\n"
"v_accvgpr_write_b32 a46, 0\n"
"v_accvgpr_write_b32 a47, 0\n"
"v_accvgpr_write_b32 a48, 0\n"
"v_accvgpr_write_b32 a49, 0\n"
"v_accvgpr_write_b32 a50, 0\n"
"v_accvgpr_write_b32 a51, 0\n"
"v_accvgpr_write_b32 a52, 0\n"
"v_accvgpr_write_b32 a53, 0\n"
"v_accvgpr_write_b32 a54, 0\n"
"v_accvgpr_write_b32 a55, 0\n"
"v_accvgpr_write_b32 a56, 0\n"
"v_accvgpr_write_b32 a57, 0\n"
"v_accvgpr_write_b32 a58, 0\n"
"v_accvgpr_write_b32 a59, 0\n"
"v_accvgpr_write_b32 a60, 0\n"
"v_accvgpr_write_b32 a61, 0\n"
"v_accvgpr_write_b32 a62, 0\n"
"v_accvgpr_write_b32 a63, 0\n"
"s_waitcnt vmcnt(19)\n"
"v_mul_f32 v29, 0x3e0293ee, v29\n"
"s_waitcnt vmcnt(18)\n"
"v_mul_f32 v31, 0x3e0293ee, v31\n"
"s_waitcnt vmcnt(17)\n"
"v_mul_f32 v33, 0x3e0293ee, v33\n"
"s_waitcnt vmcnt(16)\n"
"v_mul_f32 v35, 0x3e0293ee, v35\n"
"s_waitcnt lgkmcnt(0)\n"
"s_waitcnt vmcnt(8)\n"
"s_barrier\n"
"ds_read_b128 v[64:67], v37 offset:0\n"
"ds_read_b128 v[68:71], v37 offset:64\n"
"ds_read_b128 v[72:75], v37 offset:128\n"
"ds_read_b128 v[76:79], v37 offset:192\n"
"ds_read_b128 v[80:83], v37 offset:256\n"
"ds_read_b128 v[84:87], v37 offset:320\n"
"ds_read_b128 v[88:91], v37 offset:384\n"
"ds_read_b128 v[92:95], v37 offset:448\n"
"s_waitcnt lgkmcnt(7)\n"
"v_pk_fma_f32 v[128:129], v[28:29], v[64:65], v[128:129] op_sel_hi:[0,1,1]\n"
"v_pk_fma_f32 v[130:131], v[28:29], v[66:67], v[130:131] op_sel_hi:[0,1,1]\n"
"s_waitcnt lgkmcnt(6)\n"
"v_pk_fma_f32 v[132:133], v[28:29], v[68:69], v[132:133] op_sel_hi:[0,1,1]\n"
"v_pk_fma_f32 v[134:135], v[28:29], v[70:71], v[134:135] op_sel_hi:[0,1,1]\n"
"v_pk_mul_f32 v[128:129], v[128:129], v[28:29] op_sel:[0,1]\n"
"v_pk_mul_f32 v[130:131], v[130:131], v[28:29] op_sel:[0,1]\n"
"v_pk_mul_f32 v[132:133], v[132:133], v[28:29] op_sel:[0,1]\n"
"v_pk_mul_f32 v[134:135], v[134:135], v[28:29] op_sel:[0,1]\n"
"ds_read_b128 a[192:195], v8 offset:0\n"
"ds_read_b128 a[196:199], v9 offset:0\n"
"s_mov_b32 s62, 49152\n"
"s_add_u32 m0, s59, 49152\n"
"s_nop 0\n"
"buffer_load_dwordx4 v20, s[44:47], s62 offen offset:0 lds\n"
"v_cvt_pk_bf16_f32 v96, v128, v129\n"
"v_cvt_pk_bf16_f32 v97, v130, v131\n"
"v_cvt_pk_bf16_f32 v98, v132, v133\n"
"v_cvt_pk_bf16_f32 v99, v134, v135\n"
"s_nop 0\n"
"v_permlane32_swap_b32 v96, v98\n"
"v_permlane32_swap_b32 v97, v99\n"
"s_nop 0\n"
"v_permlane16_swap_b32 v96, v98\n"
"v_permlane16_swap_b32 v97, v99\n"
"v_accvgpr_write_b32 a128, v96\n"
"v_accvgpr_write_b32 a129, v97\n"
"v_accvgpr_write_b32 a130, v98\n"
"v_accvgpr_write_b32 a131, v99\n"
"s_waitcnt lgkmcnt(7)\n"
"v_pk_fma_f32 v[136:137], v[28:29], v[72:73], v[136:137] op_sel_hi:[0,1,1]\n"
"v_pk_fma_f32 v[138:139], v[28:29], v[74:75], v[138:139] op_sel_hi:[0,1,1]\n"
"s_waitcnt lgkmcnt(6)\n"
"v_pk_fma_f32 v[140:141], v[28:29], v[76:77], v[140:141] op_sel_hi:[0,1,1]\n"
"v_pk_fma_f32 v[142:143], v[28:29], v[78:79], v[142:143] op_sel_hi:[0,1,1]\n"
"v_pk_mul_f32 v[136:137], v[136:137], v[28:29] op_sel:[0,1]\n"
"v_pk_mul_f32 v[138:139], v[138:139], v[28:29] op_sel:[0,1]\n"
"v_pk_mul_f32 v[140:141], v[140:141], v[28:29] op_sel:[0,1]\n"
"v_pk_mul_f32 v[142:143], v[142:143], v[28:29] op_sel:[0,1]\n"
"ds_read_b128 a[200:203], v10 offset:0\n"
"ds_read_b128 a[204:207], v11 offset:0\n"
"buffer_load_dwordx4 v21, s[44:47], s62 offen offset:1024 lds\n"
"v_cvt_pk_bf16_f32 v100, v136, v137\n"
"v_cvt_pk_bf16_f32 v101, v138, v139\n"
"v_cvt_pk_bf16_f32 v102, v140, v141\n"
"v_cvt_pk_bf16_f32 v103, v142, v143\n"
"s_nop 0\n"
"v_permlane32_swap_b32 v100, v102\n"
"v_permlane32_swap_b32 v101, v103\n"
"s_nop 0\n"
"v_permlane16_swap_b32 v100, v102\n"
"v_permlane16_swap_b32 v101, v103\n"
"v_accvgpr_write_b32 a132, v100\n"
"v_accvgpr_write_b32 a133, v101\n"
"v_accvgpr_write_b32 a134, v102\n"
"v_accvgpr_write_b32 a135, v103\n"
"s_waitcnt lgkmcnt(7)\n"
"v_pk_fma_f32 v[144:145], v[28:29], v[80:81], v[144:145] op_sel_hi:[0,1,1]\n"
"v_pk_fma_f32 v[146:147], v[28:29], v[82:83], v[146:147] op_sel_hi:[0,1,1]\n"
"s_waitcnt lgkmcnt(6)\n"
"v_pk_fma_f32 v[148:149], v[28:29], v[84:85], v[148:149] op_sel_hi:[0,1,1]\n"
"v_pk_fma_f32 v[150:151], v[28:29], v[86:87], v[150:151] op_sel_hi:[0,1,1]\n"
"v_pk_mul_f32 v[144:145], v[144:145], v[28:29] op_sel:[0,1]\n"
"v_pk_mul_f32 v[146:147], v[146:147], v[28:29] op_sel:[0,1]\n"
"v_pk_mul_f32 v[148:149], v[148:149], v[28:29] op_sel:[0,1]\n"
"v_pk_mul_f32 v[150:151], v[150:151], v[28:29] op_sel:[0,1]\n"
"ds_read_b128 a[208:211], v8 offset:4096\n"
"ds_read_b128 a[212:215], v9 offset:4096\n"
"buffer_load_dwordx4 v22, s[44:47], s62 offen offset:2048 lds\n"
"v_cvt_pk_bf16_f32 v96, v144, v145\n"
"v_cvt_pk_bf16_f32 v97, v146, v147\n"
"v_cvt_pk_bf16_f32 v98, v148, v149\n"
"v_cvt_pk_bf16_f32 v99, v150, v151\n"
"s_nop 0\n"
"v_permlane32_swap_b32 v96, v98\n"
"v_permlane32_swap_b32 v97, v99\n"
"s_nop 0\n"
"v_permlane16_swap_b32 v96, v98\n"
"v_permlane16_swap_b32 v97, v99\n"
"v_accvgpr_write_b32 a136, v96\n"
"v_accvgpr_write_b32 a137, v97\n"
"v_accvgpr_write_b32 a138, v98\n"
"v_accvgpr_write_b32 a139, v99\n"
"s_waitcnt lgkmcnt(7)\n"
"v_pk_fma_f32 v[152:153], v[28:29], v[88:89], v[152:153] op_sel_hi:[0,1,1]\n"
"v_pk_fma_f32 v[154:155], v[28:29], v[90:91], v[154:155] op_sel_hi:[0,1,1]\n"
"s_waitcnt lgkmcnt(6)\n"
"v_pk_fma_f32 v[156:157], v[28:29], v[92:93], v[156:157] op_sel_hi:[0,1,1]\n"
"v_pk_fma_f32 v[158:159], v[28:29], v[94:95], v[158:159] op_sel_hi:[0,1,1]\n"
"v_pk_mul_f32 v[152:153], v[152:153], v[28:29] op_sel:[0,1]\n"
"v_pk_mul_f32 v[154:155], v[154:155], v[28:29] op_sel:[0,1]\n"
"v_pk_mul_f32 v[156:157], v[156:157], v[28:29] op_sel:[0,1]\n"
"v_pk_mul_f32 v[158:159], v[158:159], v[28:29] op_sel:[0,1]\n"
"ds_read_b128 a[216:219], v10 offset:4096\n"
"ds_read_b128 a[220:223], v11 offset:4096\n"
"buffer_load_dwordx4 v23, s[44:47], s62 offen offset:3072 lds\n"
"v_cvt_pk_bf16_f32 v100, v152, v153\n"
"v_cvt_pk_bf16_f32 v101, v154, v155\n"
"v_cvt_pk_bf16_f32 v102, v156, v157\n"
"v_cvt_pk_bf16_f32 v103, v158, v159\n"
"s_nop 0\n"
"v_permlane32_swap_b32 v100, v102\n"
"v_permlane32_swap_b32 v101, v103\n"
"s_nop 0\n"
"v_permlane16_swap_b32 v100, v102\n"
"v_permlane16_swap_b32 v101, v103\n"
"v_accvgpr_write_b32 a140, v100\n"
"v_accvgpr_write_b32 a141, v101\n"
"v_accvgpr_write_b32 a142, v102\n"
"v_accvgpr_write_b32 a143, v103\n"
"v_pk_fma_f32 v[160:161], v[30:31], v[64:65], v[160:161] op_sel_hi:[0,1,1]\n"
"v_pk_fma_f32 v[162:163], v[30:31], v[66:67], v[162:163] op_sel_hi:[0,1,1]\n"
"v_pk_fma_f32 v[164:165], v[30:31], v[68:69], v[164:165] op_sel_hi:[0,1,1]\n"
"v_pk_fma_f32 v[166:167], v[30:31], v[70:71], v[166:167] op_sel_hi:[0,1,1]\n"
"v_pk_mul_f32 v[160:161], v[160:161], v[30:31] op_sel:[0,1]\n"
"v_pk_mul_f32 v[162:163], v[162:163], v[30:31] op_sel:[0,1]\n"
"v_pk_mul_f32 v[164:165], v[164:165], v[30:31] op_sel:[0,1]\n"
"v_pk_mul_f32 v[166:167], v[166:167], v[30:31] op_sel:[0,1]\n"
"ds_read_b128 a[224:227], v8 offset:8192\n"
"ds_read_b128 a[228:231], v9 offset:8192\n"
"s_mov_b32 s62, 49152\n"
"s_add_u32 m0, s59, 114688\n"
"s_nop 0\n"
"buffer_load_dwordx4 v24, s[48:51], s62 offen offset:0 lds\n"
"v_cvt_pk_bf16_f32 v96, v160, v161\n"
"v_cvt_pk_bf16_f32 v97, v162, v163\n"
"v_cvt_pk_bf16_f32 v98, v164, v165\n"
"v_cvt_pk_bf16_f32 v99, v166, v167\n"
"s_nop 0\n"
"v_permlane32_swap_b32 v96, v98\n"
"v_permlane32_swap_b32 v97, v99\n"
"s_nop 0\n"
"v_permlane16_swap_b32 v96, v98\n"
"v_permlane16_swap_b32 v97, v99\n"
"v_accvgpr_write_b32 a144, v96\n"
"v_accvgpr_write_b32 a145, v97\n"
"v_accvgpr_write_b32 a146, v98\n"
"v_accvgpr_write_b32 a147, v99\n"
"v_pk_fma_f32 v[168:169], v[30:31], v[72:73], v[168:169] op_sel_hi:[0,1,1]\n"
"v_pk_fma_f32 v[170:171], v[30:31], v[74:75], v[170:171] op_sel_hi:[0,1,1]\n"
"v_pk_fma_f32 v[172:173], v[30:31], v[76:77], v[172:173] op_sel_hi:[0,1,1]\n"
"v_pk_fma_f32 v[174:175], v[30:31], v[78:79], v[174:175] op_sel_hi:[0,1,1]\n"
"v_pk_mul_f32 v[168:169], v[168:169], v[30:31] op_sel:[0,1]\n"
"v_pk_mul_f32 v[170:171], v[170:171], v[30:31] op_sel:[0,1]\n"
"v_pk_mul_f32 v[172:173], v[172:173], v[30:31] op_sel:[0,1]\n"
"v_pk_mul_f32 v[174:175], v[174:175], v[30:31] op_sel:[0,1]\n"
"ds_read_b128 a[232:235], v10 offset:8192\n"
"ds_read_b128 a[236:239], v11 offset:8192\n"
"buffer_load_dwordx4 v25, s[48:51], s62 offen offset:1024 lds\n"
"v_cvt_pk_bf16_f32 v100, v168, v169\n"
"v_cvt_pk_bf16_f32 v101, v170, v171\n"
"v_cvt_pk_bf16_f32 v102, v172, v173\n"
"v_cvt_pk_bf16_f32 v103, v174, v175\n"
"s_nop 0\n"
"v_permlane32_swap_b32 v100, v102\n"
"v_permlane32_swap_b32 v101, v103\n"
"s_nop 0\n"
"v_permlane16_swap_b32 v100, v102\n"
"v_permlane16_swap_b32 v101, v103\n"
"v_accvgpr_write_b32 a148, v100\n"
"v_accvgpr_write_b32 a149, v101\n"
"v_accvgpr_write_b32 a150, v102\n"
"v_accvgpr_write_b32 a151, v103\n"
"v_pk_fma_f32 v[176:177], v[30:31], v[80:81], v[176:177] op_sel_hi:[0,1,1]\n"
"v_pk_fma_f32 v[178:179], v[30:31], v[82:83], v[178:179] op_sel_hi:[0,1,1]\n"
"v_pk_fma_f32 v[180:181], v[30:31], v[84:85], v[180:181] op_sel_hi:[0,1,1]\n"
"v_pk_fma_f32 v[182:183], v[30:31], v[86:87], v[182:183] op_sel_hi:[0,1,1]\n"
"v_pk_mul_f32 v[176:177], v[176:177], v[30:31] op_sel:[0,1]\n"
"v_pk_mul_f32 v[178:179], v[178:179], v[30:31] op_sel:[0,1]\n"
"v_pk_mul_f32 v[180:181], v[180:181], v[30:31] op_sel:[0,1]\n"
"v_pk_mul_f32 v[182:183], v[182:183], v[30:31] op_sel:[0,1]\n"
"ds_read_b128 a[240:243], v8 offset:12288\n"
"ds_read_b128 a[244:247], v9 offset:12288\n"
"buffer_load_dwordx4 v26, s[48:51], s62 offen offset:2048 lds\n"
"v_cvt_pk_bf16_f32 v96, v176, v177\n"
"v_cvt_pk_bf16_f32 v97, v178, v179\n"
"v_cvt_pk_bf16_f32 v98, v180, v181\n"
"v_cvt_pk_bf16_f32 v99, v182, v183\n"
"s_nop 0\n"
"v_permlane32_swap_b32 v96, v98\n"
"v_permlane32_swap_b32 v97, v99\n"
"s_nop 0\n"
"v_permlane16_swap_b32 v96, v98\n"
"v_permlane16_swap_b32 v97, v99\n"
"v_accvgpr_write_b32 a152, v96\n"
"v_accvgpr_write_b32 a153, v97\n"
"v_accvgpr_write_b32 a154, v98\n"
"v_accvgpr_write_b32 a155, v99\n"
"v_pk_fma_f32 v[184:185], v[30:31], v[88:89], v[184:185] op_sel_hi:[0,1,1]\n"
"v_pk_fma_f32 v[186:187], v[30:31], v[90:91], v[186:187] op_sel_hi:[0,1,1]\n"
"v_pk_fma_f32 v[188:189], v[30:31], v[92:93], v[188:189] op_sel_hi:[0,1,1]\n"
"v_pk_fma_f32 v[190:191], v[30:31], v[94:95], v[190:191] op_sel_hi:[0,1,1]\n"
"v_pk_mul_f32 v[184:185], v[184:185], v[30:31] op_sel:[0,1]\n"
"v_pk_mul_f32 v[186:187], v[186:187], v[30:31] op_sel:[0,1]\n"
"v_pk_mul_f32 v[188:189], v[188:189], v[30:31] op_sel:[0,1]\n"
"v_pk_mul_f32 v[190:191], v[190:191], v[30:31] op_sel:[0,1]\n"
"ds_read_b128 a[248:251], v10 offset:12288\n"
"ds_read_b128 a[252:255], v11 offset:12288\n"
"buffer_load_dwordx4 v27, s[48:51], s62 offen offset:3072 lds\n"
"s_mov_b32 s62, 65536\n"
"v_cvt_pk_bf16_f32 v100, v184, v185\n"
"v_cvt_pk_bf16_f32 v101, v186, v187\n"
"v_cvt_pk_bf16_f32 v102, v188, v189\n"
"v_cvt_pk_bf16_f32 v103, v190, v191\n"
"s_nop 0\n"
"v_permlane32_swap_b32 v100, v102\n"
"v_permlane32_swap_b32 v101, v103\n"
"s_nop 0\n"
"v_permlane16_swap_b32 v100, v102\n"
"v_permlane16_swap_b32 v101, v103\n"
"v_accvgpr_write_b32 a156, v100\n"
"v_accvgpr_write_b32 a157, v101\n"
"v_accvgpr_write_b32 a158, v102\n"
"v_accvgpr_write_b32 a159, v103\n"
"v_pk_fma_f32 v[192:193], v[32:33], v[64:65], v[192:193] op_sel_hi:[0,1,1]\n"
"v_pk_fma_f32 v[194:195], v[32:33], v[66:67], v[194:195] op_sel_hi:[0,1,1]\n"
"v_pk_fma_f32 v[196:197], v[32:33], v[68:69], v[196:197] op_sel_hi:[0,1,1]\n"
"v_pk_fma_f32 v[198:199], v[32:33], v[70:71], v[198:199] op_sel_hi:[0,1,1]\n"
"v_pk_mul_f32 v[192:193], v[192:193], v[32:33] op_sel:[0,1]\n"
"v_pk_mul_f32 v[194:195], v[194:195], v[32:33] op_sel:[0,1]\n"
"v_pk_mul_f32 v[196:197], v[196:197], v[32:33] op_sel:[0,1]\n"
"v_pk_mul_f32 v[198:199], v[198:199], v[32:33] op_sel:[0,1]\n"
"ds_read_b64_tr_b16 v[128:129], v12 offset:0\n"
"ds_read_b64_tr_b16 v[130:131], v12 offset:4096\n"
"ds_read_b64_tr_b16 v[132:133], v13 offset:0\n"
"ds_read_b64_tr_b16 v[134:135], v13 offset:4096\n"
"v_cvt_pk_bf16_f32 v96, v192, v193\n"
"v_cvt_pk_bf16_f32 v97, v194, v195\n"
"v_cvt_pk_bf16_f32 v98, v196, v197\n"
"v_cvt_pk_bf16_f32 v99, v198, v199\n"
"s_nop 0\n"
"v_permlane32_swap_b32 v96, v98\n"
"v_permlane32_swap_b32 v97, v99\n"
"s_nop 0\n"
"v_permlane16_swap_b32 v96, v98\n"
"v_permlane16_swap_b32 v97, v99\n"
"v_accvgpr_write_b32 a160, v96\n"
"v_accvgpr_write_b32 a161, v97\n"
"v_accvgpr_write_b32 a162, v98\n"
"v_accvgpr_write_b32 a163, v99\n"
"v_pk_fma_f32 v[200:201], v[32:33], v[72:73], v[200:201] op_sel_hi:[0,1,1]\n"
"v_pk_fma_f32 v[202:203], v[32:33], v[74:75], v[202:203] op_sel_hi:[0,1,1]\n"
"v_pk_fma_f32 v[204:205], v[32:33], v[76:77], v[204:205] op_sel_hi:[0,1,1]\n"
"v_pk_fma_f32 v[206:207], v[32:33], v[78:79], v[206:207] op_sel_hi:[0,1,1]\n"
"v_pk_mul_f32 v[200:201], v[200:201], v[32:33] op_sel:[0,1]\n"
"v_pk_mul_f32 v[202:203], v[202:203], v[32:33] op_sel:[0,1]\n"
"v_pk_mul_f32 v[204:205], v[204:205], v[32:33] op_sel:[0,1]\n"
"v_pk_mul_f32 v[206:207], v[206:207], v[32:33] op_sel:[0,1]\n"
"ds_read_b64_tr_b16 v[136:137], v14 offset:0\n"
"ds_read_b64_tr_b16 v[138:139], v14 offset:4096\n"
"ds_read_b64_tr_b16 v[140:141], v15 offset:0\n"
"ds_read_b64_tr_b16 v[142:143], v15 offset:4096\n"
"v_cvt_pk_bf16_f32 v100, v200, v201\n"
"v_cvt_pk_bf16_f32 v101, v202, v203\n"
"v_cvt_pk_bf16_f32 v102, v204, v205\n"
"v_cvt_pk_bf16_f32 v103, v206, v207\n"
"s_nop 0\n"
"v_permlane32_swap_b32 v100, v102\n"
"v_permlane32_swap_b32 v101, v103\n"
"s_nop 0\n"
"v_permlane16_swap_b32 v100, v102\n"
"v_permlane16_swap_b32 v101, v103\n"
"v_accvgpr_write_b32 a164, v100\n"
"v_accvgpr_write_b32 a165, v101\n"
"v_accvgpr_write_b32 a166, v102\n"
"v_accvgpr_write_b32 a167, v103\n"
"v_pk_fma_f32 v[208:209], v[32:33], v[80:81], v[208:209] op_sel_hi:[0,1,1]\n"
"v_pk_fma_f32 v[210:211], v[32:33], v[82:83], v[210:211] op_sel_hi:[0,1,1]\n"
"v_pk_fma_f32 v[212:213], v[32:33], v[84:85], v[212:213] op_sel_hi:[0,1,1]\n"
"v_pk_fma_f32 v[214:215], v[32:33], v[86:87], v[214:215] op_sel_hi:[0,1,1]\n"
"v_pk_mul_f32 v[208:209], v[208:209], v[32:33] op_sel:[0,1]\n"
"v_pk_mul_f32 v[210:211], v[210:211], v[32:33] op_sel:[0,1]\n"
"v_pk_mul_f32 v[212:213], v[212:213], v[32:33] op_sel:[0,1]\n"
"v_pk_mul_f32 v[214:215], v[214:215], v[32:33] op_sel:[0,1]\n"
"ds_read_b64_tr_b16 v[144:145], v16 offset:0\n"
"ds_read_b64_tr_b16 v[146:147], v16 offset:4096\n"
"ds_read_b64_tr_b16 v[148:149], v17 offset:0\n"
"ds_read_b64_tr_b16 v[150:151], v17 offset:4096\n"
"v_cvt_pk_bf16_f32 v96, v208, v209\n"
"v_cvt_pk_bf16_f32 v97, v210, v211\n"
"v_cvt_pk_bf16_f32 v98, v212, v213\n"
"v_cvt_pk_bf16_f32 v99, v214, v215\n"
"s_nop 0\n"
"v_permlane32_swap_b32 v96, v98\n"
"v_permlane32_swap_b32 v97, v99\n"
"s_nop 0\n"
"v_permlane16_swap_b32 v96, v98\n"
"v_permlane16_swap_b32 v97, v99\n"
"v_accvgpr_write_b32 a168, v96\n"
"v_accvgpr_write_b32 a169, v97\n"
"v_accvgpr_write_b32 a170, v98\n"
"v_accvgpr_write_b32 a171, v99\n"
"v_pk_fma_f32 v[216:217], v[32:33], v[88:89], v[216:217] op_sel_hi:[0,1,1]\n"
"v_pk_fma_f32 v[218:219], v[32:33], v[90:91], v[218:219] op_sel_hi:[0,1,1]\n"
"v_pk_fma_f32 v[220:221], v[32:33], v[92:93], v[220:221] op_sel_hi:[0,1,1]\n"
"v_pk_fma_f32 v[222:223], v[32:33], v[94:95], v[222:223] op_sel_hi:[0,1,1]\n"
"v_pk_mul_f32 v[216:217], v[216:217], v[32:33] op_sel:[0,1]\n"
"v_pk_mul_f32 v[218:219], v[218:219], v[32:33] op_sel:[0,1]\n"
"v_pk_mul_f32 v[220:221], v[220:221], v[32:33] op_sel:[0,1]\n"
"v_pk_mul_f32 v[222:223], v[222:223], v[32:33] op_sel:[0,1]\n"
"ds_read_b64_tr_b16 v[152:153], v18 offset:0\n"
"ds_read_b64_tr_b16 v[154:155], v18 offset:4096\n"
"ds_read_b64_tr_b16 v[156:157], v19 offset:0\n"
"ds_read_b64_tr_b16 v[158:159], v19 offset:4096\n"
"v_cvt_pk_bf16_f32 v100, v216, v217\n"
"v_cvt_pk_bf16_f32 v101, v218, v219\n"
"v_cvt_pk_bf16_f32 v102, v220, v221\n"
"v_cvt_pk_bf16_f32 v103, v222, v223\n"
"s_nop 0\n"
"v_permlane32_swap_b32 v100, v102\n"
"v_permlane32_swap_b32 v101, v103\n"
"s_nop 0\n"
"v_permlane16_swap_b32 v100, v102\n"
"v_permlane16_swap_b32 v101, v103\n"
"v_accvgpr_write_b32 a172, v100\n"
"v_accvgpr_write_b32 a173, v101\n"
"v_accvgpr_write_b32 a174, v102\n"
"v_accvgpr_write_b32 a175, v103\n"
"v_pk_fma_f32 v[224:225], v[34:35], v[64:65], v[224:225] op_sel_hi:[0,1,1]\n"
"v_pk_fma_f32 v[226:227], v[34:35], v[66:67], v[226:227] op_sel_hi:[0,1,1]\n"
"v_pk_fma_f32 v[228:229], v[34:35], v[68:69], v[228:229] op_sel_hi:[0,1,1]\n"
"v_pk_fma_f32 v[230:231], v[34:35], v[70:71], v[230:231] op_sel_hi:[0,1,1]\n"
"v_pk_mul_f32 v[224:225], v[224:225], v[34:35] op_sel:[0,1]\n"
"v_pk_mul_f32 v[226:227], v[226:227], v[34:35] op_sel:[0,1]\n"
"v_pk_mul_f32 v[228:229], v[228:229], v[34:35] op_sel:[0,1]\n"
"v_pk_mul_f32 v[230:231], v[230:231], v[34:35] op_sel:[0,1]\n"
"ds_read_b64_tr_b16 v[160:161], v12 offset:8192\n"
"ds_read_b64_tr_b16 v[162:163], v12 offset:12288\n"
"ds_read_b64_tr_b16 v[164:165], v13 offset:8192\n"
"ds_read_b64_tr_b16 v[166:167], v13 offset:12288\n"
"v_cvt_pk_bf16_f32 v96, v224, v225\n"
"v_cvt_pk_bf16_f32 v97, v226, v227\n"
"v_cvt_pk_bf16_f32 v98, v228, v229\n"
"v_cvt_pk_bf16_f32 v99, v230, v231\n"
"s_nop 0\n"
"v_permlane32_swap_b32 v96, v98\n"
"v_permlane32_swap_b32 v97, v99\n"
"s_nop 0\n"
"v_permlane16_swap_b32 v96, v98\n"
"v_permlane16_swap_b32 v97, v99\n"
"v_accvgpr_write_b32 a176, v96\n"
"v_accvgpr_write_b32 a177, v97\n"
"v_accvgpr_write_b32 a178, v98\n"
"v_accvgpr_write_b32 a179, v99\n"
"v_pk_fma_f32 v[232:233], v[34:35], v[72:73], v[232:233] op_sel_hi:[0,1,1]\n"
"v_pk_fma_f32 v[234:235], v[34:35], v[74:75], v[234:235] op_sel_hi:[0,1,1]\n"
"v_pk_fma_f32 v[236:237], v[34:35], v[76:77], v[236:237] op_sel_hi:[0,1,1]\n"
"v_pk_fma_f32 v[238:239], v[34:35], v[78:79], v[238:239] op_sel_hi:[0,1,1]\n"
"v_pk_mul_f32 v[232:233], v[232:233], v[34:35] op_sel:[0,1]\n"
"v_pk_mul_f32 v[234:235], v[234:235], v[34:35] op_sel:[0,1]\n"
"v_pk_mul_f32 v[236:237], v[236:237], v[34:35] op_sel:[0,1]\n"
"v_pk_mul_f32 v[238:239], v[238:239], v[34:35] op_sel:[0,1]\n"
"ds_read_b64_tr_b16 v[168:169], v14 offset:8192\n"
"ds_read_b64_tr_b16 v[170:171], v14 offset:12288\n"
"ds_read_b64_tr_b16 v[172:173], v15 offset:8192\n"
"ds_read_b64_tr_b16 v[174:175], v15 offset:12288\n"
"v_cvt_pk_bf16_f32 v100, v232, v233\n"
"v_cvt_pk_bf16_f32 v101, v234, v235\n"
"v_cvt_pk_bf16_f32 v102, v236, v237\n"
"v_cvt_pk_bf16_f32 v103, v238, v239\n"
"s_nop 0\n"
"v_permlane32_swap_b32 v100, v102\n"
"v_permlane32_swap_b32 v101, v103\n"
"s_nop 0\n"
"v_permlane16_swap_b32 v100, v102\n"
"v_permlane16_swap_b32 v101, v103\n"
"v_accvgpr_write_b32 a180, v100\n"
"v_accvgpr_write_b32 a181, v101\n"
"v_accvgpr_write_b32 a182, v102\n"
"v_accvgpr_write_b32 a183, v103\n"
"v_pk_fma_f32 v[240:241], v[34:35], v[80:81], v[240:241] op_sel_hi:[0,1,1]\n"
"v_pk_fma_f32 v[242:243], v[34:35], v[82:83], v[242:243] op_sel_hi:[0,1,1]\n"
"v_pk_fma_f32 v[244:245], v[34:35], v[84:85], v[244:245] op_sel_hi:[0,1,1]\n"
"v_pk_fma_f32 v[246:247], v[34:35], v[86:87], v[246:247] op_sel_hi:[0,1,1]\n"
"v_pk_mul_f32 v[240:241], v[240:241], v[34:35] op_sel:[0,1]\n"
"v_pk_mul_f32 v[242:243], v[242:243], v[34:35] op_sel:[0,1]\n"
"v_pk_mul_f32 v[244:245], v[244:245], v[34:35] op_sel:[0,1]\n"
"v_pk_mul_f32 v[246:247], v[246:247], v[34:35] op_sel:[0,1]\n"
"ds_read_b64_tr_b16 v[176:177], v16 offset:8192\n"
"ds_read_b64_tr_b16 v[178:179], v16 offset:12288\n"
"ds_read_b64_tr_b16 v[180:181], v17 offset:8192\n"
"ds_read_b64_tr_b16 v[182:183], v17 offset:12288\n"
"v_cvt_pk_bf16_f32 v96, v240, v241\n"
"v_cvt_pk_bf16_f32 v97, v242, v243\n"
"v_cvt_pk_bf16_f32 v98, v244, v245\n"
"v_cvt_pk_bf16_f32 v99, v246, v247\n"
"s_nop 0\n"
"v_permlane32_swap_b32 v96, v98\n"
"v_permlane32_swap_b32 v97, v99\n"
"s_nop 0\n"
"v_permlane16_swap_b32 v96, v98\n"
"v_permlane16_swap_b32 v97, v99\n"
"v_accvgpr_write_b32 a184, v96\n"
"v_accvgpr_write_b32 a185, v97\n"
"v_accvgpr_write_b32 a186, v98\n"
"v_accvgpr_write_b32 a187, v99\n"
"v_pk_fma_f32 v[248:249], v[34:35], v[88:89], v[248:249] op_sel_hi:[0,1,1]\n"
"v_pk_fma_f32 v[250:251], v[34:35], v[90:91], v[250:251] op_sel_hi:[0,1,1]\n"
"v_pk_fma_f32 v[252:253], v[34:35], v[92:93], v[252:253] op_sel_hi:[0,1,1]\n"
"v_pk_fma_f32 v[254:255], v[34:35], v[94:95], v[254:255] op_sel_hi:[0,1,1]\n"
"v_pk_mul_f32 v[248:249], v[248:249], v[34:35] op_sel:[0,1]\n"
"v_pk_mul_f32 v[250:251], v[250:251], v[34:35] op_sel:[0,1]\n"
"v_pk_mul_f32 v[252:253], v[252:253], v[34:35] op_sel:[0,1]\n"
"v_pk_mul_f32 v[254:255], v[254:255], v[34:35] op_sel:[0,1]\n"
"ds_read_b64_tr_b16 v[184:185], v18 offset:8192\n"
"ds_read_b64_tr_b16 v[186:187], v18 offset:12288\n"
"ds_read_b64_tr_b16 v[188:189], v19 offset:8192\n"
"ds_read_b64_tr_b16 v[190:191], v19 offset:12288\n"
"v_cvt_pk_bf16_f32 v100, v248, v249\n"
"v_cvt_pk_bf16_f32 v101, v250, v251\n"
"v_cvt_pk_bf16_f32 v102, v252, v253\n"
"v_cvt_pk_bf16_f32 v103, v254, v255\n"
"s_nop 0\n"
"v_permlane32_swap_b32 v100, v102\n"
"v_permlane32_swap_b32 v101, v103\n"
"s_nop 0\n"
"v_permlane16_swap_b32 v100, v102\n"
"v_permlane16_swap_b32 v101, v103\n"
"v_accvgpr_write_b32 a188, v100\n"
"v_accvgpr_write_b32 a189, v101\n"
"v_accvgpr_write_b32 a190, v102\n"
"v_accvgpr_write_b32 a191, v103\n"
"s_waitcnt lgkmcnt(15)\n"
"v_mfma_f32_16x16x32_bf16 v[96:99], a[192:195], a[128:131], 0\n"
"v_mfma_f32_16x16x32_bf16 v[96:99], a[196:199], a[132:135], v[96:99]\n"
"v_mfma_f32_16x16x32_bf16 v[96:99], a[200:203], a[136:139], v[96:99]\n"
"v_mfma_f32_16x16x32_bf16 v[96:99], a[204:207], a[140:143], v[96:99]\n"
"v_mfma_f32_16x16x32_bf16 v[100:103], a[192:195], a[144:147], 0\n"
"v_mfma_f32_16x16x32_bf16 v[100:103], a[196:199], a[148:151], v[100:103]\n"
"v_mfma_f32_16x16x32_bf16 v[100:103], a[200:203], a[152:155], v[100:103]\n"
"v_mfma_f32_16x16x32_bf16 v[100:103], a[204:207], a[156:159], v[100:103]\n"
"v_mfma_f32_16x16x32_bf16 v[104:107], a[192:195], a[160:163], 0\n"
"v_mfma_f32_16x16x32_bf16 v[104:107], a[196:199], a[164:167], v[104:107]\n"
"v_mfma_f32_16x16x32_bf16 v[104:107], a[200:203], a[168:171], v[104:107]\n"
"v_mfma_f32_16x16x32_bf16 v[104:107], a[204:207], a[172:175], v[104:107]\n"
"v_mfma_f32_16x16x32_bf16 v[108:111], a[192:195], a[176:179], 0\n"
"v_mfma_f32_16x16x32_bf16 v[108:111], a[196:199], a[180:183], v[108:111]\n"
"v_mfma_f32_16x16x32_bf16 v[108:111], a[200:203], a[184:187], v[108:111]\n"
"v_mfma_f32_16x16x32_bf16 v[108:111], a[204:207], a[188:191], v[108:111]\n"
"v_mov_b32 v28, 0\n"
"v_mov_b32 v29, 0\n"
"v_mov_b32 v30, 0\n"
"v_mov_b32 v31, 0\n"
"v_max3_f32 v48, v96, v97, v98\n"
"v_max_f32 v48, v48, v99\n"
"v_max3_f32 v50, v100, v101, v102\n"
"v_max_f32 v50, v50, v103\n"
"v_max3_f32 v52, v104, v105, v106\n"
"v_max_f32 v52, v52, v107\n"
"v_max3_f32 v54, v108, v109, v110\n"
"v_max_f32 v54, v54, v111\n"
"v_mov_b32 v49, v48\n"
"v_mov_b32 v51, v50\n"
"v_mov_b32 v53, v52\n"
"v_mov_b32 v55, v54\n"
"v_permlane32_swap_b32 v48, v49\n"
"v_permlane32_swap_b32 v50, v51\n"
"v_permlane32_swap_b32 v52, v53\n"
"v_permlane32_swap_b32 v54, v55\n"
"v_max_f32 v48, v48, v49\n"
"v_max_f32 v50, v50, v51\n"
"v_max_f32 v52, v52, v53\n"
"v_max_f32 v54, v54, v55\n"
"v_mov_b32 v49, v48\n"
"v_mov_b32 v51, v50\n"
"v_mov_b32 v53, v52\n"
"v_mov_b32 v55, v54\n"
"v_permlane16_swap_b32 v48, v49\n"
"v_permlane16_swap_b32 v50, v51\n"
"v_permlane16_swap_b32 v52, v53\n"
"v_permlane16_swap_b32 v54, v55\n"
"v_max_f32 v48, v48, v49\n"
"v_max_f32 v50, v50, v51\n"
"v_max_f32 v52, v52, v53\n"
"v_max_f32 v54, v54, v55\n"
"v_sub_f32 v64, 0, v48\n"
"v_sub_f32 v65, 0, v48\n"
"v_sub_f32 v66, 0, v48\n"
"v_sub_f32 v67, 0, v48\n"
"v_sub_f32 v68, 0, v50\n"
"v_sub_f32 v69, 0, v50\n"
"v_sub_f32 v70, 0, v50\n"
"v_sub_f32 v71, 0, v50\n"
"v_sub_f32 v72, 0, v52\n"
"v_sub_f32 v73, 0, v52\n"
"v_sub_f32 v74, 0, v52\n"
"v_sub_f32 v75, 0, v52\n"
"v_sub_f32 v76, 0, v54\n"
"v_sub_f32 v77, 0, v54\n"
"v_sub_f32 v78, 0, v54\n"
"v_sub_f32 v79, 0, v54\n"
"s_mov_b32 s60, 8\n"
"s_waitcnt vmcnt(16) lgkmcnt(0)\n"
"s_waitcnt vmcnt(12)\n"
"s_barrier\n"
"v_mfma_f32_16x16x32_bf16 v[96:99], a[192:195], a[128:131], v[64:67]\n"
"v_mfma_f32_16x16x32_bf16 v[96:99], a[196:199], a[132:135], v[96:99]\n"
"s_add_u32 m0, s59, 0\n"
"v_mfma_f32_16x16x32_bf16 v[96:99], a[200:203], a[136:139], v[96:99]\n"
"v_mfma_f32_16x16x32_bf16 v[96:99], a[204:207], a[140:143], v[96:99]\n"
"buffer_load_dwordx4 v20, s[44:47], s62 offen offset:0 lds\n"
"v_mfma_f32_16x16x32_bf16 v[100:103], a[208:211], a[128:131], v[64:67]\n"
"v_mfma_f32_16x16x32_bf16 v[100:103], a[212:215], a[132:135], v[100:103]\n"
"v_mfma_f32_16x16x32_bf16 v[100:103], a[216:219], a[136:139], v[100:103]\n"
"v_mfma_f32_16x16x32_bf16 v[100:103], a[220:223], a[140:143], v[100:103]\n"
"s_nop 2\n"
"v_exp_f32 v32, v96\n"
"buffer_load_dwordx4 v21, s[44:47], s62 offen offset:1024 lds\n"
"v_mfma_f32_16x16x32_bf16 v[104:107], a[224:227], a[128:131], v[64:67]\n"
"v_exp_f32 v33, v97\n"
"v_mfma_f32_16x16x32_bf16 v[104:107], a[228:231], a[132:135], v[104:107]\n"
"v_cvt_pk_bf16_f32 v80, v32, v33\n"
"v_mfma_f32_16x16x32_bf16 v[104:107], a[232:235], a[136:139], v[104:107]\n"
"v_exp_f32 v34, v98\n"
"v_mfma_f32_16x16x32_bf16 v[104:107], a[236:239], a[140:143], v[104:107]\n"
"v_mfma_f32_16x16x32_bf16 v[108:111], a[240:243], a[128:131], v[64:67]\n"
"v_exp_f32 v35, v99\n"
"v_mfma_f32_16x16x32_bf16 v[108:111], a[244:247], a[132:135], v[108:111]\n"
"v_cvt_pk_bf16_f32 v81, v34, v35\n"
"v_mfma_f32_16x16x32_bf16 v[108:111], a[248:251], a[136:139], v[108:111]\n"
"v_exp_f32 v36, v100\n"
"v_mfma_f32_16x16x32_bf16 v[108:111], a[252:255], a[140:143], v[108:111]\n"
"v_exp_f32 v37, v101\n"
"ds_read_b64_tr_b16 v[192:193], v12 offset:16384\n"
"v_cvt_pk_bf16_f32 v82, v36, v37\n"
"v_exp_f32 v32, v102\n"
"ds_read_b64_tr_b16 v[194:195], v12 offset:20480\n"
"v_exp_f32 v33, v103\n"
"s_nop 0\n"
"v_cvt_pk_bf16_f32 v83, v32, v33\n"
"v_exp_f32 v34, v104\n"
"buffer_load_dwordx4 v22, s[44:47], s62 offen offset:2048 lds\n"
"v_exp_f32 v35, v105\n"
"ds_read_b64_tr_b16 v[224:225], v12 offset:24576\n"
"v_cvt_pk_bf16_f32 v84, v34, v35\n"
"v_exp_f32 v36, v106\n"
"ds_read_b64_tr_b16 v[226:227], v12 offset:28672\n"
"ds_read_b64_tr_b16 v[196:197], v13 offset:16384\n"
"v_exp_f32 v37, v107\n"
"ds_read_b64_tr_b16 v[198:199], v13 offset:20480\n"
"v_cvt_pk_bf16_f32 v85, v36, v37\n"
"v_exp_f32 v32, v108\n"
"buffer_load_dwordx4 v23, s[44:47], s62 offen offset:3072 lds\n"
"s_branch AT_P1_0\n"
"AT_LOOP:\n"
"s_waitcnt vmcnt(16) lgkmcnt(0)\n"
"s_barrier\n"
"v_mfma_f32_16x16x32_bf16 v[96:99], a[192:195], a[128:131], v[64:67]\n"
"v_exp_f32 v33, v125\n"
"v_mfma_f32_16x16x32_bf16 v[96:99], a[196:199], a[132:135], v[96:99]\n"
"s_add_u32 m0, s59, 0\n"
"v_cvt_pk_bf16_f32 v94, v32, v33\n"
"v_mfma_f32_16x16x32_bf16 v[96:99], a[200:203], a[136:139], v[96:99]\n"
"v_exp_f32 v34, v126\n"
"v_mfma_f32_16x16x32_bf16 v[96:99], a[204:207], a[140:143], v[96:99]\n"
"buffer_load_dwordx4 v20, s[44:47], s62 offen offset:0 lds\n"
"v_mfma_f32_16x16x32_bf16 v[100:103], a[208:211], a[128:131], v[64:67]\n"
"v_exp_f32 v35, v127\n"
"v_mfma_f32_16x16x32_bf16 v[100:103], a[212:215], a[132:135], v[100:103]\n"
"v_cvt_pk_bf16_f32 v95, v34, v35\n"
"v_mfma_f32_16x16x32_bf16 v[100:103], a[216:219], a[136:139], v[100:103]\n"
"v_mfma_f32_16x16x32_bf16 v[100:103], a[220:223], a[140:143], v[100:103]\n"
"v_mfma_f32_16x16x32_bf16 v[44:47], v[56:59], v[88:91], 0\n"
"v_exp_f32 v32, v96\n"
"v_mfma_f32_16x16x32_bf16 v[44:47], v[56:59], v[92:95], v[44:47]\n"
"buffer_load_dwordx4 v21, s[44:47], s62 offen offset:1024 lds\n"
"v_mfma_f32_16x16x32_bf16 v[104:107], a[224:227], a[128:131], v[64:67]\n"
"v_exp_f32 v33, v97\n"
"v_mfma_f32_16x16x32_bf16 v[104:107], a[228:231], a[132:135], v[104:107]\n"
"v_cvt_pk_bf16_f32 v80, v32, v33\n"
"v_mfma_f32_16x16x32_bf16 v[104:107], a[232:235], a[136:139], v[104:107]\n"
"v_exp_f32 v34, v98\n"
"v_mfma_f32_16x16x32_bf16 v[104:107], a[236:239], a[140:143], v[104:107]\n"
"v_cmp_nge_f32 vcc, s64, v44\n"
"v_mfma_f32_16x16x32_bf16 v[108:111], a[240:243], a[128:131], v[64:67]\n"
"v_exp_f32 v35, v99\n"
"v_mfma_f32_16x16x32_bf16 v[108:111], a[244:247], a[132:135], v[108:111]\n"
"s_cbranch_vccnz AT_R0\n"
"AT_B0:\n"
"v_cvt_pk_bf16_f32 v81, v34, v35\n"
"v_mfma_f32_16x16x32_bf16 v[108:111], a[248:251], a[136:139], v[108:111]\n"
"v_exp_f32 v36, v100\n"
"v_mfma_f32_16x16x32_bf16 v[108:111], a[252:255], a[140:143], v[108:111]\n"
"v_add_f32 v31, v31, v44\n"
"v_mfma_f32_16x16x32_bf16 a[96:99], v[192:195], v[88:91], a[96:99]\n"
"v_exp_f32 v37, v101\n"
"v_mfma_f32_16x16x32_bf16 a[96:99], v[224:227], v[92:95], a[96:99]\n"
"ds_read_b64_tr_b16 v[192:193], v12 offset:16384\n"
"v_cvt_pk_bf16_f32 v82, v36, v37\n"
"v_mfma_f32_16x16x32_bf16 a[100:103], v[196:199], v[88:91], a[100:103]\n"
"v_exp_f32 v32, v102\n"
"v_mfma_f32_16x16x32_bf16 a[100:103], v[228:231], v[92:95], a[100:103]\n"
"ds_read_b64_tr_b16 v[194:195], v12 offset:20480\n"
"v_mfma_f32_16x16x32_bf16 a[104:107], v[200:203], v[88:91], a[104:107]\n"
"v_exp_f32 v33, v103\n"
"v_mfma_f32_16x16x32_bf16 a[104:107], v[232:235], v[92:95], a[104:107]\n"
"v_cvt_pk_bf16_f32 v83, v32, v33\n"
"v_mfma_f32_16x16x32_bf16 a[108:111], v[204:207], v[88:91], a[108:111]\n"
"v_exp_f32 v34, v104\n"
"v_mfma_f32_16x16x32_bf16 a[108:111], v[236:239], v[92:95], a[108:111]\n"
"buffer_load_dwordx4 v22, s[44:47], s62 offen offset:2048 lds\n"
"v_mfma_f32_16x16x32_bf16 a[112:115], v[208:211], v[88:91], a[112:115]\n"
"v_exp_f32 v35, v105\n"
"v_mfma_f32_16x16x32_bf16 a[112:115], v[240:243], v[92:95], a[112:115]\n"
"ds_read_b64_tr_b16 v[224:225], v12 offset:24576\n"
"v_cvt_pk_bf16_f32 v84, v34, v35\n"
"v_mfma_f32_16x16x32_bf16 a[116:119], v[212:215], v[88:91], a[116:119]\n"
"v_exp_f32 v36, v106\n"
"v_mfma_f32_16x16x32_bf16 a[116:119], v[244:247], v[92:95], a[116:119]\n"
"ds_read_b64_tr_b16 v[226:227], v12 offset:28672\n"
"ds_read_b64_tr_b16 v[196:197], v13 offset:16384\n"
"v_mfma_f32_16x16x32_bf16 a[120:123], v[216:219], v[88:91], a[120:123]\n"
"v_exp_f32 v37, v107\n"
"v_mfma_f32_16x16x32_bf16 a[120:123], v[248:251], v[92:95], a[120:123]\n"
"ds_read_b64_tr_b16 v[198:199], v13 offset:20480\n"
"v_cvt_pk_bf16_f32 v85, v36, v37\n"
"v_mfma_f32_16x16x32_bf16 a[124:127], v[220:223], v[88:91], a[124:127]\n"
"v_exp_f32 v32, v108\n"
"v_mfma_f32_16x16x32_bf16 a[124:127], v[252:255], v[92:95], a[124:127]\n"
"buffer_load_dwordx4 v23, s[44:47], s62 offen offset:3072 lds\n"
"AT_P1_0:\n"
"v_mfma_f32_16x16x32_bf16 v[112:115], a[192:195], a[144:147], v[68:71]\n"
"v_exp_f32 v33, v109\n"
"v_mfma_f32_16x16x32_bf16 v[112:115], a[196:199], a[148:151], v[112:115]\n"
"ds_read_b64_tr_b16 v[228:229], v13 offset:24576\n"
"v_cvt_pk_bf16_f32 v86, v32, v33\n"
"v_mfma_f32_16x16x32_bf16 v[112:115], a[200:203], a[152:155], v[112:115]\n"
"v_exp_f32 v34, v110\n"
"v_mfma_f32_16x16x32_bf16 v[112:115], a[204:207], a[156:159], v[112:115]\n"
"ds_read_b64_tr_b16 v[230:231], v13 offset:28672\n"
"v_mfma_f32_16x16x32_bf16 v[116:119], a[208:211], a[144:147], v[68:71]\n"
"v_exp_f32 v35, v111\n"
"v_mfma_f32_16x16x32_bf16 v[116:119], a[212:215], a[148:151], v[116:119]\n"
"s_add_u32 m0, s59, 65536\n"
"v_cvt_pk_bf16_f32 v87, v34, v35\n"
"v_mfma_f32_16x16x32_bf16 v[116:119], a[216:219], a[152:155], v[116:119]\n"
"ds_read_b64_tr_b16 v[200:201], v14 offset:16384\n"
"v_mfma_f32_16x16x32_bf16 v[116:119], a[220:223], a[156:159], v[116:119]\n"
"buffer_load_dwordx4 v24, s[48:51], s62 offen offset:0 lds\n"
"v_mfma_f32_16x16x32_bf16 v[40:43], v[56:59], v[80:83], 0\n"
"v_exp_f32 v32, v112\n"
"v_mfma_f32_16x16x32_bf16 v[40:43], v[56:59], v[84:87], v[40:43]\n"
"ds_read_b64_tr_b16 v[202:203], v14 offset:20480\n"
"v_mfma_f32_16x16x32_bf16 v[120:123], a[224:227], a[144:147], v[68:71]\n"
"v_exp_f32 v33, v113\n"
"v_mfma_f32_16x16x32_bf16 v[120:123], a[228:231], a[148:151], v[120:123]\n"
"ds_read_b64_tr_b16 v[232:233], v14 offset:24576\n"
"v_cvt_pk_bf16_f32 v88, v32, v33\n"
"v_mfma_f32_16x16x32_bf16 v[120:123], a[232:235], a[152:155], v[120:123]\n"
"v_exp_f32 v34, v114\n"
"v_mfma_f32_16x16x32_bf16 v[120:123], a[236:239], a[156:159], v[120:123]\n"
"ds_read_b64_tr_b16 v[234:235], v14 offset:28672\n"
"v_cmp_nge_f32 vcc, s64, v40\n"
"v_mfma_f32_16x16x32_bf16 v[124:127], a[240:243], a[144:147], v[68:71]\n"
"v_exp_f32 v35, v115\n"
"v_mfma_f32_16x16x32_bf16 v[124:127], a[244:247], a[148:151], v[124:127]\n"
"s_cbranch_vccnz AT_R1\n"
"AT_B1:\n"
"v_cvt_pk_bf16_f32 v89, v34, v35\n"
"v_mfma_f32_16x16x32_bf16 v[124:127], a[248:251], a[152:155], v[124:127]\n"
"v_exp_f32 v36, v116\n"
"v_mfma_f32_16x16x32_bf16 v[124:127], a[252:255], a[156:159], v[124:127]\n"
"ds_read_b64_tr_b16 v[204:205], v15 offset:16384\n"
"v_add_f32 v28, v28, v40\n"
"v_mfma_f32_16x16x32_bf16 a[0:3], v[128:131], v[80:83], a[0:3]\n"
"v_exp_f32 v37, v117\n"
"v_mfma_f32_16x16x32_bf16 a[0:3], v[160:163], v[84:87], a[0:3]\n"
"ds_read_b64_tr_b16 v[206:207], v15 offset:20480\n"
"v_cvt_pk_bf16_f32 v90, v36, v37\n"
"v_mfma_f32_16x16x32_bf16 a[4:7], v[132:135], v[80:83], a[4:7]\n"
"v_exp_f32 v32, v118\n"
"v_mfma_f32_16x16x32_bf16 a[4:7], v[164:167], v[84:87], a[4:7]\n"
"buffer_load_dwordx4 v25, s[48:51], s62 offen offset:1024 lds\n"
"v_mfma_f32_16x16x32_bf16 a[8:11], v[136:139], v[80:83], a[8:11]\n"
"v_exp_f32 v33, v119\n"
"v_mfma_f32_16x16x32_bf16 a[8:11], v[168:171], v[84:87], a[8:11]\n"
"ds_read_b64_tr_b16 v[236:237], v15 offset:24576\n"
"v_cvt_pk_bf16_f32 v91, v32, v33\n"
"v_mfma_f32_16x16x32_bf16 a[12:15], v[140:143], v[80:83], a[12:15]\n"
"v_exp_f32 v34, v120\n"
"v_mfma_f32_16x16x32_bf16 a[12:15], v[172:175], v[84:87], a[12:15]\n"
"ds_read_b64_tr_b16 v[238:239], v15 offset:28672\n"
"ds_read_b64_tr_b16 v[208:209], v16 offset:16384\n"
"v_mfma_f32_16x16x32_bf16 a[16:19], v[144:147], v[80:83], a[16:19]\n"
"v_exp_f32 v35, v121\n"
"v_mfma_f32_16x16x32_bf16 a[16:19], v[176:179], v[84:87], a[16:19]\n"
"v_cvt_pk_bf16_f32 v92, v34, v35\n"
"v_mfma_f32_16x16x32_bf16 a[20:23], v[148:151], v[80:83], a[20:23]\n"
"v_exp_f32 v36, v122\n"
"v_mfma_f32_16x16x32_bf16 a[20:23], v[180:183], v[84:87], a[20:23]\n"
"ds_read_b64_tr_b16 v[210:211], v16 offset:20480\n"
"v_mfma_f32_16x16x32_bf16 a[24:27], v[152:155], v[80:83], a[24:27]\n"
"v_exp_f32 v37, v123\n"
"v_mfma_f32_16x16x32_bf16 a[24:27], v[184:187], v[84:87], a[24:27]\n"
"ds_read_b64_tr_b16 v[240:241], v16 offset:24576\n"
"v_cvt_pk_bf16_f32 v93, v36, v37\n"
"v_mfma_f32_16x16x32_bf16 a[28:31], v[156:159], v[80:83], a[28:31]\n"
"v_exp_f32 v32, v124\n"
"v_mfma_f32_16x16x32_bf16 a[28:31], v[188:191], v[84:87], a[28:31]\n"
"ds_read_b64_tr_b16 v[242:243], v16 offset:28672\n"
"v_mfma_f32_16x16x32_bf16 v[96:99], a[192:195], a[160:163], v[72:75]\n"
"v_exp_f32 v33, v125\n"
"v_mfma_f32_16x16x32_bf16 v[96:99], a[196:199], a[164:167], v[96:99]\n"
"ds_read_b64_tr_b16 v[212:213], v17 offset:16384\n"
"v_cvt_pk_bf16_f32 v94, v32, v33\n"
"v_mfma_f32_16x16x32_bf16 v[96:99], a[200:203], a[168:171], v[96:99]\n"
"v_exp_f32 v34, v126\n"
"v_mfma_f32_16x16x32_bf16 v[96:99], a[204:207], a[172:175], v[96:99]\n"
"buffer_load_dwordx4 v26, s[48:51], s62 offen offset:2048 lds\n"
"v_mfma_f32_16x16x32_bf16 v[100:103], a[208:211], a[160:163], v[72:75]\n"
"v_exp_f32 v35, v127\n"
"v_mfma_f32_16x16x32_bf16 v[100:103], a[212:215], a[164:167], v[100:103]\n"
"ds_read_b64_tr_b16 v[214:215], v17 offset:20480\n"
"v_cvt_pk_bf16_f32 v95, v34, v35\n"
"v_mfma_f32_16x16x32_bf16 v[100:103], a[216:219], a[168:171], v[100:103]\n"
"ds_read_b64_tr_b16 v[244:245], v17 offset:24576\n"
"v_mfma_f32_16x16x32_bf16 v[100:103], a[220:223], a[172:175], v[100:103]\n"
"v_mfma_f32_16x16x32_bf16 v[44:47], v[56:59], v[88:91], 0\n"
"v_exp_f32 v32, v96\n"
"v_mfma_f32_16x16x32_bf16 v[44:47], v[56:59], v[92:95], v[44:47]\n"
"ds_read_b64_tr_b16 v[246:247], v17 offset:28672\n"
"v_mfma_f32_16x16x32_bf16 v[104:107], a[224:227], a[160:163], v[72:75]\n"
"v_exp_f32 v33, v97\n"
"v_mfma_f32_16x16x32_bf16 v[104:107], a[228:231], a[164:167], v[104:107]\n"
"ds_read_b64_tr_b16 v[216:217], v18 offset:16384\n"
"v_cvt_pk_bf16_f32 v80, v32, v33\n"
"v_mfma_f32_16x16x32_bf16 v[104:107], a[232:235], a[168:171], v[104:107]\n"
"v_exp_f32 v34, v98\n"
"v_mfma_f32_16x16x32_bf16 v[104:107], a[236:239], a[172:175], v[104:107]\n"
"v_cmp_nge_f32 vcc, s64, v44\n"
"v_mfma_f32_16x16x32_bf16 v[108:111], a[240:243], a[160:163], v[72:75]\n"
"v_exp_f32 v35, v99\n"
"v_mfma_f32_16x16x32_bf16 v[108:111], a[244:247], a[164:167], v[108:111]\n"
"s_cbranch_vccnz AT_R2\n"
"AT_B2:\n"
"v_cvt_pk_bf16_f32 v81, v34, v35\n"
"v_mfma_f32_16x16x32_bf16 v[108:111], a[248:251], a[168:171], v[108:111]\n"
"v_exp_f32 v36, v100\n"
"v_mfma_f32_16x16x32_bf16 v[108:111], a[252:255], a[172:175], v[108:111]\n"
"ds_read_b64_tr_b16 v[218:219], v18 offset:20480\n"
"v_add_f32 v29, v29, v44\n"
"v_mfma_f32_16x16x32_bf16 a[32:35], v[128:131], v[88:91], a[32:35]\n"
"v_exp_f32 v37, v101\n"
"v_mfma_f32_16x16x32_bf16 a[32:35], v[160:163], v[92:95], a[32:35]\n"
"ds_read_b64_tr_b16 v[248:249], v18 offset:24576\n"
"v_cvt_pk_bf16_f32 v82, v36, v37\n"
"v_mfma_f32_16x16x32_bf16 a[36:39], v[132:135], v[88:91], a[36:39]\n"
"v_exp_f32 v32, v102\n"
"v_mfma_f32_16x16x32_bf16 a[36:39], v[164:167], v[92:95], a[36:39]\n"
"buffer_load_dwordx4 v27, s[48:51], s62 offen offset:3072 lds\n"
"v_mfma_f32_16x16x32_bf16 a[40:43], v[136:139], v[88:91], a[40:43]\n"
"v_exp_f32 v33, v103\n"
"v_mfma_f32_16x16x32_bf16 a[40:43], v[168:171], v[92:95], a[40:43]\n"
"ds_read_b64_tr_b16 v[250:251], v18 offset:28672\n"
"v_cvt_pk_bf16_f32 v83, v32, v33\n"
"v_mfma_f32_16x16x32_bf16 a[44:47], v[140:143], v[88:91], a[44:47]\n"
"v_exp_f32 v34, v104\n"
"v_mfma_f32_16x16x32_bf16 a[44:47], v[172:175], v[92:95], a[44:47]\n"
"ds_read_b64_tr_b16 v[220:221], v19 offset:16384\n"
"ds_read_b64_tr_b16 v[222:223], v19 offset:20480\n"
"v_mfma_f32_16x16x32_bf16 a[48:51], v[144:147], v[88:91], a[48:51]\n"
"v_exp_f32 v35, v105\n"
"v_mfma_f32_16x16x32_bf16 a[48:51], v[176:179], v[92:95], a[48:51]\n"
"ds_read_b64_tr_b16 v[252:253], v19 offset:24576\n"
"v_cvt_pk_bf16_f32 v84, v34, v35\n"
"v_mfma_f32_16x16x32_bf16 a[52:55], v[148:151], v[88:91], a[52:55]\n"
"v_exp_f32 v36, v106\n"
"v_mfma_f32_16x16x32_bf16 a[52:55], v[180:183], v[92:95], a[52:55]\n"
"ds_read_b64_tr_b16 v[254:255], v19 offset:28672\n"
"v_mfma_f32_16x16x32_bf16 a[56:59], v[152:155], v[88:91], a[56:59]\n"
"v_exp_f32 v37, v107\n"
"v_mfma_f32_16x16x32_bf16 a[56:59], v[184:187], v[92:95], a[56:59]\n"
"s_add_u32 s62, s62, s54\n"
"v_cvt_pk_bf16_f32 v85, v36, v37\n"
"v_mfma_f32_16x16x32_bf16 a[60:63], v[156:159], v[88:91], a[60:63]\n"
"v_exp_f32 v32, v108\n"
"v_mfma_f32_16x16x32_bf16 a[60:63], v[188:191], v[92:95], a[60:63]\n"
"v_mfma_f32_16x16x32_bf16 v[112:115], a[192:195], a[176:179], v[76:79]\n"
"v_exp_f32 v33, v109\n"
"v_mfma_f32_16x16x32_bf16 v[112:115], a[196:199], a[180:183], v[112:115]\n"
"ds_read_b128 a[192:195], v8 offset:16384\n"
"v_cvt_pk_bf16_f32 v86, v32, v33\n"
"v_mfma_f32_16x16x32_bf16 v[112:115], a[200:203], a[184:187], v[112:115]\n"
"v_exp_f32 v34, v110\n"
"v_mfma_f32_16x16x32_bf16 v[112:115], a[204:207], a[188:191], v[112:115]\n"
"ds_read_b128 a[196:199], v9 offset:16384\n"
"ds_read_b128 a[200:203], v10 offset:16384\n"
"v_mfma_f32_16x16x32_bf16 v[116:119], a[208:211], a[176:179], v[76:79]\n"
"v_exp_f32 v35, v111\n"
"v_mfma_f32_16x16x32_bf16 v[116:119], a[212:215], a[180:183], v[116:119]\n"
"ds_read_b128 a[204:207], v11 offset:16384\n"
"v_cvt_pk_bf16_f32 v87, v34, v35\n"
"v_mfma_f32_16x16x32_bf16 v[116:119], a[216:219], a[184:187], v[116:119]\n"
"ds_read_b128 a[208:211], v8 offset:20480\n"
"ds_read_b128 a[212:215], v9 offset:20480\n"
"v_mfma_f32_16x16x32_bf16 v[116:119], a[220:223], a[188:191], v[116:119]\n"
"ds_read_b128 a[216:219], v10 offset:20480\n"
"ds_read_b128 a[220:223], v11 offset:20480\n"
"v_mfma_f32_16x16x32_bf16 v[40:43], v[56:59], v[80:83], 0\n"
"v_exp_f32 v32, v112\n"
"v_mfma_f32_16x16x32_bf16 v[40:43], v[56:59], v[84:87], v[40:43]\n"
"v_mfma_f32_16x16x32_bf16 v[120:123], a[224:227], a[176:179], v[76:79]\n"
"v_exp_f32 v33, v113\n"
"v_mfma_f32_16x16x32_bf16 v[120:123], a[228:231], a[180:183], v[120:123]\n"
"ds_read_b128 a[224:227], v8 offset:24576\n"
"v_cvt_pk_bf16_f32 v88, v32, v33\n"
"v_mfma_f32_16x16x32_bf16 v[120:123], a[232:235], a[184:187], v[120:123]\n"
"v_exp_f32 v34, v114\n"
"v_mfma_f32_16x16x32_bf16 v[120:123], a[236:239], a[188:191], v[120:123]\n"
"ds_read_b128 a[228:231], v9 offset:24576\n"
"v_cmp_nge_f32 vcc, s64, v40\n"
"v_mfma_f32_16x16x32_bf16 v[124:127], a[240:243], a[176:179], v[76:79]\n"
"v_exp_f32 v35, v115\n"
"v_mfma_f32_16x16x32_bf16 v[124:127], a[244:247], a[180:183], v[124:127]\n"
"s_cbranch_vccnz AT_R3\n"
"AT_B3:\n"
"v_cvt_pk_bf16_f32 v89, v34, v35\n"
"v_mfma_f32_16x16x32_bf16 v[124:127], a[248:251], a[184:187], v[124:127]\n"
"v_exp_f32 v36, v116\n"
"v_mfma_f32_16x16x32_bf16 v[124:127], a[252:255], a[188:191], v[124:127]\n"
"ds_read_b128 a[232:235], v10 offset:24576\n"
"v_add_f32 v30, v30, v40\n"
"v_mfma_f32_16x16x32_bf16 a[64:67], v[128:131], v[80:83], a[64:67]\n"
"v_exp_f32 v37, v117\n"
"v_mfma_f32_16x16x32_bf16 a[64:67], v[160:163], v[84:87], a[64:67]\n"
"ds_read_b128 a[236:239], v11 offset:24576\n"
"v_cvt_pk_bf16_f32 v90, v36, v37\n"
"v_mfma_f32_16x16x32_bf16 a[68:71], v[132:135], v[80:83], a[68:71]\n"
"v_exp_f32 v32, v118\n"
"v_mfma_f32_16x16x32_bf16 a[68:71], v[164:167], v[84:87], a[68:71]\n"
"ds_read_b128 a[240:243], v8 offset:28672\n"
"ds_read_b128 a[244:247], v9 offset:28672\n"
"v_mfma_f32_16x16x32_bf16 a[72:75], v[136:139], v[80:83], a[72:75]\n"
"v_exp_f32 v33, v119\n"
"v_mfma_f32_16x16x32_bf16 a[72:75], v[168:171], v[84:87], a[72:75]\n"
"ds_read_b128 a[248:251], v10 offset:28672\n"
"v_cvt_pk_bf16_f32 v91, v32, v33\n"
"v_mfma_f32_16x16x32_bf16 a[76:79], v[140:143], v[80:83], a[76:79]\n"
"v_exp_f32 v34, v120\n"
"v_mfma_f32_16x16x32_bf16 a[76:79], v[172:175], v[84:87], a[76:79]\n"
"ds_read_b128 a[252:255], v11 offset:28672\n"
"v_mfma_f32_16x16x32_bf16 a[80:83], v[144:147], v[80:83], a[80:83]\n"
"v_exp_f32 v35, v121\n"
"v_mfma_f32_16x16x32_bf16 a[80:83], v[176:179], v[84:87], a[80:83]\n"
"v_cvt_pk_bf16_f32 v92, v34, v35\n"
"v_mfma_f32_16x16x32_bf16 a[84:87], v[148:151], v[80:83], a[84:87]\n"
"v_exp_f32 v36, v122\n"
"v_mfma_f32_16x16x32_bf16 a[84:87], v[180:183], v[84:87], a[84:87]\n"
"buffer_load_dwordx4 v[60:63], v38, s[40:43], s52 offen\n"
"v_mfma_f32_16x16x32_bf16 a[88:91], v[152:155], v[80:83], a[88:91]\n"
"v_exp_f32 v37, v123\n"
"v_mfma_f32_16x16x32_bf16 a[88:91], v[184:187], v[84:87], a[88:91]\n"
"v_cvt_pk_bf16_f32 v93, v36, v37\n"
"v_mfma_f32_16x16x32_bf16 a[92:95], v[156:159], v[80:83], a[92:95]\n"
"v_exp_f32 v32, v124\n"
"v_mfma_f32_16x16x32_bf16 a[92:95], v[188:191], v[84:87], a[92:95]\n"
"s_waitcnt vmcnt(16) lgkmcnt(0)\n"
"s_barrier\n"
"v_mfma_f32_16x16x32_bf16 v[96:99], a[192:195], a[128:131], v[64:67]\n"
"v_exp_f32 v33, v125\n"
"v_mfma_f32_16x16x32_bf16 v[96:99], a[196:199], a[132:135], v[96:99]\n"
"s_add_u32 m0, s59, 16384\n"
"v_cvt_pk_bf16_f32 v94, v32, v33\n"
"v_mfma_f32_16x16x32_bf16 v[96:99], a[200:203], a[136:139], v[96:99]\n"
"v_exp_f32 v34, v126\n"
"v_mfma_f32_16x16x32_bf16 v[96:99], a[204:207], a[140:143], v[96:99]\n"
"buffer_load_dwordx4 v20, s[44:47], s62 offen offset:0 lds\n"
"v_mfma_f32_16x16x32_bf16 v[100:103], a[208:211], a[128:131], v[64:67]\n"
"v_exp_f32 v35, v127\n"
"v_mfma_f32_16x16x32_bf16 v[100:103], a[212:215], a[132:135], v[100:103]\n"
"v_cvt_pk_bf16_f32 v95, v34, v35\n"
"v_mfma_f32_16x16x32_bf16 v[100:103], a[216:219], a[136:139], v[100:103]\n"
"v_mfma_f32_16x16x32_bf16 v[100:103], a[220:223], a[140:143], v[100:103]\n"
"v_mfma_f32_16x16x32_bf16 v[44:47], v[56:59], v[88:91], 0\n"
"v_exp_f32 v32, v96\n"
"v_mfma_f32_16x16x32_bf16 v[44:47], v[56:59], v[92:95], v[44:47]\n"
"buffer_load_dwordx4 v21, s[44:47], s62 offen offset:1024 lds\n"
"v_mfma_f32_16x16x32_bf16 v[104:107], a[224:227], a[128:131], v[64:67]\n"
"v_exp_f32 v33, v97\n"
"v_mfma_f32_16x16x32_bf16 v[104:107], a[228:231], a[132:135], v[104:107]\n"
"v_cvt_pk_bf16_f32 v80, v32, v33\n"
"v_mfma_f32_16x16x32_bf16 v[104:107], a[232:235], a[136:139], v[104:107]\n"
"v_exp_f32 v34, v98\n"
"v_mfma_f32_16x16x32_bf16 v[104:107], a[236:239], a[140:143], v[104:107]\n"
"v_cmp_nge_f32 vcc, s64, v44\n"
"v_mfma_f32_16x16x32_bf16 v[108:111], a[240:243], a[128:131], v[64:67]\n"
"v_exp_f32 v35, v99\n"
"v_mfma_f32_16x16x32_bf16 v[108:111], a[244:247], a[132:135], v[108:111]\n"
"s_cbranch_vccnz AT_R4\n"
"AT_B4:\n"
"v_cvt_pk_bf16_f32 v81, v34, v35\n"
"v_mfma_f32_16x16x32_bf16 v[108:111], a[248:251], a[136:139], v[108:111]\n"
"v_exp_f32 v36, v100\n"
"v_mfma_f32_16x16x32_bf16 v[108:111], a[252:255], a[140:143], v[108:111]\n"
"v_add_f32 v31, v31, v44\n"
"v_mfma_f32_16x16x32_bf16 a[96:99], v[128:131], v[88:91], a[96:99]\n"
"v_exp_f32 v37, v101\n"
"v_mfma_f32_16x16x32_bf16 a[96:99], v[160:163], v[92:95], a[96:99]\n"
"ds_read_b64_tr_b16 v[128:129], v12 offset:32768\n"
"v_cvt_pk_bf16_f32 v82, v36, v37\n"
"v_mfma_f32_16x16x32_bf16 a[100:103], v[132:135], v[88:91], a[100:103]\n"
"v_exp_f32 v32, v102\n"
"v_mfma_f32_16x16x32_bf16 a[100:103], v[164:167], v[92:95], a[100:103]\n"
"ds_read_b64_tr_b16 v[130:131], v12 offset:36864\n"
"v_mfma_f32_16x16x32_bf16 a[104:107], v[136:139], v[88:91], a[104:107]\n"
"v_exp_f32 v33, v103\n"
"v_mfma_f32_16x16x32_bf16 a[104:107], v[168:171], v[92:95], a[104:107]\n"
"v_cvt_pk_bf16_f32 v83, v32, v33\n"
"v_mfma_f32_16x16x32_bf16 a[108:111], v[140:143], v[88:91], a[108:111]\n"
"v_exp_f32 v34, v104\n"
"v_mfma_f32_16x16x32_bf16 a[108:111], v[172:175], v[92:95], a[108:111]\n"
"buffer_load_dwordx4 v22, s[44:47], s62 offen offset:2048 lds\n"
"v_mfma_f32_16x16x32_bf16 a[112:115], v[144:147], v[88:91], a[112:115]\n"
"v_exp_f32 v35, v105\n"
"v_mfma_f32_16x16x32_bf16 a[112:115], v[176:179], v[92:95], a[112:115]\n"
"ds_read_b64_tr_b16 v[160:161], v12 offset:40960\n"
"v_cvt_pk_bf16_f32 v84, v34, v35\n"
"v_mfma_f32_16x16x32_bf16 a[116:119], v[148:151], v[88:91], a[116:119]\n"
"v_exp_f32 v36, v106\n"
"v_mfma_f32_16x16x32_bf16 a[116:119], v[180:183], v[92:95], a[116:119]\n"
"ds_read_b64_tr_b16 v[162:163], v12 offset:45056\n"
"ds_read_b64_tr_b16 v[132:133], v13 offset:32768\n"
"v_mfma_f32_16x16x32_bf16 a[120:123], v[152:155], v[88:91], a[120:123]\n"
"v_exp_f32 v37, v107\n"
"v_mfma_f32_16x16x32_bf16 a[120:123], v[184:187], v[92:95], a[120:123]\n"
"ds_read_b64_tr_b16 v[134:135], v13 offset:36864\n"
"v_cvt_pk_bf16_f32 v85, v36, v37\n"
"v_mfma_f32_16x16x32_bf16 a[124:127], v[156:159], v[88:91], a[124:127]\n"
"v_exp_f32 v32, v108\n"
"v_mfma_f32_16x16x32_bf16 a[124:127], v[188:191], v[92:95], a[124:127]\n"
"buffer_load_dwordx4 v23, s[44:47], s62 offen offset:3072 lds\n"
"AT_P1_1:\n"
"v_mfma_f32_16x16x32_bf16 v[112:115], a[192:195], a[144:147], v[68:71]\n"
"v_exp_f32 v33, v109\n"
"v_mfma_f32_16x16x32_bf16 v[112:115], a[196:199], a[148:151], v[112:115]\n"
"ds_read_b64_tr_b16 v[164:165], v13 offset:40960\n"
"v_cvt_pk_bf16_f32 v86, v32, v33\n"
"v_mfma_f32_16x16x32_bf16 v[112:115], a[200:203], a[152:155], v[112:115]\n"
"v_exp_f32 v34, v110\n"
"v_mfma_f32_16x16x32_bf16 v[112:115], a[204:207], a[156:159], v[112:115]\n"
"ds_read_b64_tr_b16 v[166:167], v13 offset:45056\n"
"v_mfma_f32_16x16x32_bf16 v[116:119], a[208:211], a[144:147], v[68:71]\n"
"v_exp_f32 v35, v111\n"
"v_mfma_f32_16x16x32_bf16 v[116:119], a[212:215], a[148:151], v[116:119]\n"
"s_add_u32 m0, s59, 81920\n"
"v_cvt_pk_bf16_f32 v87, v34, v35\n"
"v_mfma_f32_16x16x32_bf16 v[116:119], a[216:219], a[152:155], v[116:119]\n"
"ds_read_b64_tr_b16 v[136:137], v14 offset:32768\n"
"v_mfma_f32_16x16x32_bf16 v[116:119], a[220:223], a[156:159], v[116:119]\n"
"buffer_load_dwordx4 v24, s[48:51], s62 offen offset:0 lds\n"
"v_mfma_f32_16x16x32_bf16 v[40:43], v[56:59], v[80:83], 0\n"
"v_exp_f32 v32, v112\n"
"v_mfma_f32_16x16x32_bf16 v[40:43], v[56:59], v[84:87], v[40:43]\n"
"ds_read_b64_tr_b16 v[138:139], v14 offset:36864\n"
"v_mfma_f32_16x16x32_bf16 v[120:123], a[224:227], a[144:147], v[68:71]\n"
"v_exp_f32 v33, v113\n"
"v_mfma_f32_16x16x32_bf16 v[120:123], a[228:231], a[148:151], v[120:123]\n"
"ds_read_b64_tr_b16 v[168:169], v14 offset:40960\n"
"v_cvt_pk_bf16_f32 v88, v32, v33\n"
"v_mfma_f32_16x16x32_bf16 v[120:123], a[232:235], a[152:155], v[120:123]\n"
"v_exp_f32 v34, v114\n"
"v_mfma_f32_16x16x32_bf16 v[120:123], a[236:239], a[156:159], v[120:123]\n"
"ds_read_b64_tr_b16 v[170:171], v14 offset:45056\n"
"v_cmp_nge_f32 vcc, s64, v40\n"
"v_mfma_f32_16x16x32_bf16 v[124:127], a[240:243], a[144:147], v[68:71]\n"
"v_exp_f32 v35, v115\n"
"v_mfma_f32_16x16x32_bf16 v[124:127], a[244:247], a[148:151], v[124:127]\n"
"s_cbranch_vccnz AT_R5\n"
"AT_B5:\n"
"v_cvt_pk_bf16_f32 v89, v34, v35\n"
"v_mfma_f32_16x16x32_bf16 v[124:127], a[248:251], a[152:155], v[124:127]\n"
"v_exp_f32 v36, v116\n"
"v_mfma_f32_16x16x32_bf16 v[124:127], a[252:255], a[156:159], v[124:127]\n"
"ds_read_b64_tr_b16 v[140:141], v15 offset:32768\n"
"v_add_f32 v28, v28, v40\n"
"v_mfma_f32_16x16x32_bf16 a[0:3], v[192:195], v[80:83], a[0:3]\n"
"v_exp_f32 v37, v117\n"
"v_mfma_f32_16x16x32_bf16 a[0:3], v[224:227], v[84:87], a[0:3]\n"
"ds_read_b64_tr_b16 v[142:143], v15 offset:36864\n"
"v_cvt_pk_bf16_f32 v90, v36, v37\n"
"v_mfma_f32_16x16x32_bf16 a[4:7], v[196:199], v[80:83], a[4:7]\n"
"v_exp_f32 v32, v118\n"
"v_mfma_f32_16x16x32_bf16 a[4:7], v[228:231], v[84:87], a[4:7]\n"
"buffer_load_dwordx4 v25, s[48:51], s62 offen offset:1024 lds\n"
"v_mfma_f32_16x16x32_bf16 a[8:11], v[200:203], v[80:83], a[8:11]\n"
"v_exp_f32 v33, v119\n"
"v_mfma_f32_16x16x32_bf16 a[8:11], v[232:235], v[84:87], a[8:11]\n"
"ds_read_b64_tr_b16 v[172:173], v15 offset:40960\n"
"v_cvt_pk_bf16_f32 v91, v32, v33\n"
"v_mfma_f32_16x16x32_bf16 a[12:15], v[204:207], v[80:83], a[12:15]\n"
"v_exp_f32 v34, v120\n"
"v_mfma_f32_16x16x32_bf16 a[12:15], v[236:239], v[84:87], a[12:15]\n"
"ds_read_b64_tr_b16 v[174:175], v15 offset:45056\n"
"ds_read_b64_tr_b16 v[144:145], v16 offset:32768\n"
"v_mfma_f32_16x16x32_bf16 a[16:19], v[208:211], v[80:83], a[16:19]\n"
"v_exp_f32 v35, v121\n"
"v_mfma_f32_16x16x32_bf16 a[16:19], v[240:243], v[84:87], a[16:19]\n"
"v_cvt_pk_bf16_f32 v92, v34, v35\n"
"v_mfma_f32_16x16x32_bf16 a[20:23], v[212:215], v[80:83], a[20:23]\n"
"v_exp_f32 v36, v122\n"
"v_mfma_f32_16x16x32_bf16 a[20:23], v[244:247], v[84:87], a[20:23]\n"
"ds_read_b64_tr_b16 v[146:147], v16 offset:36864\n"
"v_mfma_f32_16x16x32_bf16 a[24:27], v[216:219], v[80:83], a[24:27]\n"
"v_exp_f32 v37, v123\n"
"v_mfma_f32_16x16x32_bf16 a[24:27], v[248:251], v[84:87], a[24:27]\n"
"ds_read_b64_tr_b16 v[176:177], v16 offset:40960\n"
"v_cvt_pk_bf16_f32 v93, v36, v37\n"
"v_mfma_f32_16x16x32_bf16 a[28:31], v[220:223], v[80:83], a[28:31]\n"
"v_exp_f32 v32, v124\n"
"v_mfma_f32_16x16x32_bf16 a[28:31], v[252:255], v[84:87], a[28:31]\n"
"ds_read_b64_tr_b16 v[178:179], v16 offset:45056\n"
"v_mfma_f32_16x16x32_bf16 v[96:99], a[192:195], a[160:163], v[72:75]\n"
"v_exp_f32 v33, v125\n"
"v_mfma_f32_16x16x32_bf16 v[96:99], a[196:199], a[164:167], v[96:99]\n"
"ds_read_b64_tr_b16 v[148:149], v17 offset:32768\n"
"v_cvt_pk_bf16_f32 v94, v32, v33\n"
"v_mfma_f32_16x16x32_bf16 v[96:99], a[200:203], a[168:171], v[96:99]\n"
"v_exp_f32 v34, v126\n"
"v_mfma_f32_16x16x32_bf16 v[96:99], a[204:207], a[172:175], v[96:99]\n"
"buffer_load_dwordx4 v26, s[48:51], s62 offen offset:2048 lds\n"
"v_mfma_f32_16x16x32_bf16 v[100:103], a[208:211], a[160:163], v[72:75]\n"
"v_exp_f32 v35, v127\n"
"v_mfma_f32_16x16x32_bf16 v[100:103], a[212:215], a[164:167], v[100:103]\n"
"ds_read_b64_tr_b16 v[150:151], v17 offset:36864\n"
"v_cvt_pk_bf16_f32 v95, v34, v35\n"
"v_mfma_f32_16x16x32_bf16 v[100:103], a[216:219], a[168:171], v[100:103]\n"
"ds_read_b64_tr_b16 v[180:181], v17 offset:40960\n"
"v_mfma_f32_16x16x32_bf16 v[100:103], a[220:223], a[172:175], v[100:103]\n"
"v_mfma_f32_16x16x32_bf16 v[44:47], v[56:59], v[88:91], 0\n"
"v_exp_f32 v32, v96\n"
"v_mfma_f32_16x16x32_bf16 v[44:47], v[56:59], v[92:95], v[44:47]\n"
"ds_read_b64_tr_b16 v[182:183], v17 offset:45056\n"
"v_mfma_f32_16x16x32_bf16 v[104:107], a[224:227], a[160:163], v[72:75]\n"
"v_exp_f32 v33, v97\n"
"v_mfma_f32_16x16x32_bf16 v[104:107], a[228:231], a[164:167], v[104:107]\n"
"ds_read_b64_tr_b16 v[152:153], v18 offset:32768\n"
"v_cvt_pk_bf16_f32 v80, v32, v33\n"
"v_mfma_f32_16x16x32_bf16 v[104:107], a[232:235], a[168:171], v[104:107]\n"
"v_exp_f32 v34, v98\n"
"v_mfma_f32_16x16x32_bf16 v[104:107], a[236:239], a[172:175], v[104:107]\n"
"v_cmp_nge_f32 vcc, s64, v44\n"
"v_mfma_f32_16x16x32_bf16 v[108:111], a[240:243], a[160:163], v[72:75]\n"
"v_exp_f32 v35, v99\n"
"v_mfma_f32_16x16x32_bf16 v[108:111], a[244:247], a[164:167], v[108:111]\n"
"s_cbranch_vccnz AT_R6\n"
"AT_B6:\n"
"v_cvt_pk_bf16_f32 v81, v34, v35\n"
"v_mfma_f32_16x16x32_bf16 v[108:111], a[248:251], a[168:171], v[108:111]\n"
"v_exp_f32 v36, v100\n"
"v_mfma_f32_16x16x32_bf16 v[108:111], a[252:255], a[172:175], v[108:111]\n"
"ds_read_b64_tr_b16 v[154:155], v18 offset:36864\n"
"v_add_f32 v29, v29, v44\n"
"v_mfma_f32_16x16x32_bf16 a[32:35], v[192:195], v[88:91], a[32:35]\n"
"v_exp_f32 v37, v101\n"
"v_mfma_f32_16x16x32_bf16 a[32:35], v[224:227], v[92:95], a[32:35]\n"
"ds_read_b64_tr_b16 v[184:185], v18 offset:40960\n"
"v_cvt_pk_bf16_f32 v82, v36, v37\n"
"v_mfma_f32_16x16x32_bf16 a[36:39], v[196:199], v[88:91], a[36:39]\n"
"v_exp_f32 v32, v102\n"
"v_mfma_f32_16x16x32_bf16 a[36:39], v[228:231], v[92:95], a[36:39]\n"
"buffer_load_dwordx4 v27, s[48:51], s62 offen offset:3072 lds\n"
"v_mfma_f32_16x16x32_bf16 a[40:43], v[200:203], v[88:91], a[40:43]\n"
"v_exp_f32 v33, v103\n"
"v_mfma_f32_16x16x32_bf16 a[40:43], v[232:235], v[92:95], a[40:43]\n"
"ds_read_b64_tr_b16 v[186:187], v18 offset:45056\n"
"v_cvt_pk_bf16_f32 v83, v32, v33\n"
"v_mfma_f32_16x16x32_bf16 a[44:47], v[204:207], v[88:91], a[44:47]\n"
"v_exp_f32 v34, v104\n"
"v_mfma_f32_16x16x32_bf16 a[44:47], v[236:239], v[92:95], a[44:47]\n"
"ds_read_b64_tr_b16 v[156:157], v19 offset:32768\n"
"ds_read_b64_tr_b16 v[158:159], v19 offset:36864\n"
"v_mfma_f32_16x16x32_bf16 a[48:51], v[208:211], v[88:91], a[48:51]\n"
"v_exp_f32 v35, v105\n"
"v_mfma_f32_16x16x32_bf16 a[48:51], v[240:243], v[92:95], a[48:51]\n"
"ds_read_b64_tr_b16 v[188:189], v19 offset:40960\n"
"v_cvt_pk_bf16_f32 v84, v34, v35\n"
"v_mfma_f32_16x16x32_bf16 a[52:55], v[212:215], v[88:91], a[52:55]\n"
"v_exp_f32 v36, v106\n"
"v_mfma_f32_16x16x32_bf16 a[52:55], v[244:247], v[92:95], a[52:55]\n"
"ds_read_b64_tr_b16 v[190:191], v19 offset:45056\n"
"v_mfma_f32_16x16x32_bf16 a[56:59], v[216:219], v[88:91], a[56:59]\n"
"v_exp_f32 v37, v107\n"
"v_mfma_f32_16x16x32_bf16 a[56:59], v[248:251], v[92:95], a[56:59]\n"
"s_add_u32 s62, s62, s54\n"
"v_cvt_pk_bf16_f32 v85, v36, v37\n"
"v_mfma_f32_16x16x32_bf16 a[60:63], v[220:223], v[88:91], a[60:63]\n"
"v_exp_f32 v32, v108\n"
"v_mfma_f32_16x16x32_bf16 a[60:63], v[252:255], v[92:95], a[60:63]\n"
"v_mfma_f32_16x16x32_bf16 v[112:115], a[192:195], a[176:179], v[76:79]\n"
"v_exp_f32 v33, v109\n"
"v_mfma_f32_16x16x32_bf16 v[112:115], a[196:199], a[180:183], v[112:115]\n"
"ds_read_b128 a[192:195], v8 offset:32768\n"
"v_cvt_pk_bf16_f32 v86, v32, v33\n"
"v_mfma_f32_16x16x32_bf16 v[112:115], a[200:203], a[184:187], v[112:115]\n"
"v_exp_f32 v34, v110\n"
"v_mfma_f32_16x16x32_bf16 v[112:115], a[204:207], a[188:191], v[112:115]\n"
"ds_read_b128 a[196:199], v9 offset:32768\n"
"ds_read_b128 a[200:203], v10 offset:32768\n"
"v_mfma_f32_16x16x32_bf16 v[116:119], a[208:211], a[176:179], v[76:79]\n"
"v_exp_f32 v35, v111\n"
"v_mfma_f32_16x16x32_bf16 v[116:119], a[212:215], a[180:183], v[116:119]\n"
"ds_read_b128 a[204:207], v11 offset:32768\n"
"v_cvt_pk_bf16_f32 v87, v34, v35\n"
"v_mfma_f32_16x16x32_bf16 v[116:119], a[216:219], a[184:187], v[116:119]\n"
"ds_read_b128 a[208:211], v8 offset:36864\n"
"ds_read_b128 a[212:215], v9 offset:36864\n"
"v_mfma_f32_16x16x32_bf16 v[116:119], a[220:223], a[188:191], v[116:119]\n"
"ds_read_b128 a[216:219], v10 offset:36864\n"
"ds_read_b128 a[220:223], v11 offset:36864\n"
"v_mfma_f32_16x16x32_bf16 v[40:43], v[56:59], v[80:83], 0\n"
"v_exp_f32 v32, v112\n"
"v_mfma_f32_16x16x32_bf16 v[40:43], v[56:59], v[84:87], v[40:43]\n"
"v_mfma_f32_16x16x32_bf16 v[120:123], a[224:227], a[176:179], v[76:79]\n"
"v_exp_f32 v33, v113\n"
"v_mfma_f32_16x16x32_bf16 v[120:123], a[228:231], a[180:183], v[120:123]\n"
"ds_read_b128 a[224:227], v8 offset:40960\n"
"v_cvt_pk_bf16_f32 v88, v32, v33\n"
"v_mfma_f32_16x16x32_bf16 v[120:123], a[232:235], a[184:187], v[120:123]\n"
"v_exp_f32 v34, v114\n"
"v_mfma_f32_16x16x32_bf16 v[120:123], a[236:239], a[188:191], v[120:123]\n"
"ds_read_b128 a[228:231], v9 offset:40960\n"
"v_cmp_nge_f32 vcc, s64, v40\n"
"v_mfma_f32_16x16x32_bf16 v[124:127], a[240:243], a[176:179], v[76:79]\n"
"v_exp_f32 v35, v115\n"
"v_mfma_f32_16x16x32_bf16 v[124:127], a[244:247], a[180:183], v[124:127]\n"
"s_cbranch_vccnz AT_R7\n"
"AT_B7:\n"
"v_cvt_pk_bf16_f32 v89, v34, v35\n"
"v_mfma_f32_16x16x32_bf16 v[124:127], a[248:251], a[184:187], v[124:127]\n"
"v_exp_f32 v36, v116\n"
"v_mfma_f32_16x16x32_bf16 v[124:127], a[252:255], a[188:191], v[124:127]\n"
"ds_read_b128 a[232:235], v10 offset:40960\n"
"v_add_f32 v30, v30, v40\n"
"v_mfma_f32_16x16x32_bf16 a[64:67], v[192:195], v[80:83], a[64:67]\n"
"v_exp_f32 v37, v117\n"
"v_mfma_f32_16x16x32_bf16 a[64:67], v[224:227], v[84:87], a[64:67]\n"
"ds_read_b128 a[236:239], v11 offset:40960\n"
"v_cvt_pk_bf16_f32 v90, v36, v37\n"
"v_mfma_f32_16x16x32_bf16 a[68:71], v[196:199], v[80:83], a[68:71]\n"
"v_exp_f32 v32, v118\n"
"v_mfma_f32_16x16x32_bf16 a[68:71], v[228:231], v[84:87], a[68:71]\n"
"ds_read_b128 a[240:243], v8 offset:45056\n"
"ds_read_b128 a[244:247], v9 offset:45056\n"
"v_mfma_f32_16x16x32_bf16 a[72:75], v[200:203], v[80:83], a[72:75]\n"
"v_exp_f32 v33, v119\n"
"v_mfma_f32_16x16x32_bf16 a[72:75], v[232:235], v[84:87], a[72:75]\n"
"ds_read_b128 a[248:251], v10 offset:45056\n"
"v_cvt_pk_bf16_f32 v91, v32, v33\n"
"v_mfma_f32_16x16x32_bf16 a[76:79], v[204:207], v[80:83], a[76:79]\n"
"v_exp_f32 v34, v120\n"
"v_mfma_f32_16x16x32_bf16 a[76:79], v[236:239], v[84:87], a[76:79]\n"
"ds_read_b128 a[252:255], v11 offset:45056\n"
"v_mfma_f32_16x16x32_bf16 a[80:83], v[208:211], v[80:83], a[80:83]\n"
"v_exp_f32 v35, v121\n"
"v_mfma_f32_16x16x32_bf16 a[80:83], v[240:243], v[84:87], a[80:83]\n"
"v_cvt_pk_bf16_f32 v92, v34, v35\n"
"v_mfma_f32_16x16x32_bf16 a[84:87], v[212:215], v[80:83], a[84:87]\n"
"v_exp_f32 v36, v122\n"
"v_mfma_f32_16x16x32_bf16 a[84:87], v[244:247], v[84:87], a[84:87]\n"
"buffer_load_dwordx4 v[60:63], v38, s[84:87], s52 offen\n"
"v_mfma_f32_16x16x32_bf16 a[88:91], v[216:219], v[80:83], a[88:91]\n"
"v_exp_f32 v37, v123\n"
"v_mfma_f32_16x16x32_bf16 a[88:91], v[248:251], v[84:87], a[88:91]\n"
"v_cvt_pk_bf16_f32 v93, v36, v37\n"
"v_mfma_f32_16x16x32_bf16 a[92:95], v[220:223], v[80:83], a[92:95]\n"
"v_exp_f32 v32, v124\n"
"v_mfma_f32_16x16x32_bf16 a[92:95], v[252:255], v[84:87], a[92:95]\n"
"s_waitcnt vmcnt(16) lgkmcnt(0)\n"
"s_barrier\n"
"v_mfma_f32_16x16x32_bf16 v[96:99], a[192:195], a[128:131], v[64:67]\n"
"v_exp_f32 v33, v125\n"
"v_mfma_f32_16x16x32_bf16 v[96:99], a[196:199], a[132:135], v[96:99]\n"
"s_add_u32 m0, s59, 32768\n"
"v_cvt_pk_bf16_f32 v94, v32, v33\n"
"v_mfma_f32_16x16x32_bf16 v[96:99], a[200:203], a[136:139], v[96:99]\n"
"v_exp_f32 v34, v126\n"
"v_mfma_f32_16x16x32_bf16 v[96:99], a[204:207], a[140:143], v[96:99]\n"
"buffer_load_dwordx4 v20, s[44:47], s62 offen offset:0 lds\n"
"v_mfma_f32_16x16x32_bf16 v[100:103], a[208:211], a[128:131], v[64:67]\n"
"v_exp_f32 v35, v127\n"
"v_mfma_f32_16x16x32_bf16 v[100:103], a[212:215], a[132:135], v[100:103]\n"
"v_cvt_pk_bf16_f32 v95, v34, v35\n"
"v_mfma_f32_16x16x32_bf16 v[100:103], a[216:219], a[136:139], v[100:103]\n"
"v_mfma_f32_16x16x32_bf16 v[100:103], a[220:223], a[140:143], v[100:103]\n"
"v_mfma_f32_16x16x32_bf16 v[44:47], v[56:59], v[88:91], 0\n"
"v_exp_f32 v32, v96\n"
"v_mfma_f32_16x16x32_bf16 v[44:47], v[56:59], v[92:95], v[44:47]\n"
"buffer_load_dwordx4 v21, s[44:47], s62 offen offset:1024 lds\n"
"v_mfma_f32_16x16x32_bf16 v[104:107], a[224:227], a[128:131], v[64:67]\n"
"v_exp_f32 v33, v97\n"
"v_mfma_f32_16x16x32_bf16 v[104:107], a[228:231], a[132:135], v[104:107]\n"
"v_cvt_pk_bf16_f32 v80, v32, v33\n"
"v_mfma_f32_16x16x32_bf16 v[104:107], a[232:235], a[136:139], v[104:107]\n"
"v_exp_f32 v34, v98\n"
"v_mfma_f32_16x16x32_bf16 v[104:107], a[236:239], a[140:143], v[104:107]\n"
"v_cmp_nge_f32 vcc, s64, v44\n"
"v_mfma_f32_16x16x32_bf16 v[108:111], a[240:243], a[128:131], v[64:67]\n"
"v_exp_f32 v35, v99\n"
"v_mfma_f32_16x16x32_bf16 v[108:111], a[244:247], a[132:135], v[108:111]\n"
"s_cbranch_vccnz AT_R8\n"
"AT_B8:\n"
"v_cvt_pk_bf16_f32 v81, v34, v35\n"
"v_mfma_f32_16x16x32_bf16 v[108:111], a[248:251], a[136:139], v[108:111]\n"
"v_exp_f32 v36, v100\n"
"v_mfma_f32_16x16x32_bf16 v[108:111], a[252:255], a[140:143], v[108:111]\n"
"v_add_f32 v31, v31, v44\n"
"v_mfma_f32_16x16x32_bf16 a[96:99], v[192:195], v[88:91], a[96:99]\n"
"v_exp_f32 v37, v101\n"
"v_mfma_f32_16x16x32_bf16 a[96:99], v[224:227], v[92:95], a[96:99]\n"
"ds_read_b64_tr_b16 v[192:193], v12 offset:49152\n"
"v_cvt_pk_bf16_f32 v82, v36, v37\n"
"v_mfma_f32_16x16x32_bf16 a[100:103], v[196:199], v[88:91], a[100:103]\n"
"v_exp_f32 v32, v102\n"
"v_mfma_f32_16x16x32_bf16 a[100:103], v[228:231], v[92:95], a[100:103]\n"
"ds_read_b64_tr_b16 v[194:195], v12 offset:53248\n"
"v_mfma_f32_16x16x32_bf16 a[104:107], v[200:203], v[88:91], a[104:107]\n"
"v_exp_f32 v33, v103\n"
"v_mfma_f32_16x16x32_bf16 a[104:107], v[232:235], v[92:95], a[104:107]\n"
"v_cvt_pk_bf16_f32 v83, v32, v33\n"
"v_mfma_f32_16x16x32_bf16 a[108:111], v[204:207], v[88:91], a[108:111]\n"
"v_exp_f32 v34, v104\n"
"v_mfma_f32_16x16x32_bf16 a[108:111], v[236:239], v[92:95], a[108:111]\n"
"buffer_load_dwordx4 v22, s[44:47], s62 offen offset:2048 lds\n"
"v_mfma_f32_16x16x32_bf16 a[112:115], v[208:211], v[88:91], a[112:115]\n"
"v_exp_f32 v35, v105\n"
"v_mfma_f32_16x16x32_bf16 a[112:115], v[240:243], v[92:95], a[112:115]\n"
"ds_read_b64_tr_b16 v[224:225], v12 offset:57344\n"
"v_cvt_pk_bf16_f32 v84, v34, v35\n"
"v_mfma_f32_16x16x32_bf16 a[116:119], v[212:215], v[88:91], a[116:119]\n"
"v_exp_f32 v36, v106\n"
"v_mfma_f32_16x16x32_bf16 a[116:119], v[244:247], v[92:95], a[116:119]\n"
"ds_read_b64_tr_b16 v[226:227], v12 offset:61440\n"
"ds_read_b64_tr_b16 v[196:197], v13 offset:49152\n"
"v_mfma_f32_16x16x32_bf16 a[120:123], v[216:219], v[88:91], a[120:123]\n"
"v_exp_f32 v37, v107\n"
"v_mfma_f32_16x16x32_bf16 a[120:123], v[248:251], v[92:95], a[120:123]\n"
"ds_read_b64_tr_b16 v[198:199], v13 offset:53248\n"
"v_cvt_pk_bf16_f32 v85, v36, v37\n"
"v_mfma_f32_16x16x32_bf16 a[124:127], v[220:223], v[88:91], a[124:127]\n"
"v_exp_f32 v32, v108\n"
"v_mfma_f32_16x16x32_bf16 a[124:127], v[252:255], v[92:95], a[124:127]\n"
"buffer_load_dwordx4 v23, s[44:47], s62 offen offset:3072 lds\n"
"AT_P1_2:\n"
"v_mfma_f32_16x16x32_bf16 v[112:115], a[192:195], a[144:147], v[68:71]\n"
"v_exp_f32 v33, v109\n"
"v_mfma_f32_16x16x32_bf16 v[112:115], a[196:199], a[148:151], v[112:115]\n"
"ds_read_b64_tr_b16 v[228:229], v13 offset:57344\n"
"v_cvt_pk_bf16_f32 v86, v32, v33\n"
"v_mfma_f32_16x16x32_bf16 v[112:115], a[200:203], a[152:155], v[112:115]\n"
"v_exp_f32 v34, v110\n"
"v_mfma_f32_16x16x32_bf16 v[112:115], a[204:207], a[156:159], v[112:115]\n"
"ds_read_b64_tr_b16 v[230:231], v13 offset:61440\n"
"v_mfma_f32_16x16x32_bf16 v[116:119], a[208:211], a[144:147], v[68:71]\n"
"v_exp_f32 v35, v111\n"
"v_mfma_f32_16x16x32_bf16 v[116:119], a[212:215], a[148:151], v[116:119]\n"
"s_add_u32 m0, s59, 98304\n"
"v_cvt_pk_bf16_f32 v87, v34, v35\n"
"v_mfma_f32_16x16x32_bf16 v[116:119], a[216:219], a[152:155], v[116:119]\n"
"ds_read_b64_tr_b16 v[200:201], v14 offset:49152\n"
"v_mfma_f32_16x16x32_bf16 v[116:119], a[220:223], a[156:159], v[116:119]\n"
"buffer_load_dwordx4 v24, s[48:51], s62 offen offset:0 lds\n"
"v_mfma_f32_16x16x32_bf16 v[40:43], v[56:59], v[80:83], 0\n"
"v_exp_f32 v32, v112\n"
"v_mfma_f32_16x16x32_bf16 v[40:43], v[56:59], v[84:87], v[40:43]\n"
"ds_read_b64_tr_b16 v[202:203], v14 offset:53248\n"
"v_mfma_f32_16x16x32_bf16 v[120:123], a[224:227], a[144:147], v[68:71]\n"
"v_exp_f32 v33, v113\n"
"v_mfma_f32_16x16x32_bf16 v[120:123], a[228:231], a[148:151], v[120:123]\n"
"ds_read_b64_tr_b16 v[232:233], v14 offset:57344\n"
"v_cvt_pk_bf16_f32 v88, v32, v33\n"
"v_mfma_f32_16x16x32_bf16 v[120:123], a[232:235], a[152:155], v[120:123]\n"
"v_exp_f32 v34, v114\n"
"v_mfma_f32_16x16x32_bf16 v[120:123], a[236:239], a[156:159], v[120:123]\n"
"ds_read_b64_tr_b16 v[234:235], v14 offset:61440\n"
"v_cmp_nge_f32 vcc, s64, v40\n"
"v_mfma_f32_16x16x32_bf16 v[124:127], a[240:243], a[144:147], v[68:71]\n"
"v_exp_f32 v35, v115\n"
"v_mfma_f32_16x16x32_bf16 v[124:127], a[244:247], a[148:151], v[124:127]\n"
"s_cbranch_vccnz AT_R9\n"
"AT_B9:\n"
"v_cvt_pk_bf16_f32 v89, v34, v35\n"
"v_mfma_f32_16x16x32_bf16 v[124:127], a[248:251], a[152:155], v[124:127]\n"
"v_exp_f32 v36, v116\n"
"v_mfma_f32_16x16x32_bf16 v[124:127], a[252:255], a[156:159], v[124:127]\n"
"ds_read_b64_tr_b16 v[204:205], v15 offset:49152\n"
"v_add_f32 v28, v28, v40\n"
"v_mfma_f32_16x16x32_bf16 a[0:3], v[128:131], v[80:83], a[0:3]\n"
"v_exp_f32 v37, v117\n"
"v_mfma_f32_16x16x32_bf16 a[0:3], v[160:163], v[84:87], a[0:3]\n"
"ds_read_b64_tr_b16 v[206:207], v15 offset:53248\n"
"v_cvt_pk_bf16_f32 v90, v36, v37\n"
"v_mfma_f32_16x16x32_bf16 a[4:7], v[132:135], v[80:83], a[4:7]\n"
"v_exp_f32 v32, v118\n"
"v_mfma_f32_16x16x32_bf16 a[4:7], v[164:167], v[84:87], a[4:7]\n"
"buffer_load_dwordx4 v25, s[48:51], s62 offen offset:1024 lds\n"
"v_mfma_f32_16x16x32_bf16 a[8:11], v[136:139], v[80:83], a[8:11]\n"
"v_exp_f32 v33, v119\n"
"v_mfma_f32_16x16x32_bf16 a[8:11], v[168:171], v[84:87], a[8:11]\n"
"ds_read_b64_tr_b16 v[236:237], v15 offset:57344\n"
"v_cvt_pk_bf16_f32 v91, v32, v33\n"
"v_mfma_f32_16x16x32_bf16 a[12:15], v[140:143], v[80:83], a[12:15]\n"
"v_exp_f32 v34, v120\n"
"v_mfma_f32_16x16x32_bf16 a[12:15], v[172:175], v[84:87], a[12:15]\n"
"ds_read_b64_tr_b16 v[238:239], v15 offset:61440\n"
"ds_read_b64_tr_b16 v[208:209], v16 offset:49152\n"
"v_mfma_f32_16x16x32_bf16 a[16:19], v[144:147], v[80:83], a[16:19]\n"
"v_exp_f32 v35, v121\n"
"v_mfma_f32_16x16x32_bf16 a[16:19], v[176:179], v[84:87], a[16:19]\n"
"v_cvt_pk_bf16_f32 v92, v34, v35\n"
"v_mfma_f32_16x16x32_bf16 a[20:23], v[148:151], v[80:83], a[20:23]\n"
"v_exp_f32 v36, v122\n"
"v_mfma_f32_16x16x32_bf16 a[20:23], v[180:183], v[84:87], a[20:23]\n"
"ds_read_b64_tr_b16 v[210:211], v16 offset:53248\n"
"v_mfma_f32_16x16x32_bf16 a[24:27], v[152:155], v[80:83], a[24:27]\n"
"v_exp_f32 v37, v123\n"
"v_mfma_f32_16x16x32_bf16 a[24:27], v[184:187], v[84:87], a[24:27]\n"
"ds_read_b64_tr_b16 v[240:241], v16 offset:57344\n"
"v_cvt_pk_bf16_f32 v93, v36, v37\n"
"v_mfma_f32_16x16x32_bf16 a[28:31], v[156:159], v[80:83], a[28:31]\n"
"v_exp_f32 v32, v124\n"
"v_mfma_f32_16x16x32_bf16 a[28:31], v[188:191], v[84:87], a[28:31]\n"
"ds_read_b64_tr_b16 v[242:243], v16 offset:61440\n"
"v_mfma_f32_16x16x32_bf16 v[96:99], a[192:195], a[160:163], v[72:75]\n"
"v_exp_f32 v33, v125\n"
"v_mfma_f32_16x16x32_bf16 v[96:99], a[196:199], a[164:167], v[96:99]\n"
"ds_read_b64_tr_b16 v[212:213], v17 offset:49152\n"
"v_cvt_pk_bf16_f32 v94, v32, v33\n"
"v_mfma_f32_16x16x32_bf16 v[96:99], a[200:203], a[168:171], v[96:99]\n"
"v_exp_f32 v34, v126\n"
"v_mfma_f32_16x16x32_bf16 v[96:99], a[204:207], a[172:175], v[96:99]\n"
"buffer_load_dwordx4 v26, s[48:51], s62 offen offset:2048 lds\n"
"v_mfma_f32_16x16x32_bf16 v[100:103], a[208:211], a[160:163], v[72:75]\n"
"v_exp_f32 v35, v127\n"
"v_mfma_f32_16x16x32_bf16 v[100:103], a[212:215], a[164:167], v[100:103]\n"
"ds_read_b64_tr_b16 v[214:215], v17 offset:53248\n"
"v_cvt_pk_bf16_f32 v95, v34, v35\n"
"v_mfma_f32_16x16x32_bf16 v[100:103], a[216:219], a[168:171], v[100:103]\n"
"ds_read_b64_tr_b16 v[244:245], v17 offset:57344\n"
"v_mfma_f32_16x16x32_bf16 v[100:103], a[220:223], a[172:175], v[100:103]\n"
"v_mfma_f32_16x16x32_bf16 v[44:47], v[56:59], v[88:91], 0\n"
"v_exp_f32 v32, v96\n"
"v_mfma_f32_16x16x32_bf16 v[44:47], v[56:59], v[92:95], v[44:47]\n"
"ds_read_b64_tr_b16 v[246:247], v17 offset:61440\n"
"v_mfma_f32_16x16x32_bf16 v[104:107], a[224:227], a[160:163], v[72:75]\n"
"v_exp_f32 v33, v97\n"
"v_mfma_f32_16x16x32_bf16 v[104:107], a[228:231], a[164:167], v[104:107]\n"
"ds_read_b64_tr_b16 v[216:217], v18 offset:49152\n"
"v_cvt_pk_bf16_f32 v80, v32, v33\n"
"v_mfma_f32_16x16x32_bf16 v[104:107], a[232:235], a[168:171], v[104:107]\n"
"v_exp_f32 v34, v98\n"
"v_mfma_f32_16x16x32_bf16 v[104:107], a[236:239], a[172:175], v[104:107]\n"
"v_cmp_nge_f32 vcc, s64, v44\n"
"v_mfma_f32_16x16x32_bf16 v[108:111], a[240:243], a[160:163], v[72:75]\n"
"v_exp_f32 v35, v99\n"
"v_mfma_f32_16x16x32_bf16 v[108:111], a[244:247], a[164:167], v[108:111]\n"
"s_cbranch_vccnz AT_R10\n"
"AT_B10:\n"
"v_cvt_pk_bf16_f32 v81, v34, v35\n"
"v_mfma_f32_16x16x32_bf16 v[108:111], a[248:251], a[168:171], v[108:111]\n"
"v_exp_f32 v36, v100\n"
"v_mfma_f32_16x16x32_bf16 v[108:111], a[252:255], a[172:175], v[108:111]\n"
"ds_read_b64_tr_b16 v[218:219], v18 offset:53248\n"
"v_add_f32 v29, v29, v44\n"
"v_mfma_f32_16x16x32_bf16 a[32:35], v[128:131], v[88:91], a[32:35]\n"
"v_exp_f32 v37, v101\n"
"v_mfma_f32_16x16x32_bf16 a[32:35], v[160:163], v[92:95], a[32:35]\n"
"ds_read_b64_tr_b16 v[248:249], v18 offset:57344\n"
"v_cvt_pk_bf16_f32 v82, v36, v37\n"
"v_mfma_f32_16x16x32_bf16 a[36:39], v[132:135], v[88:91], a[36:39]\n"
"v_exp_f32 v32, v102\n"
"v_mfma_f32_16x16x32_bf16 a[36:39], v[164:167], v[92:95], a[36:39]\n"
"buffer_load_dwordx4 v27, s[48:51], s62 offen offset:3072 lds\n"
"v_mfma_f32_16x16x32_bf16 a[40:43], v[136:139], v[88:91], a[40:43]\n"
"v_exp_f32 v33, v103\n"
"v_mfma_f32_16x16x32_bf16 a[40:43], v[168:171], v[92:95], a[40:43]\n"
"ds_read_b64_tr_b16 v[250:251], v18 offset:61440\n"
"v_cvt_pk_bf16_f32 v83, v32, v33\n"
"v_mfma_f32_16x16x32_bf16 a[44:47], v[140:143], v[88:91], a[44:47]\n"
"v_exp_f32 v34, v104\n"
"v_mfma_f32_16x16x32_bf16 a[44:47], v[172:175], v[92:95], a[44:47]\n"
"ds_read_b64_tr_b16 v[220:221], v19 offset:49152\n"
"ds_read_b64_tr_b16 v[222:223], v19 offset:53248\n"
"v_mfma_f32_16x16x32_bf16 a[48:51], v[144:147], v[88:91], a[48:51]\n"
"v_exp_f32 v35, v105\n"
"v_mfma_f32_16x16x32_bf16 a[48:51], v[176:179], v[92:95], a[48:51]\n"
"ds_read_b64_tr_b16 v[252:253], v19 offset:57344\n"
"v_cvt_pk_bf16_f32 v84, v34, v35\n"
"v_mfma_f32_16x16x32_bf16 a[52:55], v[148:151], v[88:91], a[52:55]\n"
"v_exp_f32 v36, v106\n"
"v_mfma_f32_16x16x32_bf16 a[52:55], v[180:183], v[92:95], a[52:55]\n"
"ds_read_b64_tr_b16 v[254:255], v19 offset:61440\n"
"v_mfma_f32_16x16x32_bf16 a[56:59], v[152:155], v[88:91], a[56:59]\n"
"v_exp_f32 v37, v107\n"
"v_mfma_f32_16x16x32_bf16 a[56:59], v[184:187], v[92:95], a[56:59]\n"
"s_add_u32 s62, s62, s54\n"
"v_cvt_pk_bf16_f32 v85, v36, v37\n"
"v_mfma_f32_16x16x32_bf16 a[60:63], v[156:159], v[88:91], a[60:63]\n"
"v_exp_f32 v32, v108\n"
"v_mfma_f32_16x16x32_bf16 a[60:63], v[188:191], v[92:95], a[60:63]\n"
"v_mfma_f32_16x16x32_bf16 v[112:115], a[192:195], a[176:179], v[76:79]\n"
"v_exp_f32 v33, v109\n"
"v_mfma_f32_16x16x32_bf16 v[112:115], a[196:199], a[180:183], v[112:115]\n"
"ds_read_b128 a[192:195], v8 offset:49152\n"
"v_cvt_pk_bf16_f32 v86, v32, v33\n"
"v_mfma_f32_16x16x32_bf16 v[112:115], a[200:203], a[184:187], v[112:115]\n"
"v_exp_f32 v34, v110\n"
"v_mfma_f32_16x16x32_bf16 v[112:115], a[204:207], a[188:191], v[112:115]\n"
"ds_read_b128 a[196:199], v9 offset:49152\n"
"ds_read_b128 a[200:203], v10 offset:49152\n"
"v_mfma_f32_16x16x32_bf16 v[116:119], a[208:211], a[176:179], v[76:79]\n"
"v_exp_f32 v35, v111\n"
"v_mfma_f32_16x16x32_bf16 v[116:119], a[212:215], a[180:183], v[116:119]\n"
"ds_read_b128 a[204:207], v11 offset:49152\n"
"v_cvt_pk_bf16_f32 v87, v34, v35\n"
"v_mfma_f32_16x16x32_bf16 v[116:119], a[216:219], a[184:187], v[116:119]\n"
"ds_read_b128 a[208:211], v8 offset:53248\n"
"ds_read_b128 a[212:215], v9 offset:53248\n"
"v_mfma_f32_16x16x32_bf16 v[116:119], a[220:223], a[188:191], v[116:119]\n"
"ds_read_b128 a[216:219], v10 offset:53248\n"
"ds_read_b128 a[220:223], v11 offset:53248\n"
"v_mfma_f32_16x16x32_bf16 v[40:43], v[56:59], v[80:83], 0\n"
"v_exp_f32 v32, v112\n"
"v_mfma_f32_16x16x32_bf16 v[40:43], v[56:59], v[84:87], v[40:43]\n"
"v_mfma_f32_16x16x32_bf16 v[120:123], a[224:227], a[176:179], v[76:79]\n"
"v_exp_f32 v33, v113\n"
"v_mfma_f32_16x16x32_bf16 v[120:123], a[228:231], a[180:183], v[120:123]\n"
"ds_read_b128 a[224:227], v8 offset:57344\n"
"v_cvt_pk_bf16_f32 v88, v32, v33\n"
"v_mfma_f32_16x16x32_bf16 v[120:123], a[232:235], a[184:187], v[120:123]\n"
"v_exp_f32 v34, v114\n"
"v_mfma_f32_16x16x32_bf16 v[120:123], a[236:239], a[188:191], v[120:123]\n"
"ds_read_b128 a[228:231], v9 offset:57344\n"
"v_cmp_nge_f32 vcc, s64, v40\n"
"v_mfma_f32_16x16x32_bf16 v[124:127], a[240:243], a[176:179], v[76:79]\n"
"v_exp_f32 v35, v115\n"
"v_mfma_f32_16x16x32_bf16 v[124:127], a[244:247], a[180:183], v[124:127]\n"
"s_cbranch_vccnz AT_R11\n"
"AT_B11:\n"
"v_cvt_pk_bf16_f32 v89, v34, v35\n"
"v_mfma_f32_16x16x32_bf16 v[124:127], a[248:251], a[184:187], v[124:127]\n"
"v_exp_f32 v36, v116\n"
"v_mfma_f32_16x16x32_bf16 v[124:127], a[252:255], a[188:191], v[124:127]\n"
"ds_read_b128 a[232:235], v10 offset:57344\n"
"v_add_f32 v30, v30, v40\n"
"v_mfma_f32_16x16x32_bf16 a[64:67], v[128:131], v[80:83], a[64:67]\n"
"v_exp_f32 v37, v117\n"
"v_mfma_f32_16x16x32_bf16 a[64:67], v[160:163], v[84:87], a[64:67]\n"
"ds_read_b128 a[236:239], v11 offset:57344\n"
"v_cvt_pk_bf16_f32 v90, v36, v37\n"
"v_mfma_f32_16x16x32_bf16 a[68:71], v[132:135], v[80:83], a[68:71]\n"
"v_exp_f32 v32, v118\n"
"v_mfma_f32_16x16x32_bf16 a[68:71], v[164:167], v[84:87], a[68:71]\n"
"ds_read_b128 a[240:243], v8 offset:61440\n"
"ds_read_b128 a[244:247], v9 offset:61440\n"
"v_mfma_f32_16x16x32_bf16 a[72:75], v[136:139], v[80:83], a[72:75]\n"
"v_exp_f32 v33, v119\n"
"v_mfma_f32_16x16x32_bf16 a[72:75], v[168:171], v[84:87], a[72:75]\n"
"ds_read_b128 a[248:251], v10 offset:61440\n"
"v_cvt_pk_bf16_f32 v91, v32, v33\n"
"v_mfma_f32_16x16x32_bf16 a[76:79], v[140:143], v[80:83], a[76:79]\n"
"v_exp_f32 v34, v120\n"
"v_mfma_f32_16x16x32_bf16 a[76:79], v[172:175], v[84:87], a[76:79]\n"
"ds_read_b128 a[252:255], v11 offset:61440\n"
"v_mfma_f32_16x16x32_bf16 a[80:83], v[144:147], v[80:83], a[80:83]\n"
"v_exp_f32 v35, v121\n"
"v_mfma_f32_16x16x32_bf16 a[80:83], v[176:179], v[84:87], a[80:83]\n"
"v_cvt_pk_bf16_f32 v92, v34, v35\n"
"v_mfma_f32_16x16x32_bf16 a[84:87], v[148:151], v[80:83], a[84:87]\n"
"v_exp_f32 v36, v122\n"
"v_mfma_f32_16x16x32_bf16 a[84:87], v[180:183], v[84:87], a[84:87]\n"
"buffer_load_dwordx4 v[60:63], v38, s[88:91], s52 offen\n"
"v_mfma_f32_16x16x32_bf16 a[88:91], v[152:155], v[80:83], a[88:91]\n"
"v_exp_f32 v37, v123\n"
"v_mfma_f32_16x16x32_bf16 a[88:91], v[184:187], v[84:87], a[88:91]\n"
"v_cvt_pk_bf16_f32 v93, v36, v37\n"
"v_mfma_f32_16x16x32_bf16 a[92:95], v[156:159], v[80:83], a[92:95]\n"
"v_exp_f32 v32, v124\n"
"v_mfma_f32_16x16x32_bf16 a[92:95], v[188:191], v[84:87], a[92:95]\n"
"s_waitcnt vmcnt(16) lgkmcnt(0)\n"
"s_barrier\n"
"v_mfma_f32_16x16x32_bf16 v[96:99], a[192:195], a[128:131], v[64:67]\n"
"v_exp_f32 v33, v125\n"
"v_mfma_f32_16x16x32_bf16 v[96:99], a[196:199], a[132:135], v[96:99]\n"
"s_add_u32 m0, s59, 49152\n"
"v_cvt_pk_bf16_f32 v94, v32, v33\n"
"v_mfma_f32_16x16x32_bf16 v[96:99], a[200:203], a[136:139], v[96:99]\n"
"v_exp_f32 v34, v126\n"
"v_mfma_f32_16x16x32_bf16 v[96:99], a[204:207], a[140:143], v[96:99]\n"
"buffer_load_dwordx4 v20, s[44:47], s62 offen offset:0 lds\n"
"v_mfma_f32_16x16x32_bf16 v[100:103], a[208:211], a[128:131], v[64:67]\n"
"v_exp_f32 v35, v127\n"
"v_mfma_f32_16x16x32_bf16 v[100:103], a[212:215], a[132:135], v[100:103]\n"
"v_cvt_pk_bf16_f32 v95, v34, v35\n"
"v_mfma_f32_16x16x32_bf16 v[100:103], a[216:219], a[136:139], v[100:103]\n"
"v_mfma_f32_16x16x32_bf16 v[100:103], a[220:223], a[140:143], v[100:103]\n"
"v_mfma_f32_16x16x32_bf16 v[44:47], v[56:59], v[88:91], 0\n"
"v_exp_f32 v32, v96\n"
"v_mfma_f32_16x16x32_bf16 v[44:47], v[56:59], v[92:95], v[44:47]\n"
"buffer_load_dwordx4 v21, s[44:47], s62 offen offset:1024 lds\n"
"v_mfma_f32_16x16x32_bf16 v[104:107], a[224:227], a[128:131], v[64:67]\n"
"v_exp_f32 v33, v97\n"
"v_mfma_f32_16x16x32_bf16 v[104:107], a[228:231], a[132:135], v[104:107]\n"
"v_cvt_pk_bf16_f32 v80, v32, v33\n"
"v_mfma_f32_16x16x32_bf16 v[104:107], a[232:235], a[136:139], v[104:107]\n"
"v_exp_f32 v34, v98\n"
"v_mfma_f32_16x16x32_bf16 v[104:107], a[236:239], a[140:143], v[104:107]\n"
"v_cmp_nge_f32 vcc, s64, v44\n"
"v_mfma_f32_16x16x32_bf16 v[108:111], a[240:243], a[128:131], v[64:67]\n"
"v_exp_f32 v35, v99\n"
"v_mfma_f32_16x16x32_bf16 v[108:111], a[244:247], a[132:135], v[108:111]\n"
"s_cbranch_vccnz AT_R12\n"
"AT_B12:\n"
"v_cvt_pk_bf16_f32 v81, v34, v35\n"
"v_mfma_f32_16x16x32_bf16 v[108:111], a[248:251], a[136:139], v[108:111]\n"
"v_exp_f32 v36, v100\n"
"v_mfma_f32_16x16x32_bf16 v[108:111], a[252:255], a[140:143], v[108:111]\n"
"v_add_f32 v31, v31, v44\n"
"v_mfma_f32_16x16x32_bf16 a[96:99], v[128:131], v[88:91], a[96:99]\n"
"v_exp_f32 v37, v101\n"
"v_mfma_f32_16x16x32_bf16 a[96:99], v[160:163], v[92:95], a[96:99]\n"
"ds_read_b64_tr_b16 v[128:129], v12 offset:0\n"
"v_cvt_pk_bf16_f32 v82, v36, v37\n"
"v_mfma_f32_16x16x32_bf16 a[100:103], v[132:135], v[88:91], a[100:103]\n"
"v_exp_f32 v32, v102\n"
"v_mfma_f32_16x16x32_bf16 a[100:103], v[164:167], v[92:95], a[100:103]\n"
"ds_read_b64_tr_b16 v[130:131], v12 offset:4096\n"
"v_mfma_f32_16x16x32_bf16 a[104:107], v[136:139], v[88:91], a[104:107]\n"
"v_exp_f32 v33, v103\n"
"v_mfma_f32_16x16x32_bf16 a[104:107], v[168:171], v[92:95], a[104:107]\n"
"v_cvt_pk_bf16_f32 v83, v32, v33\n"
"v_mfma_f32_16x16x32_bf16 a[108:111], v[140:143], v[88:91], a[108:111]\n"
"v_exp_f32 v34, v104\n"
"v_mfma_f32_16x16x32_bf16 a[108:111], v[172:175], v[92:95], a[108:111]\n"
"buffer_load_dwordx4 v22, s[44:47], s62 offen offset:2048 lds\n"
"v_mfma_f32_16x16x32_bf16 a[112:115], v[144:147], v[88:91], a[112:115]\n"
"v_exp_f32 v35, v105\n"
"v_mfma_f32_16x16x32_bf16 a[112:115], v[176:179], v[92:95], a[112:115]\n"
"ds_read_b64_tr_b16 v[160:161], v12 offset:8192\n"
"v_cvt_pk_bf16_f32 v84, v34, v35\n"
"v_mfma_f32_16x16x32_bf16 a[116:119], v[148:151], v[88:91], a[116:119]\n"
"v_exp_f32 v36, v106\n"
"v_mfma_f32_16x16x32_bf16 a[116:119], v[180:183], v[92:95], a[116:119]\n"
"ds_read_b64_tr_b16 v[162:163], v12 offset:12288\n"
"ds_read_b64_tr_b16 v[132:133], v13 offset:0\n"
"v_mfma_f32_16x16x32_bf16 a[120:123], v[152:155], v[88:91], a[120:123]\n"
"v_exp_f32 v37, v107\n"
"v_mfma_f32_16x16x32_bf16 a[120:123], v[184:187], v[92:95], a[120:123]\n"
"ds_read_b64_tr_b16 v[134:135], v13 offset:4096\n"
"v_cvt_pk_bf16_f32 v85, v36, v37\n"
"v_mfma_f32_16x16x32_bf16 a[124:127], v[156:159], v[88:91], a[124:127]\n"
"v_exp_f32 v32, v108\n"
"v_mfma_f32_16x16x32_bf16 a[124:127], v[188:191], v[92:95], a[124:127]\n"
"buffer_load_dwordx4 v23, s[44:47], s62 offen offset:3072 lds\n"
"AT_P1_3:\n"
"v_mfma_f32_16x16x32_bf16 v[112:115], a[192:195], a[144:147], v[68:71]\n"
"v_exp_f32 v33, v109\n"
"v_mfma_f32_16x16x32_bf16 v[112:115], a[196:199], a[148:151], v[112:115]\n"
"ds_read_b64_tr_b16 v[164:165], v13 offset:8192\n"
"v_cvt_pk_bf16_f32 v86, v32, v33\n"
"v_mfma_f32_16x16x32_bf16 v[112:115], a[200:203], a[152:155], v[112:115]\n"
"v_exp_f32 v34, v110\n"
"v_mfma_f32_16x16x32_bf16 v[112:115], a[204:207], a[156:159], v[112:115]\n"
"ds_read_b64_tr_b16 v[166:167], v13 offset:12288\n"
"v_mfma_f32_16x16x32_bf16 v[116:119], a[208:211], a[144:147], v[68:71]\n"
"v_exp_f32 v35, v111\n"
"v_mfma_f32_16x16x32_bf16 v[116:119], a[212:215], a[148:151], v[116:119]\n"
"s_add_u32 m0, s59, 114688\n"
"v_cvt_pk_bf16_f32 v87, v34, v35\n"
"v_mfma_f32_16x16x32_bf16 v[116:119], a[216:219], a[152:155], v[116:119]\n"
"ds_read_b64_tr_b16 v[136:137], v14 offset:0\n"
"v_mfma_f32_16x16x32_bf16 v[116:119], a[220:223], a[156:159], v[116:119]\n"
"buffer_load_dwordx4 v24, s[48:51], s62 offen offset:0 lds\n"
"v_mfma_f32_16x16x32_bf16 v[40:43], v[56:59], v[80:83], 0\n"
"v_exp_f32 v32, v112\n"
"v_mfma_f32_16x16x32_bf16 v[40:43], v[56:59], v[84:87], v[40:43]\n"
"ds_read_b64_tr_b16 v[138:139], v14 offset:4096\n"
"v_mfma_f32_16x16x32_bf16 v[120:123], a[224:227], a[144:147], v[68:71]\n"
"v_exp_f32 v33, v113\n"
"v_mfma_f32_16x16x32_bf16 v[120:123], a[228:231], a[148:151], v[120:123]\n"
"ds_read_b64_tr_b16 v[168:169], v14 offset:8192\n"
"v_cvt_pk_bf16_f32 v88, v32, v33\n"
"v_mfma_f32_16x16x32_bf16 v[120:123], a[232:235], a[152:155], v[120:123]\n"
"v_exp_f32 v34, v114\n"
"v_mfma_f32_16x16x32_bf16 v[120:123], a[236:239], a[156:159], v[120:123]\n"
"ds_read_b64_tr_b16 v[170:171], v14 offset:12288\n"
"v_cmp_nge_f32 vcc, s64, v40\n"
"v_mfma_f32_16x16x32_bf16 v[124:127], a[240:243], a[144:147], v[68:71]\n"
"v_exp_f32 v35, v115\n"
"v_mfma_f32_16x16x32_bf16 v[124:127], a[244:247], a[148:151], v[124:127]\n"
"s_cbranch_vccnz AT_R13\n"
"AT_B13:\n"
"v_cvt_pk_bf16_f32 v89, v34, v35\n"
"v_mfma_f32_16x16x32_bf16 v[124:127], a[248:251], a[152:155], v[124:127]\n"
"v_exp_f32 v36, v116\n"
"v_mfma_f32_16x16x32_bf16 v[124:127], a[252:255], a[156:159], v[124:127]\n"
"ds_read_b64_tr_b16 v[140:141], v15 offset:0\n"
"v_add_f32 v28, v28, v40\n"
"v_mfma_f32_16x16x32_bf16 a[0:3], v[192:195], v[80:83], a[0:3]\n"
"v_exp_f32 v37, v117\n"
"v_mfma_f32_16x16x32_bf16 a[0:3], v[224:227], v[84:87], a[0:3]\n"
"ds_read_b64_tr_b16 v[142:143], v15 offset:4096\n"
"v_cvt_pk_bf16_f32 v90, v36, v37\n"
"v_mfma_f32_16x16x32_bf16 a[4:7], v[196:199], v[80:83], a[4:7]\n"
"v_exp_f32 v32, v118\n"
"v_mfma_f32_16x16x32_bf16 a[4:7], v[228:231], v[84:87], a[4:7]\n"
"buffer_load_dwordx4 v25, s[48:51], s62 offen offset:1024 lds\n"
"v_mfma_f32_16x16x32_bf16 a[8:11], v[200:203], v[80:83], a[8:11]\n"
"v_exp_f32 v33, v119\n"
"v_mfma_f32_16x16x32_bf16 a[8:11], v[232:235], v[84:87], a[8:11]\n"
"ds_read_b64_tr_b16 v[172:173], v15 offset:8192\n"
"v_cvt_pk_bf16_f32 v91, v32, v33\n"
"v_mfma_f32_16x16x32_bf16 a[12:15], v[204:207], v[80:83], a[12:15]\n"
"v_exp_f32 v34, v120\n"
"v_mfma_f32_16x16x32_bf16 a[12:15], v[236:239], v[84:87], a[12:15]\n"
"ds_read_b64_tr_b16 v[174:175], v15 offset:12288\n"
"ds_read_b64_tr_b16 v[144:145], v16 offset:0\n"
"v_mfma_f32_16x16x32_bf16 a[16:19], v[208:211], v[80:83], a[16:19]\n"
"v_exp_f32 v35, v121\n"
"v_mfma_f32_16x16x32_bf16 a[16:19], v[240:243], v[84:87], a[16:19]\n"
"v_cvt_pk_bf16_f32 v92, v34, v35\n"
"v_mfma_f32_16x16x32_bf16 a[20:23], v[212:215], v[80:83], a[20:23]\n"
"v_exp_f32 v36, v122\n"
"v_mfma_f32_16x16x32_bf16 a[20:23], v[244:247], v[84:87], a[20:23]\n"
"ds_read_b64_tr_b16 v[146:147], v16 offset:4096\n"
"v_mfma_f32_16x16x32_bf16 a[24:27], v[216:219], v[80:83], a[24:27]\n"
"v_exp_f32 v37, v123\n"
"v_mfma_f32_16x16x32_bf16 a[24:27], v[248:251], v[84:87], a[24:27]\n"
"ds_read_b64_tr_b16 v[176:177], v16 offset:8192\n"
"v_cvt_pk_bf16_f32 v93, v36, v37\n"
"v_mfma_f32_16x16x32_bf16 a[28:31], v[220:223], v[80:83], a[28:31]\n"
"v_exp_f32 v32, v124\n"
"v_mfma_f32_16x16x32_bf16 a[28:31], v[252:255], v[84:87], a[28:31]\n"
"ds_read_b64_tr_b16 v[178:179], v16 offset:12288\n"
"v_mfma_f32_16x16x32_bf16 v[96:99], a[192:195], a[160:163], v[72:75]\n"
"v_exp_f32 v33, v125\n"
"v_mfma_f32_16x16x32_bf16 v[96:99], a[196:199], a[164:167], v[96:99]\n"
"ds_read_b64_tr_b16 v[148:149], v17 offset:0\n"
"v_cvt_pk_bf16_f32 v94, v32, v33\n"
"v_mfma_f32_16x16x32_bf16 v[96:99], a[200:203], a[168:171], v[96:99]\n"
"v_exp_f32 v34, v126\n"
"v_mfma_f32_16x16x32_bf16 v[96:99], a[204:207], a[172:175], v[96:99]\n"
"buffer_load_dwordx4 v26, s[48:51], s62 offen offset:2048 lds\n"
"v_mfma_f32_16x16x32_bf16 v[100:103], a[208:211], a[160:163], v[72:75]\n"
"v_exp_f32 v35, v127\n"
"v_mfma_f32_16x16x32_bf16 v[100:103], a[212:215], a[164:167], v[100:103]\n"
"ds_read_b64_tr_b16 v[150:151], v17 offset:4096\n"
"v_cvt_pk_bf16_f32 v95, v34, v35\n"
"v_mfma_f32_16x16x32_bf16 v[100:103], a[216:219], a[168:171], v[100:103]\n"
"ds_read_b64_tr_b16 v[180:181], v17 offset:8192\n"
"v_mfma_f32_16x16x32_bf16 v[100:103], a[220:223], a[172:175], v[100:103]\n"
"v_mfma_f32_16x16x32_bf16 v[44:47], v[56:59], v[88:91], 0\n"
"v_exp_f32 v32, v96\n"
"v_mfma_f32_16x16x32_bf16 v[44:47], v[56:59], v[92:95], v[44:47]\n"
"ds_read_b64_tr_b16 v[182:183], v17 offset:12288\n"
"v_mfma_f32_16x16x32_bf16 v[104:107], a[224:227], a[160:163], v[72:75]\n"
"v_exp_f32 v33, v97\n"
"v_mfma_f32_16x16x32_bf16 v[104:107], a[228:231], a[164:167], v[104:107]\n"
"ds_read_b64_tr_b16 v[152:153], v18 offset:0\n"
"v_cvt_pk_bf16_f32 v80, v32, v33\n"
"v_mfma_f32_16x16x32_bf16 v[104:107], a[232:235], a[168:171], v[104:107]\n"
"v_exp_f32 v34, v98\n"
"v_mfma_f32_16x16x32_bf16 v[104:107], a[236:239], a[172:175], v[104:107]\n"
"v_cmp_nge_f32 vcc, s64, v44\n"
"v_mfma_f32_16x16x32_bf16 v[108:111], a[240:243], a[160:163], v[72:75]\n"
"v_exp_f32 v35, v99\n"
"v_mfma_f32_16x16x32_bf16 v[108:111], a[244:247], a[164:167], v[108:111]\n"
"s_cbranch_vccnz AT_R14\n"
"AT_B14:\n"
"v_cvt_pk_bf16_f32 v81, v34, v35\n"
"v_mfma_f32_16x16x32_bf16 v[108:111], a[248:251], a[168:171], v[108:111]\n"
"v_exp_f32 v36, v100\n"
"v_mfma_f32_16x16x32_bf16 v[108:111], a[252:255], a[172:175], v[108:111]\n"
"ds_read_b64_tr_b16 v[154:155], v18 offset:4096\n"
"v_add_f32 v29, v29, v44\n"
"v_mfma_f32_16x16x32_bf16 a[32:35], v[192:195], v[88:91], a[32:35]\n"
"v_exp_f32 v37, v101\n"
"v_mfma_f32_16x16x32_bf16 a[32:35], v[224:227], v[92:95], a[32:35]\n"
"ds_read_b64_tr_b16 v[184:185], v18 offset:8192\n"
"v_cvt_pk_bf16_f32 v82, v36, v37\n"
"v_mfma_f32_16x16x32_bf16 a[36:39], v[196:199], v[88:91], a[36:39]\n"
"v_exp_f32 v32, v102\n"
"v_mfma_f32_16x16x32_bf16 a[36:39], v[228:231], v[92:95], a[36:39]\n"
"buffer_load_dwordx4 v27, s[48:51], s62 offen offset:3072 lds\n"
"v_mfma_f32_16x16x32_bf16 a[40:43], v[200:203], v[88:91], a[40:43]\n"
"v_exp_f32 v33, v103\n"
"v_mfma_f32_16x16x32_bf16 a[40:43], v[232:235], v[92:95], a[40:43]\n"
"ds_read_b64_tr_b16 v[186:187], v18 offset:12288\n"
"v_cvt_pk_bf16_f32 v83, v32, v33\n"
"v_mfma_f32_16x16x32_bf16 a[44:47], v[204:207], v[88:91], a[44:47]\n"
"v_exp_f32 v34, v104\n"
"v_mfma_f32_16x16x32_bf16 a[44:47], v[236:239], v[92:95], a[44:47]\n"
"ds_read_b64_tr_b16 v[156:157], v19 offset:0\n"
"ds_read_b64_tr_b16 v[158:159], v19 offset:4096\n"
"v_mfma_f32_16x16x32_bf16 a[48:51], v[208:211], v[88:91], a[48:51]\n"
"v_exp_f32 v35, v105\n"
"v_mfma_f32_16x16x32_bf16 a[48:51], v[240:243], v[92:95], a[48:51]\n"
"ds_read_b64_tr_b16 v[188:189], v19 offset:8192\n"
"v_cvt_pk_bf16_f32 v84, v34, v35\n"
"v_mfma_f32_16x16x32_bf16 a[52:55], v[212:215], v[88:91], a[52:55]\n"
"v_exp_f32 v36, v106\n"
"v_mfma_f32_16x16x32_bf16 a[52:55], v[244:247], v[92:95], a[52:55]\n"
"ds_read_b64_tr_b16 v[190:191], v19 offset:12288\n"
"v_mfma_f32_16x16x32_bf16 a[56:59], v[216:219], v[88:91], a[56:59]\n"
"v_exp_f32 v37, v107\n"
"v_mfma_f32_16x16x32_bf16 a[56:59], v[248:251], v[92:95], a[56:59]\n"
"s_add_u32 s62, s62, s54\n"
"v_cvt_pk_bf16_f32 v85, v36, v37\n"
"v_mfma_f32_16x16x32_bf16 a[60:63], v[220:223], v[88:91], a[60:63]\n"
"v_exp_f32 v32, v108\n"
"v_mfma_f32_16x16x32_bf16 a[60:63], v[252:255], v[92:95], a[60:63]\n"
"v_mfma_f32_16x16x32_bf16 v[112:115], a[192:195], a[176:179], v[76:79]\n"
"v_exp_f32 v33, v109\n"
"v_mfma_f32_16x16x32_bf16 v[112:115], a[196:199], a[180:183], v[112:115]\n"
"ds_read_b128 a[192:195], v8 offset:0\n"
"v_cvt_pk_bf16_f32 v86, v32, v33\n"
"v_mfma_f32_16x16x32_bf16 v[112:115], a[200:203], a[184:187], v[112:115]\n"
"v_exp_f32 v34, v110\n"
"v_mfma_f32_16x16x32_bf16 v[112:115], a[204:207], a[188:191], v[112:115]\n"
"ds_read_b128 a[196:199], v9 offset:0\n"
"ds_read_b128 a[200:203], v10 offset:0\n"
"v_mfma_f32_16x16x32_bf16 v[116:119], a[208:211], a[176:179], v[76:79]\n"
"v_exp_f32 v35, v111\n"
"v_mfma_f32_16x16x32_bf16 v[116:119], a[212:215], a[180:183], v[116:119]\n"
"ds_read_b128 a[204:207], v11 offset:0\n"
"v_cvt_pk_bf16_f32 v87, v34, v35\n"
"v_mfma_f32_16x16x32_bf16 v[116:119], a[216:219], a[184:187], v[116:119]\n"
"ds_read_b128 a[208:211], v8 offset:4096\n"
"ds_read_b128 a[212:215], v9 offset:4096\n"
"v_mfma_f32_16x16x32_bf16 v[116:119], a[220:223], a[188:191], v[116:119]\n"
"ds_read_b128 a[216:219], v10 offset:4096\n"
"ds_read_b128 a[220:223], v11 offset:4096\n"
"v_mfma_f32_16x16x32_bf16 v[40:43], v[56:59], v[80:83], 0\n"
"v_exp_f32 v32, v112\n"
"v_mfma_f32_16x16x32_bf16 v[40:43], v[56:59], v[84:87], v[40:43]\n"
"v_mfma_f32_16x16x32_bf16 v[120:123], a[224:227], a[176:179], v[76:79]\n"
"v_exp_f32 v33, v113\n"
"v_mfma_f32_16x16x32_bf16 v[120:123], a[228:231], a[180:183], v[120:123]\n"
"ds_read_b128 a[224:227], v8 offset:8192\n"
"v_cvt_pk_bf16_f32 v88, v32, v33\n"
"v_mfma_f32_16x16x32_bf16 v[120:123], a[232:235], a[184:187], v[120:123]\n"
"v_exp_f32 v34, v114\n"
"v_mfma_f32_16x16x32_bf16 v[120:123], a[236:239], a[188:191], v[120:123]\n"
"ds_read_b128 a[228:231], v9 offset:8192\n"
"v_cmp_nge_f32 vcc, s64, v40\n"
"v_mfma_f32_16x16x32_bf16 v[124:127], a[240:243], a[176:179], v[76:79]\n"
"v_exp_f32 v35, v115\n"
"v_mfma_f32_16x16x32_bf16 v[124:127], a[244:247], a[180:183], v[124:127]\n"
"s_cbranch_vccnz AT_R15\n"
"AT_B15:\n"
"v_cvt_pk_bf16_f32 v89, v34, v35\n"
"v_mfma_f32_16x16x32_bf16 v[124:127], a[248:251], a[184:187], v[124:127]\n"
"v_exp_f32 v36, v116\n"
"v_mfma_f32_16x16x32_bf16 v[124:127], a[252:255], a[188:191], v[124:127]\n"
"ds_read_b128 a[232:235], v10 offset:8192\n"
"v_add_f32 v30, v30, v40\n"
"v_mfma_f32_16x16x32_bf16 a[64:67], v[192:195], v[80:83], a[64:67]\n"
"v_exp_f32 v37, v117\n"
"v_mfma_f32_16x16x32_bf16 a[64:67], v[224:227], v[84:87], a[64:67]\n"
"ds_read_b128 a[236:239], v11 offset:8192\n"
"v_cvt_pk_bf16_f32 v90, v36, v37\n"
"v_mfma_f32_16x16x32_bf16 a[68:71], v[196:199], v[80:83], a[68:71]\n"
"v_exp_f32 v32, v118\n"
"v_mfma_f32_16x16x32_bf16 a[68:71], v[228:231], v[84:87], a[68:71]\n"
"ds_read_b128 a[240:243], v8 offset:12288\n"
"ds_read_b128 a[244:247], v9 offset:12288\n"
"v_mfma_f32_16x16x32_bf16 a[72:75], v[200:203], v[80:83], a[72:75]\n"
"v_exp_f32 v33, v119\n"
"v_mfma_f32_16x16x32_bf16 a[72:75], v[232:235], v[84:87], a[72:75]\n"
"ds_read_b128 a[248:251], v10 offset:12288\n"
"v_cvt_pk_bf16_f32 v91, v32, v33\n"
"v_mfma_f32_16x16x32_bf16 a[76:79], v[204:207], v[80:83], a[76:79]\n"
"v_exp_f32 v34, v120\n"
"v_mfma_f32_16x16x32_bf16 a[76:79], v[236:239], v[84:87], a[76:79]\n"
"ds_read_b128 a[252:255], v11 offset:12288\n"
"v_mfma_f32_16x16x32_bf16 a[80:83], v[208:211], v[80:83], a[80:83]\n"
"v_exp_f32 v35, v121\n"
"v_mfma_f32_16x16x32_bf16 a[80:83], v[240:243], v[84:87], a[80:83]\n"
"v_cvt_pk_bf16_f32 v92, v34, v35\n"
"v_mfma_f32_16x16x32_bf16 a[84:87], v[212:215], v[80:83], a[84:87]\n"
"v_exp_f32 v36, v122\n"
"v_mfma_f32_16x16x32_bf16 a[84:87], v[244:247], v[84:87], a[84:87]\n"
"buffer_load_dwordx4 v[60:63], v38, s[92:95], s52 offen\n"
"v_mfma_f32_16x16x32_bf16 a[88:91], v[216:219], v[80:83], a[88:91]\n"
"v_exp_f32 v37, v123\n"
"v_mfma_f32_16x16x32_bf16 a[88:91], v[248:251], v[84:87], a[88:91]\n"
"v_cvt_pk_bf16_f32 v93, v36, v37\n"
"v_mfma_f32_16x16x32_bf16 a[92:95], v[220:223], v[80:83], a[92:95]\n"
"v_exp_f32 v32, v124\n"
"v_mfma_f32_16x16x32_bf16 a[92:95], v[252:255], v[84:87], a[92:95]\n"
"s_add_u32 s52, s52, 0x100000\n"
"s_sub_u32 s60, s60, 1\n"
"s_cmp_gt_u32 s60, 1\n"
"s_cbranch_scc1 AT_LOOP\n"
"s_cmp_eq_u32 s60, 1\n"
"s_cbranch_scc1 AT_SWAP\n"
"s_mov_b64 s[36:37], %0\n"
"s_add_u32 s36, s36, %11\n"
"s_addc_u32 s37, s37, 0\n"
"s_mov_b32 s38, 524288\n"
"s_mov_b32 s39, 0x20000\n"
"s_mov_b32 s56, 0\n"
"s_mov_b32 s57, 8192\n"
"v_exp_f32 v33, v125\n"
"v_exp_f32 v34, v126\n"
"v_exp_f32 v35, v127\n"
"v_cvt_pk_bf16_f32 v94, v32, v33\n"
"v_cvt_pk_bf16_f32 v95, v34, v35\n"
"v_mfma_f32_16x16x32_bf16 v[44:47], v[56:59], v[88:91], 0\n"
"s_nop 0\n"
"v_mfma_f32_16x16x32_bf16 v[44:47], v[56:59], v[92:95], v[44:47]\n"
"s_waitcnt lgkmcnt(0)\n"
"buffer_load_dwordx4 a[128:131], v39, s[36:39], s56 offen offset:0 sc1\n"
"buffer_load_dwordx4 a[136:139], v39, s[36:39], s56 offen offset:1024 sc1\n"
"buffer_load_dwordx4 a[144:147], v39, s[36:39], s56 offen offset:2048 sc1\n"
"buffer_load_dwordx4 a[152:155], v39, s[36:39], s56 offen offset:3072 sc1\n"
"s_add_u32 s56, s56, 16384\n"
"s_nop 1\n"
"v_cmp_nge_f32 vcc, s64, v44\n"
"s_nop 0\n"
"s_cbranch_vccnz AT_R16\n"
"AT_B16:\n"
"v_add_f32 v31, v31, v44\n"
"v_mfma_f32_16x16x32_bf16 a[96:99], v[192:195], v[88:91], a[96:99]\n"
"buffer_load_dwordx4 a[132:135], v39, s[36:39], s57 offen offset:0 sc1\n"
"v_mfma_f32_16x16x32_bf16 a[96:99], v[224:227], v[92:95], a[96:99]\n"
"buffer_load_dwordx4 a[140:143], v39, s[36:39], s57 offen offset:1024 sc1\n"
"v_mfma_f32_16x16x32_bf16 a[100:103], v[196:199], v[88:91], a[100:103]\n"
"buffer_load_dwordx4 a[148:151], v39, s[36:39], s57 offen offset:2048 sc1\n"
"v_mfma_f32_16x16x32_bf16 a[100:103], v[228:231], v[92:95], a[100:103]\n"
"buffer_load_dwordx4 a[156:159], v39, s[36:39], s57 offen offset:3072 sc1\n"
"s_add_u32 s57, s57, 16384\n"
"v_mfma_f32_16x16x32_bf16 a[104:107], v[200:203], v[88:91], a[104:107]\n"
"v_mfma_f32_16x16x32_bf16 a[104:107], v[232:235], v[92:95], a[104:107]\n"
"v_mfma_f32_16x16x32_bf16 a[108:111], v[204:207], v[88:91], a[108:111]\n"
"v_mfma_f32_16x16x32_bf16 a[108:111], v[236:239], v[92:95], a[108:111]\n"
"v_mfma_f32_16x16x32_bf16 a[112:115], v[208:211], v[88:91], a[112:115]\n"
"v_mfma_f32_16x16x32_bf16 a[112:115], v[240:243], v[92:95], a[112:115]\n"
"v_mfma_f32_16x16x32_bf16 a[116:119], v[212:215], v[88:91], a[116:119]\n"
"v_mfma_f32_16x16x32_bf16 a[116:119], v[244:247], v[92:95], a[116:119]\n"
"v_mfma_f32_16x16x32_bf16 a[120:123], v[216:219], v[88:91], a[120:123]\n"
"v_mfma_f32_16x16x32_bf16 a[120:123], v[248:251], v[92:95], a[120:123]\n"
"v_mfma_f32_16x16x32_bf16 a[124:127], v[220:223], v[88:91], a[124:127]\n"
"v_mfma_f32_16x16x32_bf16 a[124:127], v[252:255], v[92:95], a[124:127]\n"
"v_rcp_f32 v48, v28\n"
"v_rcp_f32 v50, v29\n"
"v_rcp_f32 v52, v30\n"
"v_rcp_f32 v54, v31\n"
"v_mov_b32 v49, v48\n"
"v_mov_b32 v51, v50\n"
"v_mov_b32 v53, v52\n"
"v_mov_b32 v55, v54\n"
"s_lshr_b32 s98, s55, 1\n"
"s_mul_i32 s98, s98, 1048576\n"
"s_and_b32 s99, s55, 1\n"
"s_lshl_b32 s99, s99, 12\n"
"s_add_u32 s98, s98, s99\n"
"v_add_u32 v32, s98, v2\n"
"v_add_u32 v32, %12, v32\n"
"v_add_u32 v33, 8192, v32\n"
"v_add_u32 v34, 16384, v32\n"
"v_add_u32 v35, 24576, v32\n"
"v_mov_b32 v40, v48\n"
"v_mov_b32 v41, v48\n"
"v_mov_b32 v42, v50\n"
"v_mov_b32 v43, v50\n"
"v_mov_b32 v44, v52\n"
"v_mov_b32 v45, v52\n"
"v_mov_b32 v46, v54\n"
"v_mov_b32 v47, v54\n"
"s_lshl_b32 s98, s55, 13\n"
"s_add_u32 s98, s98, 131072\n"
"v_add_u32 v8, s98, v2\n"
"v_accvgpr_read_b32 v64, a0\n"
"v_accvgpr_read_b32 v65, a1\n"
"v_accvgpr_read_b32 v66, a2\n"
"v_accvgpr_read_b32 v67, a3\n"
"v_accvgpr_read_b32 v68, a4\n"
"v_accvgpr_read_b32 v69, a5\n"
"v_accvgpr_read_b32 v70, a6\n"
"v_accvgpr_read_b32 v71, a7\n"
"v_pk_mul_f32 v[64:65], v[64:65], v[40:41]\n"
"v_pk_mul_f32 v[66:67], v[66:67], v[40:41]\n"
"v_pk_mul_f32 v[68:69], v[68:69], v[40:41]\n"
"v_pk_mul_f32 v[70:71], v[70:71], v[40:41]\n"
"v_cvt_pk_bf16_f32 v72, v64, v65\n"
"v_cvt_pk_bf16_f32 v73, v66, v67\n"
"v_cvt_pk_bf16_f32 v74, v68, v69\n"
"v_cvt_pk_bf16_f32 v75, v70, v71\n"
"global_store_dwordx4 v32, v[72:75], %0 offset:0 sc1\n"
"v_accvgpr_read_b32 v76, a8\n"
"v_accvgpr_read_b32 v77, a9\n"
"v_accvgpr_read_b32 v78, a10\n"
"v_accvgpr_read_b32 v79, a11\n"
"v_accvgpr_read_b32 v80, a12\n"
"v_accvgpr_read_b32 v81, a13\n"
"v_accvgpr_read_b32 v82, a14\n"
"v_accvgpr_read_b32 v83, a15\n"
"v_pk_mul_f32 v[76:77], v[76:77], v[40:41]\n"
"v_pk_mul_f32 v[78:79], v[78:79], v[40:41]\n"
"v_pk_mul_f32 v[80:81], v[80:81], v[40:41]\n"
"v_pk_mul_f32 v[82:83], v[82:83], v[40:41]\n"
"v_cvt_pk_bf16_f32 v96, v76, v77\n"
"v_cvt_pk_bf16_f32 v97, v78, v79\n"
"v_cvt_pk_bf16_f32 v98, v80, v81\n"
"v_cvt_pk_bf16_f32 v99, v82, v83\n"
"v_accvgpr_read_b32 v64, a16\n"
"v_accvgpr_read_b32 v65, a17\n"
"v_accvgpr_read_b32 v66, a18\n"
"v_accvgpr_read_b32 v67, a19\n"
"v_accvgpr_read_b32 v68, a20\n"
"v_accvgpr_read_b32 v69, a21\n"
"v_accvgpr_read_b32 v70, a22\n"
"v_accvgpr_read_b32 v71, a23\n"
"v_pk_mul_f32 v[64:65], v[64:65], v[40:41]\n"
"v_pk_mul_f32 v[66:67], v[66:67], v[40:41]\n"
"v_pk_mul_f32 v[68:69], v[68:69], v[40:41]\n"
"v_pk_mul_f32 v[70:71], v[70:71], v[40:41]\n"
"v_cvt_pk_bf16_f32 v72, v64, v65\n"
"v_cvt_pk_bf16_f32 v73, v66, v67\n"
"v_cvt_pk_bf16_f32 v74, v68, v69\n"
"v_cvt_pk_bf16_f32 v75, v70, v71\n"
"ds_write_b128 v8, v[72:75] offset:0\n"
"v_accvgpr_read_b32 v76, a24\n"
"v_accvgpr_read_b32 v77, a25\n"
"v_accvgpr_read_b32 v78, a26\n"
"v_accvgpr_read_b32 v79, a27\n"
"v_accvgpr_read_b32 v80, a28\n"
"v_accvgpr_read_b32 v81, a29\n"
"v_accvgpr_read_b32 v82, a30\n"
"v_accvgpr_read_b32 v83, a31\n"
"v_pk_mul_f32 v[76:77], v[76:77], v[40:41]\n"
"v_pk_mul_f32 v[78:79], v[78:79], v[40:41]\n"
"v_pk_mul_f32 v[80:81], v[80:81], v[40:41]\n"
"v_pk_mul_f32 v[82:83], v[82:83], v[40:41]\n"
"v_cvt_pk_bf16_f32 v84, v76, v77\n"
"v_cvt_pk_bf16_f32 v85, v78, v79\n"
"v_cvt_pk_bf16_f32 v86, v80, v81\n"
"v_cvt_pk_bf16_f32 v87, v82, v83\n"
"ds_write_b128 v8, v[84:87] offset:4096\n"
"v_accvgpr_read_b32 v64, a32\n"
"v_accvgpr_read_b32 v65, a33\n"
"v_accvgpr_read_b32 v66, a34\n"
"v_accvgpr_read_b32 v67, a35\n"
"v_accvgpr_read_b32 v68, a36\n"
"v_accvgpr_read_b32 v69, a37\n"
"v_accvgpr_read_b32 v70, a38\n"
"v_accvgpr_read_b32 v71, a39\n"
"v_pk_mul_f32 v[64:65], v[64:65], v[42:43]\n"
"v_pk_mul_f32 v[66:67], v[66:67], v[42:43]\n"
"v_pk_mul_f32 v[68:69], v[68:69], v[42:43]\n"
"v_pk_mul_f32 v[70:71], v[70:71], v[42:43]\n"
"v_cvt_pk_bf16_f32 v72, v64, v65\n"
"v_cvt_pk_bf16_f32 v73, v66, v67\n"
"v_cvt_pk_bf16_f32 v74, v68, v69\n"
"v_cvt_pk_bf16_f32 v75, v70, v71\n"
"global_store_dwordx4 v32, v[72:75], %0 offset:1024 sc1\n"
"v_accvgpr_read_b32 v76, a40\n"
"v_accvgpr_read_b32 v77, a41\n"
"v_accvgpr_read_b32 v78, a42\n"
"v_accvgpr_read_b32 v79, a43\n"
"v_accvgpr_read_b32 v80, a44\n"
"v_accvgpr_read_b32 v81, a45\n"
"v_accvgpr_read_b32 v82, a46\n"
"v_accvgpr_read_b32 v83, a47\n"
"v_pk_mul_f32 v[76:77], v[76:77], v[42:43]\n"
"v_pk_mul_f32 v[78:79], v[78:79], v[42:43]\n"
"v_pk_mul_f32 v[80:81], v[80:81], v[42:43]\n"
"v_pk_mul_f32 v[82:83], v[82:83], v[42:43]\n"
"v_cvt_pk_bf16_f32 v100, v76, v77\n"
"v_cvt_pk_bf16_f32 v101, v78, v79\n"
"v_cvt_pk_bf16_f32 v102, v80, v81\n"
"v_cvt_pk_bf16_f32 v103, v82, v83\n"
"v_accvgpr_read_b32 v64, a48\n"
"v_accvgpr_read_b32 v65, a49\n"
"v_accvgpr_read_b32 v66, a50\n"
"v_accvgpr_read_b32 v67, a51\n"
"v_accvgpr_read_b32 v68, a52\n"
"v_accvgpr_read_b32 v69, a53\n"
"v_accvgpr_read_b32 v70, a54\n"
"v_accvgpr_read_b32 v71, a55\n"
"v_pk_mul_f32 v[64:65], v[64:65], v[42:43]\n"
"v_pk_mul_f32 v[66:67], v[66:67], v[42:43]\n"
"v_pk_mul_f32 v[68:69], v[68:69], v[42:43]\n"
"v_pk_mul_f32 v[70:71], v[70:71], v[42:43]\n"
"v_cvt_pk_bf16_f32 v72, v64, v65\n"
"v_cvt_pk_bf16_f32 v73, v66, v67\n"
"v_cvt_pk_bf16_f32 v74, v68, v69\n"
"v_cvt_pk_bf16_f32 v75, v70, v71\n"
"ds_write_b128 v8, v[72:75] offset:1024\n"
"v_accvgpr_read_b32 v76, a56\n"
"v_accvgpr_read_b32 v77, a57\n"
"v_accvgpr_read_b32 v78, a58\n"
"v_accvgpr_read_b32 v79, a59\n"
"v_accvgpr_read_b32 v80, a60\n"
"v_accvgpr_read_b32 v81, a61\n"
"v_accvgpr_read_b32 v82, a62\n"
"v_accvgpr_read_b32 v83, a63\n"
"v_pk_mul_f32 v[76:77], v[76:77], v[42:43]\n"
"v_pk_mul_f32 v[78:79], v[78:79], v[42:43]\n"
"v_pk_mul_f32 v[80:81], v[80:81], v[42:43]\n"
"v_pk_mul_f32 v[82:83], v[82:83], v[42:43]\n"
"v_cvt_pk_bf16_f32 v84, v76, v77\n"
"v_cvt_pk_bf16_f32 v85, v78, v79\n"
"v_cvt_pk_bf16_f32 v86, v80, v81\n"
"v_cvt_pk_bf16_f32 v87, v82, v83\n"
"ds_write_b128 v8, v[84:87] offset:5120\n"
"v_accvgpr_read_b32 v64, a64\n"
"v_accvgpr_read_b32 v65, a65\n"
"v_accvgpr_read_b32 v66, a66\n"
"v_accvgpr_read_b32 v67, a67\n"
"v_accvgpr_read_b32 v68, a68\n"
"v_accvgpr_read_b32 v69, a69\n"
"v_accvgpr_read_b32 v70, a70\n"
"v_accvgpr_read_b32 v71, a71\n"
"v_pk_mul_f32 v[64:65], v[64:65], v[44:45]\n"
"v_pk_mul_f32 v[66:67], v[66:67], v[44:45]\n"
"v_pk_mul_f32 v[68:69], v[68:69], v[44:45]\n"
"v_pk_mul_f32 v[70:71], v[70:71], v[44:45]\n"
"v_cvt_pk_bf16_f32 v72, v64, v65\n"
"v_cvt_pk_bf16_f32 v73, v66, v67\n"
"v_cvt_pk_bf16_f32 v74, v68, v69\n"
"v_cvt_pk_bf16_f32 v75, v70, v71\n"
"global_store_dwordx4 v32, v[72:75], %0 offset:2048 sc1\n"
"v_accvgpr_read_b32 v76, a72\n"
"v_accvgpr_read_b32 v77, a73\n"
"v_accvgpr_read_b32 v78, a74\n"
"v_accvgpr_read_b32 v79, a75\n"
"v_accvgpr_read_b32 v80, a76\n"
"v_accvgpr_read_b32 v81, a77\n"
"v_accvgpr_read_b32 v82, a78\n"
"v_accvgpr_read_b32 v83, a79\n"
"v_pk_mul_f32 v[76:77], v[76:77], v[44:45]\n"
"v_pk_mul_f32 v[78:79], v[78:79], v[44:45]\n"
"v_pk_mul_f32 v[80:81], v[80:81], v[44:45]\n"
"v_pk_mul_f32 v[82:83], v[82:83], v[44:45]\n"
"v_cvt_pk_bf16_f32 v104, v76, v77\n"
"v_cvt_pk_bf16_f32 v105, v78, v79\n"
"v_cvt_pk_bf16_f32 v106, v80, v81\n"
"v_cvt_pk_bf16_f32 v107, v82, v83\n"
"v_accvgpr_read_b32 v64, a80\n"
"v_accvgpr_read_b32 v65, a81\n"
"v_accvgpr_read_b32 v66, a82\n"
"v_accvgpr_read_b32 v67, a83\n"
"v_accvgpr_read_b32 v68, a84\n"
"v_accvgpr_read_b32 v69, a85\n"
"v_accvgpr_read_b32 v70, a86\n"
"v_accvgpr_read_b32 v71, a87\n"
"v_pk_mul_f32 v[64:65], v[64:65], v[44:45]\n"
"v_pk_mul_f32 v[66:67], v[66:67], v[44:45]\n"
"v_pk_mul_f32 v[68:69], v[68:69], v[44:45]\n"
"v_pk_mul_f32 v[70:71], v[70:71], v[44:45]\n"
"v_cvt_pk_bf16_f32 v72, v64, v65\n"
"v_cvt_pk_bf16_f32 v73, v66, v67\n"
"v_cvt_pk_bf16_f32 v74, v68, v69\n"
"v_cvt_pk_bf16_f32 v75, v70, v71\n"
"ds_write_b128 v8, v[72:75] offset:2048\n"
"v_accvgpr_read_b32 v76, a88\n"
"v_accvgpr_read_b32 v77, a89\n"
"v_accvgpr_read_b32 v78, a90\n"
"v_accvgpr_read_b32 v79, a91\n"
"v_accvgpr_read_b32 v80, a92\n"
"v_accvgpr_read_b32 v81, a93\n"
"v_accvgpr_read_b32 v82, a94\n"
"v_accvgpr_read_b32 v83, a95\n"
"v_pk_mul_f32 v[76:77], v[76:77], v[44:45]\n"
"v_pk_mul_f32 v[78:79], v[78:79], v[44:45]\n"
"v_pk_mul_f32 v[80:81], v[80:81], v[44:45]\n"
"v_pk_mul_f32 v[82:83], v[82:83], v[44:45]\n"
"v_cvt_pk_bf16_f32 v84, v76, v77\n"
"v_cvt_pk_bf16_f32 v85, v78, v79\n"
"v_cvt_pk_bf16_f32 v86, v80, v81\n"
"v_cvt_pk_bf16_f32 v87, v82, v83\n"
"ds_write_b128 v8, v[84:87] offset:6144\n"
"v_accvgpr_read_b32 v64, a96\n"
"v_accvgpr_read_b32 v65, a97\n"
"v_accvgpr_read_b32 v66, a98\n"
"v_accvgpr_read_b32 v67, a99\n"
"v_accvgpr_read_b32 v68, a100\n"
"v_accvgpr_read_b32 v69, a101\n"
"v_accvgpr_read_b32 v70, a102\n"
"v_accvgpr_read_b32 v71, a103\n"
"v_pk_mul_f32 v[64:65], v[64:65], v[46:47]\n"
"v_pk_mul_f32 v[66:67], v[66:67], v[46:47]\n"
"v_pk_mul_f32 v[68:69], v[68:69], v[46:47]\n"
"v_pk_mul_f32 v[70:71], v[70:71], v[46:47]\n"
"v_cvt_pk_bf16_f32 v72, v64, v65\n"
"v_cvt_pk_bf16_f32 v73, v66, v67\n"
"v_cvt_pk_bf16_f32 v74, v68, v69\n"
"v_cvt_pk_bf16_f32 v75, v70, v71\n"
"global_store_dwordx4 v32, v[72:75], %0 offset:3072 sc1\n"
"v_accvgpr_read_b32 v76, a104\n"
"v_accvgpr_read_b32 v77, a105\n"
"v_accvgpr_read_b32 v78, a106\n"
"v_accvgpr_read_b32 v79, a107\n"
"v_accvgpr_read_b32 v80, a108\n"
"v_accvgpr_read_b32 v81, a109\n"
"v_accvgpr_read_b32 v82, a110\n"
"v_accvgpr_read_b32 v83, a111\n"
"v_pk_mul_f32 v[76:77], v[76:77], v[46:47]\n"
"v_pk_mul_f32 v[78:79], v[78:79], v[46:47]\n"
"v_pk_mul_f32 v[80:81], v[80:81], v[46:47]\n"
"v_pk_mul_f32 v[82:83], v[82:83], v[46:47]\n"
"v_cvt_pk_bf16_f32 v108, v76, v77\n"
"v_cvt_pk_bf16_f32 v109, v78, v79\n"
"v_cvt_pk_bf16_f32 v110, v80, v81\n"
"v_cvt_pk_bf16_f32 v111, v82, v83\n"
"v_accvgpr_read_b32 v64, a112\n"
"v_accvgpr_read_b32 v65, a113\n"
"v_accvgpr_read_b32 v66, a114\n"
"v_accvgpr_read_b32 v67, a115\n"
"v_accvgpr_read_b32 v68, a116\n"
"v_accvgpr_read_b32 v69, a117\n"
"v_accvgpr_read_b32 v70, a118\n"
"v_accvgpr_read_b32 v71, a119\n"
"v_pk_mul_f32 v[64:65], v[64:65], v[46:47]\n"
"v_pk_mul_f32 v[66:67], v[66:67], v[46:47]\n"
"v_pk_mul_f32 v[68:69], v[68:69], v[46:47]\n"
"v_pk_mul_f32 v[70:71], v[70:71], v[46:47]\n"
"v_cvt_pk_bf16_f32 v72, v64, v65\n"
"v_cvt_pk_bf16_f32 v73, v66, v67\n"
"v_cvt_pk_bf16_f32 v74, v68, v69\n"
"v_cvt_pk_bf16_f32 v75, v70, v71\n"
"ds_write_b128 v8, v[72:75] offset:3072\n"
"v_accvgpr_read_b32 v76, a120\n"
"v_accvgpr_read_b32 v77, a121\n"
"v_accvgpr_read_b32 v78, a122\n"
"v_accvgpr_read_b32 v79, a123\n"
"v_accvgpr_read_b32 v80, a124\n"
"v_accvgpr_read_b32 v81, a125\n"
"v_accvgpr_read_b32 v82, a126\n"
"v_accvgpr_read_b32 v83, a127\n"
"v_pk_mul_f32 v[76:77], v[76:77], v[46:47]\n"
"v_pk_mul_f32 v[78:79], v[78:79], v[46:47]\n"
"v_pk_mul_f32 v[80:81], v[80:81], v[46:47]\n"
"v_pk_mul_f32 v[82:83], v[82:83], v[46:47]\n"
"v_cvt_pk_bf16_f32 v84, v76, v77\n"
"v_cvt_pk_bf16_f32 v85, v78, v79\n"
"v_cvt_pk_bf16_f32 v86, v80, v81\n"
"v_cvt_pk_bf16_f32 v87, v82, v83\n"
"ds_write_b128 v8, v[84:87] offset:7168\n"
"s_lshr_b32 s61, %17, 5\n"
"s_lshl_b32 s61, s61, 8\n"
"s_add_u32 s61, s61, 42008576\n"
"v_mov_b32 v36, s61\n"
"v_mov_b32 v37, 1\n"
"v_lshrrev_b32 v5, 2, v1\n"
"v_and_b32 v6, 7, v5\n"
"v_and_b32 v7, 3, v1\n"
"v_lshlrev_b32 v7, 3, v7\n"
"v_lshl_add_u32 v7, v5, 8, v7\n"
"s_lshl_b32 s61, s58, 1\n"
"s_cmp_lg_u32 s58, 0\n"
"s_cselect_b64 vcc, -1, 0\n"
"v_xor_b32 v5, 0, v6\n"
"v_lshl_add_u32 v40, v5, 5, v7\n"
"v_add_u32 v12, 0x10000, v40\n"
"v_xor_b32 v5, 1, v6\n"
"v_lshl_add_u32 v41, v5, 5, v7\n"
"v_add_u32 v13, 0x10000, v41\n"
"v_xor_b32 v5, 2, v6\n"
"v_lshl_add_u32 v42, v5, 5, v7\n"
"v_add_u32 v14, 0x10000, v42\n"
"v_xor_b32 v5, 3, v6\n"
"v_lshl_add_u32 v43, v5, 5, v7\n"
"v_add_u32 v15, 0x10000, v43\n"
"v_xor_b32 v5, 4, v6\n"
"v_lshl_add_u32 v44, v5, 5, v7\n"
"v_add_u32 v16, 0x10000, v44\n"
"v_xor_b32 v5, 5, v6\n"
"v_lshl_add_u32 v45, v5, 5, v7\n"
"v_add_u32 v17, 0x10000, v45\n"
"s_sub_u32 s98, 6, s61\n"
"v_xor_b32 v5, s98, v6\n"
"v_lshl_add_u32 v46, v5, 5, v7\n"
"v_add_u32 v18, 0x10000, v46\n"
"s_sub_u32 s98, 7, s61\n"
"v_xor_b32 v5, s98, v6\n"
"v_lshl_add_u32 v47, v5, 5, v7\n"
"v_add_u32 v19, 0x10000, v47\n"
"s_waitcnt vmcnt(4)\n"
"s_barrier\n"
"ds_read_b64_tr_b16 v[64:65], v40 offset:0\n"
"ds_read_b64_tr_b16 v[66:67], v40 offset:4096\n"
"ds_read_b64_tr_b16 v[68:69], v40 offset:8192\n"
"ds_read_b64_tr_b16 v[70:71], v40 offset:12288\n"
"ds_read_b64_tr_b16 v[72:73], v12 offset:0\n"
"ds_read_b64_tr_b16 v[74:75], v12 offset:4096\n"
"ds_read_b64_tr_b16 v[76:77], v12 offset:8192\n"
"ds_read_b64_tr_b16 v[78:79], v12 offset:12288\n"
"s_waitcnt lgkmcnt(4)\n"
"v_mfma_f32_16x16x32_bf16 v[128:131], v[64:67], a[128:131], 0\n"
"ds_read_b64_tr_b16 v[80:81], v41 offset:0\n"
"v_mfma_f32_16x16x32_bf16 v[128:131], v[68:71], a[132:135], v[128:131]\n"
"buffer_load_dwordx4 a[160:163], v39, s[36:39], s56 offen offset:0 sc1\n"
"v_mfma_f32_16x16x32_bf16 v[160:163], v[64:67], a[136:139], 0\n"
"ds_read_b64_tr_b16 v[82:83], v41 offset:4096\n"
"v_mfma_f32_16x16x32_bf16 v[160:163], v[68:71], a[140:143], v[160:163]\n"
"buffer_load_dwordx4 a[168:171], v39, s[36:39], s56 offen offset:1024 sc1\n"
"v_mfma_f32_16x16x32_bf16 v[192:195], v[64:67], a[144:147], 0\n"
"ds_read_b64_tr_b16 v[84:85], v41 offset:8192\n"
"v_mfma_f32_16x16x32_bf16 v[192:195], v[68:71], a[148:151], v[192:195]\n"
"v_mfma_f32_16x16x32_bf16 v[224:227], v[64:67], a[152:155], 0\n"
"ds_read_b64_tr_b16 v[86:87], v41 offset:12288\n"
"v_mfma_f32_16x16x32_bf16 v[224:227], v[68:71], a[156:159], v[224:227]\n"
"buffer_load_dwordx4 a[176:179], v39, s[36:39], s56 offen offset:2048 sc1\n"
"s_waitcnt lgkmcnt(4)\n"
"v_mfma_f32_16x16x32_bf16 a[0:3], v[72:75], a[128:131], 0\n"
"ds_read_b64_tr_b16 v[88:89], v13 offset:0\n"
"v_mfma_f32_16x16x32_bf16 a[0:3], v[76:79], a[132:135], a[0:3]\n"
"v_mfma_f32_16x16x32_bf16 a[32:35], v[72:75], a[136:139], 0\n"
"ds_read_b64_tr_b16 v[90:91], v13 offset:4096\n"
"v_mfma_f32_16x16x32_bf16 a[32:35], v[76:79], a[140:143], a[32:35]\n"
"buffer_load_dwordx4 a[184:187], v39, s[36:39], s56 offen offset:3072 sc1\n"
"s_add_u32 s56, s56, 16384\n"
"v_mfma_f32_16x16x32_bf16 a[64:67], v[72:75], a[144:147], 0\n"
"ds_read_b64_tr_b16 v[92:93], v13 offset:8192\n"
"v_mfma_f32_16x16x32_bf16 a[64:67], v[76:79], a[148:151], a[64:67]\n"
"v_mfma_f32_16x16x32_bf16 a[96:99], v[72:75], a[152:155], 0\n"
"ds_read_b64_tr_b16 v[94:95], v13 offset:12288\n"
"v_mfma_f32_16x16x32_bf16 a[96:99], v[76:79], a[156:159], a[96:99]\n"
"buffer_load_dwordx4 a[164:167], v39, s[36:39], s57 offen offset:0 sc1\n"
"s_waitcnt lgkmcnt(4)\n"
"v_mfma_f32_16x16x32_bf16 v[132:135], v[80:83], a[128:131], 0\n"
"ds_read_b64_tr_b16 v[64:65], v42 offset:0\n"
"v_mfma_f32_16x16x32_bf16 v[132:135], v[84:87], a[132:135], v[132:135]\n"
"buffer_load_dwordx4 a[172:175], v39, s[36:39], s57 offen offset:1024 sc1\n"
"v_mfma_f32_16x16x32_bf16 v[164:167], v[80:83], a[136:139], 0\n"
"ds_read_b64_tr_b16 v[66:67], v42 offset:4096\n"
"v_mfma_f32_16x16x32_bf16 v[164:167], v[84:87], a[140:143], v[164:167]\n"
"v_mfma_f32_16x16x32_bf16 v[196:199], v[80:83], a[144:147], 0\n"
"ds_read_b64_tr_b16 v[68:69], v42 offset:8192\n"
"v_mfma_f32_16x16x32_bf16 v[196:199], v[84:87], a[148:151], v[196:199]\n"
"buffer_load_dwordx4 a[180:183], v39, s[36:39], s57 offen offset:2048 sc1\n"
"v_mfma_f32_16x16x32_bf16 v[228:231], v[80:83], a[152:155], 0\n"
"ds_read_b64_tr_b16 v[70:71], v42 offset:12288\n"
"v_mfma_f32_16x16x32_bf16 v[228:231], v[84:87], a[156:159], v[228:231]\n"
"s_waitcnt lgkmcnt(4)\n"
"v_mfma_f32_16x16x32_bf16 a[4:7], v[88:91], a[128:131], 0\n"
"ds_read_b64_tr_b16 v[72:73], v14 offset:0\n"
"v_mfma_f32_16x16x32_bf16 a[4:7], v[92:95], a[132:135], a[4:7]\n"
"buffer_load_dwordx4 a[188:191], v39, s[36:39], s57 offen offset:3072 sc1\n"
"s_add_u32 s57, s57, 16384\n"
"v_mfma_f32_16x16x32_bf16 a[36:39], v[88:91], a[136:139], 0\n"
"ds_read_b64_tr_b16 v[74:75], v14 offset:4096\n"
"v_mfma_f32_16x16x32_bf16 a[36:39], v[92:95], a[140:143], a[36:39]\n"
"v_mfma_f32_16x16x32_bf16 a[68:71], v[88:91], a[144:147], 0\n"
"ds_read_b64_tr_b16 v[76:77], v14 offset:8192\n"
"v_mfma_f32_16x16x32_bf16 a[68:71], v[92:95], a[148:151], a[68:71]\n"
"buffer_load_dwordx4 a[192:195], v39, s[36:39], s56 offen offset:0 sc1\n"
"v_mfma_f32_16x16x32_bf16 a[100:103], v[88:91], a[152:155], 0\n"
"ds_read_b64_tr_b16 v[78:79], v14 offset:12288\n"
"v_mfma_f32_16x16x32_bf16 a[100:103], v[92:95], a[156:159], a[100:103]\n"
"buffer_load_dwordx4 a[200:203], v39, s[36:39], s56 offen offset:1024 sc1\n"
"s_waitcnt lgkmcnt(4)\n"
"v_mfma_f32_16x16x32_bf16 v[136:139], v[64:67], a[128:131], 0\n"
"ds_read_b64_tr_b16 v[80:81], v43 offset:0\n"
"v_mfma_f32_16x16x32_bf16 v[136:139], v[68:71], a[132:135], v[136:139]\n"
"v_mfma_f32_16x16x32_bf16 v[168:171], v[64:67], a[136:139], 0\n"
"ds_read_b64_tr_b16 v[82:83], v43 offset:4096\n"
"v_mfma_f32_16x16x32_bf16 v[168:171], v[68:71], a[140:143], v[168:171]\n"
"buffer_load_dwordx4 a[208:211], v39, s[36:39], s56 offen offset:2048 sc1\n"
"v_mfma_f32_16x16x32_bf16 v[200:203], v[64:67], a[144:147], 0\n"
"ds_read_b64_tr_b16 v[84:85], v43 offset:8192\n"
"v_mfma_f32_16x16x32_bf16 v[200:203], v[68:71], a[148:151], v[200:203]\n"
"v_mfma_f32_16x16x32_bf16 v[232:235], v[64:67], a[152:155], 0\n"
"ds_read_b64_tr_b16 v[86:87], v43 offset:12288\n"
"v_mfma_f32_16x16x32_bf16 v[232:235], v[68:71], a[156:159], v[232:235]\n"
"buffer_load_dwordx4 a[216:219], v39, s[36:39], s56 offen offset:3072 sc1\n"
"s_add_u32 s56, s56, 16384\n"
"s_waitcnt lgkmcnt(4)\n"
"v_mfma_f32_16x16x32_bf16 a[8:11], v[72:75], a[128:131], 0\n"
"ds_read_b64_tr_b16 v[88:89], v15 offset:0\n"
"v_mfma_f32_16x16x32_bf16 a[8:11], v[76:79], a[132:135], a[8:11]\n"
"v_mfma_f32_16x16x32_bf16 a[40:43], v[72:75], a[136:139], 0\n"
"ds_read_b64_tr_b16 v[90:91], v15 offset:4096\n"
"v_mfma_f32_16x16x32_bf16 a[40:43], v[76:79], a[140:143], a[40:43]\n"
"buffer_load_dwordx4 a[196:199], v39, s[36:39], s57 offen offset:0 sc1\n"
"v_mfma_f32_16x16x32_bf16 a[72:75], v[72:75], a[144:147], 0\n"
"ds_read_b64_tr_b16 v[92:93], v15 offset:8192\n"
"v_mfma_f32_16x16x32_bf16 a[72:75], v[76:79], a[148:151], a[72:75]\n"
"buffer_load_dwordx4 a[204:207], v39, s[36:39], s57 offen offset:1024 sc1\n"
"v_mfma_f32_16x16x32_bf16 a[104:107], v[72:75], a[152:155], 0\n"
"ds_read_b64_tr_b16 v[94:95], v15 offset:12288\n"
"v_mfma_f32_16x16x32_bf16 a[104:107], v[76:79], a[156:159], a[104:107]\n"
"s_waitcnt lgkmcnt(4)\n"
"v_mfma_f32_16x16x32_bf16 v[140:143], v[80:83], a[128:131], 0\n"
"ds_read_b64_tr_b16 v[64:65], v44 offset:0\n"
"v_mfma_f32_16x16x32_bf16 v[140:143], v[84:87], a[132:135], v[140:143]\n"
"buffer_load_dwordx4 a[212:215], v39, s[36:39], s57 offen offset:2048 sc1\n"
"v_mfma_f32_16x16x32_bf16 v[172:175], v[80:83], a[136:139], 0\n"
"ds_read_b64_tr_b16 v[66:67], v44 offset:4096\n"
"v_mfma_f32_16x16x32_bf16 v[172:175], v[84:87], a[140:143], v[172:175]\n"
"v_mfma_f32_16x16x32_bf16 v[204:207], v[80:83], a[144:147], 0\n"
"ds_read_b64_tr_b16 v[68:69], v44 offset:8192\n"
"v_mfma_f32_16x16x32_bf16 v[204:207], v[84:87], a[148:151], v[204:207]\n"
"buffer_load_dwordx4 a[220:223], v39, s[36:39], s57 offen offset:3072 sc1\n"
"s_add_u32 s57, s57, 16384\n"
"v_mfma_f32_16x16x32_bf16 v[236:239], v[80:83], a[152:155], 0\n"
"ds_read_b64_tr_b16 v[70:71], v44 offset:12288\n"
"v_mfma_f32_16x16x32_bf16 v[236:239], v[84:87], a[156:159], v[236:239]\n"
"s_waitcnt lgkmcnt(4)\n"
"v_mfma_f32_16x16x32_bf16 a[12:15], v[88:91], a[128:131], 0\n"
"ds_read_b64_tr_b16 v[72:73], v16 offset:0\n"
"v_mfma_f32_16x16x32_bf16 a[12:15], v[92:95], a[132:135], a[12:15]\n"
"buffer_load_dwordx4 a[224:227], v39, s[36:39], s56 offen offset:0 sc1\n"
"v_mfma_f32_16x16x32_bf16 a[44:47], v[88:91], a[136:139], 0\n"
"ds_read_b64_tr_b16 v[74:75], v16 offset:4096\n"
"v_mfma_f32_16x16x32_bf16 a[44:47], v[92:95], a[140:143], a[44:47]\n"
"buffer_load_dwordx4 a[232:235], v39, s[36:39], s56 offen offset:1024 sc1\n"
"v_mfma_f32_16x16x32_bf16 a[76:79], v[88:91], a[144:147], 0\n"
"ds_read_b64_tr_b16 v[76:77], v16 offset:8192\n"
"v_mfma_f32_16x16x32_bf16 a[76:79], v[92:95], a[148:151], a[76:79]\n"
"v_mfma_f32_16x16x32_bf16 a[108:111], v[88:91], a[152:155], 0\n"
"ds_read_b64_tr_b16 v[78:79], v16 offset:12288\n"
"v_mfma_f32_16x16x32_bf16 a[108:111], v[92:95], a[156:159], a[108:111]\n"
"s_cbranch_vccnz SKP0\n"
"s_waitcnt lgkmcnt(4)\n"
"v_mfma_f32_16x16x32_bf16 v[144:147], v[64:67], a[128:131], 0\n"
"ds_read_b64_tr_b16 v[80:81], v45 offset:0\n"
"v_mfma_f32_16x16x32_bf16 v[144:147], v[68:71], a[132:135], v[144:147]\n"
"v_mfma_f32_16x16x32_bf16 v[176:179], v[64:67], a[136:139], 0\n"
"ds_read_b64_tr_b16 v[82:83], v45 offset:4096\n"
"v_mfma_f32_16x16x32_bf16 v[176:179], v[68:71], a[140:143], v[176:179]\n"
"v_mfma_f32_16x16x32_bf16 v[208:211], v[64:67], a[144:147], 0\n"
"ds_read_b64_tr_b16 v[84:85], v45 offset:8192\n"
"v_mfma_f32_16x16x32_bf16 v[208:211], v[68:71], a[148:151], v[208:211]\n"
"v_mfma_f32_16x16x32_bf16 v[240:243], v[64:67], a[152:155], 0\n"
"ds_read_b64_tr_b16 v[86:87], v45 offset:12288\n"
"v_mfma_f32_16x16x32_bf16 v[240:243], v[68:71], a[156:159], v[240:243]\n"
"s_waitcnt lgkmcnt(4)\n"
"v_mfma_f32_16x16x32_bf16 a[16:19], v[72:75], a[128:131], 0\n"
"ds_read_b64_tr_b16 v[88:89], v17 offset:0\n"
"v_mfma_f32_16x16x32_bf16 a[16:19], v[76:79], a[132:135], a[16:19]\n"
"v_mfma_f32_16x16x32_bf16 a[48:51], v[72:75], a[136:139], 0\n"
"ds_read_b64_tr_b16 v[90:91], v17 offset:4096\n"
"v_mfma_f32_16x16x32_bf16 a[48:51], v[76:79], a[140:143], a[48:51]\n"
"v_mfma_f32_16x16x32_bf16 a[80:83], v[72:75], a[144:147], 0\n"
"ds_read_b64_tr_b16 v[92:93], v17 offset:8192\n"
"v_mfma_f32_16x16x32_bf16 a[80:83], v[76:79], a[148:151], a[80:83]\n"
"v_mfma_f32_16x16x32_bf16 a[112:115], v[72:75], a[152:155], 0\n"
"ds_read_b64_tr_b16 v[94:95], v17 offset:12288\n"
"v_mfma_f32_16x16x32_bf16 a[112:115], v[76:79], a[156:159], a[112:115]\n"
"s_waitcnt lgkmcnt(4)\n"
"v_mfma_f32_16x16x32_bf16 v[148:151], v[80:83], a[128:131], 0\n"
"ds_read_b64_tr_b16 v[64:65], v46 offset:0\n"
"v_mfma_f32_16x16x32_bf16 v[148:151], v[84:87], a[132:135], v[148:151]\n"
"v_mfma_f32_16x16x32_bf16 v[180:183], v[80:83], a[136:139], 0\n"
"ds_read_b64_tr_b16 v[66:67], v46 offset:4096\n"
"v_mfma_f32_16x16x32_bf16 v[180:183], v[84:87], a[140:143], v[180:183]\n"
"v_mfma_f32_16x16x32_bf16 v[212:215], v[80:83], a[144:147], 0\n"
"ds_read_b64_tr_b16 v[68:69], v46 offset:8192\n"
"v_mfma_f32_16x16x32_bf16 v[212:215], v[84:87], a[148:151], v[212:215]\n"
"v_mfma_f32_16x16x32_bf16 v[244:247], v[80:83], a[152:155], 0\n"
"ds_read_b64_tr_b16 v[70:71], v46 offset:12288\n"
"v_mfma_f32_16x16x32_bf16 v[244:247], v[84:87], a[156:159], v[244:247]\n"
"s_waitcnt lgkmcnt(4)\n"
"v_mfma_f32_16x16x32_bf16 a[20:23], v[88:91], a[128:131], 0\n"
"ds_read_b64_tr_b16 v[72:73], v18 offset:0\n"
"v_mfma_f32_16x16x32_bf16 a[20:23], v[92:95], a[132:135], a[20:23]\n"
"v_mfma_f32_16x16x32_bf16 a[52:55], v[88:91], a[136:139], 0\n"
"ds_read_b64_tr_b16 v[74:75], v18 offset:4096\n"
"v_mfma_f32_16x16x32_bf16 a[52:55], v[92:95], a[140:143], a[52:55]\n"
"v_mfma_f32_16x16x32_bf16 a[84:87], v[88:91], a[144:147], 0\n"
"ds_read_b64_tr_b16 v[76:77], v18 offset:8192\n"
"v_mfma_f32_16x16x32_bf16 a[84:87], v[92:95], a[148:151], a[84:87]\n"
"v_mfma_f32_16x16x32_bf16 a[116:119], v[88:91], a[152:155], 0\n"
"ds_read_b64_tr_b16 v[78:79], v18 offset:12288\n"
"v_mfma_f32_16x16x32_bf16 a[116:119], v[92:95], a[156:159], a[116:119]\n"
"SKP0:\n"
"s_waitcnt lgkmcnt(4)\n"
"v_mfma_f32_16x16x32_bf16 v[152:155], v[64:67], a[128:131], 0\n"
"ds_read_b64_tr_b16 v[80:81], v47 offset:0\n"
"v_mfma_f32_16x16x32_bf16 v[152:155], v[68:71], a[132:135], v[152:155]\n"
"buffer_load_dwordx4 a[240:243], v39, s[36:39], s56 offen offset:2048 sc1\n"
"v_mfma_f32_16x16x32_bf16 v[184:187], v[64:67], a[136:139], 0\n"
"ds_read_b64_tr_b16 v[82:83], v47 offset:4096\n"
"v_mfma_f32_16x16x32_bf16 v[184:187], v[68:71], a[140:143], v[184:187]\n"
"v_mfma_f32_16x16x32_bf16 v[216:219], v[64:67], a[144:147], 0\n"
"ds_read_b64_tr_b16 v[84:85], v47 offset:8192\n"
"v_mfma_f32_16x16x32_bf16 v[216:219], v[68:71], a[148:151], v[216:219]\n"
"buffer_load_dwordx4 a[248:251], v39, s[36:39], s56 offen offset:3072 sc1\n"
"s_add_u32 s56, s56, 16384\n"
"v_mfma_f32_16x16x32_bf16 v[248:251], v[64:67], a[152:155], 0\n"
"ds_read_b64_tr_b16 v[86:87], v47 offset:12288\n"
"v_mfma_f32_16x16x32_bf16 v[248:251], v[68:71], a[156:159], v[248:251]\n"
"s_waitcnt lgkmcnt(4)\n"
"v_mfma_f32_16x16x32_bf16 a[24:27], v[72:75], a[128:131], 0\n"
"ds_read_b64_tr_b16 v[88:89], v19 offset:0\n"
"v_mfma_f32_16x16x32_bf16 a[24:27], v[76:79], a[132:135], a[24:27]\n"
"buffer_load_dwordx4 a[228:231], v39, s[36:39], s57 offen offset:0 sc1\n"
"v_mfma_f32_16x16x32_bf16 a[56:59], v[72:75], a[136:139], 0\n"
"ds_read_b64_tr_b16 v[90:91], v19 offset:4096\n"
"v_mfma_f32_16x16x32_bf16 a[56:59], v[76:79], a[140:143], a[56:59]\n"
"buffer_load_dwordx4 a[236:239], v39, s[36:39], s57 offen offset:1024 sc1\n"
"v_mfma_f32_16x16x32_bf16 a[88:91], v[72:75], a[144:147], 0\n"
"ds_read_b64_tr_b16 v[92:93], v19 offset:8192\n"
"v_mfma_f32_16x16x32_bf16 a[88:91], v[76:79], a[148:151], a[88:91]\n"
"v_mfma_f32_16x16x32_bf16 a[120:123], v[72:75], a[152:155], 0\n"
"ds_read_b64_tr_b16 v[94:95], v19 offset:12288\n"
"v_mfma_f32_16x16x32_bf16 a[120:123], v[76:79], a[156:159], a[120:123]\n"
"s_waitcnt vmcnt(14)\n"
"s_barrier\n"
"s_waitcnt lgkmcnt(4)\n"
"v_mfma_f32_16x16x32_bf16 v[156:159], v[80:83], a[128:131], 0\n"
"ds_read_b64_tr_b16 v[64:65], v40 offset:16384\n"
"v_mfma_f32_16x16x32_bf16 v[156:159], v[84:87], a[132:135], v[156:159]\n"
"v_mfma_f32_16x16x32_bf16 v[188:191], v[80:83], a[136:139], 0\n"
"ds_read_b64_tr_b16 v[66:67], v40 offset:20480\n"
"v_mfma_f32_16x16x32_bf16 v[188:191], v[84:87], a[140:143], v[188:191]\n"
"buffer_load_dwordx4 a[244:247], v39, s[36:39], s57 offen offset:2048 sc1\n"
"v_mfma_f32_16x16x32_bf16 v[220:223], v[80:83], a[144:147], 0\n"
"ds_read_b64_tr_b16 v[68:69], v40 offset:24576\n"
"v_mfma_f32_16x16x32_bf16 v[220:223], v[84:87], a[148:151], v[220:223]\n"
"v_mfma_f32_16x16x32_bf16 v[252:255], v[80:83], a[152:155], 0\n"
"ds_read_b64_tr_b16 v[70:71], v40 offset:28672\n"
"v_mfma_f32_16x16x32_bf16 v[252:255], v[84:87], a[156:159], v[252:255]\n"
"buffer_load_dwordx4 a[252:255], v39, s[36:39], s57 offen offset:3072 sc1\n"
"s_add_u32 s57, s57, 16384\n"
"s_waitcnt lgkmcnt(4)\n"
"v_mfma_f32_16x16x32_bf16 a[28:31], v[88:91], a[128:131], 0\n"
"ds_read_b64_tr_b16 v[72:73], v12 offset:16384\n"
"v_mfma_f32_16x16x32_bf16 a[28:31], v[92:95], a[132:135], a[28:31]\n"
"v_mfma_f32_16x16x32_bf16 a[60:63], v[88:91], a[136:139], 0\n"
"ds_read_b64_tr_b16 v[74:75], v12 offset:20480\n"
"v_mfma_f32_16x16x32_bf16 a[60:63], v[92:95], a[140:143], a[60:63]\n"
"global_store_dwordx4 v33, v[96:99], %0 offset:0 sc1\n"
"v_mfma_f32_16x16x32_bf16 a[92:95], v[88:91], a[144:147], 0\n"
"ds_read_b64_tr_b16 v[76:77], v12 offset:24576\n"
"v_mfma_f32_16x16x32_bf16 a[92:95], v[92:95], a[148:151], a[92:95]\n"
"v_mfma_f32_16x16x32_bf16 a[124:127], v[88:91], a[152:155], 0\n"
"ds_read_b64_tr_b16 v[78:79], v12 offset:28672\n"
"v_mfma_f32_16x16x32_bf16 a[124:127], v[92:95], a[156:159], a[124:127]\n"
"s_waitcnt lgkmcnt(4)\n"
"v_mfma_f32_16x16x32_bf16 v[128:131], v[64:67], a[160:163], v[128:131]\n"
"ds_read_b64_tr_b16 v[80:81], v41 offset:16384\n"
"v_mfma_f32_16x16x32_bf16 v[128:131], v[68:71], a[164:167], v[128:131]\n"
"buffer_load_dwordx4 a[128:131], v39, s[36:39], s56 offen offset:0 sc1\n"
"v_mfma_f32_16x16x32_bf16 v[160:163], v[64:67], a[168:171], v[160:163]\n"
"ds_read_b64_tr_b16 v[82:83], v41 offset:20480\n"
"v_mfma_f32_16x16x32_bf16 v[160:163], v[68:71], a[172:175], v[160:163]\n"
"v_mfma_f32_16x16x32_bf16 v[192:195], v[64:67], a[176:179], v[192:195]\n"
"ds_read_b64_tr_b16 v[84:85], v41 offset:24576\n"
"v_mfma_f32_16x16x32_bf16 v[192:195], v[68:71], a[180:183], v[192:195]\n"
"v_mfma_f32_16x16x32_bf16 v[224:227], v[64:67], a[184:187], v[224:227]\n"
"ds_read_b64_tr_b16 v[86:87], v41 offset:28672\n"
"v_mfma_f32_16x16x32_bf16 v[224:227], v[68:71], a[188:191], v[224:227]\n"
"s_waitcnt lgkmcnt(4)\n"
"v_mfma_f32_16x16x32_bf16 a[0:3], v[72:75], a[160:163], a[0:3]\n"
"ds_read_b64_tr_b16 v[88:89], v13 offset:16384\n"
"v_mfma_f32_16x16x32_bf16 a[0:3], v[76:79], a[164:167], a[0:3]\n"
"buffer_load_dwordx4 a[136:139], v39, s[36:39], s56 offen offset:1024 sc1\n"
"v_mfma_f32_16x16x32_bf16 a[32:35], v[72:75], a[168:171], a[32:35]\n"
"ds_read_b64_tr_b16 v[90:91], v13 offset:20480\n"
"v_mfma_f32_16x16x32_bf16 a[32:35], v[76:79], a[172:175], a[32:35]\n"
"v_mfma_f32_16x16x32_bf16 a[64:67], v[72:75], a[176:179], a[64:67]\n"
"ds_read_b64_tr_b16 v[92:93], v13 offset:24576\n"
"v_mfma_f32_16x16x32_bf16 a[64:67], v[76:79], a[180:183], a[64:67]\n"
"v_mfma_f32_16x16x32_bf16 a[96:99], v[72:75], a[184:187], a[96:99]\n"
"ds_read_b64_tr_b16 v[94:95], v13 offset:28672\n"
"v_mfma_f32_16x16x32_bf16 a[96:99], v[76:79], a[188:191], a[96:99]\n"
"s_waitcnt lgkmcnt(4)\n"
"v_mfma_f32_16x16x32_bf16 v[132:135], v[80:83], a[160:163], v[132:135]\n"
"ds_read_b64_tr_b16 v[64:65], v42 offset:16384\n"
"v_mfma_f32_16x16x32_bf16 v[132:135], v[84:87], a[164:167], v[132:135]\n"
"v_mfma_f32_16x16x32_bf16 v[164:167], v[80:83], a[168:171], v[164:167]\n"
"ds_read_b64_tr_b16 v[66:67], v42 offset:20480\n"
"v_mfma_f32_16x16x32_bf16 v[164:167], v[84:87], a[172:175], v[164:167]\n"
"buffer_load_dwordx4 a[144:147], v39, s[36:39], s56 offen offset:2048 sc1\n"
"v_mfma_f32_16x16x32_bf16 v[196:199], v[80:83], a[176:179], v[196:199]\n"
"ds_read_b64_tr_b16 v[68:69], v42 offset:24576\n"
"v_mfma_f32_16x16x32_bf16 v[196:199], v[84:87], a[180:183], v[196:199]\n"
"v_mfma_f32_16x16x32_bf16 v[228:231], v[80:83], a[184:187], v[228:231]\n"
"ds_read_b64_tr_b16 v[70:71], v42 offset:28672\n"
"v_mfma_f32_16x16x32_bf16 v[228:231], v[84:87], a[188:191], v[228:231]\n"
"s_waitcnt lgkmcnt(4)\n"
"v_mfma_f32_16x16x32_bf16 a[4:7], v[88:91], a[160:163], a[4:7]\n"
"ds_read_b64_tr_b16 v[72:73], v14 offset:16384\n"
"v_mfma_f32_16x16x32_bf16 a[4:7], v[92:95], a[164:167], a[4:7]\n"
"v_mfma_f32_16x16x32_bf16 a[36:39], v[88:91], a[168:171], a[36:39]\n"
"ds_read_b64_tr_b16 v[74:75], v14 offset:20480\n"
"v_mfma_f32_16x16x32_bf16 a[36:39], v[92:95], a[172:175], a[36:39]\n"
"v_mfma_f32_16x16x32_bf16 a[68:71], v[88:91], a[176:179], a[68:71]\n"
"ds_read_b64_tr_b16 v[76:77], v14 offset:24576\n"
"v_mfma_f32_16x16x32_bf16 a[68:71], v[92:95], a[180:183], a[68:71]\n"
"buffer_load_dwordx4 a[152:155], v39, s[36:39], s56 offen offset:3072 sc1\n"
"s_add_u32 s56, s56, 16384\n"
"v_mfma_f32_16x16x32_bf16 a[100:103], v[88:91], a[184:187], a[100:103]\n"
"ds_read_b64_tr_b16 v[78:79], v14 offset:28672\n"
"v_mfma_f32_16x16x32_bf16 a[100:103], v[92:95], a[188:191], a[100:103]\n"
"s_waitcnt lgkmcnt(4)\n"
"v_mfma_f32_16x16x32_bf16 v[136:139], v[64:67], a[160:163], v[136:139]\n"
"ds_read_b64_tr_b16 v[80:81], v43 offset:16384\n"
"v_mfma_f32_16x16x32_bf16 v[136:139], v[68:71], a[164:167], v[136:139]\n"
"v_mfma_f32_16x16x32_bf16 v[168:171], v[64:67], a[168:171], v[168:171]\n"
"ds_read_b64_tr_b16 v[82:83], v43 offset:20480\n"
"v_mfma_f32_16x16x32_bf16 v[168:171], v[68:71], a[172:175], v[168:171]\n"
"v_mfma_f32_16x16x32_bf16 v[200:203], v[64:67], a[176:179], v[200:203]\n"
"ds_read_b64_tr_b16 v[84:85], v43 offset:24576\n"
"v_mfma_f32_16x16x32_bf16 v[200:203], v[68:71], a[180:183], v[200:203]\n"
"v_mfma_f32_16x16x32_bf16 v[232:235], v[64:67], a[184:187], v[232:235]\n"
"ds_read_b64_tr_b16 v[86:87], v43 offset:28672\n"
"v_mfma_f32_16x16x32_bf16 v[232:235], v[68:71], a[188:191], v[232:235]\n"
"buffer_load_dwordx4 a[132:135], v39, s[36:39], s57 offen offset:0 sc1\n"
"s_waitcnt lgkmcnt(4)\n"
"v_mfma_f32_16x16x32_bf16 a[8:11], v[72:75], a[160:163], a[8:11]\n"
"ds_read_b64_tr_b16 v[88:89], v15 offset:16384\n"
"v_mfma_f32_16x16x32_bf16 a[8:11], v[76:79], a[164:167], a[8:11]\n"
"v_mfma_f32_16x16x32_bf16 a[40:43], v[72:75], a[168:171], a[40:43]\n"
"ds_read_b64_tr_b16 v[90:91], v15 offset:20480\n"
"v_mfma_f32_16x16x32_bf16 a[40:43], v[76:79], a[172:175], a[40:43]\n"
"v_mfma_f32_16x16x32_bf16 a[72:75], v[72:75], a[176:179], a[72:75]\n"
"ds_read_b64_tr_b16 v[92:93], v15 offset:24576\n"
"v_mfma_f32_16x16x32_bf16 a[72:75], v[76:79], a[180:183], a[72:75]\n"
"v_mfma_f32_16x16x32_bf16 a[104:107], v[72:75], a[184:187], a[104:107]\n"
"ds_read_b64_tr_b16 v[94:95], v15 offset:28672\n"
"v_mfma_f32_16x16x32_bf16 a[104:107], v[76:79], a[188:191], a[104:107]\n"
"s_waitcnt lgkmcnt(4)\n"
"v_mfma_f32_16x16x32_bf16 v[140:143], v[80:83], a[160:163], v[140:143]\n"
"ds_read_b64_tr_b16 v[64:65], v44 offset:16384\n"
"v_mfma_f32_16x16x32_bf16 v[140:143], v[84:87], a[164:167], v[140:143]\n"
"buffer_load_dwordx4 a[140:143], v39, s[36:39], s57 offen offset:1024 sc1\n"
"v_mfma_f32_16x16x32_bf16 v[172:175], v[80:83], a[168:171], v[172:175]\n"
"ds_read_b64_tr_b16 v[66:67], v44 offset:20480\n"
"v_mfma_f32_16x16x32_bf16 v[172:175], v[84:87], a[172:175], v[172:175]\n"
"v_mfma_f32_16x16x32_bf16 v[204:207], v[80:83], a[176:179], v[204:207]\n"
"ds_read_b64_tr_b16 v[68:69], v44 offset:24576\n"
"v_mfma_f32_16x16x32_bf16 v[204:207], v[84:87], a[180:183], v[204:207]\n"
"v_mfma_f32_16x16x32_bf16 v[236:239], v[80:83], a[184:187], v[236:239]\n"
"ds_read_b64_tr_b16 v[70:71], v44 offset:28672\n"
"v_mfma_f32_16x16x32_bf16 v[236:239], v[84:87], a[188:191], v[236:239]\n"
"s_waitcnt lgkmcnt(4)\n"
"v_mfma_f32_16x16x32_bf16 a[12:15], v[88:91], a[160:163], a[12:15]\n"
"ds_read_b64_tr_b16 v[72:73], v16 offset:16384\n"
"v_mfma_f32_16x16x32_bf16 a[12:15], v[92:95], a[164:167], a[12:15]\n"
"v_mfma_f32_16x16x32_bf16 a[44:47], v[88:91], a[168:171], a[44:47]\n"
"ds_read_b64_tr_b16 v[74:75], v16 offset:20480\n"
"v_mfma_f32_16x16x32_bf16 a[44:47], v[92:95], a[172:175], a[44:47]\n"
"buffer_load_dwordx4 a[148:151], v39, s[36:39], s57 offen offset:2048 sc1\n"
"v_mfma_f32_16x16x32_bf16 a[76:79], v[88:91], a[176:179], a[76:79]\n"
"ds_read_b64_tr_b16 v[76:77], v16 offset:24576\n"
"v_mfma_f32_16x16x32_bf16 a[76:79], v[92:95], a[180:183], a[76:79]\n"
"v_mfma_f32_16x16x32_bf16 a[108:111], v[88:91], a[184:187], a[108:111]\n"
"ds_read_b64_tr_b16 v[78:79], v16 offset:28672\n"
"v_mfma_f32_16x16x32_bf16 a[108:111], v[92:95], a[188:191], a[108:111]\n"
"s_cbranch_vccnz SKP1\n"
"s_waitcnt lgkmcnt(4)\n"
"v_mfma_f32_16x16x32_bf16 v[144:147], v[64:67], a[160:163], v[144:147]\n"
"ds_read_b64_tr_b16 v[80:81], v45 offset:16384\n"
"v_mfma_f32_16x16x32_bf16 v[144:147], v[68:71], a[164:167], v[144:147]\n"
"v_mfma_f32_16x16x32_bf16 v[176:179], v[64:67], a[168:171], v[176:179]\n"
"ds_read_b64_tr_b16 v[82:83], v45 offset:20480\n"
"v_mfma_f32_16x16x32_bf16 v[176:179], v[68:71], a[172:175], v[176:179]\n"
"v_mfma_f32_16x16x32_bf16 v[208:211], v[64:67], a[176:179], v[208:211]\n"
"ds_read_b64_tr_b16 v[84:85], v45 offset:24576\n"
"v_mfma_f32_16x16x32_bf16 v[208:211], v[68:71], a[180:183], v[208:211]\n"
"v_mfma_f32_16x16x32_bf16 v[240:243], v[64:67], a[184:187], v[240:243]\n"
"ds_read_b64_tr_b16 v[86:87], v45 offset:28672\n"
"v_mfma_f32_16x16x32_bf16 v[240:243], v[68:71], a[188:191], v[240:243]\n"
"s_waitcnt lgkmcnt(4)\n"
"v_mfma_f32_16x16x32_bf16 a[16:19], v[72:75], a[160:163], a[16:19]\n"
"ds_read_b64_tr_b16 v[88:89], v17 offset:16384\n"
"v_mfma_f32_16x16x32_bf16 a[16:19], v[76:79], a[164:167], a[16:19]\n"
"v_mfma_f32_16x16x32_bf16 a[48:51], v[72:75], a[168:171], a[48:51]\n"
"ds_read_b64_tr_b16 v[90:91], v17 offset:20480\n"
"v_mfma_f32_16x16x32_bf16 a[48:51], v[76:79], a[172:175], a[48:51]\n"
"v_mfma_f32_16x16x32_bf16 a[80:83], v[72:75], a[176:179], a[80:83]\n"
"ds_read_b64_tr_b16 v[92:93], v17 offset:24576\n"
"v_mfma_f32_16x16x32_bf16 a[80:83], v[76:79], a[180:183], a[80:83]\n"
"v_mfma_f32_16x16x32_bf16 a[112:115], v[72:75], a[184:187], a[112:115]\n"
"ds_read_b64_tr_b16 v[94:95], v17 offset:28672\n"
"v_mfma_f32_16x16x32_bf16 a[112:115], v[76:79], a[188:191], a[112:115]\n"
"s_waitcnt lgkmcnt(4)\n"
"v_mfma_f32_16x16x32_bf16 v[148:151], v[80:83], a[160:163], v[148:151]\n"
"ds_read_b64_tr_b16 v[64:65], v46 offset:16384\n"
"v_mfma_f32_16x16x32_bf16 v[148:151], v[84:87], a[164:167], v[148:151]\n"
"v_mfma_f32_16x16x32_bf16 v[180:183], v[80:83], a[168:171], v[180:183]\n"
"ds_read_b64_tr_b16 v[66:67], v46 offset:20480\n"
"v_mfma_f32_16x16x32_bf16 v[180:183], v[84:87], a[172:175], v[180:183]\n"
"v_mfma_f32_16x16x32_bf16 v[212:215], v[80:83], a[176:179], v[212:215]\n"
"ds_read_b64_tr_b16 v[68:69], v46 offset:24576\n"
"v_mfma_f32_16x16x32_bf16 v[212:215], v[84:87], a[180:183], v[212:215]\n"
"v_mfma_f32_16x16x32_bf16 v[244:247], v[80:83], a[184:187], v[244:247]\n"
"ds_read_b64_tr_b16 v[70:71], v46 offset:28672\n"
"v_mfma_f32_16x16x32_bf16 v[244:247], v[84:87], a[188:191], v[244:247]\n"
"s_waitcnt lgkmcnt(4)\n"
"v_mfma_f32_16x16x32_bf16 a[20:23], v[88:91], a[160:163], a[20:23]\n"
"ds_read_b64_tr_b16 v[72:73], v18 offset:16384\n"
"v_mfma_f32_16x16x32_bf16 a[20:23], v[92:95], a[164:167], a[20:23]\n"
"v_mfma_f32_16x16x32_bf16 a[52:55], v[88:91], a[168:171], a[52:55]\n"
"ds_read_b64_tr_b16 v[74:75], v18 offset:20480\n"
"v_mfma_f32_16x16x32_bf16 a[52:55], v[92:95], a[172:175], a[52:55]\n"
"v_mfma_f32_16x16x32_bf16 a[84:87], v[88:91], a[176:179], a[84:87]\n"
"ds_read_b64_tr_b16 v[76:77], v18 offset:24576\n"
"v_mfma_f32_16x16x32_bf16 a[84:87], v[92:95], a[180:183], a[84:87]\n"
"v_mfma_f32_16x16x32_bf16 a[116:119], v[88:91], a[184:187], a[116:119]\n"
"ds_read_b64_tr_b16 v[78:79], v18 offset:28672\n"
"v_mfma_f32_16x16x32_bf16 a[116:119], v[92:95], a[188:191], a[116:119]\n"
"SKP1:\n"
"s_waitcnt lgkmcnt(4)\n"
"v_mfma_f32_16x16x32_bf16 v[152:155], v[64:67], a[160:163], v[152:155]\n"
"ds_read_b64_tr_b16 v[80:81], v47 offset:16384\n"
"v_mfma_f32_16x16x32_bf16 v[152:155], v[68:71], a[164:167], v[152:155]\n"
"v_mfma_f32_16x16x32_bf16 v[184:187], v[64:67], a[168:171], v[184:187]\n"
"ds_read_b64_tr_b16 v[82:83], v47 offset:20480\n"
"v_mfma_f32_16x16x32_bf16 v[184:187], v[68:71], a[172:175], v[184:187]\n"
"v_mfma_f32_16x16x32_bf16 v[216:219], v[64:67], a[176:179], v[216:219]\n"
"ds_read_b64_tr_b16 v[84:85], v47 offset:24576\n"
"v_mfma_f32_16x16x32_bf16 v[216:219], v[68:71], a[180:183], v[216:219]\n"
"v_mfma_f32_16x16x32_bf16 v[248:251], v[64:67], a[184:187], v[248:251]\n"
"ds_read_b64_tr_b16 v[86:87], v47 offset:28672\n"
"v_mfma_f32_16x16x32_bf16 v[248:251], v[68:71], a[188:191], v[248:251]\n"
"buffer_load_dwordx4 a[156:159], v39, s[36:39], s57 offen offset:3072 sc1\n"
"s_add_u32 s57, s57, 16384\n"
"s_waitcnt lgkmcnt(4)\n"
"v_mfma_f32_16x16x32_bf16 a[24:27], v[72:75], a[160:163], a[24:27]\n"
"ds_read_b64_tr_b16 v[88:89], v19 offset:16384\n"
"v_mfma_f32_16x16x32_bf16 a[24:27], v[76:79], a[164:167], a[24:27]\n"
"v_mfma_f32_16x16x32_bf16 a[56:59], v[72:75], a[168:171], a[56:59]\n"
"ds_read_b64_tr_b16 v[90:91], v19 offset:20480\n"
"v_mfma_f32_16x16x32_bf16 a[56:59], v[76:79], a[172:175], a[56:59]\n"
"v_mfma_f32_16x16x32_bf16 a[88:91], v[72:75], a[176:179], a[88:91]\n"
"ds_read_b64_tr_b16 v[92:93], v19 offset:24576\n"
"v_mfma_f32_16x16x32_bf16 a[88:91], v[76:79], a[180:183], a[88:91]\n"
"v_mfma_f32_16x16x32_bf16 a[120:123], v[72:75], a[184:187], a[120:123]\n"
"ds_read_b64_tr_b16 v[94:95], v19 offset:28672\n"
"v_mfma_f32_16x16x32_bf16 a[120:123], v[76:79], a[188:191], a[120:123]\n"
"s_waitcnt vmcnt(17)\n"
"s_barrier\n"
"s_waitcnt lgkmcnt(4)\n"
"v_mfma_f32_16x16x32_bf16 v[156:159], v[80:83], a[160:163], v[156:159]\n"
"ds_read_b64_tr_b16 v[64:65], v40 offset:32768\n"
"v_mfma_f32_16x16x32_bf16 v[156:159], v[84:87], a[164:167], v[156:159]\n"
"v_mfma_f32_16x16x32_bf16 v[188:191], v[80:83], a[168:171], v[188:191]\n"
"ds_read_b64_tr_b16 v[66:67], v40 offset:36864\n"
"v_mfma_f32_16x16x32_bf16 v[188:191], v[84:87], a[172:175], v[188:191]\n"
"v_mfma_f32_16x16x32_bf16 v[220:223], v[80:83], a[176:179], v[220:223]\n"
"ds_read_b64_tr_b16 v[68:69], v40 offset:40960\n"
"v_mfma_f32_16x16x32_bf16 v[220:223], v[84:87], a[180:183], v[220:223]\n"
"global_store_dwordx4 v33, v[100:103], %0 offset:1024 sc1\n"
"v_mfma_f32_16x16x32_bf16 v[252:255], v[80:83], a[184:187], v[252:255]\n"
"ds_read_b64_tr_b16 v[70:71], v40 offset:45056\n"
"v_mfma_f32_16x16x32_bf16 v[252:255], v[84:87], a[188:191], v[252:255]\n"
"s_waitcnt lgkmcnt(4)\n"
"v_mfma_f32_16x16x32_bf16 a[28:31], v[88:91], a[160:163], a[28:31]\n"
"ds_read_b64_tr_b16 v[72:73], v12 offset:32768\n"
"v_mfma_f32_16x16x32_bf16 a[28:31], v[92:95], a[164:167], a[28:31]\n"
"v_mfma_f32_16x16x32_bf16 a[60:63], v[88:91], a[168:171], a[60:63]\n"
"ds_read_b64_tr_b16 v[74:75], v12 offset:36864\n"
"v_mfma_f32_16x16x32_bf16 a[60:63], v[92:95], a[172:175], a[60:63]\n"
"v_mfma_f32_16x16x32_bf16 a[92:95], v[88:91], a[176:179], a[92:95]\n"
"ds_read_b64_tr_b16 v[76:77], v12 offset:40960\n"
"v_mfma_f32_16x16x32_bf16 a[92:95], v[92:95], a[180:183], a[92:95]\n"
"v_mfma_f32_16x16x32_bf16 a[124:127], v[88:91], a[184:187], a[124:127]\n"
"ds_read_b64_tr_b16 v[78:79], v12 offset:45056\n"
"v_mfma_f32_16x16x32_bf16 a[124:127], v[92:95], a[188:191], a[124:127]\n"
"s_waitcnt lgkmcnt(4)\n"
"v_mfma_f32_16x16x32_bf16 v[128:131], v[64:67], a[192:195], v[128:131]\n"
"ds_read_b64_tr_b16 v[80:81], v41 offset:32768\n"
"s_add_u32 m0, s59, 0\n"
"v_mfma_f32_16x16x32_bf16 v[128:131], v[68:71], a[196:199], v[128:131]\n"
"buffer_load_dwordx4 v20, s[44:47], s62 offen offset:0 lds\n"
"v_mfma_f32_16x16x32_bf16 v[160:163], v[64:67], a[200:203], v[160:163]\n"
"ds_read_b64_tr_b16 v[82:83], v41 offset:36864\n"
"v_mfma_f32_16x16x32_bf16 v[160:163], v[68:71], a[204:207], v[160:163]\n"
"v_mfma_f32_16x16x32_bf16 v[192:195], v[64:67], a[208:211], v[192:195]\n"
"ds_read_b64_tr_b16 v[84:85], v41 offset:40960\n"
"v_mfma_f32_16x16x32_bf16 v[192:195], v[68:71], a[212:215], v[192:195]\n"
"buffer_load_dwordx4 v21, s[44:47], s62 offen offset:1024 lds\n"
"v_mfma_f32_16x16x32_bf16 v[224:227], v[64:67], a[216:219], v[224:227]\n"
"ds_read_b64_tr_b16 v[86:87], v41 offset:45056\n"
"v_mfma_f32_16x16x32_bf16 v[224:227], v[68:71], a[220:223], v[224:227]\n"
"s_waitcnt lgkmcnt(4)\n"
"v_mfma_f32_16x16x32_bf16 a[0:3], v[72:75], a[192:195], a[0:3]\n"
"ds_read_b64_tr_b16 v[88:89], v13 offset:32768\n"
"v_mfma_f32_16x16x32_bf16 a[0:3], v[76:79], a[196:199], a[0:3]\n"
"v_mfma_f32_16x16x32_bf16 a[32:35], v[72:75], a[200:203], a[32:35]\n"
"ds_read_b64_tr_b16 v[90:91], v13 offset:36864\n"
"v_mfma_f32_16x16x32_bf16 a[32:35], v[76:79], a[204:207], a[32:35]\n"
"buffer_load_dwordx4 v22, s[44:47], s62 offen offset:2048 lds\n"
"v_mfma_f32_16x16x32_bf16 a[64:67], v[72:75], a[208:211], a[64:67]\n"
"ds_read_b64_tr_b16 v[92:93], v13 offset:40960\n"
"v_mfma_f32_16x16x32_bf16 a[64:67], v[76:79], a[212:215], a[64:67]\n"
"v_mfma_f32_16x16x32_bf16 a[96:99], v[72:75], a[216:219], a[96:99]\n"
"ds_read_b64_tr_b16 v[94:95], v13 offset:45056\n"
"v_mfma_f32_16x16x32_bf16 a[96:99], v[76:79], a[220:223], a[96:99]\n"
"buffer_load_dwordx4 v23, s[44:47], s62 offen offset:3072 lds\n"
"s_waitcnt lgkmcnt(4)\n"
"v_mfma_f32_16x16x32_bf16 v[132:135], v[80:83], a[192:195], v[132:135]\n"
"ds_read_b64_tr_b16 v[64:65], v42 offset:32768\n"
"v_mfma_f32_16x16x32_bf16 v[132:135], v[84:87], a[196:199], v[132:135]\n"
"v_mfma_f32_16x16x32_bf16 v[164:167], v[80:83], a[200:203], v[164:167]\n"
"ds_read_b64_tr_b16 v[66:67], v42 offset:36864\n"
"v_mfma_f32_16x16x32_bf16 v[164:167], v[84:87], a[204:207], v[164:167]\n"
"v_mfma_f32_16x16x32_bf16 v[196:199], v[80:83], a[208:211], v[196:199]\n"
"ds_read_b64_tr_b16 v[68:69], v42 offset:40960\n"
"s_add_u32 m0, s59, 65536\n"
"v_mfma_f32_16x16x32_bf16 v[196:199], v[84:87], a[212:215], v[196:199]\n"
"buffer_load_dwordx4 v20, s[48:51], s62 offen offset:0 lds\n"
"v_mfma_f32_16x16x32_bf16 v[228:231], v[80:83], a[216:219], v[228:231]\n"
"ds_read_b64_tr_b16 v[70:71], v42 offset:45056\n"
"v_mfma_f32_16x16x32_bf16 v[228:231], v[84:87], a[220:223], v[228:231]\n"
"s_waitcnt lgkmcnt(4)\n"
"v_mfma_f32_16x16x32_bf16 a[4:7], v[88:91], a[192:195], a[4:7]\n"
"ds_read_b64_tr_b16 v[72:73], v14 offset:32768\n"
"v_mfma_f32_16x16x32_bf16 a[4:7], v[92:95], a[196:199], a[4:7]\n"
"buffer_load_dwordx4 v21, s[48:51], s62 offen offset:1024 lds\n"
"v_mfma_f32_16x16x32_bf16 a[36:39], v[88:91], a[200:203], a[36:39]\n"
"ds_read_b64_tr_b16 v[74:75], v14 offset:36864\n"
"v_mfma_f32_16x16x32_bf16 a[36:39], v[92:95], a[204:207], a[36:39]\n"
"v_mfma_f32_16x16x32_bf16 a[68:71], v[88:91], a[208:211], a[68:71]\n"
"ds_read_b64_tr_b16 v[76:77], v14 offset:40960\n"
"v_mfma_f32_16x16x32_bf16 a[68:71], v[92:95], a[212:215], a[68:71]\n"
"v_mfma_f32_16x16x32_bf16 a[100:103], v[88:91], a[216:219], a[100:103]\n"
"ds_read_b64_tr_b16 v[78:79], v14 offset:45056\n"
"v_mfma_f32_16x16x32_bf16 a[100:103], v[92:95], a[220:223], a[100:103]\n"
"buffer_load_dwordx4 v22, s[48:51], s62 offen offset:2048 lds\n"
"s_waitcnt lgkmcnt(4)\n"
"v_mfma_f32_16x16x32_bf16 v[136:139], v[64:67], a[192:195], v[136:139]\n"
"ds_read_b64_tr_b16 v[80:81], v43 offset:32768\n"
"v_mfma_f32_16x16x32_bf16 v[136:139], v[68:71], a[196:199], v[136:139]\n"
"v_mfma_f32_16x16x32_bf16 v[168:171], v[64:67], a[200:203], v[168:171]\n"
"ds_read_b64_tr_b16 v[82:83], v43 offset:36864\n"
"v_mfma_f32_16x16x32_bf16 v[168:171], v[68:71], a[204:207], v[168:171]\n"
"v_mfma_f32_16x16x32_bf16 v[200:203], v[64:67], a[208:211], v[200:203]\n"
"ds_read_b64_tr_b16 v[84:85], v43 offset:40960\n"
"v_mfma_f32_16x16x32_bf16 v[200:203], v[68:71], a[212:215], v[200:203]\n"
"buffer_load_dwordx4 v23, s[48:51], s62 offen offset:3072 lds\n"
"s_add_u32 s62, s62, 458752\n"
"v_mfma_f32_16x16x32_bf16 v[232:235], v[64:67], a[216:219], v[232:235]\n"
"ds_read_b64_tr_b16 v[86:87], v43 offset:45056\n"
"v_mfma_f32_16x16x32_bf16 v[232:235], v[68:71], a[220:223], v[232:235]\n"
"s_waitcnt lgkmcnt(4)\n"
"v_mfma_f32_16x16x32_bf16 a[8:11], v[72:75], a[192:195], a[8:11]\n"
"ds_read_b64_tr_b16 v[88:89], v15 offset:32768\n"
"v_mfma_f32_16x16x32_bf16 a[8:11], v[76:79], a[196:199], a[8:11]\n"
"buffer_load_dwordx4 a[160:163], v39, s[36:39], s56 offen offset:0 sc1\n"
"v_mfma_f32_16x16x32_bf16 a[40:43], v[72:75], a[200:203], a[40:43]\n"
"ds_read_b64_tr_b16 v[90:91], v15 offset:36864\n"
"v_mfma_f32_16x16x32_bf16 a[40:43], v[76:79], a[204:207], a[40:43]\n"
"v_mfma_f32_16x16x32_bf16 a[72:75], v[72:75], a[208:211], a[72:75]\n"
"ds_read_b64_tr_b16 v[92:93], v15 offset:40960\n"
"v_mfma_f32_16x16x32_bf16 a[72:75], v[76:79], a[212:215], a[72:75]\n"
"v_mfma_f32_16x16x32_bf16 a[104:107], v[72:75], a[216:219], a[104:107]\n"
"ds_read_b64_tr_b16 v[94:95], v15 offset:45056\n"
"v_mfma_f32_16x16x32_bf16 a[104:107], v[76:79], a[220:223], a[104:107]\n"
"buffer_load_dwordx4 a[168:171], v39, s[36:39], s56 offen offset:1024 sc1\n"
"s_waitcnt lgkmcnt(4)\n"
"v_mfma_f32_16x16x32_bf16 v[140:143], v[80:83], a[192:195], v[140:143]\n"
"ds_read_b64_tr_b16 v[64:65], v44 offset:32768\n"
"v_mfma_f32_16x16x32_bf16 v[140:143], v[84:87], a[196:199], v[140:143]\n"
"v_mfma_f32_16x16x32_bf16 v[172:175], v[80:83], a[200:203], v[172:175]\n"
"ds_read_b64_tr_b16 v[66:67], v44 offset:36864\n"
"v_mfma_f32_16x16x32_bf16 v[172:175], v[84:87], a[204:207], v[172:175]\n"
"buffer_load_dwordx4 a[176:179], v39, s[36:39], s56 offen offset:2048 sc1\n"
"v_mfma_f32_16x16x32_bf16 v[204:207], v[80:83], a[208:211], v[204:207]\n"
"ds_read_b64_tr_b16 v[68:69], v44 offset:40960\n"
"v_mfma_f32_16x16x32_bf16 v[204:207], v[84:87], a[212:215], v[204:207]\n"
"v_mfma_f32_16x16x32_bf16 v[236:239], v[80:83], a[216:219], v[236:239]\n"
"ds_read_b64_tr_b16 v[70:71], v44 offset:45056\n"
"v_mfma_f32_16x16x32_bf16 v[236:239], v[84:87], a[220:223], v[236:239]\n"
"s_waitcnt lgkmcnt(4)\n"
"v_mfma_f32_16x16x32_bf16 a[12:15], v[88:91], a[192:195], a[12:15]\n"
"ds_read_b64_tr_b16 v[72:73], v16 offset:32768\n"
"v_mfma_f32_16x16x32_bf16 a[12:15], v[92:95], a[196:199], a[12:15]\n"
"buffer_load_dwordx4 a[184:187], v39, s[36:39], s56 offen offset:3072 sc1\n"
"s_add_u32 s56, s56, 16384\n"
"v_mfma_f32_16x16x32_bf16 a[44:47], v[88:91], a[200:203], a[44:47]\n"
"ds_read_b64_tr_b16 v[74:75], v16 offset:36864\n"
"v_mfma_f32_16x16x32_bf16 a[44:47], v[92:95], a[204:207], a[44:47]\n"
"v_mfma_f32_16x16x32_bf16 a[76:79], v[88:91], a[208:211], a[76:79]\n"
"ds_read_b64_tr_b16 v[76:77], v16 offset:40960\n"
"v_mfma_f32_16x16x32_bf16 a[76:79], v[92:95], a[212:215], a[76:79]\n"
"v_mfma_f32_16x16x32_bf16 a[108:111], v[88:91], a[216:219], a[108:111]\n"
"ds_read_b64_tr_b16 v[78:79], v16 offset:45056\n"
"v_mfma_f32_16x16x32_bf16 a[108:111], v[92:95], a[220:223], a[108:111]\n"
"s_cbranch_vccnz SKP2\n"
"s_waitcnt lgkmcnt(4)\n"
"v_mfma_f32_16x16x32_bf16 v[144:147], v[64:67], a[192:195], v[144:147]\n"
"ds_read_b64_tr_b16 v[80:81], v45 offset:32768\n"
"v_mfma_f32_16x16x32_bf16 v[144:147], v[68:71], a[196:199], v[144:147]\n"
"v_mfma_f32_16x16x32_bf16 v[176:179], v[64:67], a[200:203], v[176:179]\n"
"ds_read_b64_tr_b16 v[82:83], v45 offset:36864\n"
"v_mfma_f32_16x16x32_bf16 v[176:179], v[68:71], a[204:207], v[176:179]\n"
"v_mfma_f32_16x16x32_bf16 v[208:211], v[64:67], a[208:211], v[208:211]\n"
"ds_read_b64_tr_b16 v[84:85], v45 offset:40960\n"
"v_mfma_f32_16x16x32_bf16 v[208:211], v[68:71], a[212:215], v[208:211]\n"
"v_mfma_f32_16x16x32_bf16 v[240:243], v[64:67], a[216:219], v[240:243]\n"
"ds_read_b64_tr_b16 v[86:87], v45 offset:45056\n"
"v_mfma_f32_16x16x32_bf16 v[240:243], v[68:71], a[220:223], v[240:243]\n"
"s_waitcnt lgkmcnt(4)\n"
"v_mfma_f32_16x16x32_bf16 a[16:19], v[72:75], a[192:195], a[16:19]\n"
"ds_read_b64_tr_b16 v[88:89], v17 offset:32768\n"
"v_mfma_f32_16x16x32_bf16 a[16:19], v[76:79], a[196:199], a[16:19]\n"
"v_mfma_f32_16x16x32_bf16 a[48:51], v[72:75], a[200:203], a[48:51]\n"
"ds_read_b64_tr_b16 v[90:91], v17 offset:36864\n"
"v_mfma_f32_16x16x32_bf16 a[48:51], v[76:79], a[204:207], a[48:51]\n"
"v_mfma_f32_16x16x32_bf16 a[80:83], v[72:75], a[208:211], a[80:83]\n"
"ds_read_b64_tr_b16 v[92:93], v17 offset:40960\n"
"v_mfma_f32_16x16x32_bf16 a[80:83], v[76:79], a[212:215], a[80:83]\n"
"v_mfma_f32_16x16x32_bf16 a[112:115], v[72:75], a[216:219], a[112:115]\n"
"ds_read_b64_tr_b16 v[94:95], v17 offset:45056\n"
"v_mfma_f32_16x16x32_bf16 a[112:115], v[76:79], a[220:223], a[112:115]\n"
"s_waitcnt lgkmcnt(4)\n"
"v_mfma_f32_16x16x32_bf16 v[148:151], v[80:83], a[192:195], v[148:151]\n"
"ds_read_b64_tr_b16 v[64:65], v46 offset:32768\n"
"v_mfma_f32_16x16x32_bf16 v[148:151], v[84:87], a[196:199], v[148:151]\n"
"v_mfma_f32_16x16x32_bf16 v[180:183], v[80:83], a[200:203], v[180:183]\n"
"ds_read_b64_tr_b16 v[66:67], v46 offset:36864\n"
"v_mfma_f32_16x16x32_bf16 v[180:183], v[84:87], a[204:207], v[180:183]\n"
"v_mfma_f32_16x16x32_bf16 v[212:215], v[80:83], a[208:211], v[212:215]\n"
"ds_read_b64_tr_b16 v[68:69], v46 offset:40960\n"
"v_mfma_f32_16x16x32_bf16 v[212:215], v[84:87], a[212:215], v[212:215]\n"
"v_mfma_f32_16x16x32_bf16 v[244:247], v[80:83], a[216:219], v[244:247]\n"
"ds_read_b64_tr_b16 v[70:71], v46 offset:45056\n"
"v_mfma_f32_16x16x32_bf16 v[244:247], v[84:87], a[220:223], v[244:247]\n"
"s_waitcnt lgkmcnt(4)\n"
"v_mfma_f32_16x16x32_bf16 a[20:23], v[88:91], a[192:195], a[20:23]\n"
"ds_read_b64_tr_b16 v[72:73], v18 offset:32768\n"
"v_mfma_f32_16x16x32_bf16 a[20:23], v[92:95], a[196:199], a[20:23]\n"
"v_mfma_f32_16x16x32_bf16 a[52:55], v[88:91], a[200:203], a[52:55]\n"
"ds_read_b64_tr_b16 v[74:75], v18 offset:36864\n"
"v_mfma_f32_16x16x32_bf16 a[52:55], v[92:95], a[204:207], a[52:55]\n"
"v_mfma_f32_16x16x32_bf16 a[84:87], v[88:91], a[208:211], a[84:87]\n"
"ds_read_b64_tr_b16 v[76:77], v18 offset:40960\n"
"v_mfma_f32_16x16x32_bf16 a[84:87], v[92:95], a[212:215], a[84:87]\n"
"v_mfma_f32_16x16x32_bf16 a[116:119], v[88:91], a[216:219], a[116:119]\n"
"ds_read_b64_tr_b16 v[78:79], v18 offset:45056\n"
"v_mfma_f32_16x16x32_bf16 a[116:119], v[92:95], a[220:223], a[116:119]\n"
"SKP2:\n"
"s_waitcnt lgkmcnt(4)\n"
"v_mfma_f32_16x16x32_bf16 v[152:155], v[64:67], a[192:195], v[152:155]\n"
"ds_read_b64_tr_b16 v[80:81], v47 offset:32768\n"
"v_mfma_f32_16x16x32_bf16 v[152:155], v[68:71], a[196:199], v[152:155]\n"
"buffer_load_dwordx4 a[164:167], v39, s[36:39], s57 offen offset:0 sc1\n"
"v_mfma_f32_16x16x32_bf16 v[184:187], v[64:67], a[200:203], v[184:187]\n"
"ds_read_b64_tr_b16 v[82:83], v47 offset:36864\n"
"v_mfma_f32_16x16x32_bf16 v[184:187], v[68:71], a[204:207], v[184:187]\n"
"v_mfma_f32_16x16x32_bf16 v[216:219], v[64:67], a[208:211], v[216:219]\n"
"ds_read_b64_tr_b16 v[84:85], v47 offset:40960\n"
"v_mfma_f32_16x16x32_bf16 v[216:219], v[68:71], a[212:215], v[216:219]\n"
"buffer_load_dwordx4 a[172:175], v39, s[36:39], s57 offen offset:1024 sc1\n"
"v_mfma_f32_16x16x32_bf16 v[248:251], v[64:67], a[216:219], v[248:251]\n"
"ds_read_b64_tr_b16 v[86:87], v47 offset:45056\n"
"v_mfma_f32_16x16x32_bf16 v[248:251], v[68:71], a[220:223], v[248:251]\n"
"s_waitcnt lgkmcnt(4)\n"
"v_mfma_f32_16x16x32_bf16 a[24:27], v[72:75], a[192:195], a[24:27]\n"
"ds_read_b64_tr_b16 v[88:89], v19 offset:32768\n"
"v_mfma_f32_16x16x32_bf16 a[24:27], v[76:79], a[196:199], a[24:27]\n"
"v_mfma_f32_16x16x32_bf16 a[56:59], v[72:75], a[200:203], a[56:59]\n"
"ds_read_b64_tr_b16 v[90:91], v19 offset:36864\n"
"v_mfma_f32_16x16x32_bf16 a[56:59], v[76:79], a[204:207], a[56:59]\n"
"buffer_load_dwordx4 a[180:183], v39, s[36:39], s57 offen offset:2048 sc1\n"
"v_mfma_f32_16x16x32_bf16 a[88:91], v[72:75], a[208:211], a[88:91]\n"
"ds_read_b64_tr_b16 v[92:93], v19 offset:40960\n"
"v_mfma_f32_16x16x32_bf16 a[88:91], v[76:79], a[212:215], a[88:91]\n"
"v_mfma_f32_16x16x32_bf16 a[120:123], v[72:75], a[216:219], a[120:123]\n"
"ds_read_b64_tr_b16 v[94:95], v19 offset:45056\n"
"v_mfma_f32_16x16x32_bf16 a[120:123], v[76:79], a[220:223], a[120:123]\n"
"s_waitcnt vmcnt(25)\n"
"s_barrier\n"
"s_waitcnt lgkmcnt(4)\n"
"v_mfma_f32_16x16x32_bf16 v[156:159], v[80:83], a[192:195], v[156:159]\n"
"ds_read_b64_tr_b16 v[64:65], v40 offset:49152\n"
"v_mfma_f32_16x16x32_bf16 v[156:159], v[84:87], a[196:199], v[156:159]\n"
"v_mfma_f32_16x16x32_bf16 v[188:191], v[80:83], a[200:203], v[188:191]\n"
"ds_read_b64_tr_b16 v[66:67], v40 offset:53248\n"
"v_mfma_f32_16x16x32_bf16 v[188:191], v[84:87], a[204:207], v[188:191]\n"
"buffer_load_dwordx4 a[188:191], v39, s[36:39], s57 offen offset:3072 sc1\n"
"s_add_u32 s57, s57, 16384\n"
"v_mfma_f32_16x16x32_bf16 v[220:223], v[80:83], a[208:211], v[220:223]\n"
"ds_read_b64_tr_b16 v[68:69], v40 offset:57344\n"
"v_mfma_f32_16x16x32_bf16 v[220:223], v[84:87], a[212:215], v[220:223]\n"
"v_mfma_f32_16x16x32_bf16 v[252:255], v[80:83], a[216:219], v[252:255]\n"
"ds_read_b64_tr_b16 v[70:71], v40 offset:61440\n"
"v_mfma_f32_16x16x32_bf16 v[252:255], v[84:87], a[220:223], v[252:255]\n"
"s_waitcnt lgkmcnt(4)\n"
"v_mfma_f32_16x16x32_bf16 a[28:31], v[88:91], a[192:195], a[28:31]\n"
"ds_read_b64_tr_b16 v[72:73], v12 offset:49152\n"
"v_mfma_f32_16x16x32_bf16 a[28:31], v[92:95], a[196:199], a[28:31]\n"
"global_store_dwordx4 v33, v[104:107], %0 offset:2048 sc1\n"
"v_mfma_f32_16x16x32_bf16 a[60:63], v[88:91], a[200:203], a[60:63]\n"
"ds_read_b64_tr_b16 v[74:75], v12 offset:53248\n"
"v_mfma_f32_16x16x32_bf16 a[60:63], v[92:95], a[204:207], a[60:63]\n"
"v_mfma_f32_16x16x32_bf16 a[92:95], v[88:91], a[208:211], a[92:95]\n"
"ds_read_b64_tr_b16 v[76:77], v12 offset:57344\n"
"v_mfma_f32_16x16x32_bf16 a[92:95], v[92:95], a[212:215], a[92:95]\n"
"v_mfma_f32_16x16x32_bf16 a[124:127], v[88:91], a[216:219], a[124:127]\n"
"ds_read_b64_tr_b16 v[78:79], v12 offset:61440\n"
"v_mfma_f32_16x16x32_bf16 a[124:127], v[92:95], a[220:223], a[124:127]\n"
"s_waitcnt lgkmcnt(4)\n"
"v_mfma_f32_16x16x32_bf16 v[128:131], v[64:67], a[224:227], v[128:131]\n"
"ds_read_b64_tr_b16 v[80:81], v41 offset:49152\n"
"s_add_u32 m0, s59, 16384\n"
"v_mfma_f32_16x16x32_bf16 v[128:131], v[68:71], a[228:231], v[128:131]\n"
"buffer_load_dwordx4 v20, s[44:47], s62 offen offset:0 lds\n"
"v_mfma_f32_16x16x32_bf16 v[160:163], v[64:67], a[232:235], v[160:163]\n"
"ds_read_b64_tr_b16 v[82:83], v41 offset:53248\n"
"v_mfma_f32_16x16x32_bf16 v[160:163], v[68:71], a[236:239], v[160:163]\n"
"v_mfma_f32_16x16x32_bf16 v[192:195], v[64:67], a[240:243], v[192:195]\n"
"ds_read_b64_tr_b16 v[84:85], v41 offset:57344\n"
"v_mfma_f32_16x16x32_bf16 v[192:195], v[68:71], a[244:247], v[192:195]\n"
"buffer_load_dwordx4 v21, s[44:47], s62 offen offset:1024 lds\n"
"v_mfma_f32_16x16x32_bf16 v[224:227], v[64:67], a[248:251], v[224:227]\n"
"ds_read_b64_tr_b16 v[86:87], v41 offset:61440\n"
"v_mfma_f32_16x16x32_bf16 v[224:227], v[68:71], a[252:255], v[224:227]\n"
"s_waitcnt lgkmcnt(4)\n"
"v_mfma_f32_16x16x32_bf16 a[0:3], v[72:75], a[224:227], a[0:3]\n"
"ds_read_b64_tr_b16 v[88:89], v13 offset:49152\n"
"v_mfma_f32_16x16x32_bf16 a[0:3], v[76:79], a[228:231], a[0:3]\n"
"v_mfma_f32_16x16x32_bf16 a[32:35], v[72:75], a[232:235], a[32:35]\n"
"ds_read_b64_tr_b16 v[90:91], v13 offset:53248\n"
"v_mfma_f32_16x16x32_bf16 a[32:35], v[76:79], a[236:239], a[32:35]\n"
"buffer_load_dwordx4 v22, s[44:47], s62 offen offset:2048 lds\n"
"v_mfma_f32_16x16x32_bf16 a[64:67], v[72:75], a[240:243], a[64:67]\n"
"ds_read_b64_tr_b16 v[92:93], v13 offset:57344\n"
"v_mfma_f32_16x16x32_bf16 a[64:67], v[76:79], a[244:247], a[64:67]\n"
"v_mfma_f32_16x16x32_bf16 a[96:99], v[72:75], a[248:251], a[96:99]\n"
"ds_read_b64_tr_b16 v[94:95], v13 offset:61440\n"
"v_mfma_f32_16x16x32_bf16 a[96:99], v[76:79], a[252:255], a[96:99]\n"
"buffer_load_dwordx4 v23, s[44:47], s62 offen offset:3072 lds\n"
"s_waitcnt lgkmcnt(4)\n"
"v_mfma_f32_16x16x32_bf16 v[132:135], v[80:83], a[224:227], v[132:135]\n"
"ds_read_b64_tr_b16 v[64:65], v42 offset:49152\n"
"v_mfma_f32_16x16x32_bf16 v[132:135], v[84:87], a[228:231], v[132:135]\n"
"v_mfma_f32_16x16x32_bf16 v[164:167], v[80:83], a[232:235], v[164:167]\n"
"ds_read_b64_tr_b16 v[66:67], v42 offset:53248\n"
"v_mfma_f32_16x16x32_bf16 v[164:167], v[84:87], a[236:239], v[164:167]\n"
"v_mfma_f32_16x16x32_bf16 v[196:199], v[80:83], a[240:243], v[196:199]\n"
"ds_read_b64_tr_b16 v[68:69], v42 offset:57344\n"
"s_add_u32 m0, s59, 81920\n"
"v_mfma_f32_16x16x32_bf16 v[196:199], v[84:87], a[244:247], v[196:199]\n"
"buffer_load_dwordx4 v20, s[48:51], s62 offen offset:0 lds\n"
"v_mfma_f32_16x16x32_bf16 v[228:231], v[80:83], a[248:251], v[228:231]\n"
"ds_read_b64_tr_b16 v[70:71], v42 offset:61440\n"
"v_mfma_f32_16x16x32_bf16 v[228:231], v[84:87], a[252:255], v[228:231]\n"
"s_waitcnt lgkmcnt(4)\n"
"v_mfma_f32_16x16x32_bf16 a[4:7], v[88:91], a[224:227], a[4:7]\n"
"ds_read_b64_tr_b16 v[72:73], v14 offset:49152\n"
"v_mfma_f32_16x16x32_bf16 a[4:7], v[92:95], a[228:231], a[4:7]\n"
"buffer_load_dwordx4 v21, s[48:51], s62 offen offset:1024 lds\n"
"v_mfma_f32_16x16x32_bf16 a[36:39], v[88:91], a[232:235], a[36:39]\n"
"ds_read_b64_tr_b16 v[74:75], v14 offset:53248\n"
"v_mfma_f32_16x16x32_bf16 a[36:39], v[92:95], a[236:239], a[36:39]\n"
"v_mfma_f32_16x16x32_bf16 a[68:71], v[88:91], a[240:243], a[68:71]\n"
"ds_read_b64_tr_b16 v[76:77], v14 offset:57344\n"
"v_mfma_f32_16x16x32_bf16 a[68:71], v[92:95], a[244:247], a[68:71]\n"
"v_mfma_f32_16x16x32_bf16 a[100:103], v[88:91], a[248:251], a[100:103]\n"
"ds_read_b64_tr_b16 v[78:79], v14 offset:61440\n"
"v_mfma_f32_16x16x32_bf16 a[100:103], v[92:95], a[252:255], a[100:103]\n"
"buffer_load_dwordx4 v22, s[48:51], s62 offen offset:2048 lds\n"
"s_waitcnt lgkmcnt(4)\n"
"v_mfma_f32_16x16x32_bf16 v[136:139], v[64:67], a[224:227], v[136:139]\n"
"ds_read_b64_tr_b16 v[80:81], v43 offset:49152\n"
"v_mfma_f32_16x16x32_bf16 v[136:139], v[68:71], a[228:231], v[136:139]\n"
"v_mfma_f32_16x16x32_bf16 v[168:171], v[64:67], a[232:235], v[168:171]\n"
"ds_read_b64_tr_b16 v[82:83], v43 offset:53248\n"
"v_mfma_f32_16x16x32_bf16 v[168:171], v[68:71], a[236:239], v[168:171]\n"
"v_mfma_f32_16x16x32_bf16 v[200:203], v[64:67], a[240:243], v[200:203]\n"
"ds_read_b64_tr_b16 v[84:85], v43 offset:57344\n"
"v_mfma_f32_16x16x32_bf16 v[200:203], v[68:71], a[244:247], v[200:203]\n"
"buffer_load_dwordx4 v23, s[48:51], s62 offen offset:3072 lds\n"
"s_add_u32 s62, s62, 458752\n"
"v_mfma_f32_16x16x32_bf16 v[232:235], v[64:67], a[248:251], v[232:235]\n"
"ds_read_b64_tr_b16 v[86:87], v43 offset:61440\n"
"v_mfma_f32_16x16x32_bf16 v[232:235], v[68:71], a[252:255], v[232:235]\n"
"s_waitcnt lgkmcnt(4)\n"
"v_mfma_f32_16x16x32_bf16 a[8:11], v[72:75], a[224:227], a[8:11]\n"
"ds_read_b64_tr_b16 v[88:89], v15 offset:49152\n"
"v_mfma_f32_16x16x32_bf16 a[8:11], v[76:79], a[228:231], a[8:11]\n"
"buffer_load_dwordx4 a[192:195], v39, s[36:39], s56 offen offset:0 sc1\n"
"v_mfma_f32_16x16x32_bf16 a[40:43], v[72:75], a[232:235], a[40:43]\n"
"ds_read_b64_tr_b16 v[90:91], v15 offset:53248\n"
"v_mfma_f32_16x16x32_bf16 a[40:43], v[76:79], a[236:239], a[40:43]\n"
"v_mfma_f32_16x16x32_bf16 a[72:75], v[72:75], a[240:243], a[72:75]\n"
"ds_read_b64_tr_b16 v[92:93], v15 offset:57344\n"
"v_mfma_f32_16x16x32_bf16 a[72:75], v[76:79], a[244:247], a[72:75]\n"
"v_mfma_f32_16x16x32_bf16 a[104:107], v[72:75], a[248:251], a[104:107]\n"
"ds_read_b64_tr_b16 v[94:95], v15 offset:61440\n"
"v_mfma_f32_16x16x32_bf16 a[104:107], v[76:79], a[252:255], a[104:107]\n"
"buffer_load_dwordx4 a[200:203], v39, s[36:39], s56 offen offset:1024 sc1\n"
"s_waitcnt lgkmcnt(4)\n"
"v_mfma_f32_16x16x32_bf16 v[140:143], v[80:83], a[224:227], v[140:143]\n"
"ds_read_b64_tr_b16 v[64:65], v44 offset:49152\n"
"v_mfma_f32_16x16x32_bf16 v[140:143], v[84:87], a[228:231], v[140:143]\n"
"v_mfma_f32_16x16x32_bf16 v[172:175], v[80:83], a[232:235], v[172:175]\n"
"ds_read_b64_tr_b16 v[66:67], v44 offset:53248\n"
"v_mfma_f32_16x16x32_bf16 v[172:175], v[84:87], a[236:239], v[172:175]\n"
"buffer_load_dwordx4 a[208:211], v39, s[36:39], s56 offen offset:2048 sc1\n"
"v_mfma_f32_16x16x32_bf16 v[204:207], v[80:83], a[240:243], v[204:207]\n"
"ds_read_b64_tr_b16 v[68:69], v44 offset:57344\n"
"v_mfma_f32_16x16x32_bf16 v[204:207], v[84:87], a[244:247], v[204:207]\n"
"v_mfma_f32_16x16x32_bf16 v[236:239], v[80:83], a[248:251], v[236:239]\n"
"ds_read_b64_tr_b16 v[70:71], v44 offset:61440\n"
"v_mfma_f32_16x16x32_bf16 v[236:239], v[84:87], a[252:255], v[236:239]\n"
"s_waitcnt lgkmcnt(4)\n"
"v_mfma_f32_16x16x32_bf16 a[12:15], v[88:91], a[224:227], a[12:15]\n"
"ds_read_b64_tr_b16 v[72:73], v16 offset:49152\n"
"v_mfma_f32_16x16x32_bf16 a[12:15], v[92:95], a[228:231], a[12:15]\n"
"buffer_load_dwordx4 a[216:219], v39, s[36:39], s56 offen offset:3072 sc1\n"
"s_add_u32 s56, s56, 16384\n"
"v_mfma_f32_16x16x32_bf16 a[44:47], v[88:91], a[232:235], a[44:47]\n"
"ds_read_b64_tr_b16 v[74:75], v16 offset:53248\n"
"v_mfma_f32_16x16x32_bf16 a[44:47], v[92:95], a[236:239], a[44:47]\n"
"v_mfma_f32_16x16x32_bf16 a[76:79], v[88:91], a[240:243], a[76:79]\n"
"ds_read_b64_tr_b16 v[76:77], v16 offset:57344\n"
"v_mfma_f32_16x16x32_bf16 a[76:79], v[92:95], a[244:247], a[76:79]\n"
"v_mfma_f32_16x16x32_bf16 a[108:111], v[88:91], a[248:251], a[108:111]\n"
"ds_read_b64_tr_b16 v[78:79], v16 offset:61440\n"
"v_mfma_f32_16x16x32_bf16 a[108:111], v[92:95], a[252:255], a[108:111]\n"
"s_cbranch_vccnz SKP3\n"
"s_waitcnt lgkmcnt(4)\n"
"v_mfma_f32_16x16x32_bf16 v[144:147], v[64:67], a[224:227], v[144:147]\n"
"ds_read_b64_tr_b16 v[80:81], v45 offset:49152\n"
"v_mfma_f32_16x16x32_bf16 v[144:147], v[68:71], a[228:231], v[144:147]\n"
"v_mfma_f32_16x16x32_bf16 v[176:179], v[64:67], a[232:235], v[176:179]\n"
"ds_read_b64_tr_b16 v[82:83], v45 offset:53248\n"
"v_mfma_f32_16x16x32_bf16 v[176:179], v[68:71], a[236:239], v[176:179]\n"
"v_mfma_f32_16x16x32_bf16 v[208:211], v[64:67], a[240:243], v[208:211]\n"
"ds_read_b64_tr_b16 v[84:85], v45 offset:57344\n"
"v_mfma_f32_16x16x32_bf16 v[208:211], v[68:71], a[244:247], v[208:211]\n"
"v_mfma_f32_16x16x32_bf16 v[240:243], v[64:67], a[248:251], v[240:243]\n"
"ds_read_b64_tr_b16 v[86:87], v45 offset:61440\n"
"v_mfma_f32_16x16x32_bf16 v[240:243], v[68:71], a[252:255], v[240:243]\n"
"s_waitcnt lgkmcnt(4)\n"
"v_mfma_f32_16x16x32_bf16 a[16:19], v[72:75], a[224:227], a[16:19]\n"
"ds_read_b64_tr_b16 v[88:89], v17 offset:49152\n"
"v_mfma_f32_16x16x32_bf16 a[16:19], v[76:79], a[228:231], a[16:19]\n"
"v_mfma_f32_16x16x32_bf16 a[48:51], v[72:75], a[232:235], a[48:51]\n"
"ds_read_b64_tr_b16 v[90:91], v17 offset:53248\n"
"v_mfma_f32_16x16x32_bf16 a[48:51], v[76:79], a[236:239], a[48:51]\n"
"v_mfma_f32_16x16x32_bf16 a[80:83], v[72:75], a[240:243], a[80:83]\n"
"ds_read_b64_tr_b16 v[92:93], v17 offset:57344\n"
"v_mfma_f32_16x16x32_bf16 a[80:83], v[76:79], a[244:247], a[80:83]\n"
"v_mfma_f32_16x16x32_bf16 a[112:115], v[72:75], a[248:251], a[112:115]\n"
"ds_read_b64_tr_b16 v[94:95], v17 offset:61440\n"
"v_mfma_f32_16x16x32_bf16 a[112:115], v[76:79], a[252:255], a[112:115]\n"
"s_waitcnt lgkmcnt(4)\n"
"v_mfma_f32_16x16x32_bf16 v[148:151], v[80:83], a[224:227], v[148:151]\n"
"ds_read_b64_tr_b16 v[64:65], v46 offset:49152\n"
"v_mfma_f32_16x16x32_bf16 v[148:151], v[84:87], a[228:231], v[148:151]\n"
"v_mfma_f32_16x16x32_bf16 v[180:183], v[80:83], a[232:235], v[180:183]\n"
"ds_read_b64_tr_b16 v[66:67], v46 offset:53248\n"
"v_mfma_f32_16x16x32_bf16 v[180:183], v[84:87], a[236:239], v[180:183]\n"
"v_mfma_f32_16x16x32_bf16 v[212:215], v[80:83], a[240:243], v[212:215]\n"
"ds_read_b64_tr_b16 v[68:69], v46 offset:57344\n"
"v_mfma_f32_16x16x32_bf16 v[212:215], v[84:87], a[244:247], v[212:215]\n"
"v_mfma_f32_16x16x32_bf16 v[244:247], v[80:83], a[248:251], v[244:247]\n"
"ds_read_b64_tr_b16 v[70:71], v46 offset:61440\n"
"v_mfma_f32_16x16x32_bf16 v[244:247], v[84:87], a[252:255], v[244:247]\n"
"s_waitcnt lgkmcnt(4)\n"
"v_mfma_f32_16x16x32_bf16 a[20:23], v[88:91], a[224:227], a[20:23]\n"
"ds_read_b64_tr_b16 v[72:73], v18 offset:49152\n"
"v_mfma_f32_16x16x32_bf16 a[20:23], v[92:95], a[228:231], a[20:23]\n"
"v_mfma_f32_16x16x32_bf16 a[52:55], v[88:91], a[232:235], a[52:55]\n"
"ds_read_b64_tr_b16 v[74:75], v18 offset:53248\n"
"v_mfma_f32_16x16x32_bf16 a[52:55], v[92:95], a[236:239], a[52:55]\n"
"v_mfma_f32_16x16x32_bf16 a[84:87], v[88:91], a[240:243], a[84:87]\n"
"ds_read_b64_tr_b16 v[76:77], v18 offset:57344\n"
"v_mfma_f32_16x16x32_bf16 a[84:87], v[92:95], a[244:247], a[84:87]\n"
"v_mfma_f32_16x16x32_bf16 a[116:119], v[88:91], a[248:251], a[116:119]\n"
"ds_read_b64_tr_b16 v[78:79], v18 offset:61440\n"
"v_mfma_f32_16x16x32_bf16 a[116:119], v[92:95], a[252:255], a[116:119]\n"
"SKP3:\n"
"s_waitcnt lgkmcnt(4)\n"
"v_mfma_f32_16x16x32_bf16 v[152:155], v[64:67], a[224:227], v[152:155]\n"
"ds_read_b64_tr_b16 v[80:81], v47 offset:49152\n"
"v_mfma_f32_16x16x32_bf16 v[152:155], v[68:71], a[228:231], v[152:155]\n"
"buffer_load_dwordx4 a[196:199], v39, s[36:39], s57 offen offset:0 sc1\n"
"v_mfma_f32_16x16x32_bf16 v[184:187], v[64:67], a[232:235], v[184:187]\n"
"ds_read_b64_tr_b16 v[82:83], v47 offset:53248\n"
"v_mfma_f32_16x16x32_bf16 v[184:187], v[68:71], a[236:239], v[184:187]\n"
"v_mfma_f32_16x16x32_bf16 v[216:219], v[64:67], a[240:243], v[216:219]\n"
"ds_read_b64_tr_b16 v[84:85], v47 offset:57344\n"
"v_mfma_f32_16x16x32_bf16 v[216:219], v[68:71], a[244:247], v[216:219]\n"
"buffer_load_dwordx4 a[204:207], v39, s[36:39], s57 offen offset:1024 sc1\n"
"v_mfma_f32_16x16x32_bf16 v[248:251], v[64:67], a[248:251], v[248:251]\n"
"ds_read_b64_tr_b16 v[86:87], v47 offset:61440\n"
"v_mfma_f32_16x16x32_bf16 v[248:251], v[68:71], a[252:255], v[248:251]\n"
"s_waitcnt lgkmcnt(4)\n"
"v_mfma_f32_16x16x32_bf16 a[24:27], v[72:75], a[224:227], a[24:27]\n"
"ds_read_b64_tr_b16 v[88:89], v19 offset:49152\n"
"v_mfma_f32_16x16x32_bf16 a[24:27], v[76:79], a[228:231], a[24:27]\n"
"v_mfma_f32_16x16x32_bf16 a[56:59], v[72:75], a[232:235], a[56:59]\n"
"ds_read_b64_tr_b16 v[90:91], v19 offset:53248\n"
"v_mfma_f32_16x16x32_bf16 a[56:59], v[76:79], a[236:239], a[56:59]\n"
"buffer_load_dwordx4 a[212:215], v39, s[36:39], s57 offen offset:2048 sc1\n"
"v_mfma_f32_16x16x32_bf16 a[88:91], v[72:75], a[240:243], a[88:91]\n"
"ds_read_b64_tr_b16 v[92:93], v19 offset:57344\n"
"v_mfma_f32_16x16x32_bf16 a[88:91], v[76:79], a[244:247], a[88:91]\n"
"v_mfma_f32_16x16x32_bf16 a[120:123], v[72:75], a[248:251], a[120:123]\n"
"ds_read_b64_tr_b16 v[94:95], v19 offset:61440\n"
"v_mfma_f32_16x16x32_bf16 a[120:123], v[76:79], a[252:255], a[120:123]\n"
"s_waitcnt vmcnt(24)\n"
"s_barrier\n"
"s_waitcnt lgkmcnt(4)\n"
"v_mfma_f32_16x16x32_bf16 v[156:159], v[80:83], a[224:227], v[156:159]\n"
"ds_read_b64_tr_b16 v[64:65], v40 offset:0\n"
"v_mfma_f32_16x16x32_bf16 v[156:159], v[84:87], a[228:231], v[156:159]\n"
"v_mfma_f32_16x16x32_bf16 v[188:191], v[80:83], a[232:235], v[188:191]\n"
"ds_read_b64_tr_b16 v[66:67], v40 offset:4096\n"
"v_mfma_f32_16x16x32_bf16 v[188:191], v[84:87], a[236:239], v[188:191]\n"
"buffer_load_dwordx4 a[220:223], v39, s[36:39], s57 offen offset:3072 sc1\n"
"s_add_u32 s57, s57, 16384\n"
"v_mfma_f32_16x16x32_bf16 v[220:223], v[80:83], a[240:243], v[220:223]\n"
"ds_read_b64_tr_b16 v[68:69], v40 offset:8192\n"
"v_mfma_f32_16x16x32_bf16 v[220:223], v[84:87], a[244:247], v[220:223]\n"
"v_mfma_f32_16x16x32_bf16 v[252:255], v[80:83], a[248:251], v[252:255]\n"
"ds_read_b64_tr_b16 v[70:71], v40 offset:12288\n"
"v_mfma_f32_16x16x32_bf16 v[252:255], v[84:87], a[252:255], v[252:255]\n"
"s_waitcnt lgkmcnt(4)\n"
"v_mfma_f32_16x16x32_bf16 a[28:31], v[88:91], a[224:227], a[28:31]\n"
"ds_read_b64_tr_b16 v[72:73], v12 offset:0\n"
"v_mfma_f32_16x16x32_bf16 a[28:31], v[92:95], a[228:231], a[28:31]\n"
"global_store_dwordx4 v33, v[108:111], %0 offset:3072 sc1\n"
"v_mfma_f32_16x16x32_bf16 a[60:63], v[88:91], a[232:235], a[60:63]\n"
"ds_read_b64_tr_b16 v[74:75], v12 offset:4096\n"
"v_mfma_f32_16x16x32_bf16 a[60:63], v[92:95], a[236:239], a[60:63]\n"
"v_mfma_f32_16x16x32_bf16 a[92:95], v[88:91], a[240:243], a[92:95]\n"
"ds_read_b64_tr_b16 v[76:77], v12 offset:8192\n"
"v_mfma_f32_16x16x32_bf16 a[92:95], v[92:95], a[244:247], a[92:95]\n"
"v_mfma_f32_16x16x32_bf16 a[124:127], v[88:91], a[248:251], a[124:127]\n"
"ds_read_b64_tr_b16 v[78:79], v12 offset:12288\n"
"v_mfma_f32_16x16x32_bf16 a[124:127], v[92:95], a[252:255], a[124:127]\n"
"s_mov_b32 s60, 2\n"
"G1_LOOP:\n"
"s_waitcnt lgkmcnt(4)\n"
"v_mfma_f32_16x16x32_bf16 v[128:131], v[64:67], a[128:131], v[128:131]\n"
"ds_read_b64_tr_b16 v[80:81], v41 offset:0\n"
"s_add_u32 m0, s59, 32768\n"
"v_mfma_f32_16x16x32_bf16 v[128:131], v[68:71], a[132:135], v[128:131]\n"
"buffer_load_dwordx4 v20, s[44:47], s62 offen offset:0 lds\n"
"v_mfma_f32_16x16x32_bf16 v[160:163], v[64:67], a[136:139], v[160:163]\n"
"ds_read_b64_tr_b16 v[82:83], v41 offset:4096\n"
"v_mfma_f32_16x16x32_bf16 v[160:163], v[68:71], a[140:143], v[160:163]\n"
"ds_read_b128 v[96:99], v8 offset:0\n"
"v_mfma_f32_16x16x32_bf16 v[192:195], v[64:67], a[144:147], v[192:195]\n"
"ds_read_b64_tr_b16 v[84:85], v41 offset:8192\n"
"v_mfma_f32_16x16x32_bf16 v[192:195], v[68:71], a[148:151], v[192:195]\n"
"buffer_load_dwordx4 v21, s[44:47], s62 offen offset:1024 lds\n"
"v_mfma_f32_16x16x32_bf16 v[224:227], v[64:67], a[152:155], v[224:227]\n"
"ds_read_b64_tr_b16 v[86:87], v41 offset:12288\n"
"v_mfma_f32_16x16x32_bf16 v[224:227], v[68:71], a[156:159], v[224:227]\n"
"s_waitcnt lgkmcnt(5)\n"
"v_mfma_f32_16x16x32_bf16 a[0:3], v[72:75], a[128:131], a[0:3]\n"
"ds_read_b64_tr_b16 v[88:89], v13 offset:0\n"
"v_mfma_f32_16x16x32_bf16 a[0:3], v[76:79], a[132:135], a[0:3]\n"
"v_mfma_f32_16x16x32_bf16 a[32:35], v[72:75], a[136:139], a[32:35]\n"
"ds_read_b64_tr_b16 v[90:91], v13 offset:4096\n"
"v_mfma_f32_16x16x32_bf16 a[32:35], v[76:79], a[140:143], a[32:35]\n"
"buffer_load_dwordx4 v22, s[44:47], s62 offen offset:2048 lds\n"
"v_mfma_f32_16x16x32_bf16 a[64:67], v[72:75], a[144:147], a[64:67]\n"
"ds_read_b64_tr_b16 v[92:93], v13 offset:8192\n"
"v_mfma_f32_16x16x32_bf16 a[64:67], v[76:79], a[148:151], a[64:67]\n"
"v_mfma_f32_16x16x32_bf16 a[96:99], v[72:75], a[152:155], a[96:99]\n"
"ds_read_b64_tr_b16 v[94:95], v13 offset:12288\n"
"v_mfma_f32_16x16x32_bf16 a[96:99], v[76:79], a[156:159], a[96:99]\n"
"buffer_load_dwordx4 v23, s[44:47], s62 offen offset:3072 lds\n"
"s_waitcnt lgkmcnt(4)\n"
"v_mfma_f32_16x16x32_bf16 v[132:135], v[80:83], a[128:131], v[132:135]\n"
"ds_read_b64_tr_b16 v[64:65], v42 offset:0\n"
"v_mfma_f32_16x16x32_bf16 v[132:135], v[84:87], a[132:135], v[132:135]\n"
"v_mfma_f32_16x16x32_bf16 v[164:167], v[80:83], a[136:139], v[164:167]\n"
"ds_read_b64_tr_b16 v[66:67], v42 offset:4096\n"
"v_mfma_f32_16x16x32_bf16 v[164:167], v[84:87], a[140:143], v[164:167]\n"
"v_mfma_f32_16x16x32_bf16 v[196:199], v[80:83], a[144:147], v[196:199]\n"
"ds_read_b64_tr_b16 v[68:69], v42 offset:8192\n"
"s_add_u32 m0, s59, 98304\n"
"v_mfma_f32_16x16x32_bf16 v[196:199], v[84:87], a[148:151], v[196:199]\n"
"buffer_load_dwordx4 v20, s[48:51], s62 offen offset:0 lds\n"
"v_mfma_f32_16x16x32_bf16 v[228:231], v[80:83], a[152:155], v[228:231]\n"
"ds_read_b64_tr_b16 v[70:71], v42 offset:12288\n"
"v_mfma_f32_16x16x32_bf16 v[228:231], v[84:87], a[156:159], v[228:231]\n"
"s_waitcnt lgkmcnt(4)\n"
"v_mfma_f32_16x16x32_bf16 a[4:7], v[88:91], a[128:131], a[4:7]\n"
"ds_read_b64_tr_b16 v[72:73], v14 offset:0\n"
"v_mfma_f32_16x16x32_bf16 a[4:7], v[92:95], a[132:135], a[4:7]\n"
"buffer_load_dwordx4 v21, s[48:51], s62 offen offset:1024 lds\n"
"v_mfma_f32_16x16x32_bf16 a[36:39], v[88:91], a[136:139], a[36:39]\n"
"ds_read_b64_tr_b16 v[74:75], v14 offset:4096\n"
"v_mfma_f32_16x16x32_bf16 a[36:39], v[92:95], a[140:143], a[36:39]\n"
"v_mfma_f32_16x16x32_bf16 a[68:71], v[88:91], a[144:147], a[68:71]\n"
"ds_read_b64_tr_b16 v[76:77], v14 offset:8192\n"
"v_mfma_f32_16x16x32_bf16 a[68:71], v[92:95], a[148:151], a[68:71]\n"
"v_mfma_f32_16x16x32_bf16 a[100:103], v[88:91], a[152:155], a[100:103]\n"
"ds_read_b64_tr_b16 v[78:79], v14 offset:12288\n"
"v_mfma_f32_16x16x32_bf16 a[100:103], v[92:95], a[156:159], a[100:103]\n"
"buffer_load_dwordx4 v22, s[48:51], s62 offen offset:2048 lds\n"
"s_waitcnt lgkmcnt(4)\n"
"v_mfma_f32_16x16x32_bf16 v[136:139], v[64:67], a[128:131], v[136:139]\n"
"ds_read_b64_tr_b16 v[80:81], v43 offset:0\n"
"v_mfma_f32_16x16x32_bf16 v[136:139], v[68:71], a[132:135], v[136:139]\n"
"v_mfma_f32_16x16x32_bf16 v[168:171], v[64:67], a[136:139], v[168:171]\n"
"ds_read_b64_tr_b16 v[82:83], v43 offset:4096\n"
"v_mfma_f32_16x16x32_bf16 v[168:171], v[68:71], a[140:143], v[168:171]\n"
"v_mfma_f32_16x16x32_bf16 v[200:203], v[64:67], a[144:147], v[200:203]\n"
"ds_read_b64_tr_b16 v[84:85], v43 offset:8192\n"
"v_mfma_f32_16x16x32_bf16 v[200:203], v[68:71], a[148:151], v[200:203]\n"
"buffer_load_dwordx4 v23, s[48:51], s62 offen offset:3072 lds\n"
"s_add_u32 s62, s62, 458752\n"
"v_mfma_f32_16x16x32_bf16 v[232:235], v[64:67], a[152:155], v[232:235]\n"
"ds_read_b64_tr_b16 v[86:87], v43 offset:12288\n"
"v_mfma_f32_16x16x32_bf16 v[232:235], v[68:71], a[156:159], v[232:235]\n"
"s_waitcnt lgkmcnt(4)\n"
"v_mfma_f32_16x16x32_bf16 a[8:11], v[72:75], a[128:131], a[8:11]\n"
"ds_read_b64_tr_b16 v[88:89], v15 offset:0\n"
"v_mfma_f32_16x16x32_bf16 a[8:11], v[76:79], a[132:135], a[8:11]\n"
"buffer_load_dwordx4 a[224:227], v39, s[36:39], s56 offen offset:0 sc1\n"
"v_mfma_f32_16x16x32_bf16 a[40:43], v[72:75], a[136:139], a[40:43]\n"
"ds_read_b64_tr_b16 v[90:91], v15 offset:4096\n"
"v_mfma_f32_16x16x32_bf16 a[40:43], v[76:79], a[140:143], a[40:43]\n"
"v_mfma_f32_16x16x32_bf16 a[72:75], v[72:75], a[144:147], a[72:75]\n"
"ds_read_b64_tr_b16 v[92:93], v15 offset:8192\n"
"v_mfma_f32_16x16x32_bf16 a[72:75], v[76:79], a[148:151], a[72:75]\n"
"v_mfma_f32_16x16x32_bf16 a[104:107], v[72:75], a[152:155], a[104:107]\n"
"ds_read_b64_tr_b16 v[94:95], v15 offset:12288\n"
"v_mfma_f32_16x16x32_bf16 a[104:107], v[76:79], a[156:159], a[104:107]\n"
"buffer_load_dwordx4 a[232:235], v39, s[36:39], s56 offen offset:1024 sc1\n"
"s_waitcnt lgkmcnt(4)\n"
"v_mfma_f32_16x16x32_bf16 v[140:143], v[80:83], a[128:131], v[140:143]\n"
"ds_read_b64_tr_b16 v[64:65], v44 offset:0\n"
"v_mfma_f32_16x16x32_bf16 v[140:143], v[84:87], a[132:135], v[140:143]\n"
"v_mfma_f32_16x16x32_bf16 v[172:175], v[80:83], a[136:139], v[172:175]\n"
"ds_read_b64_tr_b16 v[66:67], v44 offset:4096\n"
"v_mfma_f32_16x16x32_bf16 v[172:175], v[84:87], a[140:143], v[172:175]\n"
"buffer_load_dwordx4 a[240:243], v39, s[36:39], s56 offen offset:2048 sc1\n"
"v_mfma_f32_16x16x32_bf16 v[204:207], v[80:83], a[144:147], v[204:207]\n"
"ds_read_b64_tr_b16 v[68:69], v44 offset:8192\n"
"v_mfma_f32_16x16x32_bf16 v[204:207], v[84:87], a[148:151], v[204:207]\n"
"v_mfma_f32_16x16x32_bf16 v[236:239], v[80:83], a[152:155], v[236:239]\n"
"ds_read_b64_tr_b16 v[70:71], v44 offset:12288\n"
"v_mfma_f32_16x16x32_bf16 v[236:239], v[84:87], a[156:159], v[236:239]\n"
"s_waitcnt lgkmcnt(4)\n"
"v_mfma_f32_16x16x32_bf16 a[12:15], v[88:91], a[128:131], a[12:15]\n"
"ds_read_b64_tr_b16 v[72:73], v16 offset:0\n"
"v_mfma_f32_16x16x32_bf16 a[12:15], v[92:95], a[132:135], a[12:15]\n"
"buffer_load_dwordx4 a[248:251], v39, s[36:39], s56 offen offset:3072 sc1\n"
"s_add_u32 s56, s56, 16384\n"
"v_mfma_f32_16x16x32_bf16 a[44:47], v[88:91], a[136:139], a[44:47]\n"
"ds_read_b64_tr_b16 v[74:75], v16 offset:4096\n"
"v_mfma_f32_16x16x32_bf16 a[44:47], v[92:95], a[140:143], a[44:47]\n"
"v_mfma_f32_16x16x32_bf16 a[76:79], v[88:91], a[144:147], a[76:79]\n"
"ds_read_b64_tr_b16 v[76:77], v16 offset:8192\n"
"v_mfma_f32_16x16x32_bf16 a[76:79], v[92:95], a[148:151], a[76:79]\n"
"v_mfma_f32_16x16x32_bf16 a[108:111], v[88:91], a[152:155], a[108:111]\n"
"ds_read_b64_tr_b16 v[78:79], v16 offset:12288\n"
"v_mfma_f32_16x16x32_bf16 a[108:111], v[92:95], a[156:159], a[108:111]\n"
"s_cbranch_vccnz SKP4\n"
"s_waitcnt lgkmcnt(4)\n"
"v_mfma_f32_16x16x32_bf16 v[144:147], v[64:67], a[128:131], v[144:147]\n"
"ds_read_b64_tr_b16 v[80:81], v45 offset:0\n"
"v_mfma_f32_16x16x32_bf16 v[144:147], v[68:71], a[132:135], v[144:147]\n"
"v_mfma_f32_16x16x32_bf16 v[176:179], v[64:67], a[136:139], v[176:179]\n"
"ds_read_b64_tr_b16 v[82:83], v45 offset:4096\n"
"v_mfma_f32_16x16x32_bf16 v[176:179], v[68:71], a[140:143], v[176:179]\n"
"v_mfma_f32_16x16x32_bf16 v[208:211], v[64:67], a[144:147], v[208:211]\n"
"ds_read_b64_tr_b16 v[84:85], v45 offset:8192\n"
"v_mfma_f32_16x16x32_bf16 v[208:211], v[68:71], a[148:151], v[208:211]\n"
"v_mfma_f32_16x16x32_bf16 v[240:243], v[64:67], a[152:155], v[240:243]\n"
"ds_read_b64_tr_b16 v[86:87], v45 offset:12288\n"
"v_mfma_f32_16x16x32_bf16 v[240:243], v[68:71], a[156:159], v[240:243]\n"
"s_waitcnt lgkmcnt(4)\n"
"v_mfma_f32_16x16x32_bf16 a[16:19], v[72:75], a[128:131], a[16:19]\n"
"ds_read_b64_tr_b16 v[88:89], v17 offset:0\n"
"v_mfma_f32_16x16x32_bf16 a[16:19], v[76:79], a[132:135], a[16:19]\n"
"v_mfma_f32_16x16x32_bf16 a[48:51], v[72:75], a[136:139], a[48:51]\n"
"ds_read_b64_tr_b16 v[90:91], v17 offset:4096\n"
"v_mfma_f32_16x16x32_bf16 a[48:51], v[76:79], a[140:143], a[48:51]\n"
"v_mfma_f32_16x16x32_bf16 a[80:83], v[72:75], a[144:147], a[80:83]\n"
"ds_read_b64_tr_b16 v[92:93], v17 offset:8192\n"
"v_mfma_f32_16x16x32_bf16 a[80:83], v[76:79], a[148:151], a[80:83]\n"
"v_mfma_f32_16x16x32_bf16 a[112:115], v[72:75], a[152:155], a[112:115]\n"
"ds_read_b64_tr_b16 v[94:95], v17 offset:12288\n"
"v_mfma_f32_16x16x32_bf16 a[112:115], v[76:79], a[156:159], a[112:115]\n"
"s_waitcnt lgkmcnt(4)\n"
"v_mfma_f32_16x16x32_bf16 v[148:151], v[80:83], a[128:131], v[148:151]\n"
"ds_read_b64_tr_b16 v[64:65], v46 offset:0\n"
"v_mfma_f32_16x16x32_bf16 v[148:151], v[84:87], a[132:135], v[148:151]\n"
"v_mfma_f32_16x16x32_bf16 v[180:183], v[80:83], a[136:139], v[180:183]\n"
"ds_read_b64_tr_b16 v[66:67], v46 offset:4096\n"
"v_mfma_f32_16x16x32_bf16 v[180:183], v[84:87], a[140:143], v[180:183]\n"
"v_mfma_f32_16x16x32_bf16 v[212:215], v[80:83], a[144:147], v[212:215]\n"
"ds_read_b64_tr_b16 v[68:69], v46 offset:8192\n"
"v_mfma_f32_16x16x32_bf16 v[212:215], v[84:87], a[148:151], v[212:215]\n"
"v_mfma_f32_16x16x32_bf16 v[244:247], v[80:83], a[152:155], v[244:247]\n"
"ds_read_b64_tr_b16 v[70:71], v46 offset:12288\n"
"v_mfma_f32_16x16x32_bf16 v[244:247], v[84:87], a[156:159], v[244:247]\n"
"s_waitcnt lgkmcnt(4)\n"
"v_mfma_f32_16x16x32_bf16 a[20:23], v[88:91], a[128:131], a[20:23]\n"
"ds_read_b64_tr_b16 v[72:73], v18 offset:0\n"
"v_mfma_f32_16x16x32_bf16 a[20:23], v[92:95], a[132:135], a[20:23]\n"
"v_mfma_f32_16x16x32_bf16 a[52:55], v[88:91], a[136:139], a[52:55]\n"
"ds_read_b64_tr_b16 v[74:75], v18 offset:4096\n"
"v_mfma_f32_16x16x32_bf16 a[52:55], v[92:95], a[140:143], a[52:55]\n"
"v_mfma_f32_16x16x32_bf16 a[84:87], v[88:91], a[144:147], a[84:87]\n"
"ds_read_b64_tr_b16 v[76:77], v18 offset:8192\n"
"v_mfma_f32_16x16x32_bf16 a[84:87], v[92:95], a[148:151], a[84:87]\n"
"v_mfma_f32_16x16x32_bf16 a[116:119], v[88:91], a[152:155], a[116:119]\n"
"ds_read_b64_tr_b16 v[78:79], v18 offset:12288\n"
"v_mfma_f32_16x16x32_bf16 a[116:119], v[92:95], a[156:159], a[116:119]\n"
"SKP4:\n"
"s_waitcnt lgkmcnt(4)\n"
"v_mfma_f32_16x16x32_bf16 v[152:155], v[64:67], a[128:131], v[152:155]\n"
"ds_read_b64_tr_b16 v[80:81], v47 offset:0\n"
"v_mfma_f32_16x16x32_bf16 v[152:155], v[68:71], a[132:135], v[152:155]\n"
"buffer_load_dwordx4 a[228:231], v39, s[36:39], s57 offen offset:0 sc1\n"
"v_mfma_f32_16x16x32_bf16 v[184:187], v[64:67], a[136:139], v[184:187]\n"
"ds_read_b64_tr_b16 v[82:83], v47 offset:4096\n"
"v_mfma_f32_16x16x32_bf16 v[184:187], v[68:71], a[140:143], v[184:187]\n"
"v_mfma_f32_16x16x32_bf16 v[216:219], v[64:67], a[144:147], v[216:219]\n"
"ds_read_b64_tr_b16 v[84:85], v47 offset:8192\n"
"v_mfma_f32_16x16x32_bf16 v[216:219], v[68:71], a[148:151], v[216:219]\n"
"buffer_load_dwordx4 a[236:239], v39, s[36:39], s57 offen offset:1024 sc1\n"
"v_mfma_f32_16x16x32_bf16 v[248:251], v[64:67], a[152:155], v[248:251]\n"
"ds_read_b64_tr_b16 v[86:87], v47 offset:12288\n"
"v_mfma_f32_16x16x32_bf16 v[248:251], v[68:71], a[156:159], v[248:251]\n"
"s_waitcnt lgkmcnt(4)\n"
"v_mfma_f32_16x16x32_bf16 a[24:27], v[72:75], a[128:131], a[24:27]\n"
"ds_read_b64_tr_b16 v[88:89], v19 offset:0\n"
"v_mfma_f32_16x16x32_bf16 a[24:27], v[76:79], a[132:135], a[24:27]\n"
"v_mfma_f32_16x16x32_bf16 a[56:59], v[72:75], a[136:139], a[56:59]\n"
"ds_read_b64_tr_b16 v[90:91], v19 offset:4096\n"
"v_mfma_f32_16x16x32_bf16 a[56:59], v[76:79], a[140:143], a[56:59]\n"
"buffer_load_dwordx4 a[244:247], v39, s[36:39], s57 offen offset:2048 sc1\n"
"v_mfma_f32_16x16x32_bf16 a[88:91], v[72:75], a[144:147], a[88:91]\n"
"ds_read_b64_tr_b16 v[92:93], v19 offset:8192\n"
"v_mfma_f32_16x16x32_bf16 a[88:91], v[76:79], a[148:151], a[88:91]\n"
"v_mfma_f32_16x16x32_bf16 a[120:123], v[72:75], a[152:155], a[120:123]\n"
"ds_read_b64_tr_b16 v[94:95], v19 offset:12288\n"
"v_mfma_f32_16x16x32_bf16 a[120:123], v[76:79], a[156:159], a[120:123]\n"
"s_waitcnt vmcnt(24)\n"
"s_barrier\n"
"s_waitcnt lgkmcnt(4)\n"
"v_mfma_f32_16x16x32_bf16 v[156:159], v[80:83], a[128:131], v[156:159]\n"
"ds_read_b64_tr_b16 v[64:65], v40 offset:16384\n"
"v_mfma_f32_16x16x32_bf16 v[156:159], v[84:87], a[132:135], v[156:159]\n"
"v_mfma_f32_16x16x32_bf16 v[188:191], v[80:83], a[136:139], v[188:191]\n"
"ds_read_b64_tr_b16 v[66:67], v40 offset:20480\n"
"v_mfma_f32_16x16x32_bf16 v[188:191], v[84:87], a[140:143], v[188:191]\n"
"buffer_load_dwordx4 a[252:255], v39, s[36:39], s57 offen offset:3072 sc1\n"
"s_add_u32 s57, s57, 16384\n"
"v_mfma_f32_16x16x32_bf16 v[220:223], v[80:83], a[144:147], v[220:223]\n"
"ds_read_b64_tr_b16 v[68:69], v40 offset:24576\n"
"v_mfma_f32_16x16x32_bf16 v[220:223], v[84:87], a[148:151], v[220:223]\n"
"v_mfma_f32_16x16x32_bf16 v[252:255], v[80:83], a[152:155], v[252:255]\n"
"ds_read_b64_tr_b16 v[70:71], v40 offset:28672\n"
"v_mfma_f32_16x16x32_bf16 v[252:255], v[84:87], a[156:159], v[252:255]\n"
"s_waitcnt lgkmcnt(4)\n"
"v_mfma_f32_16x16x32_bf16 a[28:31], v[88:91], a[128:131], a[28:31]\n"
"ds_read_b64_tr_b16 v[72:73], v12 offset:16384\n"
"v_mfma_f32_16x16x32_bf16 a[28:31], v[92:95], a[132:135], a[28:31]\n"
"global_store_dwordx4 v34, v[96:99], %0 offset:0 sc1\n"
"v_mfma_f32_16x16x32_bf16 a[60:63], v[88:91], a[136:139], a[60:63]\n"
"ds_read_b64_tr_b16 v[74:75], v12 offset:20480\n"
"v_mfma_f32_16x16x32_bf16 a[60:63], v[92:95], a[140:143], a[60:63]\n"
"v_mfma_f32_16x16x32_bf16 a[92:95], v[88:91], a[144:147], a[92:95]\n"
"ds_read_b64_tr_b16 v[76:77], v12 offset:24576\n"
"v_mfma_f32_16x16x32_bf16 a[92:95], v[92:95], a[148:151], a[92:95]\n"
"v_mfma_f32_16x16x32_bf16 a[124:127], v[88:91], a[152:155], a[124:127]\n"
"ds_read_b64_tr_b16 v[78:79], v12 offset:28672\n"
"v_mfma_f32_16x16x32_bf16 a[124:127], v[92:95], a[156:159], a[124:127]\n"
"s_waitcnt lgkmcnt(4)\n"
"v_mfma_f32_16x16x32_bf16 v[128:131], v[64:67], a[160:163], v[128:131]\n"
"ds_read_b64_tr_b16 v[80:81], v41 offset:16384\n"
"s_add_u32 m0, s59, 49152\n"
"v_mfma_f32_16x16x32_bf16 v[128:131], v[68:71], a[164:167], v[128:131]\n"
"buffer_load_dwordx4 v20, s[44:47], s62 offen offset:0 lds\n"
"v_mfma_f32_16x16x32_bf16 v[160:163], v[64:67], a[168:171], v[160:163]\n"
"ds_read_b64_tr_b16 v[82:83], v41 offset:20480\n"
"v_mfma_f32_16x16x32_bf16 v[160:163], v[68:71], a[172:175], v[160:163]\n"
"ds_read_b128 v[100:103], v8 offset:1024\n"
"v_mfma_f32_16x16x32_bf16 v[192:195], v[64:67], a[176:179], v[192:195]\n"
"ds_read_b64_tr_b16 v[84:85], v41 offset:24576\n"
"v_mfma_f32_16x16x32_bf16 v[192:195], v[68:71], a[180:183], v[192:195]\n"
"buffer_load_dwordx4 v21, s[44:47], s62 offen offset:1024 lds\n"
"v_mfma_f32_16x16x32_bf16 v[224:227], v[64:67], a[184:187], v[224:227]\n"
"ds_read_b64_tr_b16 v[86:87], v41 offset:28672\n"
"v_mfma_f32_16x16x32_bf16 v[224:227], v[68:71], a[188:191], v[224:227]\n"
"s_waitcnt lgkmcnt(5)\n"
"v_mfma_f32_16x16x32_bf16 a[0:3], v[72:75], a[160:163], a[0:3]\n"
"ds_read_b64_tr_b16 v[88:89], v13 offset:16384\n"
"v_mfma_f32_16x16x32_bf16 a[0:3], v[76:79], a[164:167], a[0:3]\n"
"v_mfma_f32_16x16x32_bf16 a[32:35], v[72:75], a[168:171], a[32:35]\n"
"ds_read_b64_tr_b16 v[90:91], v13 offset:20480\n"
"v_mfma_f32_16x16x32_bf16 a[32:35], v[76:79], a[172:175], a[32:35]\n"
"buffer_load_dwordx4 v22, s[44:47], s62 offen offset:2048 lds\n"
"v_mfma_f32_16x16x32_bf16 a[64:67], v[72:75], a[176:179], a[64:67]\n"
"ds_read_b64_tr_b16 v[92:93], v13 offset:24576\n"
"v_mfma_f32_16x16x32_bf16 a[64:67], v[76:79], a[180:183], a[64:67]\n"
"v_mfma_f32_16x16x32_bf16 a[96:99], v[72:75], a[184:187], a[96:99]\n"
"ds_read_b64_tr_b16 v[94:95], v13 offset:28672\n"
"v_mfma_f32_16x16x32_bf16 a[96:99], v[76:79], a[188:191], a[96:99]\n"
"buffer_load_dwordx4 v23, s[44:47], s62 offen offset:3072 lds\n"
"s_waitcnt lgkmcnt(4)\n"
"v_mfma_f32_16x16x32_bf16 v[132:135], v[80:83], a[160:163], v[132:135]\n"
"ds_read_b64_tr_b16 v[64:65], v42 offset:16384\n"
"v_mfma_f32_16x16x32_bf16 v[132:135], v[84:87], a[164:167], v[132:135]\n"
"v_mfma_f32_16x16x32_bf16 v[164:167], v[80:83], a[168:171], v[164:167]\n"
"ds_read_b64_tr_b16 v[66:67], v42 offset:20480\n"
"v_mfma_f32_16x16x32_bf16 v[164:167], v[84:87], a[172:175], v[164:167]\n"
"v_mfma_f32_16x16x32_bf16 v[196:199], v[80:83], a[176:179], v[196:199]\n"
"ds_read_b64_tr_b16 v[68:69], v42 offset:24576\n"
"s_add_u32 m0, s59, 114688\n"
"v_mfma_f32_16x16x32_bf16 v[196:199], v[84:87], a[180:183], v[196:199]\n"
"buffer_load_dwordx4 v20, s[48:51], s62 offen offset:0 lds\n"
"v_mfma_f32_16x16x32_bf16 v[228:231], v[80:83], a[184:187], v[228:231]\n"
"ds_read_b64_tr_b16 v[70:71], v42 offset:28672\n"
"v_mfma_f32_16x16x32_bf16 v[228:231], v[84:87], a[188:191], v[228:231]\n"
"s_waitcnt lgkmcnt(4)\n"
"v_mfma_f32_16x16x32_bf16 a[4:7], v[88:91], a[160:163], a[4:7]\n"
"ds_read_b64_tr_b16 v[72:73], v14 offset:16384\n"
"v_mfma_f32_16x16x32_bf16 a[4:7], v[92:95], a[164:167], a[4:7]\n"
"buffer_load_dwordx4 v21, s[48:51], s62 offen offset:1024 lds\n"
"v_mfma_f32_16x16x32_bf16 a[36:39], v[88:91], a[168:171], a[36:39]\n"
"ds_read_b64_tr_b16 v[74:75], v14 offset:20480\n"
"v_mfma_f32_16x16x32_bf16 a[36:39], v[92:95], a[172:175], a[36:39]\n"
"v_mfma_f32_16x16x32_bf16 a[68:71], v[88:91], a[176:179], a[68:71]\n"
"ds_read_b64_tr_b16 v[76:77], v14 offset:24576\n"
"v_mfma_f32_16x16x32_bf16 a[68:71], v[92:95], a[180:183], a[68:71]\n"
"v_mfma_f32_16x16x32_bf16 a[100:103], v[88:91], a[184:187], a[100:103]\n"
"ds_read_b64_tr_b16 v[78:79], v14 offset:28672\n"
"v_mfma_f32_16x16x32_bf16 a[100:103], v[92:95], a[188:191], a[100:103]\n"
"buffer_load_dwordx4 v22, s[48:51], s62 offen offset:2048 lds\n"
"s_waitcnt lgkmcnt(4)\n"
"v_mfma_f32_16x16x32_bf16 v[136:139], v[64:67], a[160:163], v[136:139]\n"
"ds_read_b64_tr_b16 v[80:81], v43 offset:16384\n"
"v_mfma_f32_16x16x32_bf16 v[136:139], v[68:71], a[164:167], v[136:139]\n"
"v_mfma_f32_16x16x32_bf16 v[168:171], v[64:67], a[168:171], v[168:171]\n"
"ds_read_b64_tr_b16 v[82:83], v43 offset:20480\n"
"v_mfma_f32_16x16x32_bf16 v[168:171], v[68:71], a[172:175], v[168:171]\n"
"v_mfma_f32_16x16x32_bf16 v[200:203], v[64:67], a[176:179], v[200:203]\n"
"ds_read_b64_tr_b16 v[84:85], v43 offset:24576\n"
"v_mfma_f32_16x16x32_bf16 v[200:203], v[68:71], a[180:183], v[200:203]\n"
"buffer_load_dwordx4 v23, s[48:51], s62 offen offset:3072 lds\n"
"s_add_u32 s62, s62, 458752\n"
"v_mfma_f32_16x16x32_bf16 v[232:235], v[64:67], a[184:187], v[232:235]\n"
"ds_read_b64_tr_b16 v[86:87], v43 offset:28672\n"
"v_mfma_f32_16x16x32_bf16 v[232:235], v[68:71], a[188:191], v[232:235]\n"
"s_waitcnt lgkmcnt(4)\n"
"v_mfma_f32_16x16x32_bf16 a[8:11], v[72:75], a[160:163], a[8:11]\n"
"ds_read_b64_tr_b16 v[88:89], v15 offset:16384\n"
"v_mfma_f32_16x16x32_bf16 a[8:11], v[76:79], a[164:167], a[8:11]\n"
"buffer_load_dwordx4 a[128:131], v39, s[36:39], s56 offen offset:0 sc1\n"
"v_mfma_f32_16x16x32_bf16 a[40:43], v[72:75], a[168:171], a[40:43]\n"
"ds_read_b64_tr_b16 v[90:91], v15 offset:20480\n"
"v_mfma_f32_16x16x32_bf16 a[40:43], v[76:79], a[172:175], a[40:43]\n"
"v_mfma_f32_16x16x32_bf16 a[72:75], v[72:75], a[176:179], a[72:75]\n"
"ds_read_b64_tr_b16 v[92:93], v15 offset:24576\n"
"v_mfma_f32_16x16x32_bf16 a[72:75], v[76:79], a[180:183], a[72:75]\n"
"v_mfma_f32_16x16x32_bf16 a[104:107], v[72:75], a[184:187], a[104:107]\n"
"ds_read_b64_tr_b16 v[94:95], v15 offset:28672\n"
"v_mfma_f32_16x16x32_bf16 a[104:107], v[76:79], a[188:191], a[104:107]\n"
"buffer_load_dwordx4 a[136:139], v39, s[36:39], s56 offen offset:1024 sc1\n"
"s_waitcnt lgkmcnt(4)\n"
"v_mfma_f32_16x16x32_bf16 v[140:143], v[80:83], a[160:163], v[140:143]\n"
"ds_read_b64_tr_b16 v[64:65], v44 offset:16384\n"
"v_mfma_f32_16x16x32_bf16 v[140:143], v[84:87], a[164:167], v[140:143]\n"
"v_mfma_f32_16x16x32_bf16 v[172:175], v[80:83], a[168:171], v[172:175]\n"
"ds_read_b64_tr_b16 v[66:67], v44 offset:20480\n"
"v_mfma_f32_16x16x32_bf16 v[172:175], v[84:87], a[172:175], v[172:175]\n"
"buffer_load_dwordx4 a[144:147], v39, s[36:39], s56 offen offset:2048 sc1\n"
"v_mfma_f32_16x16x32_bf16 v[204:207], v[80:83], a[176:179], v[204:207]\n"
"ds_read_b64_tr_b16 v[68:69], v44 offset:24576\n"
"v_mfma_f32_16x16x32_bf16 v[204:207], v[84:87], a[180:183], v[204:207]\n"
"v_mfma_f32_16x16x32_bf16 v[236:239], v[80:83], a[184:187], v[236:239]\n"
"ds_read_b64_tr_b16 v[70:71], v44 offset:28672\n"
"v_mfma_f32_16x16x32_bf16 v[236:239], v[84:87], a[188:191], v[236:239]\n"
"s_waitcnt lgkmcnt(4)\n"
"v_mfma_f32_16x16x32_bf16 a[12:15], v[88:91], a[160:163], a[12:15]\n"
"ds_read_b64_tr_b16 v[72:73], v16 offset:16384\n"
"v_mfma_f32_16x16x32_bf16 a[12:15], v[92:95], a[164:167], a[12:15]\n"
"buffer_load_dwordx4 a[152:155], v39, s[36:39], s56 offen offset:3072 sc1\n"
"s_add_u32 s56, s56, 16384\n"
"v_mfma_f32_16x16x32_bf16 a[44:47], v[88:91], a[168:171], a[44:47]\n"
"ds_read_b64_tr_b16 v[74:75], v16 offset:20480\n"
"v_mfma_f32_16x16x32_bf16 a[44:47], v[92:95], a[172:175], a[44:47]\n"
"v_mfma_f32_16x16x32_bf16 a[76:79], v[88:91], a[176:179], a[76:79]\n"
"ds_read_b64_tr_b16 v[76:77], v16 offset:24576\n"
"v_mfma_f32_16x16x32_bf16 a[76:79], v[92:95], a[180:183], a[76:79]\n"
"v_mfma_f32_16x16x32_bf16 a[108:111], v[88:91], a[184:187], a[108:111]\n"
"ds_read_b64_tr_b16 v[78:79], v16 offset:28672\n"
"v_mfma_f32_16x16x32_bf16 a[108:111], v[92:95], a[188:191], a[108:111]\n"
"s_cbranch_vccnz SKP5\n"
"s_waitcnt lgkmcnt(4)\n"
"v_mfma_f32_16x16x32_bf16 v[144:147], v[64:67], a[160:163], v[144:147]\n"
"ds_read_b64_tr_b16 v[80:81], v45 offset:16384\n"
"v_mfma_f32_16x16x32_bf16 v[144:147], v[68:71], a[164:167], v[144:147]\n"
"v_mfma_f32_16x16x32_bf16 v[176:179], v[64:67], a[168:171], v[176:179]\n"
"ds_read_b64_tr_b16 v[82:83], v45 offset:20480\n"
"v_mfma_f32_16x16x32_bf16 v[176:179], v[68:71], a[172:175], v[176:179]\n"
"v_mfma_f32_16x16x32_bf16 v[208:211], v[64:67], a[176:179], v[208:211]\n"
"ds_read_b64_tr_b16 v[84:85], v45 offset:24576\n"
"v_mfma_f32_16x16x32_bf16 v[208:211], v[68:71], a[180:183], v[208:211]\n"
"v_mfma_f32_16x16x32_bf16 v[240:243], v[64:67], a[184:187], v[240:243]\n"
"ds_read_b64_tr_b16 v[86:87], v45 offset:28672\n"
"v_mfma_f32_16x16x32_bf16 v[240:243], v[68:71], a[188:191], v[240:243]\n"
"s_waitcnt lgkmcnt(4)\n"
"v_mfma_f32_16x16x32_bf16 a[16:19], v[72:75], a[160:163], a[16:19]\n"
"ds_read_b64_tr_b16 v[88:89], v17 offset:16384\n"
"v_mfma_f32_16x16x32_bf16 a[16:19], v[76:79], a[164:167], a[16:19]\n"
"v_mfma_f32_16x16x32_bf16 a[48:51], v[72:75], a[168:171], a[48:51]\n"
"ds_read_b64_tr_b16 v[90:91], v17 offset:20480\n"
"v_mfma_f32_16x16x32_bf16 a[48:51], v[76:79], a[172:175], a[48:51]\n"
"v_mfma_f32_16x16x32_bf16 a[80:83], v[72:75], a[176:179], a[80:83]\n"
"ds_read_b64_tr_b16 v[92:93], v17 offset:24576\n"
"v_mfma_f32_16x16x32_bf16 a[80:83], v[76:79], a[180:183], a[80:83]\n"
"v_mfma_f32_16x16x32_bf16 a[112:115], v[72:75], a[184:187], a[112:115]\n"
"ds_read_b64_tr_b16 v[94:95], v17 offset:28672\n"
"v_mfma_f32_16x16x32_bf16 a[112:115], v[76:79], a[188:191], a[112:115]\n"
"s_waitcnt lgkmcnt(4)\n"
"v_mfma_f32_16x16x32_bf16 v[148:151], v[80:83], a[160:163], v[148:151]\n"
"ds_read_b64_tr_b16 v[64:65], v46 offset:16384\n"
"v_mfma_f32_16x16x32_bf16 v[148:151], v[84:87], a[164:167], v[148:151]\n"
"v_mfma_f32_16x16x32_bf16 v[180:183], v[80:83], a[168:171], v[180:183]\n"
"ds_read_b64_tr_b16 v[66:67], v46 offset:20480\n"
"v_mfma_f32_16x16x32_bf16 v[180:183], v[84:87], a[172:175], v[180:183]\n"
"v_mfma_f32_16x16x32_bf16 v[212:215], v[80:83], a[176:179], v[212:215]\n"
"ds_read_b64_tr_b16 v[68:69], v46 offset:24576\n"
"v_mfma_f32_16x16x32_bf16 v[212:215], v[84:87], a[180:183], v[212:215]\n"
"v_mfma_f32_16x16x32_bf16 v[244:247], v[80:83], a[184:187], v[244:247]\n"
"ds_read_b64_tr_b16 v[70:71], v46 offset:28672\n"
"v_mfma_f32_16x16x32_bf16 v[244:247], v[84:87], a[188:191], v[244:247]\n"
"s_waitcnt lgkmcnt(4)\n"
"v_mfma_f32_16x16x32_bf16 a[20:23], v[88:91], a[160:163], a[20:23]\n"
"ds_read_b64_tr_b16 v[72:73], v18 offset:16384\n"
"v_mfma_f32_16x16x32_bf16 a[20:23], v[92:95], a[164:167], a[20:23]\n"
"v_mfma_f32_16x16x32_bf16 a[52:55], v[88:91], a[168:171], a[52:55]\n"
"ds_read_b64_tr_b16 v[74:75], v18 offset:20480\n"
"v_mfma_f32_16x16x32_bf16 a[52:55], v[92:95], a[172:175], a[52:55]\n"
"v_mfma_f32_16x16x32_bf16 a[84:87], v[88:91], a[176:179], a[84:87]\n"
"ds_read_b64_tr_b16 v[76:77], v18 offset:24576\n"
"v_mfma_f32_16x16x32_bf16 a[84:87], v[92:95], a[180:183], a[84:87]\n"
"v_mfma_f32_16x16x32_bf16 a[116:119], v[88:91], a[184:187], a[116:119]\n"
"ds_read_b64_tr_b16 v[78:79], v18 offset:28672\n"
"v_mfma_f32_16x16x32_bf16 a[116:119], v[92:95], a[188:191], a[116:119]\n"
"SKP5:\n"
"s_waitcnt lgkmcnt(4)\n"
"v_mfma_f32_16x16x32_bf16 v[152:155], v[64:67], a[160:163], v[152:155]\n"
"ds_read_b64_tr_b16 v[80:81], v47 offset:16384\n"
"v_mfma_f32_16x16x32_bf16 v[152:155], v[68:71], a[164:167], v[152:155]\n"
"buffer_load_dwordx4 a[132:135], v39, s[36:39], s57 offen offset:0 sc1\n"
"v_mfma_f32_16x16x32_bf16 v[184:187], v[64:67], a[168:171], v[184:187]\n"
"ds_read_b64_tr_b16 v[82:83], v47 offset:20480\n"
"v_mfma_f32_16x16x32_bf16 v[184:187], v[68:71], a[172:175], v[184:187]\n"
"v_mfma_f32_16x16x32_bf16 v[216:219], v[64:67], a[176:179], v[216:219]\n"
"ds_read_b64_tr_b16 v[84:85], v47 offset:24576\n"
"v_mfma_f32_16x16x32_bf16 v[216:219], v[68:71], a[180:183], v[216:219]\n"
"buffer_load_dwordx4 a[140:143], v39, s[36:39], s57 offen offset:1024 sc1\n"
"v_mfma_f32_16x16x32_bf16 v[248:251], v[64:67], a[184:187], v[248:251]\n"
"ds_read_b64_tr_b16 v[86:87], v47 offset:28672\n"
"v_mfma_f32_16x16x32_bf16 v[248:251], v[68:71], a[188:191], v[248:251]\n"
"s_waitcnt lgkmcnt(4)\n"
"v_mfma_f32_16x16x32_bf16 a[24:27], v[72:75], a[160:163], a[24:27]\n"
"ds_read_b64_tr_b16 v[88:89], v19 offset:16384\n"
"v_mfma_f32_16x16x32_bf16 a[24:27], v[76:79], a[164:167], a[24:27]\n"
"v_mfma_f32_16x16x32_bf16 a[56:59], v[72:75], a[168:171], a[56:59]\n"
"ds_read_b64_tr_b16 v[90:91], v19 offset:20480\n"
"v_mfma_f32_16x16x32_bf16 a[56:59], v[76:79], a[172:175], a[56:59]\n"
"buffer_load_dwordx4 a[148:151], v39, s[36:39], s57 offen offset:2048 sc1\n"
"v_mfma_f32_16x16x32_bf16 a[88:91], v[72:75], a[176:179], a[88:91]\n"
"ds_read_b64_tr_b16 v[92:93], v19 offset:24576\n"
"v_mfma_f32_16x16x32_bf16 a[88:91], v[76:79], a[180:183], a[88:91]\n"
"v_mfma_f32_16x16x32_bf16 a[120:123], v[72:75], a[184:187], a[120:123]\n"
"ds_read_b64_tr_b16 v[94:95], v19 offset:28672\n"
"v_mfma_f32_16x16x32_bf16 a[120:123], v[76:79], a[188:191], a[120:123]\n"
"s_waitcnt vmcnt(24)\n"
"s_barrier\n"
"s_waitcnt lgkmcnt(4)\n"
"v_mfma_f32_16x16x32_bf16 v[156:159], v[80:83], a[160:163], v[156:159]\n"
"ds_read_b64_tr_b16 v[64:65], v40 offset:32768\n"
"v_mfma_f32_16x16x32_bf16 v[156:159], v[84:87], a[164:167], v[156:159]\n"
"v_mfma_f32_16x16x32_bf16 v[188:191], v[80:83], a[168:171], v[188:191]\n"
"ds_read_b64_tr_b16 v[66:67], v40 offset:36864\n"
"v_mfma_f32_16x16x32_bf16 v[188:191], v[84:87], a[172:175], v[188:191]\n"
"buffer_load_dwordx4 a[156:159], v39, s[36:39], s57 offen offset:3072 sc1\n"
"s_add_u32 s57, s57, 16384\n"
"v_mfma_f32_16x16x32_bf16 v[220:223], v[80:83], a[176:179], v[220:223]\n"
"ds_read_b64_tr_b16 v[68:69], v40 offset:40960\n"
"v_mfma_f32_16x16x32_bf16 v[220:223], v[84:87], a[180:183], v[220:223]\n"
"v_mfma_f32_16x16x32_bf16 v[252:255], v[80:83], a[184:187], v[252:255]\n"
"ds_read_b64_tr_b16 v[70:71], v40 offset:45056\n"
"v_mfma_f32_16x16x32_bf16 v[252:255], v[84:87], a[188:191], v[252:255]\n"
"s_waitcnt lgkmcnt(4)\n"
"v_mfma_f32_16x16x32_bf16 a[28:31], v[88:91], a[160:163], a[28:31]\n"
"ds_read_b64_tr_b16 v[72:73], v12 offset:32768\n"
"v_mfma_f32_16x16x32_bf16 a[28:31], v[92:95], a[164:167], a[28:31]\n"
"global_store_dwordx4 v34, v[100:103], %0 offset:1024 sc1\n"
"v_mfma_f32_16x16x32_bf16 a[60:63], v[88:91], a[168:171], a[60:63]\n"
"ds_read_b64_tr_b16 v[74:75], v12 offset:36864\n"
"v_mfma_f32_16x16x32_bf16 a[60:63], v[92:95], a[172:175], a[60:63]\n"
"v_mfma_f32_16x16x32_bf16 a[92:95], v[88:91], a[176:179], a[92:95]\n"
"ds_read_b64_tr_b16 v[76:77], v12 offset:40960\n"
"v_mfma_f32_16x16x32_bf16 a[92:95], v[92:95], a[180:183], a[92:95]\n"
"v_mfma_f32_16x16x32_bf16 a[124:127], v[88:91], a[184:187], a[124:127]\n"
"ds_read_b64_tr_b16 v[78:79], v12 offset:45056\n"
"v_mfma_f32_16x16x32_bf16 a[124:127], v[92:95], a[188:191], a[124:127]\n"
"s_waitcnt lgkmcnt(4)\n"
"v_mfma_f32_16x16x32_bf16 v[128:131], v[64:67], a[192:195], v[128:131]\n"
"ds_read_b64_tr_b16 v[80:81], v41 offset:32768\n"
"s_add_u32 m0, s59, 0\n"
"v_mfma_f32_16x16x32_bf16 v[128:131], v[68:71], a[196:199], v[128:131]\n"
"buffer_load_dwordx4 v20, s[44:47], s62 offen offset:0 lds\n"
"v_mfma_f32_16x16x32_bf16 v[160:163], v[64:67], a[200:203], v[160:163]\n"
"ds_read_b64_tr_b16 v[82:83], v41 offset:36864\n"
"v_mfma_f32_16x16x32_bf16 v[160:163], v[68:71], a[204:207], v[160:163]\n"
"ds_read_b128 v[104:107], v8 offset:2048\n"
"v_mfma_f32_16x16x32_bf16 v[192:195], v[64:67], a[208:211], v[192:195]\n"
"ds_read_b64_tr_b16 v[84:85], v41 offset:40960\n"
"v_mfma_f32_16x16x32_bf16 v[192:195], v[68:71], a[212:215], v[192:195]\n"
"buffer_load_dwordx4 v21, s[44:47], s62 offen offset:1024 lds\n"
"v_mfma_f32_16x16x32_bf16 v[224:227], v[64:67], a[216:219], v[224:227]\n"
"ds_read_b64_tr_b16 v[86:87], v41 offset:45056\n"
"v_mfma_f32_16x16x32_bf16 v[224:227], v[68:71], a[220:223], v[224:227]\n"
"s_waitcnt lgkmcnt(5)\n"
"v_mfma_f32_16x16x32_bf16 a[0:3], v[72:75], a[192:195], a[0:3]\n"
"ds_read_b64_tr_b16 v[88:89], v13 offset:32768\n"
"v_mfma_f32_16x16x32_bf16 a[0:3], v[76:79], a[196:199], a[0:3]\n"
"v_mfma_f32_16x16x32_bf16 a[32:35], v[72:75], a[200:203], a[32:35]\n"
"ds_read_b64_tr_b16 v[90:91], v13 offset:36864\n"
"v_mfma_f32_16x16x32_bf16 a[32:35], v[76:79], a[204:207], a[32:35]\n"
"buffer_load_dwordx4 v22, s[44:47], s62 offen offset:2048 lds\n"
"v_mfma_f32_16x16x32_bf16 a[64:67], v[72:75], a[208:211], a[64:67]\n"
"ds_read_b64_tr_b16 v[92:93], v13 offset:40960\n"
"v_mfma_f32_16x16x32_bf16 a[64:67], v[76:79], a[212:215], a[64:67]\n"
"v_mfma_f32_16x16x32_bf16 a[96:99], v[72:75], a[216:219], a[96:99]\n"
"ds_read_b64_tr_b16 v[94:95], v13 offset:45056\n"
"v_mfma_f32_16x16x32_bf16 a[96:99], v[76:79], a[220:223], a[96:99]\n"
"buffer_load_dwordx4 v23, s[44:47], s62 offen offset:3072 lds\n"
"s_waitcnt lgkmcnt(4)\n"
"v_mfma_f32_16x16x32_bf16 v[132:135], v[80:83], a[192:195], v[132:135]\n"
"ds_read_b64_tr_b16 v[64:65], v42 offset:32768\n"
"v_mfma_f32_16x16x32_bf16 v[132:135], v[84:87], a[196:199], v[132:135]\n"
"v_mfma_f32_16x16x32_bf16 v[164:167], v[80:83], a[200:203], v[164:167]\n"
"ds_read_b64_tr_b16 v[66:67], v42 offset:36864\n"
"v_mfma_f32_16x16x32_bf16 v[164:167], v[84:87], a[204:207], v[164:167]\n"
"v_mfma_f32_16x16x32_bf16 v[196:199], v[80:83], a[208:211], v[196:199]\n"
"ds_read_b64_tr_b16 v[68:69], v42 offset:40960\n"
"s_add_u32 m0, s59, 65536\n"
"v_mfma_f32_16x16x32_bf16 v[196:199], v[84:87], a[212:215], v[196:199]\n"
"buffer_load_dwordx4 v20, s[48:51], s62 offen offset:0 lds\n"
"v_mfma_f32_16x16x32_bf16 v[228:231], v[80:83], a[216:219], v[228:231]\n"
"ds_read_b64_tr_b16 v[70:71], v42 offset:45056\n"
"v_mfma_f32_16x16x32_bf16 v[228:231], v[84:87], a[220:223], v[228:231]\n"
"s_waitcnt lgkmcnt(4)\n"
"v_mfma_f32_16x16x32_bf16 a[4:7], v[88:91], a[192:195], a[4:7]\n"
"ds_read_b64_tr_b16 v[72:73], v14 offset:32768\n"
"v_mfma_f32_16x16x32_bf16 a[4:7], v[92:95], a[196:199], a[4:7]\n"
"buffer_load_dwordx4 v21, s[48:51], s62 offen offset:1024 lds\n"
"v_mfma_f32_16x16x32_bf16 a[36:39], v[88:91], a[200:203], a[36:39]\n"
"ds_read_b64_tr_b16 v[74:75], v14 offset:36864\n"
"v_mfma_f32_16x16x32_bf16 a[36:39], v[92:95], a[204:207], a[36:39]\n"
"v_mfma_f32_16x16x32_bf16 a[68:71], v[88:91], a[208:211], a[68:71]\n"
"ds_read_b64_tr_b16 v[76:77], v14 offset:40960\n"
"v_mfma_f32_16x16x32_bf16 a[68:71], v[92:95], a[212:215], a[68:71]\n"
"v_mfma_f32_16x16x32_bf16 a[100:103], v[88:91], a[216:219], a[100:103]\n"
"ds_read_b64_tr_b16 v[78:79], v14 offset:45056\n"
"v_mfma_f32_16x16x32_bf16 a[100:103], v[92:95], a[220:223], a[100:103]\n"
"buffer_load_dwordx4 v22, s[48:51], s62 offen offset:2048 lds\n"
"s_waitcnt lgkmcnt(4)\n"
"v_mfma_f32_16x16x32_bf16 v[136:139], v[64:67], a[192:195], v[136:139]\n"
"ds_read_b64_tr_b16 v[80:81], v43 offset:32768\n"
"v_mfma_f32_16x16x32_bf16 v[136:139], v[68:71], a[196:199], v[136:139]\n"
"v_mfma_f32_16x16x32_bf16 v[168:171], v[64:67], a[200:203], v[168:171]\n"
"ds_read_b64_tr_b16 v[82:83], v43 offset:36864\n"
"v_mfma_f32_16x16x32_bf16 v[168:171], v[68:71], a[204:207], v[168:171]\n"
"v_mfma_f32_16x16x32_bf16 v[200:203], v[64:67], a[208:211], v[200:203]\n"
"ds_read_b64_tr_b16 v[84:85], v43 offset:40960\n"
"v_mfma_f32_16x16x32_bf16 v[200:203], v[68:71], a[212:215], v[200:203]\n"
"buffer_load_dwordx4 v23, s[48:51], s62 offen offset:3072 lds\n"
"s_add_u32 s62, s62, 458752\n"
"v_mfma_f32_16x16x32_bf16 v[232:235], v[64:67], a[216:219], v[232:235]\n"
"ds_read_b64_tr_b16 v[86:87], v43 offset:45056\n"
"v_mfma_f32_16x16x32_bf16 v[232:235], v[68:71], a[220:223], v[232:235]\n"
"s_waitcnt lgkmcnt(4)\n"
"v_mfma_f32_16x16x32_bf16 a[8:11], v[72:75], a[192:195], a[8:11]\n"
"ds_read_b64_tr_b16 v[88:89], v15 offset:32768\n"
"v_mfma_f32_16x16x32_bf16 a[8:11], v[76:79], a[196:199], a[8:11]\n"
"buffer_load_dwordx4 a[160:163], v39, s[36:39], s56 offen offset:0 sc1\n"
"v_mfma_f32_16x16x32_bf16 a[40:43], v[72:75], a[200:203], a[40:43]\n"
"ds_read_b64_tr_b16 v[90:91], v15 offset:36864\n"
"v_mfma_f32_16x16x32_bf16 a[40:43], v[76:79], a[204:207], a[40:43]\n"
"v_mfma_f32_16x16x32_bf16 a[72:75], v[72:75], a[208:211], a[72:75]\n"
"ds_read_b64_tr_b16 v[92:93], v15 offset:40960\n"
"v_mfma_f32_16x16x32_bf16 a[72:75], v[76:79], a[212:215], a[72:75]\n"
"v_mfma_f32_16x16x32_bf16 a[104:107], v[72:75], a[216:219], a[104:107]\n"
"ds_read_b64_tr_b16 v[94:95], v15 offset:45056\n"
"v_mfma_f32_16x16x32_bf16 a[104:107], v[76:79], a[220:223], a[104:107]\n"
"buffer_load_dwordx4 a[168:171], v39, s[36:39], s56 offen offset:1024 sc1\n"
"s_waitcnt lgkmcnt(4)\n"
"v_mfma_f32_16x16x32_bf16 v[140:143], v[80:83], a[192:195], v[140:143]\n"
"ds_read_b64_tr_b16 v[64:65], v44 offset:32768\n"
"v_mfma_f32_16x16x32_bf16 v[140:143], v[84:87], a[196:199], v[140:143]\n"
"v_mfma_f32_16x16x32_bf16 v[172:175], v[80:83], a[200:203], v[172:175]\n"
"ds_read_b64_tr_b16 v[66:67], v44 offset:36864\n"
"v_mfma_f32_16x16x32_bf16 v[172:175], v[84:87], a[204:207], v[172:175]\n"
"buffer_load_dwordx4 a[176:179], v39, s[36:39], s56 offen offset:2048 sc1\n"
"v_mfma_f32_16x16x32_bf16 v[204:207], v[80:83], a[208:211], v[204:207]\n"
"ds_read_b64_tr_b16 v[68:69], v44 offset:40960\n"
"v_mfma_f32_16x16x32_bf16 v[204:207], v[84:87], a[212:215], v[204:207]\n"
"v_mfma_f32_16x16x32_bf16 v[236:239], v[80:83], a[216:219], v[236:239]\n"
"ds_read_b64_tr_b16 v[70:71], v44 offset:45056\n"
"v_mfma_f32_16x16x32_bf16 v[236:239], v[84:87], a[220:223], v[236:239]\n"
"s_waitcnt lgkmcnt(4)\n"
"v_mfma_f32_16x16x32_bf16 a[12:15], v[88:91], a[192:195], a[12:15]\n"
"ds_read_b64_tr_b16 v[72:73], v16 offset:32768\n"
"v_mfma_f32_16x16x32_bf16 a[12:15], v[92:95], a[196:199], a[12:15]\n"
"buffer_load_dwordx4 a[184:187], v39, s[36:39], s56 offen offset:3072 sc1\n"
"s_add_u32 s56, s56, 16384\n"
"v_mfma_f32_16x16x32_bf16 a[44:47], v[88:91], a[200:203], a[44:47]\n"
"ds_read_b64_tr_b16 v[74:75], v16 offset:36864\n"
"v_mfma_f32_16x16x32_bf16 a[44:47], v[92:95], a[204:207], a[44:47]\n"
"v_mfma_f32_16x16x32_bf16 a[76:79], v[88:91], a[208:211], a[76:79]\n"
"ds_read_b64_tr_b16 v[76:77], v16 offset:40960\n"
"v_mfma_f32_16x16x32_bf16 a[76:79], v[92:95], a[212:215], a[76:79]\n"
"v_mfma_f32_16x16x32_bf16 a[108:111], v[88:91], a[216:219], a[108:111]\n"
"ds_read_b64_tr_b16 v[78:79], v16 offset:45056\n"
"v_mfma_f32_16x16x32_bf16 a[108:111], v[92:95], a[220:223], a[108:111]\n"
"s_cbranch_vccnz SKP6\n"
"s_waitcnt lgkmcnt(4)\n"
"v_mfma_f32_16x16x32_bf16 v[144:147], v[64:67], a[192:195], v[144:147]\n"
"ds_read_b64_tr_b16 v[80:81], v45 offset:32768\n"
"v_mfma_f32_16x16x32_bf16 v[144:147], v[68:71], a[196:199], v[144:147]\n"
"v_mfma_f32_16x16x32_bf16 v[176:179], v[64:67], a[200:203], v[176:179]\n"
"ds_read_b64_tr_b16 v[82:83], v45 offset:36864\n"
"v_mfma_f32_16x16x32_bf16 v[176:179], v[68:71], a[204:207], v[176:179]\n"
"v_mfma_f32_16x16x32_bf16 v[208:211], v[64:67], a[208:211], v[208:211]\n"
"ds_read_b64_tr_b16 v[84:85], v45 offset:40960\n"
"v_mfma_f32_16x16x32_bf16 v[208:211], v[68:71], a[212:215], v[208:211]\n"
"v_mfma_f32_16x16x32_bf16 v[240:243], v[64:67], a[216:219], v[240:243]\n"
"ds_read_b64_tr_b16 v[86:87], v45 offset:45056\n"
"v_mfma_f32_16x16x32_bf16 v[240:243], v[68:71], a[220:223], v[240:243]\n"
"s_waitcnt lgkmcnt(4)\n"
"v_mfma_f32_16x16x32_bf16 a[16:19], v[72:75], a[192:195], a[16:19]\n"
"ds_read_b64_tr_b16 v[88:89], v17 offset:32768\n"
"v_mfma_f32_16x16x32_bf16 a[16:19], v[76:79], a[196:199], a[16:19]\n"
"v_mfma_f32_16x16x32_bf16 a[48:51], v[72:75], a[200:203], a[48:51]\n"
"ds_read_b64_tr_b16 v[90:91], v17 offset:36864\n"
"v_mfma_f32_16x16x32_bf16 a[48:51], v[76:79], a[204:207], a[48:51]\n"
"v_mfma_f32_16x16x32_bf16 a[80:83], v[72:75], a[208:211], a[80:83]\n"
"ds_read_b64_tr_b16 v[92:93], v17 offset:40960\n"
"v_mfma_f32_16x16x32_bf16 a[80:83], v[76:79], a[212:215], a[80:83]\n"
"v_mfma_f32_16x16x32_bf16 a[112:115], v[72:75], a[216:219], a[112:115]\n"
"ds_read_b64_tr_b16 v[94:95], v17 offset:45056\n"
"v_mfma_f32_16x16x32_bf16 a[112:115], v[76:79], a[220:223], a[112:115]\n"
"s_waitcnt lgkmcnt(4)\n"
"v_mfma_f32_16x16x32_bf16 v[148:151], v[80:83], a[192:195], v[148:151]\n"
"ds_read_b64_tr_b16 v[64:65], v46 offset:32768\n"
"v_mfma_f32_16x16x32_bf16 v[148:151], v[84:87], a[196:199], v[148:151]\n"
"v_mfma_f32_16x16x32_bf16 v[180:183], v[80:83], a[200:203], v[180:183]\n"
"ds_read_b64_tr_b16 v[66:67], v46 offset:36864\n"
"v_mfma_f32_16x16x32_bf16 v[180:183], v[84:87], a[204:207], v[180:183]\n"
"v_mfma_f32_16x16x32_bf16 v[212:215], v[80:83], a[208:211], v[212:215]\n"
"ds_read_b64_tr_b16 v[68:69], v46 offset:40960\n"
"v_mfma_f32_16x16x32_bf16 v[212:215], v[84:87], a[212:215], v[212:215]\n"
"v_mfma_f32_16x16x32_bf16 v[244:247], v[80:83], a[216:219], v[244:247]\n"
"ds_read_b64_tr_b16 v[70:71], v46 offset:45056\n"
"v_mfma_f32_16x16x32_bf16 v[244:247], v[84:87], a[220:223], v[244:247]\n"
"s_waitcnt lgkmcnt(4)\n"
"v_mfma_f32_16x16x32_bf16 a[20:23], v[88:91], a[192:195], a[20:23]\n"
"ds_read_b64_tr_b16 v[72:73], v18 offset:32768\n"
"v_mfma_f32_16x16x32_bf16 a[20:23], v[92:95], a[196:199], a[20:23]\n"
"v_mfma_f32_16x16x32_bf16 a[52:55], v[88:91], a[200:203], a[52:55]\n"
"ds_read_b64_tr_b16 v[74:75], v18 offset:36864\n"
"v_mfma_f32_16x16x32_bf16 a[52:55], v[92:95], a[204:207], a[52:55]\n"
"v_mfma_f32_16x16x32_bf16 a[84:87], v[88:91], a[208:211], a[84:87]\n"
"ds_read_b64_tr_b16 v[76:77], v18 offset:40960\n"
"v_mfma_f32_16x16x32_bf16 a[84:87], v[92:95], a[212:215], a[84:87]\n"
"v_mfma_f32_16x16x32_bf16 a[116:119], v[88:91], a[216:219], a[116:119]\n"
"ds_read_b64_tr_b16 v[78:79], v18 offset:45056\n"
"v_mfma_f32_16x16x32_bf16 a[116:119], v[92:95], a[220:223], a[116:119]\n"
"SKP6:\n"
"s_waitcnt lgkmcnt(4)\n"
"v_mfma_f32_16x16x32_bf16 v[152:155], v[64:67], a[192:195], v[152:155]\n"
"ds_read_b64_tr_b16 v[80:81], v47 offset:32768\n"
"v_mfma_f32_16x16x32_bf16 v[152:155], v[68:71], a[196:199], v[152:155]\n"
"buffer_load_dwordx4 a[164:167], v39, s[36:39], s57 offen offset:0 sc1\n"
"v_mfma_f32_16x16x32_bf16 v[184:187], v[64:67], a[200:203], v[184:187]\n"
"ds_read_b64_tr_b16 v[82:83], v47 offset:36864\n"
"v_mfma_f32_16x16x32_bf16 v[184:187], v[68:71], a[204:207], v[184:187]\n"
"v_mfma_f32_16x16x32_bf16 v[216:219], v[64:67], a[208:211], v[216:219]\n"
"ds_read_b64_tr_b16 v[84:85], v47 offset:40960\n"
"v_mfma_f32_16x16x32_bf16 v[216:219], v[68:71], a[212:215], v[216:219]\n"
"buffer_load_dwordx4 a[172:175], v39, s[36:39], s57 offen offset:1024 sc1\n"
"v_mfma_f32_16x16x32_bf16 v[248:251], v[64:67], a[216:219], v[248:251]\n"
"ds_read_b64_tr_b16 v[86:87], v47 offset:45056\n"
"v_mfma_f32_16x16x32_bf16 v[248:251], v[68:71], a[220:223], v[248:251]\n"
"s_waitcnt lgkmcnt(4)\n"
"v_mfma_f32_16x16x32_bf16 a[24:27], v[72:75], a[192:195], a[24:27]\n"
"ds_read_b64_tr_b16 v[88:89], v19 offset:32768\n"
"v_mfma_f32_16x16x32_bf16 a[24:27], v[76:79], a[196:199], a[24:27]\n"
"v_mfma_f32_16x16x32_bf16 a[56:59], v[72:75], a[200:203], a[56:59]\n"
"ds_read_b64_tr_b16 v[90:91], v19 offset:36864\n"
"v_mfma_f32_16x16x32_bf16 a[56:59], v[76:79], a[204:207], a[56:59]\n"
"buffer_load_dwordx4 a[180:183], v39, s[36:39], s57 offen offset:2048 sc1\n"
"v_mfma_f32_16x16x32_bf16 a[88:91], v[72:75], a[208:211], a[88:91]\n"
"ds_read_b64_tr_b16 v[92:93], v19 offset:40960\n"
"v_mfma_f32_16x16x32_bf16 a[88:91], v[76:79], a[212:215], a[88:91]\n"
"v_mfma_f32_16x16x32_bf16 a[120:123], v[72:75], a[216:219], a[120:123]\n"
"ds_read_b64_tr_b16 v[94:95], v19 offset:45056\n"
"v_mfma_f32_16x16x32_bf16 a[120:123], v[76:79], a[220:223], a[120:123]\n"
"s_waitcnt vmcnt(24)\n"
"s_barrier\n"
"s_waitcnt lgkmcnt(4)\n"
"v_mfma_f32_16x16x32_bf16 v[156:159], v[80:83], a[192:195], v[156:159]\n"
"ds_read_b64_tr_b16 v[64:65], v40 offset:49152\n"
"v_mfma_f32_16x16x32_bf16 v[156:159], v[84:87], a[196:199], v[156:159]\n"
"v_mfma_f32_16x16x32_bf16 v[188:191], v[80:83], a[200:203], v[188:191]\n"
"ds_read_b64_tr_b16 v[66:67], v40 offset:53248\n"
"v_mfma_f32_16x16x32_bf16 v[188:191], v[84:87], a[204:207], v[188:191]\n"
"buffer_load_dwordx4 a[188:191], v39, s[36:39], s57 offen offset:3072 sc1\n"
"s_add_u32 s57, s57, 16384\n"
"v_mfma_f32_16x16x32_bf16 v[220:223], v[80:83], a[208:211], v[220:223]\n"
"ds_read_b64_tr_b16 v[68:69], v40 offset:57344\n"
"v_mfma_f32_16x16x32_bf16 v[220:223], v[84:87], a[212:215], v[220:223]\n"
"v_mfma_f32_16x16x32_bf16 v[252:255], v[80:83], a[216:219], v[252:255]\n"
"ds_read_b64_tr_b16 v[70:71], v40 offset:61440\n"
"v_mfma_f32_16x16x32_bf16 v[252:255], v[84:87], a[220:223], v[252:255]\n"
"s_waitcnt lgkmcnt(4)\n"
"v_mfma_f32_16x16x32_bf16 a[28:31], v[88:91], a[192:195], a[28:31]\n"
"ds_read_b64_tr_b16 v[72:73], v12 offset:49152\n"
"v_mfma_f32_16x16x32_bf16 a[28:31], v[92:95], a[196:199], a[28:31]\n"
"global_store_dwordx4 v34, v[104:107], %0 offset:2048 sc1\n"
"v_mfma_f32_16x16x32_bf16 a[60:63], v[88:91], a[200:203], a[60:63]\n"
"ds_read_b64_tr_b16 v[74:75], v12 offset:53248\n"
"v_mfma_f32_16x16x32_bf16 a[60:63], v[92:95], a[204:207], a[60:63]\n"
"v_mfma_f32_16x16x32_bf16 a[92:95], v[88:91], a[208:211], a[92:95]\n"
"ds_read_b64_tr_b16 v[76:77], v12 offset:57344\n"
"v_mfma_f32_16x16x32_bf16 a[92:95], v[92:95], a[212:215], a[92:95]\n"
"v_mfma_f32_16x16x32_bf16 a[124:127], v[88:91], a[216:219], a[124:127]\n"
"ds_read_b64_tr_b16 v[78:79], v12 offset:61440\n"
"v_mfma_f32_16x16x32_bf16 a[124:127], v[92:95], a[220:223], a[124:127]\n"
"s_waitcnt lgkmcnt(4)\n"
"v_mfma_f32_16x16x32_bf16 v[128:131], v[64:67], a[224:227], v[128:131]\n"
"ds_read_b64_tr_b16 v[80:81], v41 offset:49152\n"
"s_add_u32 m0, s59, 16384\n"
"v_mfma_f32_16x16x32_bf16 v[128:131], v[68:71], a[228:231], v[128:131]\n"
"buffer_load_dwordx4 v20, s[44:47], s62 offen offset:0 lds\n"
"v_mfma_f32_16x16x32_bf16 v[160:163], v[64:67], a[232:235], v[160:163]\n"
"ds_read_b64_tr_b16 v[82:83], v41 offset:53248\n"
"v_mfma_f32_16x16x32_bf16 v[160:163], v[68:71], a[236:239], v[160:163]\n"
"ds_read_b128 v[108:111], v8 offset:3072\n"
"v_mfma_f32_16x16x32_bf16 v[192:195], v[64:67], a[240:243], v[192:195]\n"
"ds_read_b64_tr_b16 v[84:85], v41 offset:57344\n"
"v_mfma_f32_16x16x32_bf16 v[192:195], v[68:71], a[244:247], v[192:195]\n"
"buffer_load_dwordx4 v21, s[44:47], s62 offen offset:1024 lds\n"
"v_mfma_f32_16x16x32_bf16 v[224:227], v[64:67], a[248:251], v[224:227]\n"
"ds_read_b64_tr_b16 v[86:87], v41 offset:61440\n"
"v_mfma_f32_16x16x32_bf16 v[224:227], v[68:71], a[252:255], v[224:227]\n"
"s_waitcnt lgkmcnt(5)\n"
"v_mfma_f32_16x16x32_bf16 a[0:3], v[72:75], a[224:227], a[0:3]\n"
"ds_read_b64_tr_b16 v[88:89], v13 offset:49152\n"
"v_mfma_f32_16x16x32_bf16 a[0:3], v[76:79], a[228:231], a[0:3]\n"
"v_mfma_f32_16x16x32_bf16 a[32:35], v[72:75], a[232:235], a[32:35]\n"
"ds_read_b64_tr_b16 v[90:91], v13 offset:53248\n"
"v_mfma_f32_16x16x32_bf16 a[32:35], v[76:79], a[236:239], a[32:35]\n"
"buffer_load_dwordx4 v22, s[44:47], s62 offen offset:2048 lds\n"
"v_mfma_f32_16x16x32_bf16 a[64:67], v[72:75], a[240:243], a[64:67]\n"
"ds_read_b64_tr_b16 v[92:93], v13 offset:57344\n"
"v_mfma_f32_16x16x32_bf16 a[64:67], v[76:79], a[244:247], a[64:67]\n"
"v_mfma_f32_16x16x32_bf16 a[96:99], v[72:75], a[248:251], a[96:99]\n"
"ds_read_b64_tr_b16 v[94:95], v13 offset:61440\n"
"v_mfma_f32_16x16x32_bf16 a[96:99], v[76:79], a[252:255], a[96:99]\n"
"buffer_load_dwordx4 v23, s[44:47], s62 offen offset:3072 lds\n"
"s_waitcnt lgkmcnt(4)\n"
"v_mfma_f32_16x16x32_bf16 v[132:135], v[80:83], a[224:227], v[132:135]\n"
"ds_read_b64_tr_b16 v[64:65], v42 offset:49152\n"
"v_mfma_f32_16x16x32_bf16 v[132:135], v[84:87], a[228:231], v[132:135]\n"
"v_mfma_f32_16x16x32_bf16 v[164:167], v[80:83], a[232:235], v[164:167]\n"
"ds_read_b64_tr_b16 v[66:67], v42 offset:53248\n"
"v_mfma_f32_16x16x32_bf16 v[164:167], v[84:87], a[236:239], v[164:167]\n"
"v_mfma_f32_16x16x32_bf16 v[196:199], v[80:83], a[240:243], v[196:199]\n"
"ds_read_b64_tr_b16 v[68:69], v42 offset:57344\n"
"s_add_u32 m0, s59, 81920\n"
"v_mfma_f32_16x16x32_bf16 v[196:199], v[84:87], a[244:247], v[196:199]\n"
"buffer_load_dwordx4 v20, s[48:51], s62 offen offset:0 lds\n"
"v_mfma_f32_16x16x32_bf16 v[228:231], v[80:83], a[248:251], v[228:231]\n"
"ds_read_b64_tr_b16 v[70:71], v42 offset:61440\n"
"v_mfma_f32_16x16x32_bf16 v[228:231], v[84:87], a[252:255], v[228:231]\n"
"s_waitcnt lgkmcnt(4)\n"
"v_mfma_f32_16x16x32_bf16 a[4:7], v[88:91], a[224:227], a[4:7]\n"
"ds_read_b64_tr_b16 v[72:73], v14 offset:49152\n"
"v_mfma_f32_16x16x32_bf16 a[4:7], v[92:95], a[228:231], a[4:7]\n"
"buffer_load_dwordx4 v21, s[48:51], s62 offen offset:1024 lds\n"
"v_mfma_f32_16x16x32_bf16 a[36:39], v[88:91], a[232:235], a[36:39]\n"
"ds_read_b64_tr_b16 v[74:75], v14 offset:53248\n"
"v_mfma_f32_16x16x32_bf16 a[36:39], v[92:95], a[236:239], a[36:39]\n"
"v_mfma_f32_16x16x32_bf16 a[68:71], v[88:91], a[240:243], a[68:71]\n"
"ds_read_b64_tr_b16 v[76:77], v14 offset:57344\n"
"v_mfma_f32_16x16x32_bf16 a[68:71], v[92:95], a[244:247], a[68:71]\n"
"v_mfma_f32_16x16x32_bf16 a[100:103], v[88:91], a[248:251], a[100:103]\n"
"ds_read_b64_tr_b16 v[78:79], v14 offset:61440\n"
"v_mfma_f32_16x16x32_bf16 a[100:103], v[92:95], a[252:255], a[100:103]\n"
"buffer_load_dwordx4 v22, s[48:51], s62 offen offset:2048 lds\n"
"s_waitcnt lgkmcnt(4)\n"
"v_mfma_f32_16x16x32_bf16 v[136:139], v[64:67], a[224:227], v[136:139]\n"
"ds_read_b64_tr_b16 v[80:81], v43 offset:49152\n"
"v_mfma_f32_16x16x32_bf16 v[136:139], v[68:71], a[228:231], v[136:139]\n"
"v_mfma_f32_16x16x32_bf16 v[168:171], v[64:67], a[232:235], v[168:171]\n"
"ds_read_b64_tr_b16 v[82:83], v43 offset:53248\n"
"v_mfma_f32_16x16x32_bf16 v[168:171], v[68:71], a[236:239], v[168:171]\n"
"v_mfma_f32_16x16x32_bf16 v[200:203], v[64:67], a[240:243], v[200:203]\n"
"ds_read_b64_tr_b16 v[84:85], v43 offset:57344\n"
"v_mfma_f32_16x16x32_bf16 v[200:203], v[68:71], a[244:247], v[200:203]\n"
"buffer_load_dwordx4 v23, s[48:51], s62 offen offset:3072 lds\n"
"s_add_u32 s62, s62, 458752\n"
"v_mfma_f32_16x16x32_bf16 v[232:235], v[64:67], a[248:251], v[232:235]\n"
"ds_read_b64_tr_b16 v[86:87], v43 offset:61440\n"
"v_mfma_f32_16x16x32_bf16 v[232:235], v[68:71], a[252:255], v[232:235]\n"
"s_waitcnt lgkmcnt(4)\n"
"v_mfma_f32_16x16x32_bf16 a[8:11], v[72:75], a[224:227], a[8:11]\n"
"ds_read_b64_tr_b16 v[88:89], v15 offset:49152\n"
"v_mfma_f32_16x16x32_bf16 a[8:11], v[76:79], a[228:231], a[8:11]\n"
"buffer_load_dwordx4 a[192:195], v39, s[36:39], s56 offen offset:0 sc1\n"
"v_mfma_f32_16x16x32_bf16 a[40:43], v[72:75], a[232:235], a[40:43]\n"
"ds_read_b64_tr_b16 v[90:91], v15 offset:53248\n"
"v_mfma_f32_16x16x32_bf16 a[40:43], v[76:79], a[236:239], a[40:43]\n"
"v_mfma_f32_16x16x32_bf16 a[72:75], v[72:75], a[240:243], a[72:75]\n"
"ds_read_b64_tr_b16 v[92:93], v15 offset:57344\n"
"v_mfma_f32_16x16x32_bf16 a[72:75], v[76:79], a[244:247], a[72:75]\n"
"v_mfma_f32_16x16x32_bf16 a[104:107], v[72:75], a[248:251], a[104:107]\n"
"ds_read_b64_tr_b16 v[94:95], v15 offset:61440\n"
"v_mfma_f32_16x16x32_bf16 a[104:107], v[76:79], a[252:255], a[104:107]\n"
"buffer_load_dwordx4 a[200:203], v39, s[36:39], s56 offen offset:1024 sc1\n"
"s_waitcnt lgkmcnt(4)\n"
"v_mfma_f32_16x16x32_bf16 v[140:143], v[80:83], a[224:227], v[140:143]\n"
"ds_read_b64_tr_b16 v[64:65], v44 offset:49152\n"
"v_mfma_f32_16x16x32_bf16 v[140:143], v[84:87], a[228:231], v[140:143]\n"
"v_mfma_f32_16x16x32_bf16 v[172:175], v[80:83], a[232:235], v[172:175]\n"
"ds_read_b64_tr_b16 v[66:67], v44 offset:53248\n"
"v_mfma_f32_16x16x32_bf16 v[172:175], v[84:87], a[236:239], v[172:175]\n"
"buffer_load_dwordx4 a[208:211], v39, s[36:39], s56 offen offset:2048 sc1\n"
"v_mfma_f32_16x16x32_bf16 v[204:207], v[80:83], a[240:243], v[204:207]\n"
"ds_read_b64_tr_b16 v[68:69], v44 offset:57344\n"
"v_mfma_f32_16x16x32_bf16 v[204:207], v[84:87], a[244:247], v[204:207]\n"
"v_mfma_f32_16x16x32_bf16 v[236:239], v[80:83], a[248:251], v[236:239]\n"
"ds_read_b64_tr_b16 v[70:71], v44 offset:61440\n"
"v_mfma_f32_16x16x32_bf16 v[236:239], v[84:87], a[252:255], v[236:239]\n"
"s_waitcnt lgkmcnt(4)\n"
"v_mfma_f32_16x16x32_bf16 a[12:15], v[88:91], a[224:227], a[12:15]\n"
"ds_read_b64_tr_b16 v[72:73], v16 offset:49152\n"
"v_mfma_f32_16x16x32_bf16 a[12:15], v[92:95], a[228:231], a[12:15]\n"
"buffer_load_dwordx4 a[216:219], v39, s[36:39], s56 offen offset:3072 sc1\n"
"s_add_u32 s56, s56, 16384\n"
"v_mfma_f32_16x16x32_bf16 a[44:47], v[88:91], a[232:235], a[44:47]\n"
"ds_read_b64_tr_b16 v[74:75], v16 offset:53248\n"
"v_mfma_f32_16x16x32_bf16 a[44:47], v[92:95], a[236:239], a[44:47]\n"
"v_mfma_f32_16x16x32_bf16 a[76:79], v[88:91], a[240:243], a[76:79]\n"
"ds_read_b64_tr_b16 v[76:77], v16 offset:57344\n"
"v_mfma_f32_16x16x32_bf16 a[76:79], v[92:95], a[244:247], a[76:79]\n"
"v_mfma_f32_16x16x32_bf16 a[108:111], v[88:91], a[248:251], a[108:111]\n"
"ds_read_b64_tr_b16 v[78:79], v16 offset:61440\n"
"v_mfma_f32_16x16x32_bf16 a[108:111], v[92:95], a[252:255], a[108:111]\n"
"s_cbranch_vccnz SKP7\n"
"s_waitcnt lgkmcnt(4)\n"
"v_mfma_f32_16x16x32_bf16 v[144:147], v[64:67], a[224:227], v[144:147]\n"
"ds_read_b64_tr_b16 v[80:81], v45 offset:49152\n"
"v_mfma_f32_16x16x32_bf16 v[144:147], v[68:71], a[228:231], v[144:147]\n"
"v_mfma_f32_16x16x32_bf16 v[176:179], v[64:67], a[232:235], v[176:179]\n"
"ds_read_b64_tr_b16 v[82:83], v45 offset:53248\n"
"v_mfma_f32_16x16x32_bf16 v[176:179], v[68:71], a[236:239], v[176:179]\n"
"v_mfma_f32_16x16x32_bf16 v[208:211], v[64:67], a[240:243], v[208:211]\n"
"ds_read_b64_tr_b16 v[84:85], v45 offset:57344\n"
"v_mfma_f32_16x16x32_bf16 v[208:211], v[68:71], a[244:247], v[208:211]\n"
"v_mfma_f32_16x16x32_bf16 v[240:243], v[64:67], a[248:251], v[240:243]\n"
"ds_read_b64_tr_b16 v[86:87], v45 offset:61440\n"
"v_mfma_f32_16x16x32_bf16 v[240:243], v[68:71], a[252:255], v[240:243]\n"
"s_waitcnt lgkmcnt(4)\n"
"v_mfma_f32_16x16x32_bf16 a[16:19], v[72:75], a[224:227], a[16:19]\n"
"ds_read_b64_tr_b16 v[88:89], v17 offset:49152\n"
"v_mfma_f32_16x16x32_bf16 a[16:19], v[76:79], a[228:231], a[16:19]\n"
"v_mfma_f32_16x16x32_bf16 a[48:51], v[72:75], a[232:235], a[48:51]\n"
"ds_read_b64_tr_b16 v[90:91], v17 offset:53248\n"
"v_mfma_f32_16x16x32_bf16 a[48:51], v[76:79], a[236:239], a[48:51]\n"
"v_mfma_f32_16x16x32_bf16 a[80:83], v[72:75], a[240:243], a[80:83]\n"
"ds_read_b64_tr_b16 v[92:93], v17 offset:57344\n"
"v_mfma_f32_16x16x32_bf16 a[80:83], v[76:79], a[244:247], a[80:83]\n"
"v_mfma_f32_16x16x32_bf16 a[112:115], v[72:75], a[248:251], a[112:115]\n"
"ds_read_b64_tr_b16 v[94:95], v17 offset:61440\n"
"v_mfma_f32_16x16x32_bf16 a[112:115], v[76:79], a[252:255], a[112:115]\n"
"s_waitcnt lgkmcnt(4)\n"
"v_mfma_f32_16x16x32_bf16 v[148:151], v[80:83], a[224:227], v[148:151]\n"
"ds_read_b64_tr_b16 v[64:65], v46 offset:49152\n"
"v_mfma_f32_16x16x32_bf16 v[148:151], v[84:87], a[228:231], v[148:151]\n"
"v_mfma_f32_16x16x32_bf16 v[180:183], v[80:83], a[232:235], v[180:183]\n"
"ds_read_b64_tr_b16 v[66:67], v46 offset:53248\n"
"v_mfma_f32_16x16x32_bf16 v[180:183], v[84:87], a[236:239], v[180:183]\n"
"v_mfma_f32_16x16x32_bf16 v[212:215], v[80:83], a[240:243], v[212:215]\n"
"ds_read_b64_tr_b16 v[68:69], v46 offset:57344\n"
"v_mfma_f32_16x16x32_bf16 v[212:215], v[84:87], a[244:247], v[212:215]\n"
"v_mfma_f32_16x16x32_bf16 v[244:247], v[80:83], a[248:251], v[244:247]\n"
"ds_read_b64_tr_b16 v[70:71], v46 offset:61440\n"
"v_mfma_f32_16x16x32_bf16 v[244:247], v[84:87], a[252:255], v[244:247]\n"
"s_waitcnt lgkmcnt(4)\n"
"v_mfma_f32_16x16x32_bf16 a[20:23], v[88:91], a[224:227], a[20:23]\n"
"ds_read_b64_tr_b16 v[72:73], v18 offset:49152\n"
"v_mfma_f32_16x16x32_bf16 a[20:23], v[92:95], a[228:231], a[20:23]\n"
"v_mfma_f32_16x16x32_bf16 a[52:55], v[88:91], a[232:235], a[52:55]\n"
"ds_read_b64_tr_b16 v[74:75], v18 offset:53248\n"
"v_mfma_f32_16x16x32_bf16 a[52:55], v[92:95], a[236:239], a[52:55]\n"
"v_mfma_f32_16x16x32_bf16 a[84:87], v[88:91], a[240:243], a[84:87]\n"
"ds_read_b64_tr_b16 v[76:77], v18 offset:57344\n"
"v_mfma_f32_16x16x32_bf16 a[84:87], v[92:95], a[244:247], a[84:87]\n"
"v_mfma_f32_16x16x32_bf16 a[116:119], v[88:91], a[248:251], a[116:119]\n"
"ds_read_b64_tr_b16 v[78:79], v18 offset:61440\n"
"v_mfma_f32_16x16x32_bf16 a[116:119], v[92:95], a[252:255], a[116:119]\n"
"SKP7:\n"
"s_waitcnt lgkmcnt(4)\n"
"v_mfma_f32_16x16x32_bf16 v[152:155], v[64:67], a[224:227], v[152:155]\n"
"ds_read_b64_tr_b16 v[80:81], v47 offset:49152\n"
"v_mfma_f32_16x16x32_bf16 v[152:155], v[68:71], a[228:231], v[152:155]\n"
"buffer_load_dwordx4 a[196:199], v39, s[36:39], s57 offen offset:0 sc1\n"
"v_mfma_f32_16x16x32_bf16 v[184:187], v[64:67], a[232:235], v[184:187]\n"
"ds_read_b64_tr_b16 v[82:83], v47 offset:53248\n"
"v_mfma_f32_16x16x32_bf16 v[184:187], v[68:71], a[236:239], v[184:187]\n"
"v_mfma_f32_16x16x32_bf16 v[216:219], v[64:67], a[240:243], v[216:219]\n"
"ds_read_b64_tr_b16 v[84:85], v47 offset:57344\n"
"v_mfma_f32_16x16x32_bf16 v[216:219], v[68:71], a[244:247], v[216:219]\n"
"buffer_load_dwordx4 a[204:207], v39, s[36:39], s57 offen offset:1024 sc1\n"
"v_mfma_f32_16x16x32_bf16 v[248:251], v[64:67], a[248:251], v[248:251]\n"
"ds_read_b64_tr_b16 v[86:87], v47 offset:61440\n"
"v_mfma_f32_16x16x32_bf16 v[248:251], v[68:71], a[252:255], v[248:251]\n"
"s_waitcnt lgkmcnt(4)\n"
"v_mfma_f32_16x16x32_bf16 a[24:27], v[72:75], a[224:227], a[24:27]\n"
"ds_read_b64_tr_b16 v[88:89], v19 offset:49152\n"
"v_mfma_f32_16x16x32_bf16 a[24:27], v[76:79], a[228:231], a[24:27]\n"
"v_mfma_f32_16x16x32_bf16 a[56:59], v[72:75], a[232:235], a[56:59]\n"
"ds_read_b64_tr_b16 v[90:91], v19 offset:53248\n"
"v_mfma_f32_16x16x32_bf16 a[56:59], v[76:79], a[236:239], a[56:59]\n"
"buffer_load_dwordx4 a[212:215], v39, s[36:39], s57 offen offset:2048 sc1\n"
"v_mfma_f32_16x16x32_bf16 a[88:91], v[72:75], a[240:243], a[88:91]\n"
"ds_read_b64_tr_b16 v[92:93], v19 offset:57344\n"
"v_mfma_f32_16x16x32_bf16 a[88:91], v[76:79], a[244:247], a[88:91]\n"
"v_mfma_f32_16x16x32_bf16 a[120:123], v[72:75], a[248:251], a[120:123]\n"
"ds_read_b64_tr_b16 v[94:95], v19 offset:61440\n"
"v_mfma_f32_16x16x32_bf16 a[120:123], v[76:79], a[252:255], a[120:123]\n"
"s_waitcnt vmcnt(24)\n"
"s_barrier\n"
"s_waitcnt lgkmcnt(4)\n"
"v_mfma_f32_16x16x32_bf16 v[156:159], v[80:83], a[224:227], v[156:159]\n"
"ds_read_b64_tr_b16 v[64:65], v40 offset:0\n"
"v_mfma_f32_16x16x32_bf16 v[156:159], v[84:87], a[228:231], v[156:159]\n"
"v_mfma_f32_16x16x32_bf16 v[188:191], v[80:83], a[232:235], v[188:191]\n"
"ds_read_b64_tr_b16 v[66:67], v40 offset:4096\n"
"v_mfma_f32_16x16x32_bf16 v[188:191], v[84:87], a[236:239], v[188:191]\n"
"buffer_load_dwordx4 a[220:223], v39, s[36:39], s57 offen offset:3072 sc1\n"
"s_add_u32 s57, s57, 16384\n"
"v_mfma_f32_16x16x32_bf16 v[220:223], v[80:83], a[240:243], v[220:223]\n"
"ds_read_b64_tr_b16 v[68:69], v40 offset:8192\n"
"v_mfma_f32_16x16x32_bf16 v[220:223], v[84:87], a[244:247], v[220:223]\n"
"v_mfma_f32_16x16x32_bf16 v[252:255], v[80:83], a[248:251], v[252:255]\n"
"ds_read_b64_tr_b16 v[70:71], v40 offset:12288\n"
"v_mfma_f32_16x16x32_bf16 v[252:255], v[84:87], a[252:255], v[252:255]\n"
"s_waitcnt lgkmcnt(4)\n"
"v_mfma_f32_16x16x32_bf16 a[28:31], v[88:91], a[224:227], a[28:31]\n"
"ds_read_b64_tr_b16 v[72:73], v12 offset:0\n"
"v_mfma_f32_16x16x32_bf16 a[28:31], v[92:95], a[228:231], a[28:31]\n"
"global_store_dwordx4 v34, v[108:111], %0 offset:3072 sc1\n"
"v_add_u32 v34, 8192, v34\n"
"v_add_u32 v8, 4096, v8\n"
"v_mfma_f32_16x16x32_bf16 a[60:63], v[88:91], a[232:235], a[60:63]\n"
"ds_read_b64_tr_b16 v[74:75], v12 offset:4096\n"
"v_mfma_f32_16x16x32_bf16 a[60:63], v[92:95], a[236:239], a[60:63]\n"
"v_mfma_f32_16x16x32_bf16 a[92:95], v[88:91], a[240:243], a[92:95]\n"
"ds_read_b64_tr_b16 v[76:77], v12 offset:8192\n"
"v_mfma_f32_16x16x32_bf16 a[92:95], v[92:95], a[244:247], a[92:95]\n"
"v_mfma_f32_16x16x32_bf16 a[124:127], v[88:91], a[248:251], a[124:127]\n"
"ds_read_b64_tr_b16 v[78:79], v12 offset:12288\n"
"v_mfma_f32_16x16x32_bf16 a[124:127], v[92:95], a[252:255], a[124:127]\n"
"s_sub_u32 s60, s60, 1\n"
"s_cmp_lg_u32 s60, 0\n"
"s_cbranch_scc1 G1_LOOP\n"
"s_lshr_b32 s98, %17, 3\n"
"s_lshl_b32 s98, s98, 12\n"
"s_lshl_b32 s99, s55, 10\n"
"s_add_u32 s98, s98, s99\n"
"v_add_u32 v5, s98, v2\n"
"v_min_u32 v5, MID_END-MID_BEGIN-16, v5\n"
"v_and_b32 v5, -16, v5\n"
"s_waitcnt lgkmcnt(4)\n"
"v_mfma_f32_16x16x32_bf16 v[128:131], v[64:67], a[128:131], v[128:131]\n"
"ds_read_b64_tr_b16 v[80:81], v41 offset:0\n"
"s_add_u32 m0, s59, 32768\n"
"v_mfma_f32_16x16x32_bf16 v[128:131], v[68:71], a[132:135], v[128:131]\n"
"buffer_load_dwordx4 v20, s[44:47], s62 offen offset:0 lds\n"
"v_mfma_f32_16x16x32_bf16 v[160:163], v[64:67], a[136:139], v[160:163]\n"
"ds_read_b64_tr_b16 v[82:83], v41 offset:4096\n"
"v_mfma_f32_16x16x32_bf16 v[160:163], v[68:71], a[140:143], v[160:163]\n"
"v_mfma_f32_16x16x32_bf16 v[192:195], v[64:67], a[144:147], v[192:195]\n"
"ds_read_b64_tr_b16 v[84:85], v41 offset:8192\n"
"v_mfma_f32_16x16x32_bf16 v[192:195], v[68:71], a[148:151], v[192:195]\n"
"buffer_load_dwordx4 v21, s[44:47], s62 offen offset:1024 lds\n"
"v_mfma_f32_16x16x32_bf16 v[224:227], v[64:67], a[152:155], v[224:227]\n"
"ds_read_b64_tr_b16 v[86:87], v41 offset:12288\n"
"v_mfma_f32_16x16x32_bf16 v[224:227], v[68:71], a[156:159], v[224:227]\n"
"s_waitcnt lgkmcnt(4)\n"
"v_mfma_f32_16x16x32_bf16 a[0:3], v[72:75], a[128:131], a[0:3]\n"
"ds_read_b64_tr_b16 v[88:89], v13 offset:0\n"
"v_mfma_f32_16x16x32_bf16 a[0:3], v[76:79], a[132:135], a[0:3]\n"
"buffer_load_dwordx4 v22, s[44:47], s62 offen offset:2048 lds\n"
"v_mfma_f32_16x16x32_bf16 a[32:35], v[72:75], a[136:139], a[32:35]\n"
"ds_read_b64_tr_b16 v[90:91], v13 offset:4096\n"
"v_mfma_f32_16x16x32_bf16 a[32:35], v[76:79], a[140:143], a[32:35]\n"
"v_mfma_f32_16x16x32_bf16 a[64:67], v[72:75], a[144:147], a[64:67]\n"
"ds_read_b64_tr_b16 v[92:93], v13 offset:8192\n"
"v_mfma_f32_16x16x32_bf16 a[64:67], v[76:79], a[148:151], a[64:67]\n"
"buffer_load_dwordx4 v23, s[44:47], s62 offen offset:3072 lds\n"
"v_mfma_f32_16x16x32_bf16 a[96:99], v[72:75], a[152:155], a[96:99]\n"
"ds_read_b64_tr_b16 v[94:95], v13 offset:12288\n"
"v_mfma_f32_16x16x32_bf16 a[96:99], v[76:79], a[156:159], a[96:99]\n"
"s_waitcnt lgkmcnt(4)\n"
"v_mfma_f32_16x16x32_bf16 v[132:135], v[80:83], a[128:131], v[132:135]\n"
"ds_read_b64_tr_b16 v[64:65], v42 offset:0\n"
"s_add_u32 m0, s59, 98304\n"
"v_mfma_f32_16x16x32_bf16 v[132:135], v[84:87], a[132:135], v[132:135]\n"
"buffer_load_dwordx4 v20, s[48:51], s62 offen offset:0 lds\n"
"v_mfma_f32_16x16x32_bf16 v[164:167], v[80:83], a[136:139], v[164:167]\n"
"ds_read_b64_tr_b16 v[66:67], v42 offset:4096\n"
"v_mfma_f32_16x16x32_bf16 v[164:167], v[84:87], a[140:143], v[164:167]\n"
"v_mfma_f32_16x16x32_bf16 v[196:199], v[80:83], a[144:147], v[196:199]\n"
"ds_read_b64_tr_b16 v[68:69], v42 offset:8192\n"
"v_mfma_f32_16x16x32_bf16 v[196:199], v[84:87], a[148:151], v[196:199]\n"
"buffer_load_dwordx4 v21, s[48:51], s62 offen offset:1024 lds\n"
"v_mfma_f32_16x16x32_bf16 v[228:231], v[80:83], a[152:155], v[228:231]\n"
"ds_read_b64_tr_b16 v[70:71], v42 offset:12288\n"
"v_mfma_f32_16x16x32_bf16 v[228:231], v[84:87], a[156:159], v[228:231]\n"
"s_waitcnt lgkmcnt(4)\n"
"v_mfma_f32_16x16x32_bf16 a[4:7], v[88:91], a[128:131], a[4:7]\n"
"ds_read_b64_tr_b16 v[72:73], v14 offset:0\n"
"v_mfma_f32_16x16x32_bf16 a[4:7], v[92:95], a[132:135], a[4:7]\n"
"buffer_load_dwordx4 v22, s[48:51], s62 offen offset:2048 lds\n"
"v_mfma_f32_16x16x32_bf16 a[36:39], v[88:91], a[136:139], a[36:39]\n"
"ds_read_b64_tr_b16 v[74:75], v14 offset:4096\n"
"v_mfma_f32_16x16x32_bf16 a[36:39], v[92:95], a[140:143], a[36:39]\n"
"v_mfma_f32_16x16x32_bf16 a[68:71], v[88:91], a[144:147], a[68:71]\n"
"ds_read_b64_tr_b16 v[76:77], v14 offset:8192\n"
"v_mfma_f32_16x16x32_bf16 a[68:71], v[92:95], a[148:151], a[68:71]\n"
"buffer_load_dwordx4 v23, s[48:51], s62 offen offset:3072 lds\n"
"s_add_u32 s62, s62, 458752\n"
"v_mfma_f32_16x16x32_bf16 a[100:103], v[88:91], a[152:155], a[100:103]\n"
"ds_read_b64_tr_b16 v[78:79], v14 offset:12288\n"
"v_mfma_f32_16x16x32_bf16 a[100:103], v[92:95], a[156:159], a[100:103]\n"
"s_waitcnt lgkmcnt(4)\n"
"v_mfma_f32_16x16x32_bf16 v[136:139], v[64:67], a[128:131], v[136:139]\n"
"ds_read_b64_tr_b16 v[80:81], v43 offset:0\n"
"v_mfma_f32_16x16x32_bf16 v[136:139], v[68:71], a[132:135], v[136:139]\n"
"buffer_load_dwordx4 a[224:227], v39, s[36:39], s56 offen offset:0 sc1\n"
"v_mfma_f32_16x16x32_bf16 v[168:171], v[64:67], a[136:139], v[168:171]\n"
"ds_read_b64_tr_b16 v[82:83], v43 offset:4096\n"
"v_mfma_f32_16x16x32_bf16 v[168:171], v[68:71], a[140:143], v[168:171]\n"
"v_mfma_f32_16x16x32_bf16 v[200:203], v[64:67], a[144:147], v[200:203]\n"
"ds_read_b64_tr_b16 v[84:85], v43 offset:8192\n"
"v_mfma_f32_16x16x32_bf16 v[200:203], v[68:71], a[148:151], v[200:203]\n"
"buffer_load_dwordx4 a[232:235], v39, s[36:39], s56 offen offset:1024 sc1\n"
"v_mfma_f32_16x16x32_bf16 v[232:235], v[64:67], a[152:155], v[232:235]\n"
"ds_read_b64_tr_b16 v[86:87], v43 offset:12288\n"
"v_mfma_f32_16x16x32_bf16 v[232:235], v[68:71], a[156:159], v[232:235]\n"
"s_waitcnt lgkmcnt(4)\n"
"v_mfma_f32_16x16x32_bf16 a[8:11], v[72:75], a[128:131], a[8:11]\n"
"ds_read_b64_tr_b16 v[88:89], v15 offset:0\n"
"v_mfma_f32_16x16x32_bf16 a[8:11], v[76:79], a[132:135], a[8:11]\n"
"buffer_load_dwordx4 a[240:243], v39, s[36:39], s56 offen offset:2048 sc1\n"
"v_mfma_f32_16x16x32_bf16 a[40:43], v[72:75], a[136:139], a[40:43]\n"
"ds_read_b64_tr_b16 v[90:91], v15 offset:4096\n"
"v_mfma_f32_16x16x32_bf16 a[40:43], v[76:79], a[140:143], a[40:43]\n"
"v_mfma_f32_16x16x32_bf16 a[72:75], v[72:75], a[144:147], a[72:75]\n"
"ds_read_b64_tr_b16 v[92:93], v15 offset:8192\n"
"v_mfma_f32_16x16x32_bf16 a[72:75], v[76:79], a[148:151], a[72:75]\n"
"buffer_load_dwordx4 a[248:251], v39, s[36:39], s56 offen offset:3072 sc1\n"
"s_add_u32 s56, s56, 16384\n"
"v_mfma_f32_16x16x32_bf16 a[104:107], v[72:75], a[152:155], a[104:107]\n"
"ds_read_b64_tr_b16 v[94:95], v15 offset:12288\n"
"v_mfma_f32_16x16x32_bf16 a[104:107], v[76:79], a[156:159], a[104:107]\n"
"s_waitcnt lgkmcnt(4)\n"
"v_mfma_f32_16x16x32_bf16 v[140:143], v[80:83], a[128:131], v[140:143]\n"
"ds_read_b64_tr_b16 v[64:65], v44 offset:0\n"
"v_mfma_f32_16x16x32_bf16 v[140:143], v[84:87], a[132:135], v[140:143]\n"
"buffer_load_dwordx4 a[228:231], v39, s[36:39], s57 offen offset:0 sc1\n"
"v_mfma_f32_16x16x32_bf16 v[172:175], v[80:83], a[136:139], v[172:175]\n"
"ds_read_b64_tr_b16 v[66:67], v44 offset:4096\n"
"v_mfma_f32_16x16x32_bf16 v[172:175], v[84:87], a[140:143], v[172:175]\n"
"v_mfma_f32_16x16x32_bf16 v[204:207], v[80:83], a[144:147], v[204:207]\n"
"ds_read_b64_tr_b16 v[68:69], v44 offset:8192\n"
"v_mfma_f32_16x16x32_bf16 v[204:207], v[84:87], a[148:151], v[204:207]\n"
"buffer_load_dwordx4 a[236:239], v39, s[36:39], s57 offen offset:1024 sc1\n"
"v_mfma_f32_16x16x32_bf16 v[236:239], v[80:83], a[152:155], v[236:239]\n"
"ds_read_b64_tr_b16 v[70:71], v44 offset:12288\n"
"v_mfma_f32_16x16x32_bf16 v[236:239], v[84:87], a[156:159], v[236:239]\n"
"s_waitcnt lgkmcnt(4)\n"
"v_mfma_f32_16x16x32_bf16 a[12:15], v[88:91], a[128:131], a[12:15]\n"
"ds_read_b64_tr_b16 v[72:73], v16 offset:0\n"
"v_mfma_f32_16x16x32_bf16 a[12:15], v[92:95], a[132:135], a[12:15]\n"
"buffer_load_dwordx4 a[244:247], v39, s[36:39], s57 offen offset:2048 sc1\n"
"v_mfma_f32_16x16x32_bf16 a[44:47], v[88:91], a[136:139], a[44:47]\n"
"ds_read_b64_tr_b16 v[74:75], v16 offset:4096\n"
"v_mfma_f32_16x16x32_bf16 a[44:47], v[92:95], a[140:143], a[44:47]\n"
"v_mfma_f32_16x16x32_bf16 a[76:79], v[88:91], a[144:147], a[76:79]\n"
"ds_read_b64_tr_b16 v[76:77], v16 offset:8192\n"
"v_mfma_f32_16x16x32_bf16 a[76:79], v[92:95], a[148:151], a[76:79]\n"
"buffer_load_dwordx4 a[252:255], v39, s[36:39], s57 offen offset:3072 sc1\n"
"s_add_u32 s57, s57, 16384\n"
"v_mfma_f32_16x16x32_bf16 a[108:111], v[88:91], a[152:155], a[108:111]\n"
"ds_read_b64_tr_b16 v[78:79], v16 offset:12288\n"
"v_mfma_f32_16x16x32_bf16 a[108:111], v[92:95], a[156:159], a[108:111]\n"
"s_cbranch_vccnz SKP8\n"
"s_waitcnt lgkmcnt(4)\n"
"v_mfma_f32_16x16x32_bf16 v[144:147], v[64:67], a[128:131], v[144:147]\n"
"ds_read_b64_tr_b16 v[80:81], v45 offset:0\n"
"v_mfma_f32_16x16x32_bf16 v[144:147], v[68:71], a[132:135], v[144:147]\n"
"v_mfma_f32_16x16x32_bf16 v[176:179], v[64:67], a[136:139], v[176:179]\n"
"ds_read_b64_tr_b16 v[82:83], v45 offset:4096\n"
"v_mfma_f32_16x16x32_bf16 v[176:179], v[68:71], a[140:143], v[176:179]\n"
"v_mfma_f32_16x16x32_bf16 v[208:211], v[64:67], a[144:147], v[208:211]\n"
"ds_read_b64_tr_b16 v[84:85], v45 offset:8192\n"
"v_mfma_f32_16x16x32_bf16 v[208:211], v[68:71], a[148:151], v[208:211]\n"
"v_mfma_f32_16x16x32_bf16 v[240:243], v[64:67], a[152:155], v[240:243]\n"
"ds_read_b64_tr_b16 v[86:87], v45 offset:12288\n"
"v_mfma_f32_16x16x32_bf16 v[240:243], v[68:71], a[156:159], v[240:243]\n"
"s_waitcnt lgkmcnt(4)\n"
"v_mfma_f32_16x16x32_bf16 a[16:19], v[72:75], a[128:131], a[16:19]\n"
"ds_read_b64_tr_b16 v[88:89], v17 offset:0\n"
"v_mfma_f32_16x16x32_bf16 a[16:19], v[76:79], a[132:135], a[16:19]\n"
"v_mfma_f32_16x16x32_bf16 a[48:51], v[72:75], a[136:139], a[48:51]\n"
"ds_read_b64_tr_b16 v[90:91], v17 offset:4096\n"
"v_mfma_f32_16x16x32_bf16 a[48:51], v[76:79], a[140:143], a[48:51]\n"
"v_mfma_f32_16x16x32_bf16 a[80:83], v[72:75], a[144:147], a[80:83]\n"
"ds_read_b64_tr_b16 v[92:93], v17 offset:8192\n"
"v_mfma_f32_16x16x32_bf16 a[80:83], v[76:79], a[148:151], a[80:83]\n"
"v_mfma_f32_16x16x32_bf16 a[112:115], v[72:75], a[152:155], a[112:115]\n"
"ds_read_b64_tr_b16 v[94:95], v17 offset:12288\n"
"v_mfma_f32_16x16x32_bf16 a[112:115], v[76:79], a[156:159], a[112:115]\n"
"s_waitcnt lgkmcnt(4)\n"
"v_mfma_f32_16x16x32_bf16 v[148:151], v[80:83], a[128:131], v[148:151]\n"
"ds_read_b64_tr_b16 v[64:65], v46 offset:0\n"
"v_mfma_f32_16x16x32_bf16 v[148:151], v[84:87], a[132:135], v[148:151]\n"
"v_mfma_f32_16x16x32_bf16 v[180:183], v[80:83], a[136:139], v[180:183]\n"
"ds_read_b64_tr_b16 v[66:67], v46 offset:4096\n"
"v_mfma_f32_16x16x32_bf16 v[180:183], v[84:87], a[140:143], v[180:183]\n"
"v_mfma_f32_16x16x32_bf16 v[212:215], v[80:83], a[144:147], v[212:215]\n"
"ds_read_b64_tr_b16 v[68:69], v46 offset:8192\n"
"v_mfma_f32_16x16x32_bf16 v[212:215], v[84:87], a[148:151], v[212:215]\n"
"v_mfma_f32_16x16x32_bf16 v[244:247], v[80:83], a[152:155], v[244:247]\n"
"ds_read_b64_tr_b16 v[70:71], v46 offset:12288\n"
"v_mfma_f32_16x16x32_bf16 v[244:247], v[84:87], a[156:159], v[244:247]\n"
"s_waitcnt lgkmcnt(4)\n"
"v_mfma_f32_16x16x32_bf16 a[20:23], v[88:91], a[128:131], a[20:23]\n"
"ds_read_b64_tr_b16 v[72:73], v18 offset:0\n"
"v_mfma_f32_16x16x32_bf16 a[20:23], v[92:95], a[132:135], a[20:23]\n"
"v_mfma_f32_16x16x32_bf16 a[52:55], v[88:91], a[136:139], a[52:55]\n"
"ds_read_b64_tr_b16 v[74:75], v18 offset:4096\n"
"v_mfma_f32_16x16x32_bf16 a[52:55], v[92:95], a[140:143], a[52:55]\n"
"v_mfma_f32_16x16x32_bf16 a[84:87], v[88:91], a[144:147], a[84:87]\n"
"ds_read_b64_tr_b16 v[76:77], v18 offset:8192\n"
"v_mfma_f32_16x16x32_bf16 a[84:87], v[92:95], a[148:151], a[84:87]\n"
"v_mfma_f32_16x16x32_bf16 a[116:119], v[88:91], a[152:155], a[116:119]\n"
"ds_read_b64_tr_b16 v[78:79], v18 offset:12288\n"
"v_mfma_f32_16x16x32_bf16 a[116:119], v[92:95], a[156:159], a[116:119]\n"
"SKP8:\n"
"s_waitcnt lgkmcnt(4)\n"
"v_mfma_f32_16x16x32_bf16 v[152:155], v[64:67], a[128:131], v[152:155]\n"
"ds_read_b64_tr_b16 v[80:81], v47 offset:0\n"
"v_mfma_f32_16x16x32_bf16 v[152:155], v[68:71], a[132:135], v[152:155]\n"
"v_mfma_f32_16x16x32_bf16 v[184:187], v[64:67], a[136:139], v[184:187]\n"
"ds_read_b64_tr_b16 v[82:83], v47 offset:4096\n"
"s_getpc_b64 s[66:67]\n"
"CW_PC1:\n"
"s_sub_u32 s66, s66, CW_PC1-MID_BEGIN\n"
"s_subb_u32 s67, s67, 0\n"
"s_and_b32 s66, s66, -16\n"
"s_add_u32 m0, s59, 131072\n"
"s_nop 0\n"
"v_mfma_f32_16x16x32_bf16 v[184:187], v[68:71], a[140:143], v[184:187]\n"
"global_load_lds_dwordx4 v5, s[66:67]\n"
"v_mfma_f32_16x16x32_bf16 v[216:219], v[64:67], a[144:147], v[216:219]\n"
"ds_read_b64_tr_b16 v[84:85], v47 offset:8192\n"
"v_mfma_f32_16x16x32_bf16 v[216:219], v[68:71], a[148:151], v[216:219]\n"
"v_mfma_f32_16x16x32_bf16 v[248:251], v[64:67], a[152:155], v[248:251]\n"
"ds_read_b64_tr_b16 v[86:87], v47 offset:12288\n"
"v_mfma_f32_16x16x32_bf16 v[248:251], v[68:71], a[156:159], v[248:251]\n"
"s_waitcnt lgkmcnt(4)\n"
"v_mfma_f32_16x16x32_bf16 a[24:27], v[72:75], a[128:131], a[24:27]\n"
"ds_read_b64_tr_b16 v[88:89], v19 offset:0\n"
"v_mfma_f32_16x16x32_bf16 a[24:27], v[76:79], a[132:135], a[24:27]\n"
"v_mfma_f32_16x16x32_bf16 a[56:59], v[72:75], a[136:139], a[56:59]\n"
"ds_read_b64_tr_b16 v[90:91], v19 offset:4096\n"
"v_mfma_f32_16x16x32_bf16 a[56:59], v[76:79], a[140:143], a[56:59]\n"
"v_mfma_f32_16x16x32_bf16 a[88:91], v[72:75], a[144:147], a[88:91]\n"
"ds_read_b64_tr_b16 v[92:93], v19 offset:8192\n"
"v_mfma_f32_16x16x32_bf16 a[88:91], v[76:79], a[148:151], a[88:91]\n"
"v_mfma_f32_16x16x32_bf16 a[120:123], v[72:75], a[152:155], a[120:123]\n"
"ds_read_b64_tr_b16 v[94:95], v19 offset:12288\n"
"v_mfma_f32_16x16x32_bf16 a[120:123], v[76:79], a[156:159], a[120:123]\n"
"s_waitcnt vmcnt(26)\n"
"s_barrier\n"
"s_waitcnt lgkmcnt(4)\n"
"v_mfma_f32_16x16x32_bf16 v[156:159], v[80:83], a[128:131], v[156:159]\n"
"ds_read_b64_tr_b16 v[64:65], v40 offset:16384\n"
"v_mfma_f32_16x16x32_bf16 v[156:159], v[84:87], a[132:135], v[156:159]\n"
"s_mov_b64 exec, 1\n"
"s_waitcnt vmcnt(17)\n"
"global_atomic_add v36, v37, %0 sc1\n"
"s_mov_b64 exec, -1\n"
"v_mfma_f32_16x16x32_bf16 v[188:191], v[80:83], a[136:139], v[188:191]\n"
"ds_read_b64_tr_b16 v[66:67], v40 offset:20480\n"
"s_and_b32 s68, %17, 1\n"
"s_lshl_b32 s68, s68, 2\n"
"s_xor_b32 s68, s68, 4\n"
"s_bfe_u32 s98, %17, 0x20001\n"
"s_add_u32 s68, s68, s98\n"
"v_mfma_f32_16x16x32_bf16 v[188:191], v[84:87], a[140:143], v[188:191]\n"
"v_mfma_f32_16x16x32_bf16 v[220:223], v[80:83], a[144:147], v[220:223]\n"
"ds_read_b64_tr_b16 v[68:69], v40 offset:24576\n"
"v_mfma_f32_16x16x32_bf16 v[220:223], v[84:87], a[148:151], v[220:223]\n"
"v_mfma_f32_16x16x32_bf16 v[252:255], v[80:83], a[152:155], v[252:255]\n"
"ds_read_b64_tr_b16 v[70:71], v40 offset:28672\n"
"v_mfma_f32_16x16x32_bf16 v[252:255], v[84:87], a[156:159], v[252:255]\n"
"s_waitcnt lgkmcnt(4)\n"
"v_mfma_f32_16x16x32_bf16 a[28:31], v[88:91], a[128:131], a[28:31]\n"
"ds_read_b64_tr_b16 v[72:73], v12 offset:16384\n"
"v_mfma_f32_16x16x32_bf16 a[28:31], v[92:95], a[132:135], a[28:31]\n"
"v_mfma_f32_16x16x32_bf16 a[60:63], v[88:91], a[136:139], a[60:63]\n"
"ds_read_b64_tr_b16 v[74:75], v12 offset:20480\n"
"v_mfma_f32_16x16x32_bf16 a[60:63], v[92:95], a[140:143], a[60:63]\n"
"v_mfma_f32_16x16x32_bf16 a[92:95], v[88:91], a[144:147], a[92:95]\n"
"ds_read_b64_tr_b16 v[76:77], v12 offset:24576\n"
"v_mfma_f32_16x16x32_bf16 a[92:95], v[92:95], a[148:151], a[92:95]\n"
"v_mfma_f32_16x16x32_bf16 a[124:127], v[88:91], a[152:155], a[124:127]\n"
"ds_read_b64_tr_b16 v[78:79], v12 offset:28672\n"
"v_mfma_f32_16x16x32_bf16 a[124:127], v[92:95], a[156:159], a[124:127]\n"
"s_waitcnt lgkmcnt(4)\n"
"v_mfma_f32_16x16x32_bf16 v[128:131], v[64:67], a[160:163], v[128:131]\n"
"ds_read_b64_tr_b16 v[80:81], v41 offset:16384\n"
"s_add_u32 m0, s59, 49152\n"
"v_mfma_f32_16x16x32_bf16 v[128:131], v[68:71], a[164:167], v[128:131]\n"
"buffer_load_dwordx4 v20, s[44:47], s62 offen offset:0 lds\n"
"v_mfma_f32_16x16x32_bf16 v[160:163], v[64:67], a[168:171], v[160:163]\n"
"ds_read_b64_tr_b16 v[82:83], v41 offset:20480\n"
"v_mfma_f32_16x16x32_bf16 v[160:163], v[68:71], a[172:175], v[160:163]\n"
"buffer_load_dwordx4 v21, s[44:47], s62 offen offset:1024 lds\n"
"v_mfma_f32_16x16x32_bf16 v[192:195], v[64:67], a[176:179], v[192:195]\n"
"ds_read_b64_tr_b16 v[84:85], v41 offset:24576\n"
"v_mfma_f32_16x16x32_bf16 v[192:195], v[68:71], a[180:183], v[192:195]\n"
"v_mfma_f32_16x16x32_bf16 v[224:227], v[64:67], a[184:187], v[224:227]\n"
"ds_read_b64_tr_b16 v[86:87], v41 offset:28672\n"
"v_mfma_f32_16x16x32_bf16 v[224:227], v[68:71], a[188:191], v[224:227]\n"
"buffer_load_dwordx4 v22, s[44:47], s62 offen offset:2048 lds\n"
"s_waitcnt lgkmcnt(4)\n"
"v_mfma_f32_16x16x32_bf16 a[0:3], v[72:75], a[160:163], a[0:3]\n"
"ds_read_b64_tr_b16 v[88:89], v13 offset:16384\n"
"v_mfma_f32_16x16x32_bf16 a[0:3], v[76:79], a[164:167], a[0:3]\n"
"buffer_load_dwordx4 v23, s[44:47], s62 offen offset:3072 lds\n"
"v_mfma_f32_16x16x32_bf16 a[32:35], v[72:75], a[168:171], a[32:35]\n"
"ds_read_b64_tr_b16 v[90:91], v13 offset:20480\n"
"v_mfma_f32_16x16x32_bf16 a[32:35], v[76:79], a[172:175], a[32:35]\n"
"v_mfma_f32_16x16x32_bf16 a[64:67], v[72:75], a[176:179], a[64:67]\n"
"ds_read_b64_tr_b16 v[92:93], v13 offset:24576\n"
"s_add_u32 m0, s59, 114688\n"
"v_mfma_f32_16x16x32_bf16 a[64:67], v[76:79], a[180:183], a[64:67]\n"
"buffer_load_dwordx4 v20, s[48:51], s62 offen offset:0 lds\n"
"v_mfma_f32_16x16x32_bf16 a[96:99], v[72:75], a[184:187], a[96:99]\n"
"ds_read_b64_tr_b16 v[94:95], v13 offset:28672\n"
"v_mfma_f32_16x16x32_bf16 a[96:99], v[76:79], a[188:191], a[96:99]\n"
"s_waitcnt lgkmcnt(4)\n"
"v_mfma_f32_16x16x32_bf16 v[132:135], v[80:83], a[160:163], v[132:135]\n"
"ds_read_b64_tr_b16 v[64:65], v42 offset:16384\n"
"v_mfma_f32_16x16x32_bf16 v[132:135], v[84:87], a[164:167], v[132:135]\n"
"buffer_load_dwordx4 v21, s[48:51], s62 offen offset:1024 lds\n"
"v_mfma_f32_16x16x32_bf16 v[164:167], v[80:83], a[168:171], v[164:167]\n"
"ds_read_b64_tr_b16 v[66:67], v42 offset:20480\n"
"v_mfma_f32_16x16x32_bf16 v[164:167], v[84:87], a[172:175], v[164:167]\n"
"buffer_load_dwordx4 v22, s[48:51], s62 offen offset:2048 lds\n"
"v_mfma_f32_16x16x32_bf16 v[196:199], v[80:83], a[176:179], v[196:199]\n"
"ds_read_b64_tr_b16 v[68:69], v42 offset:24576\n"
"v_mfma_f32_16x16x32_bf16 v[196:199], v[84:87], a[180:183], v[196:199]\n"
"v_mfma_f32_16x16x32_bf16 v[228:231], v[80:83], a[184:187], v[228:231]\n"
"ds_read_b64_tr_b16 v[70:71], v42 offset:28672\n"
"v_mfma_f32_16x16x32_bf16 v[228:231], v[84:87], a[188:191], v[228:231]\n"
"buffer_load_dwordx4 v23, s[48:51], s62 offen offset:3072 lds\n"
"s_add_u32 s62, s62, 458752\n"
"s_waitcnt lgkmcnt(4)\n"
"v_mfma_f32_16x16x32_bf16 a[4:7], v[88:91], a[160:163], a[4:7]\n"
"ds_read_b64_tr_b16 v[72:73], v14 offset:16384\n"
"v_mfma_f32_16x16x32_bf16 a[4:7], v[92:95], a[164:167], a[4:7]\n"
"s_mov_b64 s[84:85], %0\n"
"s_mov_b64 s[44:45], %9\n"
"v_mfma_f32_16x16x32_bf16 a[36:39], v[88:91], a[168:171], a[36:39]\n"
"ds_read_b64_tr_b16 v[74:75], v14 offset:20480\n"
"s_mov_b64 s[40:41], %8\n"
"v_mfma_f32_16x16x32_bf16 a[36:39], v[92:95], a[172:175], a[36:39]\n"
"s_lshr_b32 s64, s68, 2\n"
"s_xor_b32 s64, s64, 1\n"
"v_mfma_f32_16x16x32_bf16 a[68:71], v[88:91], a[176:179], a[68:71]\n"
"ds_read_b64_tr_b16 v[76:77], v14 offset:24576\n"
"s_and_b32 s61, s68, 3\n"
"v_mfma_f32_16x16x32_bf16 a[68:71], v[92:95], a[180:183], a[68:71]\n"
"s_lshl_b32 s61, s61, 2\n"
"s_bfe_u32 s65, %17, 0x20003\n"
"v_mfma_f32_16x16x32_bf16 a[100:103], v[88:91], a[184:187], a[100:103]\n"
"ds_read_b64_tr_b16 v[78:79], v14 offset:28672\n"
"s_add_u32 s61, s61, s65\n"
"v_mfma_f32_16x16x32_bf16 a[100:103], v[92:95], a[188:191], a[100:103]\n"
"s_lshr_b32 s65, %17, 5\n"
"s_lshl_b32 s63, s64, 1\n"
"s_waitcnt lgkmcnt(4)\n"
"v_mfma_f32_16x16x32_bf16 v[136:139], v[64:67], a[160:163], v[136:139]\n"
"ds_read_b64_tr_b16 v[80:81], v43 offset:16384\n"
"s_add_u32 s63, s63, 14\n"
"v_mfma_f32_16x16x32_bf16 v[136:139], v[68:71], a[164:167], v[136:139]\n"
"s_lshl_b32 s69, s61, 3\n"
"s_add_u32 s69, s69, s65\n"
"v_mfma_f32_16x16x32_bf16 v[168:171], v[64:67], a[168:171], v[168:171]\n"
"ds_read_b64_tr_b16 v[82:83], v43 offset:20480\n"
"s_lshl_b32 s60, s69, 15\n"
"v_mfma_f32_16x16x32_bf16 v[168:171], v[68:71], a[172:175], v[168:171]\n"
"s_add_u32 s60, s60, 37748736\n"
"s_add_u32 s92, s84, s60\n"
"v_mfma_f32_16x16x32_bf16 v[200:203], v[64:67], a[176:179], v[200:203]\n"
"ds_read_b64_tr_b16 v[84:85], v43 offset:24576\n"
"s_addc_u32 s93, s85, 0\n"
"v_mfma_f32_16x16x32_bf16 v[200:203], v[68:71], a[180:183], v[200:203]\n"
"s_mov_b32 s94, 32768\n"
"s_mov_b32 s95, 0x20000\n"
"v_mfma_f32_16x16x32_bf16 v[232:235], v[64:67], a[184:187], v[232:235]\n"
"ds_read_b64_tr_b16 v[86:87], v43 offset:28672\n"
"s_lshl_b32 s60, s69, 9\n"
"v_mfma_f32_16x16x32_bf16 v[232:235], v[68:71], a[188:191], v[232:235]\n"
"s_lshl_b32 s69, s55, 6\n"
"s_add_u32 s60, s60, s69\n"
"s_waitcnt lgkmcnt(4)\n"
"v_mfma_f32_16x16x32_bf16 a[8:11], v[72:75], a[160:163], a[8:11]\n"
"ds_read_b64_tr_b16 v[88:89], v15 offset:16384\n"
"s_add_u32 s60, s60, 41943040\n"
"v_mfma_f32_16x16x32_bf16 a[8:11], v[76:79], a[164:167], a[8:11]\n"
"s_add_u32 s46, s84, s60\n"
"s_addc_u32 s47, s85, 0\n"
"v_mfma_f32_16x16x32_bf16 a[40:43], v[72:75], a[168:171], a[40:43]\n"
"ds_read_b64_tr_b16 v[90:91], v15 offset:20480\n"
"s_cmp_lg_u32 s64, 0\n"
"v_mfma_f32_16x16x32_bf16 a[40:43], v[76:79], a[172:175], a[40:43]\n"
"s_cselect_b64 s[88:89], %6, %7\n"
"s_lshl_b32 s60, s64, 11\n"
"v_mfma_f32_16x16x32_bf16 a[72:75], v[72:75], a[176:179], a[72:75]\n"
"ds_read_b64_tr_b16 v[92:93], v15 offset:24576\n"
"s_sub_u32 s60, 42010624, s60\n"
"v_mfma_f32_16x16x32_bf16 a[72:75], v[76:79], a[180:183], a[72:75]\n"
"s_mul_i32 s51, s64, 96\n"
"s_add_u32 s51, s51, 32\n"
"v_mfma_f32_16x16x32_bf16 a[104:107], v[72:75], a[184:187], a[104:107]\n"
"ds_read_b64_tr_b16 v[94:95], v15 offset:28672\n"
"s_lshl_b32 s50, s64, 24\n"
"v_mfma_f32_16x16x32_bf16 a[104:107], v[76:79], a[188:191], a[104:107]\n"
"s_sub_u32 s50, 20971520, s50\n"
"s_lshr_b32 s69, s61, 1\n"
"s_waitcnt lgkmcnt(4)\n"
"v_mfma_f32_16x16x32_bf16 v[140:143], v[80:83], a[160:163], v[140:143]\n"
"ds_read_b64_tr_b16 v[64:65], v44 offset:16384\n"
"s_lshl_b32 s69, s69, 8\n"
"v_mfma_f32_16x16x32_bf16 v[140:143], v[84:87], a[164:167], v[140:143]\n"
"s_add_u32 s60, s60, s69\n"
"s_add_u32 s48, s84, s60\n"
"v_mfma_f32_16x16x32_bf16 v[172:175], v[80:83], a[168:171], v[172:175]\n"
"ds_read_b64_tr_b16 v[66:67], v44 offset:20480\n"
"s_addc_u32 s49, s85, 0\n"
"v_mfma_f32_16x16x32_bf16 v[172:175], v[84:87], a[172:175], v[172:175]\n"
"s_mul_i32 s69, s61, s63\n"
"s_lshl_b32 s69, s69, 16\n"
"v_mfma_f32_16x16x32_bf16 v[204:207], v[80:83], a[176:179], v[204:207]\n"
"ds_read_b64_tr_b16 v[68:69], v44 offset:24576\n"
"s_add_u32 s69, s69, s50\n"
"v_mfma_f32_16x16x32_bf16 v[204:207], v[84:87], a[180:183], v[204:207]\n"
"s_add_u32 s84, s84, s69\n"
"s_addc_u32 s85, s85, 0\n"
"v_mfma_f32_16x16x32_bf16 v[236:239], v[80:83], a[184:187], v[236:239]\n"
"ds_read_b64_tr_b16 v[70:71], v44 offset:28672\n"
"s_mov_b32 s86, 0\n"
"v_mfma_f32_16x16x32_bf16 v[236:239], v[84:87], a[188:191], v[236:239]\n"
"s_mov_b32 s87, 0x20000\n"
"s_lshl_b32 s65, s65, 8\n"
"s_waitcnt lgkmcnt(4)\n"
"v_mfma_f32_16x16x32_bf16 a[12:15], v[88:91], a[160:163], a[12:15]\n"
"ds_read_b64_tr_b16 v[72:73], v16 offset:16384\n"
"s_add_u32 s88, s88, s65\n"
"v_mfma_f32_16x16x32_bf16 a[12:15], v[92:95], a[164:167], a[12:15]\n"
"s_addc_u32 s89, s89, 0\n"
"s_mov_b32 s91, 0x20000\n"
"v_mfma_f32_16x16x32_bf16 a[44:47], v[88:91], a[168:171], a[44:47]\n"
"ds_read_b64_tr_b16 v[74:75], v16 offset:20480\n"
"s_lshl_b32 s61, s61, 18\n"
"v_mfma_f32_16x16x32_bf16 a[44:47], v[92:95], a[172:175], a[44:47]\n"
"s_add_u32 s61, s61, s65\n"
"s_add_u32 s40, s40, s61\n"
"v_mfma_f32_16x16x32_bf16 a[76:79], v[88:91], a[176:179], a[76:79]\n"
"ds_read_b64_tr_b16 v[76:77], v16 offset:24576\n"
"s_addc_u32 s41, s41, 0\n"
"v_mfma_f32_16x16x32_bf16 a[76:79], v[92:95], a[180:183], a[76:79]\n"
"s_mov_b32 s42, 0\n"
"s_mov_b32 s43, 0x20000\n"
"v_mfma_f32_16x16x32_bf16 a[108:111], v[88:91], a[184:187], a[108:111]\n"
"ds_read_b64_tr_b16 v[78:79], v16 offset:28672\n"
"s_add_u32 s44, s44, s61\n"
"v_mfma_f32_16x16x32_bf16 a[108:111], v[92:95], a[188:191], a[108:111]\n"
"s_cbranch_vccnz SKP9\n"
"s_waitcnt lgkmcnt(4)\n"
"v_mfma_f32_16x16x32_bf16 v[144:147], v[64:67], a[160:163], v[144:147]\n"
"ds_read_b64_tr_b16 v[80:81], v45 offset:16384\n"
"v_mfma_f32_16x16x32_bf16 v[144:147], v[68:71], a[164:167], v[144:147]\n"
"v_mfma_f32_16x16x32_bf16 v[176:179], v[64:67], a[168:171], v[176:179]\n"
"ds_read_b64_tr_b16 v[82:83], v45 offset:20480\n"
"v_mfma_f32_16x16x32_bf16 v[176:179], v[68:71], a[172:175], v[176:179]\n"
"v_mfma_f32_16x16x32_bf16 v[208:211], v[64:67], a[176:179], v[208:211]\n"
"ds_read_b64_tr_b16 v[84:85], v45 offset:24576\n"
"v_mfma_f32_16x16x32_bf16 v[208:211], v[68:71], a[180:183], v[208:211]\n"
"v_mfma_f32_16x16x32_bf16 v[240:243], v[64:67], a[184:187], v[240:243]\n"
"ds_read_b64_tr_b16 v[86:87], v45 offset:28672\n"
"v_mfma_f32_16x16x32_bf16 v[240:243], v[68:71], a[188:191], v[240:243]\n"
"s_waitcnt lgkmcnt(4)\n"
"v_mfma_f32_16x16x32_bf16 a[16:19], v[72:75], a[160:163], a[16:19]\n"
"ds_read_b64_tr_b16 v[88:89], v17 offset:16384\n"
"v_mfma_f32_16x16x32_bf16 a[16:19], v[76:79], a[164:167], a[16:19]\n"
"v_mfma_f32_16x16x32_bf16 a[48:51], v[72:75], a[168:171], a[48:51]\n"
"ds_read_b64_tr_b16 v[90:91], v17 offset:20480\n"
"v_mfma_f32_16x16x32_bf16 a[48:51], v[76:79], a[172:175], a[48:51]\n"
"v_mfma_f32_16x16x32_bf16 a[80:83], v[72:75], a[176:179], a[80:83]\n"
"ds_read_b64_tr_b16 v[92:93], v17 offset:24576\n"
"v_mfma_f32_16x16x32_bf16 a[80:83], v[76:79], a[180:183], a[80:83]\n"
"v_mfma_f32_16x16x32_bf16 a[112:115], v[72:75], a[184:187], a[112:115]\n"
"ds_read_b64_tr_b16 v[94:95], v17 offset:28672\n"
"v_mfma_f32_16x16x32_bf16 a[112:115], v[76:79], a[188:191], a[112:115]\n"
"s_waitcnt lgkmcnt(4)\n"
"v_mfma_f32_16x16x32_bf16 v[148:151], v[80:83], a[160:163], v[148:151]\n"
"ds_read_b64_tr_b16 v[64:65], v46 offset:16384\n"
"v_mfma_f32_16x16x32_bf16 v[148:151], v[84:87], a[164:167], v[148:151]\n"
"v_mfma_f32_16x16x32_bf16 v[180:183], v[80:83], a[168:171], v[180:183]\n"
"ds_read_b64_tr_b16 v[66:67], v46 offset:20480\n"
"v_mfma_f32_16x16x32_bf16 v[180:183], v[84:87], a[172:175], v[180:183]\n"
"v_mfma_f32_16x16x32_bf16 v[212:215], v[80:83], a[176:179], v[212:215]\n"
"ds_read_b64_tr_b16 v[68:69], v46 offset:24576\n"
"v_mfma_f32_16x16x32_bf16 v[212:215], v[84:87], a[180:183], v[212:215]\n"
"v_mfma_f32_16x16x32_bf16 v[244:247], v[80:83], a[184:187], v[244:247]\n"
"ds_read_b64_tr_b16 v[70:71], v46 offset:28672\n"
"v_mfma_f32_16x16x32_bf16 v[244:247], v[84:87], a[188:191], v[244:247]\n"
"s_waitcnt lgkmcnt(4)\n"
"v_mfma_f32_16x16x32_bf16 a[20:23], v[88:91], a[160:163], a[20:23]\n"
"ds_read_b64_tr_b16 v[72:73], v18 offset:16384\n"
"v_mfma_f32_16x16x32_bf16 a[20:23], v[92:95], a[164:167], a[20:23]\n"
"v_mfma_f32_16x16x32_bf16 a[52:55], v[88:91], a[168:171], a[52:55]\n"
"ds_read_b64_tr_b16 v[74:75], v18 offset:20480\n"
"v_mfma_f32_16x16x32_bf16 a[52:55], v[92:95], a[172:175], a[52:55]\n"
"v_mfma_f32_16x16x32_bf16 a[84:87], v[88:91], a[176:179], a[84:87]\n"
"ds_read_b64_tr_b16 v[76:77], v18 offset:24576\n"
"v_mfma_f32_16x16x32_bf16 a[84:87], v[92:95], a[180:183], a[84:87]\n"
"v_mfma_f32_16x16x32_bf16 a[116:119], v[88:91], a[184:187], a[116:119]\n"
"ds_read_b64_tr_b16 v[78:79], v18 offset:28672\n"
"v_mfma_f32_16x16x32_bf16 a[116:119], v[92:95], a[188:191], a[116:119]\n"
"SKP9:\n"
"s_waitcnt lgkmcnt(4)\n"
"v_mfma_f32_16x16x32_bf16 v[152:155], v[64:67], a[160:163], v[152:155]\n"
"ds_read_b64_tr_b16 v[80:81], v47 offset:16384\n"
"s_addc_u32 s45, s45, 0\n"
"v_mfma_f32_16x16x32_bf16 v[152:155], v[68:71], a[164:167], v[152:155]\n"
"s_mul_i32 s61, s55, s63\n"
"s_add_u32 s69, s61, s63\n"
"v_mfma_f32_16x16x32_bf16 v[184:187], v[64:67], a[168:171], v[184:187]\n"
"ds_read_b64_tr_b16 v[82:83], v47 offset:20480\n"
"s_lshl_b32 s50, s69, 14\n"
"v_mfma_f32_16x16x32_bf16 v[184:187], v[68:71], a[172:175], v[184:187]\n"
"s_lshl_b32 s90, s69, 17\n"
"s_sub_u32 s90, s90, s65\n"
"v_mfma_f32_16x16x32_bf16 v[216:219], v[64:67], a[176:179], v[216:219]\n"
"ds_read_b64_tr_b16 v[84:85], v47 offset:24576\n"
"s_lshl_b32 s34, s61, 14\n"
"v_mfma_f32_16x16x32_bf16 v[216:219], v[68:71], a[180:183], v[216:219]\n"
"s_add_u32 s35, s34, 8192\n"
"s_lshl_b32 s52, s61, 17\n"
"v_mfma_f32_16x16x32_bf16 v[248:251], v[64:67], a[184:187], v[248:251]\n"
"ds_read_b64_tr_b16 v[86:87], v47 offset:28672\n"
"s_and_b32 s53, s55, 1\n"
"v_mfma_f32_16x16x32_bf16 v[248:251], v[68:71], a[188:191], v[248:251]\n"
"s_lshl_b32 s53, s53, 14\n"
"s_lshr_b32 s69, s55, 1\n"
"s_waitcnt lgkmcnt(4)\n"
"v_mfma_f32_16x16x32_bf16 a[24:27], v[72:75], a[160:163], a[24:27]\n"
"ds_read_b64_tr_b16 v[88:89], v19 offset:16384\n"
"s_lshl_b32 s69, s69, 16\n"
"v_mfma_f32_16x16x32_bf16 a[24:27], v[76:79], a[164:167], a[24:27]\n"
"s_add_u32 s53, s53, s69\n"
"v_mov_b32 v38, 0\n"
"v_mfma_f32_16x16x32_bf16 a[56:59], v[72:75], a[168:171], a[56:59]\n"
"ds_read_b64_tr_b16 v[90:91], v19 offset:20480\n"
"global_load_dword v38, v38, s[48:49] sc1\n"
"v_mfma_f32_16x16x32_bf16 a[56:59], v[76:79], a[172:175], a[56:59]\n"
"v_mfma_f32_16x16x32_bf16 a[88:91], v[72:75], a[176:179], a[88:91]\n"
"ds_read_b64_tr_b16 v[92:93], v19 offset:24576\n"
"v_mfma_f32_16x16x32_bf16 a[88:91], v[76:79], a[180:183], a[88:91]\n"
"v_mfma_f32_16x16x32_bf16 a[120:123], v[72:75], a[184:187], a[120:123]\n"
"ds_read_b64_tr_b16 v[94:95], v19 offset:28672\n"
"v_mfma_f32_16x16x32_bf16 a[120:123], v[76:79], a[188:191], a[120:123]\n"
"s_waitcnt vmcnt(19)\n"
"s_barrier\n"
"s_waitcnt lgkmcnt(4)\n"
"v_mfma_f32_16x16x32_bf16 v[156:159], v[80:83], a[160:163], v[156:159]\n"
"ds_read_b64_tr_b16 v[64:65], v40 offset:32768\n"
"v_mfma_f32_16x16x32_bf16 v[156:159], v[84:87], a[164:167], v[156:159]\n"
"v_mfma_f32_16x16x32_bf16 v[188:191], v[80:83], a[168:171], v[188:191]\n"
"ds_read_b64_tr_b16 v[66:67], v40 offset:36864\n"
"v_mfma_f32_16x16x32_bf16 v[188:191], v[84:87], a[172:175], v[188:191]\n"
"v_mfma_f32_16x16x32_bf16 v[220:223], v[80:83], a[176:179], v[220:223]\n"
"ds_read_b64_tr_b16 v[68:69], v40 offset:40960\n"
"v_mfma_f32_16x16x32_bf16 v[220:223], v[84:87], a[180:183], v[220:223]\n"
"v_mfma_f32_16x16x32_bf16 v[252:255], v[80:83], a[184:187], v[252:255]\n"
"ds_read_b64_tr_b16 v[70:71], v40 offset:45056\n"
"v_mfma_f32_16x16x32_bf16 v[252:255], v[84:87], a[188:191], v[252:255]\n"
"s_waitcnt lgkmcnt(4)\n"
"v_mfma_f32_16x16x32_bf16 a[28:31], v[88:91], a[160:163], a[28:31]\n"
"ds_read_b64_tr_b16 v[72:73], v12 offset:32768\n"
"v_mfma_f32_16x16x32_bf16 a[28:31], v[92:95], a[164:167], a[28:31]\n"
"v_mfma_f32_16x16x32_bf16 a[60:63], v[88:91], a[168:171], a[60:63]\n"
"ds_read_b64_tr_b16 v[74:75], v12 offset:36864\n"
"v_mfma_f32_16x16x32_bf16 a[60:63], v[92:95], a[172:175], a[60:63]\n"
"v_mfma_f32_16x16x32_bf16 a[92:95], v[88:91], a[176:179], a[92:95]\n"
"ds_read_b64_tr_b16 v[76:77], v12 offset:40960\n"
"v_mfma_f32_16x16x32_bf16 a[92:95], v[92:95], a[180:183], a[92:95]\n"
"v_mfma_f32_16x16x32_bf16 a[124:127], v[88:91], a[184:187], a[124:127]\n"
"ds_read_b64_tr_b16 v[78:79], v12 offset:45056\n"
"v_mfma_f32_16x16x32_bf16 a[124:127], v[92:95], a[188:191], a[124:127]\n"
"s_waitcnt lgkmcnt(4)\n"
"v_mfma_f32_16x16x32_bf16 v[128:131], v[64:67], a[192:195], v[128:131]\n"
"ds_read_b64_tr_b16 v[80:81], v41 offset:32768\n"
"v_mfma_f32_16x16x32_bf16 v[128:131], v[68:71], a[196:199], v[128:131]\n"
"v_add_u32 v5, 0, v3\n"
"v_lshlrev_b32 v5, 1, v5\n"
"v_mfma_f32_16x16x32_bf16 v[160:163], v[64:67], a[200:203], v[160:163]\n"
"ds_read_b64_tr_b16 v[82:83], v41 offset:36864\n"
"v_mfma_f32_16x16x32_bf16 v[160:163], v[68:71], a[204:207], v[160:163]\n"
"v_xor_b32 v5, v5, v4\n"
"v_lshlrev_b32 v5, 4, v5\n"
"v_mfma_f32_16x16x32_bf16 v[192:195], v[64:67], a[208:211], v[192:195]\n"
"ds_read_b64_tr_b16 v[84:85], v41 offset:40960\n"
"v_mfma_f32_16x16x32_bf16 v[192:195], v[68:71], a[212:215], v[192:195]\n"
"v_lshl_add_u32 v5, v3, 11, v5\n"
"v_add_u32 v24, 0, v5\n"
"v_mfma_f32_16x16x32_bf16 v[224:227], v[64:67], a[216:219], v[224:227]\n"
"ds_read_b64_tr_b16 v[86:87], v41 offset:45056\n"
"v_mfma_f32_16x16x32_bf16 v[224:227], v[68:71], a[220:223], v[224:227]\n"
"v_add_u32 v5, 4, v3\n"
"v_lshlrev_b32 v5, 1, v5\n"
"s_waitcnt lgkmcnt(4)\n"
"v_mfma_f32_16x16x32_bf16 a[0:3], v[72:75], a[192:195], a[0:3]\n"
"ds_read_b64_tr_b16 v[88:89], v13 offset:32768\n"
"v_mfma_f32_16x16x32_bf16 a[0:3], v[76:79], a[196:199], a[0:3]\n"
"v_xor_b32 v5, v5, v4\n"
"v_lshlrev_b32 v5, 4, v5\n"
"v_mfma_f32_16x16x32_bf16 a[32:35], v[72:75], a[200:203], a[32:35]\n"
"ds_read_b64_tr_b16 v[90:91], v13 offset:36864\n"
"v_mfma_f32_16x16x32_bf16 a[32:35], v[76:79], a[204:207], a[32:35]\n"
"v_lshl_add_u32 v5, v3, 11, v5\n"
"v_add_u32 v25, 7168, v5\n"
"v_mfma_f32_16x16x32_bf16 a[64:67], v[72:75], a[208:211], a[64:67]\n"
"ds_read_b64_tr_b16 v[92:93], v13 offset:40960\n"
"v_mfma_f32_16x16x32_bf16 a[64:67], v[76:79], a[212:215], a[64:67]\n"
"v_add_u32 v5, 0, v3\n"
"v_lshlrev_b32 v5, 1, v5\n"
"v_mfma_f32_16x16x32_bf16 a[96:99], v[72:75], a[216:219], a[96:99]\n"
"ds_read_b64_tr_b16 v[94:95], v13 offset:45056\n"
"v_mfma_f32_16x16x32_bf16 a[96:99], v[76:79], a[220:223], a[96:99]\n"
"v_xor_b32 v5, v5, v4\n"
"v_lshlrev_b32 v5, 4, v5\n"
"s_waitcnt lgkmcnt(4)\n"
"v_mfma_f32_16x16x32_bf16 v[132:135], v[80:83], a[192:195], v[132:135]\n"
"ds_read_b64_tr_b16 v[64:65], v42 offset:32768\n"
"v_mfma_f32_16x16x32_bf16 v[132:135], v[84:87], a[196:199], v[132:135]\n"
"v_lshl_add_u32 v5, v3, 11, v5\n"
"v_add_u32 v26, 14336, v5\n"
"v_mfma_f32_16x16x32_bf16 v[164:167], v[80:83], a[200:203], v[164:167]\n"
"ds_read_b64_tr_b16 v[66:67], v42 offset:36864\n"
"v_mfma_f32_16x16x32_bf16 v[164:167], v[84:87], a[204:207], v[164:167]\n"
"v_add_u32 v5, 4, v3\n"
"v_lshlrev_b32 v5, 1, v5\n"
"v_mfma_f32_16x16x32_bf16 v[196:199], v[80:83], a[208:211], v[196:199]\n"
"ds_read_b64_tr_b16 v[68:69], v42 offset:40960\n"
"v_mfma_f32_16x16x32_bf16 v[196:199], v[84:87], a[212:215], v[196:199]\n"
"v_xor_b32 v5, v5, v4\n"
"v_lshlrev_b32 v5, 4, v5\n"
"v_mfma_f32_16x16x32_bf16 v[228:231], v[80:83], a[216:219], v[228:231]\n"
"ds_read_b64_tr_b16 v[70:71], v42 offset:45056\n"
"v_mfma_f32_16x16x32_bf16 v[228:231], v[84:87], a[220:223], v[228:231]\n"
"v_lshl_add_u32 v5, v3, 11, v5\n"
"v_add_u32 v27, 21504, v5\n"
"s_waitcnt lgkmcnt(4)\n"
"v_mfma_f32_16x16x32_bf16 a[4:7], v[88:91], a[192:195], a[4:7]\n"
"ds_read_b64_tr_b16 v[72:73], v14 offset:32768\n"
"v_mfma_f32_16x16x32_bf16 a[4:7], v[92:95], a[196:199], a[4:7]\n"
"v_mov_b32 v5, s55\n"
"v_xor_b32 v6, 0, v5\n"
"v_mfma_f32_16x16x32_bf16 a[36:39], v[88:91], a[200:203], a[36:39]\n"
"ds_read_b64_tr_b16 v[74:75], v14 offset:36864\n"
"v_mfma_f32_16x16x32_bf16 a[36:39], v[92:95], a[204:207], a[36:39]\n"
"v_lshl_add_u32 v8, v6, 11, v2\n"
"v_xor_b32 v6, 1, v5\n"
"v_mfma_f32_16x16x32_bf16 a[68:71], v[88:91], a[208:211], a[68:71]\n"
"ds_read_b64_tr_b16 v[76:77], v14 offset:40960\n"
"v_mfma_f32_16x16x32_bf16 a[68:71], v[92:95], a[212:215], a[68:71]\n"
"v_lshl_add_u32 v9, v6, 11, v2\n"
"v_xor_b32 v6, 2, v5\n"
"v_mfma_f32_16x16x32_bf16 a[100:103], v[88:91], a[216:219], a[100:103]\n"
"ds_read_b64_tr_b16 v[78:79], v14 offset:45056\n"
"v_mfma_f32_16x16x32_bf16 a[100:103], v[92:95], a[220:223], a[100:103]\n"
"v_lshl_add_u32 v10, v6, 11, v2\n"
"v_xor_b32 v6, 3, v5\n"
"s_waitcnt lgkmcnt(4)\n"
"v_mfma_f32_16x16x32_bf16 v[136:139], v[64:67], a[192:195], v[136:139]\n"
"ds_read_b64_tr_b16 v[80:81], v43 offset:32768\n"
"v_mfma_f32_16x16x32_bf16 v[136:139], v[68:71], a[196:199], v[136:139]\n"
"v_lshl_add_u32 v11, v6, 11, v2\n"
"s_waitcnt vmcnt(0)\n"
"v_readfirstlane_b32 s65, v38\n"
"v_mfma_f32_16x16x32_bf16 v[168:171], v[64:67], a[200:203], v[168:171]\n"
"ds_read_b64_tr_b16 v[82:83], v43 offset:36864\n"
"v_mfma_f32_16x16x32_bf16 v[168:171], v[68:71], a[204:207], v[168:171]\n"
"s_cmp_ge_u32 s65, s51\n"
"s_cselect_b32 s86, s50, 0\n"
"v_mfma_f32_16x16x32_bf16 v[200:203], v[64:67], a[208:211], v[200:203]\n"
"ds_read_b64_tr_b16 v[84:85], v43 offset:40960\n"
"v_mfma_f32_16x16x32_bf16 v[200:203], v[68:71], a[212:215], v[200:203]\n"
"v_mov_b32 v32, 0xc0135761\n"
"v_mov_b32 v33, 0xc0135761\n"
"v_mfma_f32_16x16x32_bf16 v[232:235], v[64:67], a[216:219], v[232:235]\n"
"ds_read_b64_tr_b16 v[86:87], v43 offset:45056\n"
"v_mfma_f32_16x16x32_bf16 v[232:235], v[68:71], a[220:223], v[232:235]\n"
"v_mov_b32 v34, 0xbdd2d3e8\n"
"v_mov_b32 v35, 0xbdd2d3e8\n"
"s_waitcnt lgkmcnt(4)\n"
"v_mfma_f32_16x16x32_bf16 a[8:11], v[72:75], a[192:195], a[8:11]\n"
"ds_read_b64_tr_b16 v[88:89], v15 offset:32768\n"
"v_mfma_f32_16x16x32_bf16 a[8:11], v[76:79], a[196:199], a[8:11]\n"
"s_lshr_b32 s98, s55, 1\n"
"s_mul_i32 s98, s98, 917504\n"
"v_mfma_f32_16x16x32_bf16 a[40:43], v[72:75], a[200:203], a[40:43]\n"
"ds_read_b64_tr_b16 v[90:91], v15 offset:36864\n"
"v_mfma_f32_16x16x32_bf16 a[40:43], v[76:79], a[204:207], a[40:43]\n"
"s_and_b32 s99, s55, 1\n"
"s_lshl_b32 s99, s99, 12\n"
"v_mfma_f32_16x16x32_bf16 a[72:75], v[72:75], a[208:211], a[72:75]\n"
"ds_read_b64_tr_b16 v[92:93], v15 offset:40960\n"
"v_mfma_f32_16x16x32_bf16 a[72:75], v[76:79], a[212:215], a[72:75]\n"
"s_add_u32 s98, s98, s99\n"
"v_add_u32 v28, s98, v2\n"
"v_mfma_f32_16x16x32_bf16 a[104:107], v[72:75], a[216:219], a[104:107]\n"
"ds_read_b64_tr_b16 v[94:95], v15 offset:45056\n"
"v_mfma_f32_16x16x32_bf16 a[104:107], v[76:79], a[220:223], a[104:107]\n"
"v_add_u32 v31, %10, v28\n"
"v_add_u32 v28, %13, v28\n"
"s_waitcnt lgkmcnt(4)\n"
"v_mfma_f32_16x16x32_bf16 v[140:143], v[80:83], a[192:195], v[140:143]\n"
"ds_read_b64_tr_b16 v[64:65], v44 offset:32768\n"
"v_mfma_f32_16x16x32_bf16 v[140:143], v[84:87], a[196:199], v[140:143]\n"
"v_add_u32 v29, 8192, v28\n"
"v_add_u32 v30, 16384, v28\n"
"v_mfma_f32_16x16x32_bf16 v[172:175], v[80:83], a[200:203], v[172:175]\n"
"ds_read_b64_tr_b16 v[66:67], v44 offset:36864\n"
"v_mfma_f32_16x16x32_bf16 v[172:175], v[84:87], a[204:207], v[172:175]\n"
"s_bfe_u32 s61, %17, 0x10001\n"
"s_lshl_b32 s61, s61, 2\n"
"v_mfma_f32_16x16x32_bf16 v[204:207], v[80:83], a[208:211], v[204:207]\n"
"ds_read_b64_tr_b16 v[68:69], v44 offset:40960\n"
"v_mfma_f32_16x16x32_bf16 v[204:207], v[84:87], a[212:215], v[204:207]\n"
"s_bfe_u32 s99, %17, 0x20003\n"
"s_add_u32 s61, s61, s99\n"
"v_mfma_f32_16x16x32_bf16 v[236:239], v[80:83], a[216:219], v[236:239]\n"
"ds_read_b64_tr_b16 v[70:71], v44 offset:45056\n"
"v_mfma_f32_16x16x32_bf16 v[236:239], v[84:87], a[220:223], v[236:239]\n"
"s_lshl_b32 s61, s61, 8\n"
"s_add_u32 s61, s61, 42010624\n"
"s_waitcnt lgkmcnt(4)\n"
"v_mfma_f32_16x16x32_bf16 a[12:15], v[88:91], a[192:195], a[12:15]\n"
"ds_read_b64_tr_b16 v[72:73], v16 offset:32768\n"
"v_mfma_f32_16x16x32_bf16 a[12:15], v[92:95], a[196:199], a[12:15]\n"
"v_mov_b32 v36, s61\n"
"v_mfma_f32_16x16x32_bf16 a[44:47], v[88:91], a[200:203], a[44:47]\n"
"ds_read_b64_tr_b16 v[74:75], v16 offset:36864\n"
"v_mfma_f32_16x16x32_bf16 a[44:47], v[92:95], a[204:207], a[44:47]\n"
"v_mfma_f32_16x16x32_bf16 a[76:79], v[88:91], a[208:211], a[76:79]\n"
"ds_read_b64_tr_b16 v[76:77], v16 offset:40960\n"
"v_mfma_f32_16x16x32_bf16 a[76:79], v[92:95], a[212:215], a[76:79]\n"
"v_mfma_f32_16x16x32_bf16 a[108:111], v[88:91], a[216:219], a[108:111]\n"
"ds_read_b64_tr_b16 v[78:79], v16 offset:45056\n"
"v_mfma_f32_16x16x32_bf16 a[108:111], v[92:95], a[220:223], a[108:111]\n"
"s_cbranch_vccnz SKP10\n"
"s_waitcnt lgkmcnt(4)\n"
"v_mfma_f32_16x16x32_bf16 v[144:147], v[64:67], a[192:195], v[144:147]\n"
"ds_read_b64_tr_b16 v[80:81], v45 offset:32768\n"
"v_mfma_f32_16x16x32_bf16 v[144:147], v[68:71], a[196:199], v[144:147]\n"
"v_mfma_f32_16x16x32_bf16 v[176:179], v[64:67], a[200:203], v[176:179]\n"
"ds_read_b64_tr_b16 v[82:83], v45 offset:36864\n"
"v_mfma_f32_16x16x32_bf16 v[176:179], v[68:71], a[204:207], v[176:179]\n"
"v_mfma_f32_16x16x32_bf16 v[208:211], v[64:67], a[208:211], v[208:211]\n"
"ds_read_b64_tr_b16 v[84:85], v45 offset:40960\n"
"v_mfma_f32_16x16x32_bf16 v[208:211], v[68:71], a[212:215], v[208:211]\n"
"v_mfma_f32_16x16x32_bf16 v[240:243], v[64:67], a[216:219], v[240:243]\n"
"ds_read_b64_tr_b16 v[86:87], v45 offset:45056\n"
"v_mfma_f32_16x16x32_bf16 v[240:243], v[68:71], a[220:223], v[240:243]\n"
"s_waitcnt lgkmcnt(4)\n"
"v_mfma_f32_16x16x32_bf16 a[16:19], v[72:75], a[192:195], a[16:19]\n"
"ds_read_b64_tr_b16 v[88:89], v17 offset:32768\n"
"v_mfma_f32_16x16x32_bf16 a[16:19], v[76:79], a[196:199], a[16:19]\n"
"v_mfma_f32_16x16x32_bf16 a[48:51], v[72:75], a[200:203], a[48:51]\n"
"ds_read_b64_tr_b16 v[90:91], v17 offset:36864\n"
"v_mfma_f32_16x16x32_bf16 a[48:51], v[76:79], a[204:207], a[48:51]\n"
"v_mfma_f32_16x16x32_bf16 a[80:83], v[72:75], a[208:211], a[80:83]\n"
"ds_read_b64_tr_b16 v[92:93], v17 offset:40960\n"
"v_mfma_f32_16x16x32_bf16 a[80:83], v[76:79], a[212:215], a[80:83]\n"
"v_mfma_f32_16x16x32_bf16 a[112:115], v[72:75], a[216:219], a[112:115]\n"
"ds_read_b64_tr_b16 v[94:95], v17 offset:45056\n"
"v_mfma_f32_16x16x32_bf16 a[112:115], v[76:79], a[220:223], a[112:115]\n"
"s_waitcnt lgkmcnt(4)\n"
"v_mfma_f32_16x16x32_bf16 v[148:151], v[80:83], a[192:195], v[148:151]\n"
"ds_read_b64_tr_b16 v[64:65], v46 offset:32768\n"
"v_mfma_f32_16x16x32_bf16 v[148:151], v[84:87], a[196:199], v[148:151]\n"
"v_mfma_f32_16x16x32_bf16 v[180:183], v[80:83], a[200:203], v[180:183]\n"
"ds_read_b64_tr_b16 v[66:67], v46 offset:36864\n"
"v_mfma_f32_16x16x32_bf16 v[180:183], v[84:87], a[204:207], v[180:183]\n"
"v_mfma_f32_16x16x32_bf16 v[212:215], v[80:83], a[208:211], v[212:215]\n"
"ds_read_b64_tr_b16 v[68:69], v46 offset:40960\n"
"v_mfma_f32_16x16x32_bf16 v[212:215], v[84:87], a[212:215], v[212:215]\n"
"v_mfma_f32_16x16x32_bf16 v[244:247], v[80:83], a[216:219], v[244:247]\n"
"ds_read_b64_tr_b16 v[70:71], v46 offset:45056\n"
"v_mfma_f32_16x16x32_bf16 v[244:247], v[84:87], a[220:223], v[244:247]\n"
"s_waitcnt lgkmcnt(4)\n"
"v_mfma_f32_16x16x32_bf16 a[20:23], v[88:91], a[192:195], a[20:23]\n"
"ds_read_b64_tr_b16 v[72:73], v18 offset:32768\n"
"v_mfma_f32_16x16x32_bf16 a[20:23], v[92:95], a[196:199], a[20:23]\n"
"v_mfma_f32_16x16x32_bf16 a[52:55], v[88:91], a[200:203], a[52:55]\n"
"ds_read_b64_tr_b16 v[74:75], v18 offset:36864\n"
"v_mfma_f32_16x16x32_bf16 a[52:55], v[92:95], a[204:207], a[52:55]\n"
"v_mfma_f32_16x16x32_bf16 a[84:87], v[88:91], a[208:211], a[84:87]\n"
"ds_read_b64_tr_b16 v[76:77], v18 offset:40960\n"
"v_mfma_f32_16x16x32_bf16 a[84:87], v[92:95], a[212:215], a[84:87]\n"
"v_mfma_f32_16x16x32_bf16 a[116:119], v[88:91], a[216:219], a[116:119]\n"
"ds_read_b64_tr_b16 v[78:79], v18 offset:45056\n"
"v_mfma_f32_16x16x32_bf16 a[116:119], v[92:95], a[220:223], a[116:119]\n"
"SKP10:\n"
"s_waitcnt lgkmcnt(4)\n"
"v_mfma_f32_16x16x32_bf16 v[152:155], v[64:67], a[192:195], v[152:155]\n"
"ds_read_b64_tr_b16 v[80:81], v47 offset:32768\n"
"v_mfma_f32_16x16x32_bf16 v[152:155], v[68:71], a[196:199], v[152:155]\n"
"v_mfma_f32_16x16x32_bf16 v[184:187], v[64:67], a[200:203], v[184:187]\n"
"ds_read_b64_tr_b16 v[82:83], v47 offset:36864\n"
"v_mfma_f32_16x16x32_bf16 v[184:187], v[68:71], a[204:207], v[184:187]\n"
"v_mfma_f32_16x16x32_bf16 v[216:219], v[64:67], a[208:211], v[216:219]\n"
"ds_read_b64_tr_b16 v[84:85], v47 offset:40960\n"
"v_mfma_f32_16x16x32_bf16 v[216:219], v[68:71], a[212:215], v[216:219]\n"
"v_mfma_f32_16x16x32_bf16 v[248:251], v[64:67], a[216:219], v[248:251]\n"
"ds_read_b64_tr_b16 v[86:87], v47 offset:45056\n"
"v_mfma_f32_16x16x32_bf16 v[248:251], v[68:71], a[220:223], v[248:251]\n"
"s_waitcnt lgkmcnt(4)\n"
"v_mfma_f32_16x16x32_bf16 a[24:27], v[72:75], a[192:195], a[24:27]\n"
"ds_read_b64_tr_b16 v[88:89], v19 offset:32768\n"
"v_mfma_f32_16x16x32_bf16 a[24:27], v[76:79], a[196:199], a[24:27]\n"
"v_mfma_f32_16x16x32_bf16 a[56:59], v[72:75], a[200:203], a[56:59]\n"
"ds_read_b64_tr_b16 v[90:91], v19 offset:36864\n"
"v_mfma_f32_16x16x32_bf16 a[56:59], v[76:79], a[204:207], a[56:59]\n"
"v_mfma_f32_16x16x32_bf16 a[88:91], v[72:75], a[208:211], a[88:91]\n"
"ds_read_b64_tr_b16 v[92:93], v19 offset:40960\n"
"v_mfma_f32_16x16x32_bf16 a[88:91], v[76:79], a[212:215], a[88:91]\n"
"v_mfma_f32_16x16x32_bf16 a[120:123], v[72:75], a[216:219], a[120:123]\n"
"ds_read_b64_tr_b16 v[94:95], v19 offset:45056\n"
"v_mfma_f32_16x16x32_bf16 a[120:123], v[76:79], a[220:223], a[120:123]\n"
"s_barrier\n"
"s_waitcnt lgkmcnt(4)\n"
"v_mfma_f32_16x16x32_bf16 v[156:159], v[80:83], a[192:195], v[156:159]\n"
"ds_read_b64_tr_b16 v[64:65], v40 offset:49152\n"
"v_mfma_f32_16x16x32_bf16 v[156:159], v[84:87], a[196:199], v[156:159]\n"
"v_mfma_f32_16x16x32_bf16 v[188:191], v[80:83], a[200:203], v[188:191]\n"
"ds_read_b64_tr_b16 v[66:67], v40 offset:53248\n"
"v_mfma_f32_16x16x32_bf16 v[188:191], v[84:87], a[204:207], v[188:191]\n"
"v_mfma_f32_16x16x32_bf16 v[220:223], v[80:83], a[208:211], v[220:223]\n"
"ds_read_b64_tr_b16 v[68:69], v40 offset:57344\n"
"v_mfma_f32_16x16x32_bf16 v[220:223], v[84:87], a[212:215], v[220:223]\n"
"v_mfma_f32_16x16x32_bf16 v[252:255], v[80:83], a[216:219], v[252:255]\n"
"ds_read_b64_tr_b16 v[70:71], v40 offset:61440\n"
"v_mfma_f32_16x16x32_bf16 v[252:255], v[84:87], a[220:223], v[252:255]\n"
"s_waitcnt lgkmcnt(4)\n"
"v_mfma_f32_16x16x32_bf16 a[28:31], v[88:91], a[192:195], a[28:31]\n"
"ds_read_b64_tr_b16 v[72:73], v12 offset:49152\n"
"v_mfma_f32_16x16x32_bf16 a[28:31], v[92:95], a[196:199], a[28:31]\n"
"v_mfma_f32_16x16x32_bf16 a[60:63], v[88:91], a[200:203], a[60:63]\n"
"ds_read_b64_tr_b16 v[74:75], v12 offset:53248\n"
"v_mfma_f32_16x16x32_bf16 a[60:63], v[92:95], a[204:207], a[60:63]\n"
"v_mfma_f32_16x16x32_bf16 a[92:95], v[88:91], a[208:211], a[92:95]\n"
"ds_read_b64_tr_b16 v[76:77], v12 offset:57344\n"
"v_mfma_f32_16x16x32_bf16 a[92:95], v[92:95], a[212:215], a[92:95]\n"
"v_mfma_f32_16x16x32_bf16 a[124:127], v[88:91], a[216:219], a[124:127]\n"
"ds_read_b64_tr_b16 v[78:79], v12 offset:61440\n"
"v_mfma_f32_16x16x32_bf16 a[124:127], v[92:95], a[220:223], a[124:127]\n"
"s_waitcnt lgkmcnt(4)\n"
"v_mfma_f32_16x16x32_bf16 v[128:131], v[64:67], a[224:227], v[128:131]\n"
"ds_read_b64_tr_b16 v[80:81], v41 offset:49152\n"
"s_add_u32 m0, s53, 0\n"
"v_mfma_f32_16x16x32_bf16 v[128:131], v[68:71], a[228:231], v[128:131]\n"
"buffer_load_dwordx4 v24, s[88:91], s52 offen offset:0 lds\n"
"v_mfma_f32_16x16x32_bf16 v[160:163], v[64:67], a[232:235], v[160:163]\n"
"ds_read_b64_tr_b16 v[82:83], v41 offset:53248\n"
"v_mfma_f32_16x16x32_bf16 v[160:163], v[68:71], a[236:239], v[160:163]\n"
"buffer_load_dwordx4 v25, s[88:91], s52 offen offset:1024 lds\n"
"v_mfma_f32_16x16x32_bf16 v[192:195], v[64:67], a[240:243], v[192:195]\n"
"ds_read_b64_tr_b16 v[84:85], v41 offset:57344\n"
"v_mfma_f32_16x16x32_bf16 v[192:195], v[68:71], a[244:247], v[192:195]\n"
"buffer_load_dwordx4 v26, s[88:91], s52 offen offset:2048 lds\n"
"v_mfma_f32_16x16x32_bf16 v[224:227], v[64:67], a[248:251], v[224:227]\n"
"ds_read_b64_tr_b16 v[86:87], v41 offset:61440\n"
"v_mfma_f32_16x16x32_bf16 v[224:227], v[68:71], a[252:255], v[224:227]\n"
"s_waitcnt lgkmcnt(4)\n"
"v_mfma_f32_16x16x32_bf16 a[0:3], v[72:75], a[224:227], a[0:3]\n"
"ds_read_b64_tr_b16 v[88:89], v13 offset:49152\n"
"v_mfma_f32_16x16x32_bf16 a[0:3], v[76:79], a[228:231], a[0:3]\n"
"buffer_load_dwordx4 v27, s[88:91], s52 offen offset:3072 lds\n"
"s_add_u32 s52, s52, 32768\n"
"v_mfma_f32_16x16x32_bf16 a[32:35], v[72:75], a[232:235], a[32:35]\n"
"ds_read_b64_tr_b16 v[90:91], v13 offset:53248\n"
"s_add_u32 m0, s53, 4096\n"
"v_mfma_f32_16x16x32_bf16 a[32:35], v[76:79], a[236:239], a[32:35]\n"
"buffer_load_dwordx4 v24, s[88:91], s52 offen offset:0 lds\n"
"v_mfma_f32_16x16x32_bf16 a[64:67], v[72:75], a[240:243], a[64:67]\n"
"ds_read_b64_tr_b16 v[92:93], v13 offset:57344\n"
"v_mfma_f32_16x16x32_bf16 a[64:67], v[76:79], a[244:247], a[64:67]\n"
"buffer_load_dwordx4 v25, s[88:91], s52 offen offset:1024 lds\n"
"v_mfma_f32_16x16x32_bf16 a[96:99], v[72:75], a[248:251], a[96:99]\n"
"ds_read_b64_tr_b16 v[94:95], v13 offset:61440\n"
"v_mfma_f32_16x16x32_bf16 a[96:99], v[76:79], a[252:255], a[96:99]\n"
"s_waitcnt lgkmcnt(4)\n"
"v_mfma_f32_16x16x32_bf16 v[132:135], v[80:83], a[224:227], v[132:135]\n"
"ds_read_b64_tr_b16 v[64:65], v42 offset:49152\n"
"v_mfma_f32_16x16x32_bf16 v[132:135], v[84:87], a[228:231], v[132:135]\n"
"buffer_load_dwordx4 v26, s[88:91], s52 offen offset:2048 lds\n"
"v_mfma_f32_16x16x32_bf16 v[164:167], v[80:83], a[232:235], v[164:167]\n"
"ds_read_b64_tr_b16 v[66:67], v42 offset:53248\n"
"v_mfma_f32_16x16x32_bf16 v[164:167], v[84:87], a[236:239], v[164:167]\n"
"buffer_load_dwordx4 v27, s[88:91], s52 offen offset:3072 lds\n"
"s_add_u32 s52, s52, 32768\n"
"v_mfma_f32_16x16x32_bf16 v[196:199], v[80:83], a[240:243], v[196:199]\n"
"ds_read_b64_tr_b16 v[68:69], v42 offset:57344\n"
"v_mfma_f32_16x16x32_bf16 v[196:199], v[84:87], a[244:247], v[196:199]\n"
"v_mfma_f32_16x16x32_bf16 v[228:231], v[80:83], a[248:251], v[228:231]\n"
"ds_read_b64_tr_b16 v[70:71], v42 offset:61440\n"
"s_add_u32 m0, s53, 8192\n"
"v_mfma_f32_16x16x32_bf16 v[228:231], v[84:87], a[252:255], v[228:231]\n"
"buffer_load_dwordx4 v24, s[88:91], s52 offen offset:0 lds\n"
"s_waitcnt lgkmcnt(4)\n"
"v_mfma_f32_16x16x32_bf16 a[4:7], v[88:91], a[224:227], a[4:7]\n"
"ds_read_b64_tr_b16 v[72:73], v14 offset:49152\n"
"v_mfma_f32_16x16x32_bf16 a[4:7], v[92:95], a[228:231], a[4:7]\n"
"buffer_load_dwordx4 v25, s[88:91], s52 offen offset:1024 lds\n"
"v_mfma_f32_16x16x32_bf16 a[36:39], v[88:91], a[232:235], a[36:39]\n"
"ds_read_b64_tr_b16 v[74:75], v14 offset:53248\n"
"v_mfma_f32_16x16x32_bf16 a[36:39], v[92:95], a[236:239], a[36:39]\n"
"buffer_load_dwordx4 v26, s[88:91], s52 offen offset:2048 lds\n"
"v_mfma_f32_16x16x32_bf16 a[68:71], v[88:91], a[240:243], a[68:71]\n"
"ds_read_b64_tr_b16 v[76:77], v14 offset:57344\n"
"v_mfma_f32_16x16x32_bf16 a[68:71], v[92:95], a[244:247], a[68:71]\n"
"v_mfma_f32_16x16x32_bf16 a[100:103], v[88:91], a[248:251], a[100:103]\n"
"ds_read_b64_tr_b16 v[78:79], v14 offset:61440\n"
"v_mfma_f32_16x16x32_bf16 a[100:103], v[92:95], a[252:255], a[100:103]\n"
"buffer_load_dwordx4 v27, s[88:91], s52 offen offset:3072 lds\n"
"s_add_u32 s52, s52, 32768\n"
"s_waitcnt lgkmcnt(4)\n"
"v_mfma_f32_16x16x32_bf16 v[136:139], v[64:67], a[224:227], v[136:139]\n"
"ds_read_b64_tr_b16 v[80:81], v43 offset:49152\n"
"s_add_u32 m0, s53, 12288\n"
"v_mfma_f32_16x16x32_bf16 v[136:139], v[68:71], a[228:231], v[136:139]\n"
"buffer_load_dwordx4 v24, s[88:91], s52 offen offset:0 lds\n"
"v_mfma_f32_16x16x32_bf16 v[168:171], v[64:67], a[232:235], v[168:171]\n"
"ds_read_b64_tr_b16 v[82:83], v43 offset:53248\n"
"v_mfma_f32_16x16x32_bf16 v[168:171], v[68:71], a[236:239], v[168:171]\n"
"buffer_load_dwordx4 v25, s[88:91], s52 offen offset:1024 lds\n"
"v_mfma_f32_16x16x32_bf16 v[200:203], v[64:67], a[240:243], v[200:203]\n"
"ds_read_b64_tr_b16 v[84:85], v43 offset:57344\n"
"v_mfma_f32_16x16x32_bf16 v[200:203], v[68:71], a[244:247], v[200:203]\n"
"v_mfma_f32_16x16x32_bf16 v[232:235], v[64:67], a[248:251], v[232:235]\n"
"ds_read_b64_tr_b16 v[86:87], v43 offset:61440\n"
"v_mfma_f32_16x16x32_bf16 v[232:235], v[68:71], a[252:255], v[232:235]\n"
"buffer_load_dwordx4 v26, s[88:91], s52 offen offset:2048 lds\n"
"s_waitcnt lgkmcnt(4)\n"
"v_mfma_f32_16x16x32_bf16 a[8:11], v[72:75], a[224:227], a[8:11]\n"
"ds_read_b64_tr_b16 v[88:89], v15 offset:49152\n"
"v_mfma_f32_16x16x32_bf16 a[8:11], v[76:79], a[228:231], a[8:11]\n"
"buffer_load_dwordx4 v27, s[88:91], s52 offen offset:3072 lds\n"
"s_add_u32 s52, s52, 32768\n"
"v_mfma_f32_16x16x32_bf16 a[40:43], v[72:75], a[232:235], a[40:43]\n"
"ds_read_b64_tr_b16 v[90:91], v15 offset:53248\n"
"v_mfma_f32_16x16x32_bf16 a[40:43], v[76:79], a[236:239], a[40:43]\n"
"v_mfma_f32_16x16x32_bf16 a[72:75], v[72:75], a[240:243], a[72:75]\n"
"ds_read_b64_tr_b16 v[92:93], v15 offset:57344\n"
"v_mfma_f32_16x16x32_bf16 a[72:75], v[76:79], a[244:247], a[72:75]\n"
"buffer_load_dwordx4 a[128:131], v8, s[84:87], s34 offen offset:0 sc1\n"
"v_mfma_f32_16x16x32_bf16 a[104:107], v[72:75], a[248:251], a[104:107]\n"
"ds_read_b64_tr_b16 v[94:95], v15 offset:61440\n"
"v_mfma_f32_16x16x32_bf16 a[104:107], v[76:79], a[252:255], a[104:107]\n"
"buffer_load_dwordx4 a[132:135], v8, s[84:87], s35 offen offset:0 sc1\n"
"s_waitcnt lgkmcnt(4)\n"
"v_mfma_f32_16x16x32_bf16 v[140:143], v[80:83], a[224:227], v[140:143]\n"
"ds_read_b64_tr_b16 v[64:65], v44 offset:49152\n"
"v_mfma_f32_16x16x32_bf16 v[140:143], v[84:87], a[228:231], v[140:143]\n"
"buffer_load_dwordx4 a[136:139], v8, s[84:87], s34 offen offset:1024 sc1\n"
"v_mfma_f32_16x16x32_bf16 v[172:175], v[80:83], a[232:235], v[172:175]\n"
"ds_read_b64_tr_b16 v[66:67], v44 offset:53248\n"
"v_mfma_f32_16x16x32_bf16 v[172:175], v[84:87], a[236:239], v[172:175]\n"
"v_mfma_f32_16x16x32_bf16 v[204:207], v[80:83], a[240:243], v[204:207]\n"
"ds_read_b64_tr_b16 v[68:69], v44 offset:57344\n"
"v_mfma_f32_16x16x32_bf16 v[204:207], v[84:87], a[244:247], v[204:207]\n"
"buffer_load_dwordx4 a[140:143], v8, s[84:87], s35 offen offset:1024 sc1\n"
"v_mfma_f32_16x16x32_bf16 v[236:239], v[80:83], a[248:251], v[236:239]\n"
"ds_read_b64_tr_b16 v[70:71], v44 offset:61440\n"
"v_mfma_f32_16x16x32_bf16 v[236:239], v[84:87], a[252:255], v[236:239]\n"
"buffer_load_dwordx4 a[144:147], v9, s[84:87], s34 offen offset:0 sc1\n"
"s_waitcnt lgkmcnt(4)\n"
"v_mfma_f32_16x16x32_bf16 a[12:15], v[88:91], a[224:227], a[12:15]\n"
"ds_read_b64_tr_b16 v[72:73], v16 offset:49152\n"
"v_mfma_f32_16x16x32_bf16 a[12:15], v[92:95], a[228:231], a[12:15]\n"
"buffer_load_dwordx4 a[148:151], v9, s[84:87], s35 offen offset:0 sc1\n"
"v_mfma_f32_16x16x32_bf16 a[44:47], v[88:91], a[232:235], a[44:47]\n"
"ds_read_b64_tr_b16 v[74:75], v16 offset:53248\n"
"v_mfma_f32_16x16x32_bf16 a[44:47], v[92:95], a[236:239], a[44:47]\n"
"v_mfma_f32_16x16x32_bf16 a[76:79], v[88:91], a[240:243], a[76:79]\n"
"ds_read_b64_tr_b16 v[76:77], v16 offset:57344\n"
"v_mfma_f32_16x16x32_bf16 a[76:79], v[92:95], a[244:247], a[76:79]\n"
"buffer_load_dwordx4 a[152:155], v9, s[84:87], s34 offen offset:1024 sc1\n"
"v_mfma_f32_16x16x32_bf16 a[108:111], v[88:91], a[248:251], a[108:111]\n"
"ds_read_b64_tr_b16 v[78:79], v16 offset:61440\n"
"v_mfma_f32_16x16x32_bf16 a[108:111], v[92:95], a[252:255], a[108:111]\n"
"s_cbranch_vccnz SKP11\n"
"s_waitcnt lgkmcnt(4)\n"
"v_mfma_f32_16x16x32_bf16 v[144:147], v[64:67], a[224:227], v[144:147]\n"
"ds_read_b64_tr_b16 v[80:81], v45 offset:49152\n"
"v_mfma_f32_16x16x32_bf16 v[144:147], v[68:71], a[228:231], v[144:147]\n"
"v_mfma_f32_16x16x32_bf16 v[176:179], v[64:67], a[232:235], v[176:179]\n"
"ds_read_b64_tr_b16 v[82:83], v45 offset:53248\n"
"v_mfma_f32_16x16x32_bf16 v[176:179], v[68:71], a[236:239], v[176:179]\n"
"v_mfma_f32_16x16x32_bf16 v[208:211], v[64:67], a[240:243], v[208:211]\n"
"ds_read_b64_tr_b16 v[84:85], v45 offset:57344\n"
"v_mfma_f32_16x16x32_bf16 v[208:211], v[68:71], a[244:247], v[208:211]\n"
"v_mfma_f32_16x16x32_bf16 v[240:243], v[64:67], a[248:251], v[240:243]\n"
"ds_read_b64_tr_b16 v[86:87], v45 offset:61440\n"
"v_mfma_f32_16x16x32_bf16 v[240:243], v[68:71], a[252:255], v[240:243]\n"
"s_waitcnt lgkmcnt(4)\n"
"v_mfma_f32_16x16x32_bf16 a[16:19], v[72:75], a[224:227], a[16:19]\n"
"ds_read_b64_tr_b16 v[88:89], v17 offset:49152\n"
"v_mfma_f32_16x16x32_bf16 a[16:19], v[76:79], a[228:231], a[16:19]\n"
"v_mfma_f32_16x16x32_bf16 a[48:51], v[72:75], a[232:235], a[48:51]\n"
"ds_read_b64_tr_b16 v[90:91], v17 offset:53248\n"
"v_mfma_f32_16x16x32_bf16 a[48:51], v[76:79], a[236:239], a[48:51]\n"
"v_mfma_f32_16x16x32_bf16 a[80:83], v[72:75], a[240:243], a[80:83]\n"
"ds_read_b64_tr_b16 v[92:93], v17 offset:57344\n"
"v_mfma_f32_16x16x32_bf16 a[80:83], v[76:79], a[244:247], a[80:83]\n"
"v_mfma_f32_16x16x32_bf16 a[112:115], v[72:75], a[248:251], a[112:115]\n"
"ds_read_b64_tr_b16 v[94:95], v17 offset:61440\n"
"v_mfma_f32_16x16x32_bf16 a[112:115], v[76:79], a[252:255], a[112:115]\n"
"s_waitcnt lgkmcnt(4)\n"
"v_mfma_f32_16x16x32_bf16 v[148:151], v[80:83], a[224:227], v[148:151]\n"
"ds_read_b64_tr_b16 v[64:65], v46 offset:49152\n"
"v_mfma_f32_16x16x32_bf16 v[148:151], v[84:87], a[228:231], v[148:151]\n"
"v_mfma_f32_16x16x32_bf16 v[180:183], v[80:83], a[232:235], v[180:183]\n"
"ds_read_b64_tr_b16 v[66:67], v46 offset:53248\n"
"v_mfma_f32_16x16x32_bf16 v[180:183], v[84:87], a[236:239], v[180:183]\n"
"v_mfma_f32_16x16x32_bf16 v[212:215], v[80:83], a[240:243], v[212:215]\n"
"ds_read_b64_tr_b16 v[68:69], v46 offset:57344\n"
"v_mfma_f32_16x16x32_bf16 v[212:215], v[84:87], a[244:247], v[212:215]\n"
"v_mfma_f32_16x16x32_bf16 v[244:247], v[80:83], a[248:251], v[244:247]\n"
"ds_read_b64_tr_b16 v[70:71], v46 offset:61440\n"
"v_mfma_f32_16x16x32_bf16 v[244:247], v[84:87], a[252:255], v[244:247]\n"
"s_waitcnt lgkmcnt(4)\n"
"v_mfma_f32_16x16x32_bf16 a[20:23], v[88:91], a[224:227], a[20:23]\n"
"ds_read_b64_tr_b16 v[72:73], v18 offset:49152\n"
"v_mfma_f32_16x16x32_bf16 a[20:23], v[92:95], a[228:231], a[20:23]\n"
"v_mfma_f32_16x16x32_bf16 a[52:55], v[88:91], a[232:235], a[52:55]\n"
"ds_read_b64_tr_b16 v[74:75], v18 offset:53248\n"
"v_mfma_f32_16x16x32_bf16 a[52:55], v[92:95], a[236:239], a[52:55]\n"
"v_mfma_f32_16x16x32_bf16 a[84:87], v[88:91], a[240:243], a[84:87]\n"
"ds_read_b64_tr_b16 v[76:77], v18 offset:57344\n"
"v_mfma_f32_16x16x32_bf16 a[84:87], v[92:95], a[244:247], a[84:87]\n"
"v_mfma_f32_16x16x32_bf16 a[116:119], v[88:91], a[248:251], a[116:119]\n"
"ds_read_b64_tr_b16 v[78:79], v18 offset:61440\n"
"v_mfma_f32_16x16x32_bf16 a[116:119], v[92:95], a[252:255], a[116:119]\n"
"SKP11:\n"
"s_waitcnt lgkmcnt(4)\n"
"v_mfma_f32_16x16x32_bf16 v[152:155], v[64:67], a[224:227], v[152:155]\n"
"ds_read_b64_tr_b16 v[80:81], v47 offset:49152\n"
"v_mfma_f32_16x16x32_bf16 v[152:155], v[68:71], a[228:231], v[152:155]\n"
"buffer_load_dwordx4 a[156:159], v9, s[84:87], s35 offen offset:1024 sc1\n"
"v_mfma_f32_16x16x32_bf16 v[184:187], v[64:67], a[232:235], v[184:187]\n"
"ds_read_b64_tr_b16 v[82:83], v47 offset:53248\n"
"v_mfma_f32_16x16x32_bf16 v[184:187], v[68:71], a[236:239], v[184:187]\n"
"v_mfma_f32_16x16x32_bf16 v[216:219], v[64:67], a[240:243], v[216:219]\n"
"ds_read_b64_tr_b16 v[84:85], v47 offset:57344\n"
"v_mfma_f32_16x16x32_bf16 v[216:219], v[68:71], a[244:247], v[216:219]\n"
"buffer_load_dwordx4 a[160:163], v10, s[84:87], s34 offen offset:0 sc1\n"
"v_mfma_f32_16x16x32_bf16 v[248:251], v[64:67], a[248:251], v[248:251]\n"
"ds_read_b64_tr_b16 v[86:87], v47 offset:61440\n"
"v_mfma_f32_16x16x32_bf16 v[248:251], v[68:71], a[252:255], v[248:251]\n"
"buffer_load_dwordx4 a[164:167], v10, s[84:87], s35 offen offset:0 sc1\n"
"s_waitcnt lgkmcnt(4)\n"
"v_mfma_f32_16x16x32_bf16 a[24:27], v[72:75], a[224:227], a[24:27]\n"
"ds_read_b64_tr_b16 v[88:89], v19 offset:49152\n"
"v_mfma_f32_16x16x32_bf16 a[24:27], v[76:79], a[228:231], a[24:27]\n"
"buffer_load_dwordx4 a[168:171], v10, s[84:87], s34 offen offset:1024 sc1\n"
"v_mfma_f32_16x16x32_bf16 a[56:59], v[72:75], a[232:235], a[56:59]\n"
"ds_read_b64_tr_b16 v[90:91], v19 offset:53248\n"
"v_mfma_f32_16x16x32_bf16 a[56:59], v[76:79], a[236:239], a[56:59]\n"
"v_mfma_f32_16x16x32_bf16 a[88:91], v[72:75], a[240:243], a[88:91]\n"
"ds_read_b64_tr_b16 v[92:93], v19 offset:57344\n"
"v_mfma_f32_16x16x32_bf16 a[88:91], v[76:79], a[244:247], a[88:91]\n"
"buffer_load_dwordx4 a[172:175], v10, s[84:87], s35 offen offset:1024 sc1\n"
"v_mfma_f32_16x16x32_bf16 a[120:123], v[72:75], a[248:251], a[120:123]\n"
"ds_read_b64_tr_b16 v[94:95], v19 offset:61440\n"
"v_mfma_f32_16x16x32_bf16 a[120:123], v[76:79], a[252:255], a[120:123]\n"
"s_waitcnt lgkmcnt(0)\n"
"s_barrier\n"
"v_mfma_f32_16x16x32_bf16 v[156:159], v[80:83], a[224:227], v[156:159]\n"
"v_mfma_f32_16x16x32_bf16 v[156:159], v[84:87], a[228:231], v[156:159]\n"
"v_mfma_f32_16x16x32_bf16 v[188:191], v[80:83], a[232:235], v[188:191]\n"
"v_mfma_f32_16x16x32_bf16 v[188:191], v[84:87], a[236:239], v[188:191]\n"
"buffer_load_dwordx4 a[176:179], v11, s[84:87], s34 offen offset:0 sc1\n"
"v_mfma_f32_16x16x32_bf16 v[220:223], v[80:83], a[240:243], v[220:223]\n"
"v_mfma_f32_16x16x32_bf16 v[220:223], v[84:87], a[244:247], v[220:223]\n"
"buffer_load_dwordx4 a[180:183], v11, s[84:87], s35 offen offset:0 sc1\n"
"v_mfma_f32_16x16x32_bf16 v[252:255], v[80:83], a[248:251], v[252:255]\n"
"v_mfma_f32_16x16x32_bf16 v[252:255], v[84:87], a[252:255], v[252:255]\n"
"v_mfma_f32_16x16x32_bf16 a[28:31], v[88:91], a[224:227], a[28:31]\n"
"v_mfma_f32_16x16x32_bf16 a[28:31], v[92:95], a[228:231], a[28:31]\n"
"buffer_load_dwordx4 a[184:187], v11, s[84:87], s34 offen offset:1024 sc1\n"
"v_mfma_f32_16x16x32_bf16 a[60:63], v[88:91], a[232:235], a[60:63]\n"
"v_mfma_f32_16x16x32_bf16 a[60:63], v[92:95], a[236:239], a[60:63]\n"
"buffer_load_dwordx4 a[188:191], v11, s[84:87], s35 offen offset:1024 sc1\n"
"v_mfma_f32_16x16x32_bf16 a[92:95], v[88:91], a[240:243], a[92:95]\n"
"v_mfma_f32_16x16x32_bf16 a[92:95], v[92:95], a[244:247], a[92:95]\n"
"v_mfma_f32_16x16x32_bf16 a[124:127], v[88:91], a[248:251], a[124:127]\n"
"v_mfma_f32_16x16x32_bf16 a[124:127], v[92:95], a[252:255], a[124:127]\n"
"v_pk_mul_f32 v[96:97], v[128:129], v[128:129]\n"
"v_pk_mul_f32 v[98:99], v[130:131], v[130:131]\n"
"v_pk_mul_f32 v[100:101], v[132:133], v[132:133]\n"
"v_pk_mul_f32 v[102:103], v[134:135], v[134:135]\n"
"v_accvgpr_read_b32 v104, a0\n"
"v_accvgpr_read_b32 v105, a1\n"
"v_accvgpr_read_b32 v106, a2\n"
"v_accvgpr_read_b32 v107, a3\n"
"v_accvgpr_read_b32 v108, a4\n"
"v_accvgpr_read_b32 v109, a5\n"
"v_accvgpr_read_b32 v110, a6\n"
"v_accvgpr_read_b32 v111, a7\n"
"s_add_u32 m0, s53, 32768\n"
"s_nop 0\n"
"buffer_load_dwordx4 v24, s[88:91], s52 offen offset:0 lds\n"
"v_pk_fma_f32 v[96:97], v[96:97], v[34:35], v[32:33]\n"
"v_pk_fma_f32 v[98:99], v[98:99], v[34:35], v[32:33]\n"
"v_pk_fma_f32 v[100:101], v[100:101], v[34:35], v[32:33]\n"
"v_pk_fma_f32 v[102:103], v[102:103], v[34:35], v[32:33]\n"
"v_pk_mul_f32 v[96:97], v[96:97], v[128:129]\n"
"v_pk_mul_f32 v[98:99], v[98:99], v[130:131]\n"
"v_pk_mul_f32 v[100:101], v[100:101], v[132:133]\n"
"v_pk_mul_f32 v[102:103], v[102:103], v[134:135]\n"
"v_exp_f32 v96, v96\n"
"v_exp_f32 v97, v97\n"
"v_exp_f32 v98, v98\n"
"v_exp_f32 v99, v99\n"
"v_exp_f32 v100, v100\n"
"v_exp_f32 v101, v101\n"
"v_exp_f32 v102, v102\n"
"v_exp_f32 v103, v103\n"
"buffer_load_dwordx4 v25, s[88:91], s52 offen offset:1024 lds\n"
"v_pk_mul_f32 v[104:105], v[104:105], v[128:129]\n"
"v_pk_mul_f32 v[106:107], v[106:107], v[130:131]\n"
"v_pk_mul_f32 v[108:109], v[108:109], v[132:133]\n"
"v_pk_mul_f32 v[110:111], v[110:111], v[134:135]\n"
"v_pk_add_f32 v[96:97], v[96:97], 1.0 op_sel_hi:[1,0]\n"
"v_pk_add_f32 v[98:99], v[98:99], 1.0 op_sel_hi:[1,0]\n"
"v_pk_add_f32 v[100:101], v[100:101], 1.0 op_sel_hi:[1,0]\n"
"v_pk_add_f32 v[102:103], v[102:103], 1.0 op_sel_hi:[1,0]\n"
"v_rcp_f32 v96, v96\n"
"v_rcp_f32 v97, v97\n"
"v_rcp_f32 v98, v98\n"
"v_rcp_f32 v99, v99\n"
"v_rcp_f32 v100, v100\n"
"v_rcp_f32 v101, v101\n"
"v_rcp_f32 v102, v102\n"
"v_rcp_f32 v103, v103\n"
"v_pk_mul_f32 v[96:97], v[96:97], v[104:105]\n"
"v_pk_mul_f32 v[98:99], v[98:99], v[106:107]\n"
"v_pk_mul_f32 v[100:101], v[100:101], v[108:109]\n"
"v_pk_mul_f32 v[102:103], v[102:103], v[110:111]\n"
"v_cvt_pk_bf16_f32 v128, v96, v97\n"
"v_cvt_pk_bf16_f32 v129, v98, v99\n"
"v_cvt_pk_bf16_f32 v130, v100, v101\n"
"v_cvt_pk_bf16_f32 v131, v102, v103\n"
"global_store_dwordx4 v28, v[128:131], %0 offset:0 sc1\n"
"v_pk_mul_f32 v[48:49], v[160:161], v[160:161]\n"
"v_pk_mul_f32 v[50:51], v[162:163], v[162:163]\n"
"v_pk_mul_f32 v[52:53], v[164:165], v[164:165]\n"
"v_pk_mul_f32 v[54:55], v[166:167], v[166:167]\n"
"v_accvgpr_read_b32 v56, a32\n"
"v_accvgpr_read_b32 v57, a33\n"
"v_accvgpr_read_b32 v58, a34\n"
"v_accvgpr_read_b32 v59, a35\n"
"v_accvgpr_read_b32 v60, a36\n"
"v_accvgpr_read_b32 v61, a37\n"
"v_accvgpr_read_b32 v62, a38\n"
"v_accvgpr_read_b32 v63, a39\n"
"buffer_load_dwordx4 v26, s[88:91], s52 offen offset:2048 lds\n"
"v_pk_fma_f32 v[48:49], v[48:49], v[34:35], v[32:33]\n"
"v_pk_fma_f32 v[50:51], v[50:51], v[34:35], v[32:33]\n"
"v_pk_fma_f32 v[52:53], v[52:53], v[34:35], v[32:33]\n"
"v_pk_fma_f32 v[54:55], v[54:55], v[34:35], v[32:33]\n"
"v_pk_mul_f32 v[48:49], v[48:49], v[160:161]\n"
"v_pk_mul_f32 v[50:51], v[50:51], v[162:163]\n"
"v_pk_mul_f32 v[52:53], v[52:53], v[164:165]\n"
"v_pk_mul_f32 v[54:55], v[54:55], v[166:167]\n"
"v_exp_f32 v48, v48\n"
"v_exp_f32 v49, v49\n"
"v_exp_f32 v50, v50\n"
"v_exp_f32 v51, v51\n"
"v_exp_f32 v52, v52\n"
"v_exp_f32 v53, v53\n"
"v_exp_f32 v54, v54\n"
"v_exp_f32 v55, v55\n"
"buffer_load_dwordx4 v27, s[88:91], s52 offen offset:3072 lds\n"
"s_add_u32 s52, s52, 32768\n"
"v_pk_mul_f32 v[56:57], v[56:57], v[160:161]\n"
"v_pk_mul_f32 v[58:59], v[58:59], v[162:163]\n"
"v_pk_mul_f32 v[60:61], v[60:61], v[164:165]\n"
"v_pk_mul_f32 v[62:63], v[62:63], v[166:167]\n"
"v_pk_add_f32 v[48:49], v[48:49], 1.0 op_sel_hi:[1,0]\n"
"v_pk_add_f32 v[50:51], v[50:51], 1.0 op_sel_hi:[1,0]\n"
"v_pk_add_f32 v[52:53], v[52:53], 1.0 op_sel_hi:[1,0]\n"
"v_pk_add_f32 v[54:55], v[54:55], 1.0 op_sel_hi:[1,0]\n"
"v_rcp_f32 v48, v48\n"
"v_rcp_f32 v49, v49\n"
"v_rcp_f32 v50, v50\n"
"v_rcp_f32 v51, v51\n"
"v_rcp_f32 v52, v52\n"
"v_rcp_f32 v53, v53\n"
"v_rcp_f32 v54, v54\n"
"v_rcp_f32 v55, v55\n"
"v_pk_mul_f32 v[48:49], v[48:49], v[56:57]\n"
"v_pk_mul_f32 v[50:51], v[50:51], v[58:59]\n"
"v_pk_mul_f32 v[52:53], v[52:53], v[60:61]\n"
"v_pk_mul_f32 v[54:55], v[54:55], v[62:63]\n"
"v_cvt_pk_bf16_f32 v160, v48, v49\n"
"v_cvt_pk_bf16_f32 v161, v50, v51\n"
"v_cvt_pk_bf16_f32 v162, v52, v53\n"
"v_cvt_pk_bf16_f32 v163, v54, v55\n"
"global_store_dwordx4 v28, v[160:163], %0 offset:1024 sc1\n"
"v_pk_mul_f32 v[96:97], v[192:193], v[192:193]\n"
"v_pk_mul_f32 v[98:99], v[194:195], v[194:195]\n"
"v_pk_mul_f32 v[100:101], v[196:197], v[196:197]\n"
"v_pk_mul_f32 v[102:103], v[198:199], v[198:199]\n"
"v_accvgpr_read_b32 v104, a64\n"
"v_accvgpr_read_b32 v105, a65\n"
"v_accvgpr_read_b32 v106, a66\n"
"v_accvgpr_read_b32 v107, a67\n"
"v_accvgpr_read_b32 v108, a68\n"
"v_accvgpr_read_b32 v109, a69\n"
"v_accvgpr_read_b32 v110, a70\n"
"v_accvgpr_read_b32 v111, a71\n"
"s_add_u32 m0, s53, 36864\n"
"s_nop 0\n"
"buffer_load_dwordx4 v24, s[88:91], s52 offen offset:0 lds\n"
"v_pk_fma_f32 v[96:97], v[96:97], v[34:35], v[32:33]\n"
"v_pk_fma_f32 v[98:99], v[98:99], v[34:35], v[32:33]\n"
"v_pk_fma_f32 v[100:101], v[100:101], v[34:35], v[32:33]\n"
"v_pk_fma_f32 v[102:103], v[102:103], v[34:35], v[32:33]\n"
"v_pk_mul_f32 v[96:97], v[96:97], v[192:193]\n"
"v_pk_mul_f32 v[98:99], v[98:99], v[194:195]\n"
"v_pk_mul_f32 v[100:101], v[100:101], v[196:197]\n"
"v_pk_mul_f32 v[102:103], v[102:103], v[198:199]\n"
"v_exp_f32 v96, v96\n"
"v_exp_f32 v97, v97\n"
"v_exp_f32 v98, v98\n"
"v_exp_f32 v99, v99\n"
"v_exp_f32 v100, v100\n"
"v_exp_f32 v101, v101\n"
"v_exp_f32 v102, v102\n"
"v_exp_f32 v103, v103\n"
"buffer_load_dwordx4 v25, s[88:91], s52 offen offset:1024 lds\n"
"v_pk_mul_f32 v[104:105], v[104:105], v[192:193]\n"
"v_pk_mul_f32 v[106:107], v[106:107], v[194:195]\n"
"v_pk_mul_f32 v[108:109], v[108:109], v[196:197]\n"
"v_pk_mul_f32 v[110:111], v[110:111], v[198:199]\n"
"v_pk_add_f32 v[96:97], v[96:97], 1.0 op_sel_hi:[1,0]\n"
"v_pk_add_f32 v[98:99], v[98:99], 1.0 op_sel_hi:[1,0]\n"
"v_pk_add_f32 v[100:101], v[100:101], 1.0 op_sel_hi:[1,0]\n"
"v_pk_add_f32 v[102:103], v[102:103], 1.0 op_sel_hi:[1,0]\n"
"v_rcp_f32 v96, v96\n"
"v_rcp_f32 v97, v97\n"
"v_rcp_f32 v98, v98\n"
"v_rcp_f32 v99, v99\n"
"v_rcp_f32 v100, v100\n"
"v_rcp_f32 v101, v101\n"
"v_rcp_f32 v102, v102\n"
"v_rcp_f32 v103, v103\n"
"v_pk_mul_f32 v[96:97], v[96:97], v[104:105]\n"
"v_pk_mul_f32 v[98:99], v[98:99], v[106:107]\n"
"v_pk_mul_f32 v[100:101], v[100:101], v[108:109]\n"
"v_pk_mul_f32 v[102:103], v[102:103], v[110:111]\n"
"v_cvt_pk_bf16_f32 v192, v96, v97\n"
"v_cvt_pk_bf16_f32 v193, v98, v99\n"
"v_cvt_pk_bf16_f32 v194, v100, v101\n"
"v_cvt_pk_bf16_f32 v195, v102, v103\n"
"global_store_dwordx4 v28, v[192:195], %0 offset:2048 sc1\n"
"v_pk_mul_f32 v[48:49], v[224:225], v[224:225]\n"
"v_pk_mul_f32 v[50:51], v[226:227], v[226:227]\n"
"v_pk_mul_f32 v[52:53], v[228:229], v[228:229]\n"
"v_pk_mul_f32 v[54:55], v[230:231], v[230:231]\n"
"v_accvgpr_read_b32 v56, a96\n"
"v_accvgpr_read_b32 v57, a97\n"
"v_accvgpr_read_b32 v58, a98\n"
"v_accvgpr_read_b32 v59, a99\n"
"v_accvgpr_read_b32 v60, a100\n"
"v_accvgpr_read_b32 v61, a101\n"
"v_accvgpr_read_b32 v62, a102\n"
"v_accvgpr_read_b32 v63, a103\n"
"buffer_load_dwordx4 v26, s[88:91], s52 offen offset:2048 lds\n"
"v_pk_fma_f32 v[48:49], v[48:49], v[34:35], v[32:33]\n"
"v_pk_fma_f32 v[50:51], v[50:51], v[34:35], v[32:33]\n"
"v_pk_fma_f32 v[52:53], v[52:53], v[34:35], v[32:33]\n"
"v_pk_fma_f32 v[54:55], v[54:55], v[34:35], v[32:33]\n"
"v_pk_mul_f32 v[48:49], v[48:49], v[224:225]\n"
"v_pk_mul_f32 v[50:51], v[50:51], v[226:227]\n"
"v_pk_mul_f32 v[52:53], v[52:53], v[228:229]\n"
"v_pk_mul_f32 v[54:55], v[54:55], v[230:231]\n"
"v_exp_f32 v48, v48\n"
"v_exp_f32 v49, v49\n"
"v_exp_f32 v50, v50\n"
"v_exp_f32 v51, v51\n"
"v_exp_f32 v52, v52\n"
"v_exp_f32 v53, v53\n"
"v_exp_f32 v54, v54\n"
"v_exp_f32 v55, v55\n"
"buffer_load_dwordx4 v27, s[88:91], s52 offen offset:3072 lds\n"
"s_add_u32 s52, s52, 32768\n"
"v_pk_mul_f32 v[56:57], v[56:57], v[224:225]\n"
"v_pk_mul_f32 v[58:59], v[58:59], v[226:227]\n"
"v_pk_mul_f32 v[60:61], v[60:61], v[228:229]\n"
"v_pk_mul_f32 v[62:63], v[62:63], v[230:231]\n"
"v_pk_add_f32 v[48:49], v[48:49], 1.0 op_sel_hi:[1,0]\n"
"v_pk_add_f32 v[50:51], v[50:51], 1.0 op_sel_hi:[1,0]\n"
"v_pk_add_f32 v[52:53], v[52:53], 1.0 op_sel_hi:[1,0]\n"
"v_pk_add_f32 v[54:55], v[54:55], 1.0 op_sel_hi:[1,0]\n"
"v_rcp_f32 v48, v48\n"
"v_rcp_f32 v49, v49\n"
"v_rcp_f32 v50, v50\n"
"v_rcp_f32 v51, v51\n"
"v_rcp_f32 v52, v52\n"
"v_rcp_f32 v53, v53\n"
"v_rcp_f32 v54, v54\n"
"v_rcp_f32 v55, v55\n"
"v_pk_mul_f32 v[48:49], v[48:49], v[56:57]\n"
"v_pk_mul_f32 v[50:51], v[50:51], v[58:59]\n"
"v_pk_mul_f32 v[52:53], v[52:53], v[60:61]\n"
"v_pk_mul_f32 v[54:55], v[54:55], v[62:63]\n"
"v_cvt_pk_bf16_f32 v224, v48, v49\n"
"v_cvt_pk_bf16_f32 v225, v50, v51\n"
"v_cvt_pk_bf16_f32 v226, v52, v53\n"
"v_cvt_pk_bf16_f32 v227, v54, v55\n"
"global_store_dwordx4 v28, v[224:227], %0 offset:3072 sc1\n"
"v_pk_mul_f32 v[96:97], v[136:137], v[136:137]\n"
"v_pk_mul_f32 v[98:99], v[138:139], v[138:139]\n"
"v_pk_mul_f32 v[100:101], v[140:141], v[140:141]\n"
"v_pk_mul_f32 v[102:103], v[142:143], v[142:143]\n"
"v_accvgpr_read_b32 v104, a8\n"
"v_accvgpr_read_b32 v105, a9\n"
"v_accvgpr_read_b32 v106, a10\n"
"v_accvgpr_read_b32 v107, a11\n"
"v_accvgpr_read_b32 v108, a12\n"
"v_accvgpr_read_b32 v109, a13\n"
"v_accvgpr_read_b32 v110, a14\n"
"v_accvgpr_read_b32 v111, a15\n"
"s_add_u32 m0, s53, 40960\n"
"s_nop 0\n"
"buffer_load_dwordx4 v24, s[88:91], s52 offen offset:0 lds\n"
"v_pk_fma_f32 v[96:97], v[96:97], v[34:35], v[32:33]\n"
"v_pk_fma_f32 v[98:99], v[98:99], v[34:35], v[32:33]\n"
"v_pk_fma_f32 v[100:101], v[100:101], v[34:35], v[32:33]\n"
"v_pk_fma_f32 v[102:103], v[102:103], v[34:35], v[32:33]\n"
"v_pk_mul_f32 v[96:97], v[96:97], v[136:137]\n"
"v_pk_mul_f32 v[98:99], v[98:99], v[138:139]\n"
"v_pk_mul_f32 v[100:101], v[100:101], v[140:141]\n"
"v_pk_mul_f32 v[102:103], v[102:103], v[142:143]\n"
"v_exp_f32 v96, v96\n"
"v_exp_f32 v97, v97\n"
"v_exp_f32 v98, v98\n"
"v_exp_f32 v99, v99\n"
"v_exp_f32 v100, v100\n"
"v_exp_f32 v101, v101\n"
"v_exp_f32 v102, v102\n"
"v_exp_f32 v103, v103\n"
"buffer_load_dwordx4 v25, s[88:91], s52 offen offset:1024 lds\n"
"v_pk_mul_f32 v[104:105], v[104:105], v[136:137]\n"
"v_pk_mul_f32 v[106:107], v[106:107], v[138:139]\n"
"v_pk_mul_f32 v[108:109], v[108:109], v[140:141]\n"
"v_pk_mul_f32 v[110:111], v[110:111], v[142:143]\n"
"v_pk_add_f32 v[96:97], v[96:97], 1.0 op_sel_hi:[1,0]\n"
"v_pk_add_f32 v[98:99], v[98:99], 1.0 op_sel_hi:[1,0]\n"
"v_pk_add_f32 v[100:101], v[100:101], 1.0 op_sel_hi:[1,0]\n"
"v_pk_add_f32 v[102:103], v[102:103], 1.0 op_sel_hi:[1,0]\n"
"v_rcp_f32 v96, v96\n"
"v_rcp_f32 v97, v97\n"
"v_rcp_f32 v98, v98\n"
"v_rcp_f32 v99, v99\n"
"v_rcp_f32 v100, v100\n"
"v_rcp_f32 v101, v101\n"
"v_rcp_f32 v102, v102\n"
"v_rcp_f32 v103, v103\n"
"v_pk_mul_f32 v[96:97], v[96:97], v[104:105]\n"
"v_pk_mul_f32 v[98:99], v[98:99], v[106:107]\n"
"v_pk_mul_f32 v[100:101], v[100:101], v[108:109]\n"
"v_pk_mul_f32 v[102:103], v[102:103], v[110:111]\n"
"v_cvt_pk_bf16_f32 v136, v96, v97\n"
"v_cvt_pk_bf16_f32 v137, v98, v99\n"
"v_cvt_pk_bf16_f32 v138, v100, v101\n"
"v_cvt_pk_bf16_f32 v139, v102, v103\n"
"global_store_dwordx4 v29, v[136:139], %0 offset:0 sc1\n"
"v_pk_mul_f32 v[48:49], v[168:169], v[168:169]\n"
"v_pk_mul_f32 v[50:51], v[170:171], v[170:171]\n"
"v_pk_mul_f32 v[52:53], v[172:173], v[172:173]\n"
"v_pk_mul_f32 v[54:55], v[174:175], v[174:175]\n"
"v_accvgpr_read_b32 v56, a40\n"
"v_accvgpr_read_b32 v57, a41\n"
"v_accvgpr_read_b32 v58, a42\n"
"v_accvgpr_read_b32 v59, a43\n"
"v_accvgpr_read_b32 v60, a44\n"
"v_accvgpr_read_b32 v61, a45\n"
"v_accvgpr_read_b32 v62, a46\n"
"v_accvgpr_read_b32 v63, a47\n"
"buffer_load_dwordx4 v26, s[88:91], s52 offen offset:2048 lds\n"
"v_pk_fma_f32 v[48:49], v[48:49], v[34:35], v[32:33]\n"
"v_pk_fma_f32 v[50:51], v[50:51], v[34:35], v[32:33]\n"
"v_pk_fma_f32 v[52:53], v[52:53], v[34:35], v[32:33]\n"
"v_pk_fma_f32 v[54:55], v[54:55], v[34:35], v[32:33]\n"
"v_pk_mul_f32 v[48:49], v[48:49], v[168:169]\n"
"v_pk_mul_f32 v[50:51], v[50:51], v[170:171]\n"
"v_pk_mul_f32 v[52:53], v[52:53], v[172:173]\n"
"v_pk_mul_f32 v[54:55], v[54:55], v[174:175]\n"
"v_exp_f32 v48, v48\n"
"v_exp_f32 v49, v49\n"
"v_exp_f32 v50, v50\n"
"v_exp_f32 v51, v51\n"
"v_exp_f32 v52, v52\n"
"v_exp_f32 v53, v53\n"
"v_exp_f32 v54, v54\n"
"v_exp_f32 v55, v55\n"
"buffer_load_dwordx4 v27, s[88:91], s52 offen offset:3072 lds\n"
"s_add_u32 s52, s52, 32768\n"
"v_pk_mul_f32 v[56:57], v[56:57], v[168:169]\n"
"v_pk_mul_f32 v[58:59], v[58:59], v[170:171]\n"
"v_pk_mul_f32 v[60:61], v[60:61], v[172:173]\n"
"v_pk_mul_f32 v[62:63], v[62:63], v[174:175]\n"
"v_pk_add_f32 v[48:49], v[48:49], 1.0 op_sel_hi:[1,0]\n"
"v_pk_add_f32 v[50:51], v[50:51], 1.0 op_sel_hi:[1,0]\n"
"v_pk_add_f32 v[52:53], v[52:53], 1.0 op_sel_hi:[1,0]\n"
"v_pk_add_f32 v[54:55], v[54:55], 1.0 op_sel_hi:[1,0]\n"
"v_rcp_f32 v48, v48\n"
"v_rcp_f32 v49, v49\n"
"v_rcp_f32 v50, v50\n"
"v_rcp_f32 v51, v51\n"
"v_rcp_f32 v52, v52\n"
"v_rcp_f32 v53, v53\n"
"v_rcp_f32 v54, v54\n"
"v_rcp_f32 v55, v55\n"
"v_pk_mul_f32 v[48:49], v[48:49], v[56:57]\n"
"v_pk_mul_f32 v[50:51], v[50:51], v[58:59]\n"
"v_pk_mul_f32 v[52:53], v[52:53], v[60:61]\n"
"v_pk_mul_f32 v[54:55], v[54:55], v[62:63]\n"
"v_cvt_pk_bf16_f32 v168, v48, v49\n"
"v_cvt_pk_bf16_f32 v169, v50, v51\n"
"v_cvt_pk_bf16_f32 v170, v52, v53\n"
"v_cvt_pk_bf16_f32 v171, v54, v55\n"
"global_store_dwordx4 v29, v[168:171], %0 offset:1024 sc1\n"
"v_pk_mul_f32 v[96:97], v[200:201], v[200:201]\n"
"v_pk_mul_f32 v[98:99], v[202:203], v[202:203]\n"
"v_pk_mul_f32 v[100:101], v[204:205], v[204:205]\n"
"v_pk_mul_f32 v[102:103], v[206:207], v[206:207]\n"
"v_accvgpr_read_b32 v104, a72\n"
"v_accvgpr_read_b32 v105, a73\n"
"v_accvgpr_read_b32 v106, a74\n"
"v_accvgpr_read_b32 v107, a75\n"
"v_accvgpr_read_b32 v108, a76\n"
"v_accvgpr_read_b32 v109, a77\n"
"v_accvgpr_read_b32 v110, a78\n"
"v_accvgpr_read_b32 v111, a79\n"
"s_add_u32 m0, s53, 45056\n"
"s_nop 0\n"
"buffer_load_dwordx4 v24, s[88:91], s52 offen offset:0 lds\n"
"v_pk_fma_f32 v[96:97], v[96:97], v[34:35], v[32:33]\n"
"v_pk_fma_f32 v[98:99], v[98:99], v[34:35], v[32:33]\n"
"v_pk_fma_f32 v[100:101], v[100:101], v[34:35], v[32:33]\n"
"v_pk_fma_f32 v[102:103], v[102:103], v[34:35], v[32:33]\n"
"v_pk_mul_f32 v[96:97], v[96:97], v[200:201]\n"
"v_pk_mul_f32 v[98:99], v[98:99], v[202:203]\n"
"v_pk_mul_f32 v[100:101], v[100:101], v[204:205]\n"
"v_pk_mul_f32 v[102:103], v[102:103], v[206:207]\n"
"v_exp_f32 v96, v96\n"
"v_exp_f32 v97, v97\n"
"v_exp_f32 v98, v98\n"
"v_exp_f32 v99, v99\n"
"v_exp_f32 v100, v100\n"
"v_exp_f32 v101, v101\n"
"v_exp_f32 v102, v102\n"
"v_exp_f32 v103, v103\n"
"buffer_load_dwordx4 v25, s[88:91], s52 offen offset:1024 lds\n"
"v_pk_mul_f32 v[104:105], v[104:105], v[200:201]\n"
"v_pk_mul_f32 v[106:107], v[106:107], v[202:203]\n"
"v_pk_mul_f32 v[108:109], v[108:109], v[204:205]\n"
"v_pk_mul_f32 v[110:111], v[110:111], v[206:207]\n"
"v_pk_add_f32 v[96:97], v[96:97], 1.0 op_sel_hi:[1,0]\n"
"v_pk_add_f32 v[98:99], v[98:99], 1.0 op_sel_hi:[1,0]\n"
"v_pk_add_f32 v[100:101], v[100:101], 1.0 op_sel_hi:[1,0]\n"
"v_pk_add_f32 v[102:103], v[102:103], 1.0 op_sel_hi:[1,0]\n"
"v_rcp_f32 v96, v96\n"
"v_rcp_f32 v97, v97\n"
"v_rcp_f32 v98, v98\n"
"v_rcp_f32 v99, v99\n"
"v_rcp_f32 v100, v100\n"
"v_rcp_f32 v101, v101\n"
"v_rcp_f32 v102, v102\n"
"v_rcp_f32 v103, v103\n"
"v_pk_mul_f32 v[96:97], v[96:97], v[104:105]\n"
"v_pk_mul_f32 v[98:99], v[98:99], v[106:107]\n"
"v_pk_mul_f32 v[100:101], v[100:101], v[108:109]\n"
"v_pk_mul_f32 v[102:103], v[102:103], v[110:111]\n"
"v_cvt_pk_bf16_f32 v200, v96, v97\n"
"v_cvt_pk_bf16_f32 v201, v98, v99\n"
"v_cvt_pk_bf16_f32 v202, v100, v101\n"
"v_cvt_pk_bf16_f32 v203, v102, v103\n"
"global_store_dwordx4 v29, v[200:203], %0 offset:2048 sc1\n"
"v_pk_mul_f32 v[48:49], v[232:233], v[232:233]\n"
"v_pk_mul_f32 v[50:51], v[234:235], v[234:235]\n"
"v_pk_mul_f32 v[52:53], v[236:237], v[236:237]\n"
"v_pk_mul_f32 v[54:55], v[238:239], v[238:239]\n"
"v_accvgpr_read_b32 v56, a104\n"
"v_accvgpr_read_b32 v57, a105\n"
"v_accvgpr_read_b32 v58, a106\n"
"v_accvgpr_read_b32 v59, a107\n"
"v_accvgpr_read_b32 v60, a108\n"
"v_accvgpr_read_b32 v61, a109\n"
"v_accvgpr_read_b32 v62, a110\n"
"v_accvgpr_read_b32 v63, a111\n"
"buffer_load_dwordx4 v26, s[88:91], s52 offen offset:2048 lds\n"
"v_pk_fma_f32 v[48:49], v[48:49], v[34:35], v[32:33]\n"
"v_pk_fma_f32 v[50:51], v[50:51], v[34:35], v[32:33]\n"
"v_pk_fma_f32 v[52:53], v[52:53], v[34:35], v[32:33]\n"
"v_pk_fma_f32 v[54:55], v[54:55], v[34:35], v[32:33]\n"
"v_pk_mul_f32 v[48:49], v[48:49], v[232:233]\n"
"v_pk_mul_f32 v[50:51], v[50:51], v[234:235]\n"
"v_pk_mul_f32 v[52:53], v[52:53], v[236:237]\n"
"v_pk_mul_f32 v[54:55], v[54:55], v[238:239]\n"
"v_exp_f32 v48, v48\n"
"v_exp_f32 v49, v49\n"
"v_exp_f32 v50, v50\n"
"v_exp_f32 v51, v51\n"
"v_exp_f32 v52, v52\n"
"v_exp_f32 v53, v53\n"
"v_exp_f32 v54, v54\n"
"v_exp_f32 v55, v55\n"
"buffer_load_dwordx4 v27, s[88:91], s52 offen offset:3072 lds\n"
"s_add_u32 s52, s52, 32768\n"
"v_pk_mul_f32 v[56:57], v[56:57], v[232:233]\n"
"v_pk_mul_f32 v[58:59], v[58:59], v[234:235]\n"
"v_pk_mul_f32 v[60:61], v[60:61], v[236:237]\n"
"v_pk_mul_f32 v[62:63], v[62:63], v[238:239]\n"
"v_pk_add_f32 v[48:49], v[48:49], 1.0 op_sel_hi:[1,0]\n"
"v_pk_add_f32 v[50:51], v[50:51], 1.0 op_sel_hi:[1,0]\n"
"v_pk_add_f32 v[52:53], v[52:53], 1.0 op_sel_hi:[1,0]\n"
"v_pk_add_f32 v[54:55], v[54:55], 1.0 op_sel_hi:[1,0]\n"
"v_rcp_f32 v48, v48\n"
"v_rcp_f32 v49, v49\n"
"v_rcp_f32 v50, v50\n"
"v_rcp_f32 v51, v51\n"
"v_rcp_f32 v52, v52\n"
"v_rcp_f32 v53, v53\n"
"v_rcp_f32 v54, v54\n"
"v_rcp_f32 v55, v55\n"
"v_pk_mul_f32 v[48:49], v[48:49], v[56:57]\n"
"v_pk_mul_f32 v[50:51], v[50:51], v[58:59]\n"
"v_pk_mul_f32 v[52:53], v[52:53], v[60:61]\n"
"v_pk_mul_f32 v[54:55], v[54:55], v[62:63]\n"
"v_cvt_pk_bf16_f32 v232, v48, v49\n"
"v_cvt_pk_bf16_f32 v233, v50, v51\n"
"v_cvt_pk_bf16_f32 v234, v52, v53\n"
"v_cvt_pk_bf16_f32 v235, v54, v55\n"
"global_store_dwordx4 v29, v[232:235], %0 offset:3072 sc1\n"
"s_cbranch_vccnz EP_SKIP\n"
"v_pk_mul_f32 v[96:97], v[144:145], v[144:145]\n"
"v_pk_mul_f32 v[98:99], v[146:147], v[146:147]\n"
"v_pk_mul_f32 v[100:101], v[148:149], v[148:149]\n"
"v_pk_mul_f32 v[102:103], v[150:151], v[150:151]\n"
"v_accvgpr_read_b32 v104, a16\n"
"v_accvgpr_read_b32 v105, a17\n"
"v_accvgpr_read_b32 v106, a18\n"
"v_accvgpr_read_b32 v107, a19\n"
"v_accvgpr_read_b32 v108, a20\n"
"v_accvgpr_read_b32 v109, a21\n"
"v_accvgpr_read_b32 v110, a22\n"
"v_accvgpr_read_b32 v111, a23\n"
"v_pk_fma_f32 v[96:97], v[96:97], v[34:35], v[32:33]\n"
"v_pk_fma_f32 v[98:99], v[98:99], v[34:35], v[32:33]\n"
"v_pk_fma_f32 v[100:101], v[100:101], v[34:35], v[32:33]\n"
"v_pk_fma_f32 v[102:103], v[102:103], v[34:35], v[32:33]\n"
"v_pk_mul_f32 v[96:97], v[96:97], v[144:145]\n"
"v_pk_mul_f32 v[98:99], v[98:99], v[146:147]\n"
"v_pk_mul_f32 v[100:101], v[100:101], v[148:149]\n"
"v_pk_mul_f32 v[102:103], v[102:103], v[150:151]\n"
"v_exp_f32 v96, v96\n"
"v_exp_f32 v97, v97\n"
"v_exp_f32 v98, v98\n"
"v_exp_f32 v99, v99\n"
"v_exp_f32 v100, v100\n"
"v_exp_f32 v101, v101\n"
"v_exp_f32 v102, v102\n"
"v_exp_f32 v103, v103\n"
"v_pk_mul_f32 v[104:105], v[104:105], v[144:145]\n"
"v_pk_mul_f32 v[106:107], v[106:107], v[146:147]\n"
"v_pk_mul_f32 v[108:109], v[108:109], v[148:149]\n"
"v_pk_mul_f32 v[110:111], v[110:111], v[150:151]\n"
"v_pk_add_f32 v[96:97], v[96:97], 1.0 op_sel_hi:[1,0]\n"
"v_pk_add_f32 v[98:99], v[98:99], 1.0 op_sel_hi:[1,0]\n"
"v_pk_add_f32 v[100:101], v[100:101], 1.0 op_sel_hi:[1,0]\n"
"v_pk_add_f32 v[102:103], v[102:103], 1.0 op_sel_hi:[1,0]\n"
"v_rcp_f32 v96, v96\n"
"v_rcp_f32 v97, v97\n"
"v_rcp_f32 v98, v98\n"
"v_rcp_f32 v99, v99\n"
"v_rcp_f32 v100, v100\n"
"v_rcp_f32 v101, v101\n"
"v_rcp_f32 v102, v102\n"
"v_rcp_f32 v103, v103\n"
"v_pk_mul_f32 v[96:97], v[96:97], v[104:105]\n"
"v_pk_mul_f32 v[98:99], v[98:99], v[106:107]\n"
"v_pk_mul_f32 v[100:101], v[100:101], v[108:109]\n"
"v_pk_mul_f32 v[102:103], v[102:103], v[110:111]\n"
"v_cvt_pk_bf16_f32 v144, v96, v97\n"
"v_cvt_pk_bf16_f32 v145, v98, v99\n"
"v_cvt_pk_bf16_f32 v146, v100, v101\n"
"v_cvt_pk_bf16_f32 v147, v102, v103\n"
"global_store_dwordx4 v30, v[144:147], %0 offset:0 sc1\n"
"v_pk_mul_f32 v[48:49], v[176:177], v[176:177]\n"
"v_pk_mul_f32 v[50:51], v[178:179], v[178:179]\n"
"v_pk_mul_f32 v[52:53], v[180:181], v[180:181]\n"
"v_pk_mul_f32 v[54:55], v[182:183], v[182:183]\n"
"v_accvgpr_read_b32 v56, a48\n"
"v_accvgpr_read_b32 v57, a49\n"
"v_accvgpr_read_b32 v58, a50\n"
"v_accvgpr_read_b32 v59, a51\n"
"v_accvgpr_read_b32 v60, a52\n"
"v_accvgpr_read_b32 v61, a53\n"
"v_accvgpr_read_b32 v62, a54\n"
"v_accvgpr_read_b32 v63, a55\n"
"v_pk_fma_f32 v[48:49], v[48:49], v[34:35], v[32:33]\n"
"v_pk_fma_f32 v[50:51], v[50:51], v[34:35], v[32:33]\n"
"v_pk_fma_f32 v[52:53], v[52:53], v[34:35], v[32:33]\n"
"v_pk_fma_f32 v[54:55], v[54:55], v[34:35], v[32:33]\n"
"v_pk_mul_f32 v[48:49], v[48:49], v[176:177]\n"
"v_pk_mul_f32 v[50:51], v[50:51], v[178:179]\n"
"v_pk_mul_f32 v[52:53], v[52:53], v[180:181]\n"
"v_pk_mul_f32 v[54:55], v[54:55], v[182:183]\n"
"v_exp_f32 v48, v48\n"
"v_exp_f32 v49, v49\n"
"v_exp_f32 v50, v50\n"
"v_exp_f32 v51, v51\n"
"v_exp_f32 v52, v52\n"
"v_exp_f32 v53, v53\n"
"v_exp_f32 v54, v54\n"
"v_exp_f32 v55, v55\n"
"v_pk_mul_f32 v[56:57], v[56:57], v[176:177]\n"
"v_pk_mul_f32 v[58:59], v[58:59], v[178:179]\n"
"v_pk_mul_f32 v[60:61], v[60:61], v[180:181]\n"
"v_pk_mul_f32 v[62:63], v[62:63], v[182:183]\n"
"v_pk_add_f32 v[48:49], v[48:49], 1.0 op_sel_hi:[1,0]\n"
"v_pk_add_f32 v[50:51], v[50:51], 1.0 op_sel_hi:[1,0]\n"
"v_pk_add_f32 v[52:53], v[52:53], 1.0 op_sel_hi:[1,0]\n"
"v_pk_add_f32 v[54:55], v[54:55], 1.0 op_sel_hi:[1,0]\n"
"v_rcp_f32 v48, v48\n"
"v_rcp_f32 v49, v49\n"
"v_rcp_f32 v50, v50\n"
"v_rcp_f32 v51, v51\n"
"v_rcp_f32 v52, v52\n"
"v_rcp_f32 v53, v53\n"
"v_rcp_f32 v54, v54\n"
"v_rcp_f32 v55, v55\n"
"v_pk_mul_f32 v[48:49], v[48:49], v[56:57]\n"
"v_pk_mul_f32 v[50:51], v[50:51], v[58:59]\n"
"v_pk_mul_f32 v[52:53], v[52:53], v[60:61]\n"
"v_pk_mul_f32 v[54:55], v[54:55], v[62:63]\n"
"v_cvt_pk_bf16_f32 v176, v48, v49\n"
"v_cvt_pk_bf16_f32 v177, v50, v51\n"
"v_cvt_pk_bf16_f32 v178, v52, v53\n"
"v_cvt_pk_bf16_f32 v179, v54, v55\n"
"global_store_dwordx4 v30, v[176:179], %0 offset:1024 sc1\n"
"v_pk_mul_f32 v[96:97], v[208:209], v[208:209]\n"
"v_pk_mul_f32 v[98:99], v[210:211], v[210:211]\n"
"v_pk_mul_f32 v[100:101], v[212:213], v[212:213]\n"
"v_pk_mul_f32 v[102:103], v[214:215], v[214:215]\n"
"v_accvgpr_read_b32 v104, a80\n"
"v_accvgpr_read_b32 v105, a81\n"
"v_accvgpr_read_b32 v106, a82\n"
"v_accvgpr_read_b32 v107, a83\n"
"v_accvgpr_read_b32 v108, a84\n"
"v_accvgpr_read_b32 v109, a85\n"
"v_accvgpr_read_b32 v110, a86\n"
"v_accvgpr_read_b32 v111, a87\n"
"v_pk_fma_f32 v[96:97], v[96:97], v[34:35], v[32:33]\n"
"v_pk_fma_f32 v[98:99], v[98:99], v[34:35], v[32:33]\n"
"v_pk_fma_f32 v[100:101], v[100:101], v[34:35], v[32:33]\n"
"v_pk_fma_f32 v[102:103], v[102:103], v[34:35], v[32:33]\n"
"v_pk_mul_f32 v[96:97], v[96:97], v[208:209]\n"
"v_pk_mul_f32 v[98:99], v[98:99], v[210:211]\n"
"v_pk_mul_f32 v[100:101], v[100:101], v[212:213]\n"
"v_pk_mul_f32 v[102:103], v[102:103], v[214:215]\n"
"v_exp_f32 v96, v96\n"
"v_exp_f32 v97, v97\n"
"v_exp_f32 v98, v98\n"
"v_exp_f32 v99, v99\n"
"v_exp_f32 v100, v100\n"
"v_exp_f32 v101, v101\n"
"v_exp_f32 v102, v102\n"
"v_exp_f32 v103, v103\n"
"v_pk_mul_f32 v[104:105], v[104:105], v[208:209]\n"
"v_pk_mul_f32 v[106:107], v[106:107], v[210:211]\n"
"v_pk_mul_f32 v[108:109], v[108:109], v[212:213]\n"
"v_pk_mul_f32 v[110:111], v[110:111], v[214:215]\n"
"v_pk_add_f32 v[96:97], v[96:97], 1.0 op_sel_hi:[1,0]\n"
"v_pk_add_f32 v[98:99], v[98:99], 1.0 op_sel_hi:[1,0]\n"
"v_pk_add_f32 v[100:101], v[100:101], 1.0 op_sel_hi:[1,0]\n"
"v_pk_add_f32 v[102:103], v[102:103], 1.0 op_sel_hi:[1,0]\n"
"v_rcp_f32 v96, v96\n"
"v_rcp_f32 v97, v97\n"
"v_rcp_f32 v98, v98\n"
"v_rcp_f32 v99, v99\n"
"v_rcp_f32 v100, v100\n"
"v_rcp_f32 v101, v101\n"
"v_rcp_f32 v102, v102\n"
"v_rcp_f32 v103, v103\n"
"v_pk_mul_f32 v[96:97], v[96:97], v[104:105]\n"
"v_pk_mul_f32 v[98:99], v[98:99], v[106:107]\n"
"v_pk_mul_f32 v[100:101], v[100:101], v[108:109]\n"
"v_pk_mul_f32 v[102:103], v[102:103], v[110:111]\n"
"v_cvt_pk_bf16_f32 v208, v96, v97\n"
"v_cvt_pk_bf16_f32 v209, v98, v99\n"
"v_cvt_pk_bf16_f32 v210, v100, v101\n"
"v_cvt_pk_bf16_f32 v211, v102, v103\n"
"global_store_dwordx4 v30, v[208:211], %0 offset:2048 sc1\n"
"v_pk_mul_f32 v[48:49], v[240:241], v[240:241]\n"
"v_pk_mul_f32 v[50:51], v[242:243], v[242:243]\n"
"v_pk_mul_f32 v[52:53], v[244:245], v[244:245]\n"
"v_pk_mul_f32 v[54:55], v[246:247], v[246:247]\n"
"v_accvgpr_read_b32 v56, a112\n"
"v_accvgpr_read_b32 v57, a113\n"
"v_accvgpr_read_b32 v58, a114\n"
"v_accvgpr_read_b32 v59, a115\n"
"v_accvgpr_read_b32 v60, a116\n"
"v_accvgpr_read_b32 v61, a117\n"
"v_accvgpr_read_b32 v62, a118\n"
"v_accvgpr_read_b32 v63, a119\n"
"v_pk_fma_f32 v[48:49], v[48:49], v[34:35], v[32:33]\n"
"v_pk_fma_f32 v[50:51], v[50:51], v[34:35], v[32:33]\n"
"v_pk_fma_f32 v[52:53], v[52:53], v[34:35], v[32:33]\n"
"v_pk_fma_f32 v[54:55], v[54:55], v[34:35], v[32:33]\n"
"v_pk_mul_f32 v[48:49], v[48:49], v[240:241]\n"
"v_pk_mul_f32 v[50:51], v[50:51], v[242:243]\n"
"v_pk_mul_f32 v[52:53], v[52:53], v[244:245]\n"
"v_pk_mul_f32 v[54:55], v[54:55], v[246:247]\n"
"v_exp_f32 v48, v48\n"
"v_exp_f32 v49, v49\n"
"v_exp_f32 v50, v50\n"
"v_exp_f32 v51, v51\n"
"v_exp_f32 v52, v52\n"
"v_exp_f32 v53, v53\n"
"v_exp_f32 v54, v54\n"
"v_exp_f32 v55, v55\n"
"v_pk_mul_f32 v[56:57], v[56:57], v[240:241]\n"
"v_pk_mul_f32 v[58:59], v[58:59], v[242:243]\n"
"v_pk_mul_f32 v[60:61], v[60:61], v[244:245]\n"
"v_pk_mul_f32 v[62:63], v[62:63], v[246:247]\n"
"v_pk_add_f32 v[48:49], v[48:49], 1.0 op_sel_hi:[1,0]\n"
"v_pk_add_f32 v[50:51], v[50:51], 1.0 op_sel_hi:[1,0]\n"
"v_pk_add_f32 v[52:53], v[52:53], 1.0 op_sel_hi:[1,0]\n"
"v_pk_add_f32 v[54:55], v[54:55], 1.0 op_sel_hi:[1,0]\n"
"v_rcp_f32 v48, v48\n"
"v_rcp_f32 v49, v49\n"
"v_rcp_f32 v50, v50\n"
"v_rcp_f32 v51, v51\n"
"v_rcp_f32 v52, v52\n"
"v_rcp_f32 v53, v53\n"
"v_rcp_f32 v54, v54\n"
"v_rcp_f32 v55, v55\n"
"v_pk_mul_f32 v[48:49], v[48:49], v[56:57]\n"
"v_pk_mul_f32 v[50:51], v[50:51], v[58:59]\n"
"v_pk_mul_f32 v[52:53], v[52:53], v[60:61]\n"
"v_pk_mul_f32 v[54:55], v[54:55], v[62:63]\n"
"v_cvt_pk_bf16_f32 v240, v48, v49\n"
"v_cvt_pk_bf16_f32 v241, v50, v51\n"
"v_cvt_pk_bf16_f32 v242, v52, v53\n"
"v_cvt_pk_bf16_f32 v243, v54, v55\n"
"global_store_dwordx4 v30, v[240:243], %0 offset:3072 sc1\n"
"EP_SKIP:\n"
"v_pk_mul_f32 v[96:97], v[152:153], v[152:153]\n"
"v_pk_mul_f32 v[98:99], v[154:155], v[154:155]\n"
"v_pk_mul_f32 v[100:101], v[156:157], v[156:157]\n"
"v_pk_mul_f32 v[102:103], v[158:159], v[158:159]\n"
"v_accvgpr_read_b32 v104, a24\n"
"v_accvgpr_read_b32 v105, a25\n"
"v_accvgpr_read_b32 v106, a26\n"
"v_accvgpr_read_b32 v107, a27\n"
"v_accvgpr_read_b32 v108, a28\n"
"v_accvgpr_read_b32 v109, a29\n"
"v_accvgpr_read_b32 v110, a30\n"
"v_accvgpr_read_b32 v111, a31\n"
"v_pk_fma_f32 v[96:97], v[96:97], v[34:35], v[32:33]\n"
"v_pk_fma_f32 v[98:99], v[98:99], v[34:35], v[32:33]\n"
"v_pk_fma_f32 v[100:101], v[100:101], v[34:35], v[32:33]\n"
"v_pk_fma_f32 v[102:103], v[102:103], v[34:35], v[32:33]\n"
"v_pk_mul_f32 v[96:97], v[96:97], v[152:153]\n"
"v_pk_mul_f32 v[98:99], v[98:99], v[154:155]\n"
"v_pk_mul_f32 v[100:101], v[100:101], v[156:157]\n"
"v_pk_mul_f32 v[102:103], v[102:103], v[158:159]\n"
"v_exp_f32 v96, v96\n"
"v_exp_f32 v97, v97\n"
"v_exp_f32 v98, v98\n"
"v_exp_f32 v99, v99\n"
"v_exp_f32 v100, v100\n"
"v_exp_f32 v101, v101\n"
"v_exp_f32 v102, v102\n"
"v_exp_f32 v103, v103\n"
"v_pk_mul_f32 v[104:105], v[104:105], v[152:153]\n"
"v_pk_mul_f32 v[106:107], v[106:107], v[154:155]\n"
"v_pk_mul_f32 v[108:109], v[108:109], v[156:157]\n"
"v_pk_mul_f32 v[110:111], v[110:111], v[158:159]\n"
"v_pk_add_f32 v[96:97], v[96:97], 1.0 op_sel_hi:[1,0]\n"
"v_pk_add_f32 v[98:99], v[98:99], 1.0 op_sel_hi:[1,0]\n"
"v_pk_add_f32 v[100:101], v[100:101], 1.0 op_sel_hi:[1,0]\n"
"v_pk_add_f32 v[102:103], v[102:103], 1.0 op_sel_hi:[1,0]\n"
"v_rcp_f32 v96, v96\n"
"v_rcp_f32 v97, v97\n"
"v_rcp_f32 v98, v98\n"
"v_rcp_f32 v99, v99\n"
"v_rcp_f32 v100, v100\n"
"v_rcp_f32 v101, v101\n"
"v_rcp_f32 v102, v102\n"
"v_rcp_f32 v103, v103\n"
"v_pk_mul_f32 v[96:97], v[96:97], v[104:105]\n"
"v_pk_mul_f32 v[98:99], v[98:99], v[106:107]\n"
"v_pk_mul_f32 v[100:101], v[100:101], v[108:109]\n"
"v_pk_mul_f32 v[102:103], v[102:103], v[110:111]\n"
"v_cvt_pk_bf16_f32 v152, v96, v97\n"
"v_cvt_pk_bf16_f32 v153, v98, v99\n"
"v_cvt_pk_bf16_f32 v154, v100, v101\n"
"v_cvt_pk_bf16_f32 v155, v102, v103\n"
"global_store_dwordx4 v31, v[152:155], %0 offset:0 sc1\n"
"v_pk_mul_f32 v[48:49], v[184:185], v[184:185]\n"
"v_pk_mul_f32 v[50:51], v[186:187], v[186:187]\n"
"v_pk_mul_f32 v[52:53], v[188:189], v[188:189]\n"
"v_pk_mul_f32 v[54:55], v[190:191], v[190:191]\n"
"v_accvgpr_read_b32 v56, a56\n"
"v_accvgpr_read_b32 v57, a57\n"
"v_accvgpr_read_b32 v58, a58\n"
"v_accvgpr_read_b32 v59, a59\n"
"v_accvgpr_read_b32 v60, a60\n"
"v_accvgpr_read_b32 v61, a61\n"
"v_accvgpr_read_b32 v62, a62\n"
"v_accvgpr_read_b32 v63, a63\n"
"v_pk_fma_f32 v[48:49], v[48:49], v[34:35], v[32:33]\n"
"v_pk_fma_f32 v[50:51], v[50:51], v[34:35], v[32:33]\n"
"v_pk_fma_f32 v[52:53], v[52:53], v[34:35], v[32:33]\n"
"v_pk_fma_f32 v[54:55], v[54:55], v[34:35], v[32:33]\n"
"v_pk_mul_f32 v[48:49], v[48:49], v[184:185]\n"
"v_pk_mul_f32 v[50:51], v[50:51], v[186:187]\n"
"v_pk_mul_f32 v[52:53], v[52:53], v[188:189]\n"
"v_pk_mul_f32 v[54:55], v[54:55], v[190:191]\n"
"v_exp_f32 v48, v48\n"
"v_exp_f32 v49, v49\n"
"v_exp_f32 v50, v50\n"
"v_exp_f32 v51, v51\n"
"v_exp_f32 v52, v52\n"
"v_exp_f32 v53, v53\n"
"v_exp_f32 v54, v54\n"
"v_exp_f32 v55, v55\n"
"v_pk_mul_f32 v[56:57], v[56:57], v[184:185]\n"
"v_pk_mul_f32 v[58:59], v[58:59], v[186:187]\n"
"v_pk_mul_f32 v[60:61], v[60:61], v[188:189]\n"
"v_pk_mul_f32 v[62:63], v[62:63], v[190:191]\n"
"v_pk_add_f32 v[48:49], v[48:49], 1.0 op_sel_hi:[1,0]\n"
"v_pk_add_f32 v[50:51], v[50:51], 1.0 op_sel_hi:[1,0]\n"
"v_pk_add_f32 v[52:53], v[52:53], 1.0 op_sel_hi:[1,0]\n"
"v_pk_add_f32 v[54:55], v[54:55], 1.0 op_sel_hi:[1,0]\n"
"v_rcp_f32 v48, v48\n"
"v_rcp_f32 v49, v49\n"
"v_rcp_f32 v50, v50\n"
"v_rcp_f32 v51, v51\n"
"v_rcp_f32 v52, v52\n"
"v_rcp_f32 v53, v53\n"
"v_rcp_f32 v54, v54\n"
"v_rcp_f32 v55, v55\n"
"v_pk_mul_f32 v[48:49], v[48:49], v[56:57]\n"
"v_pk_mul_f32 v[50:51], v[50:51], v[58:59]\n"
"v_pk_mul_f32 v[52:53], v[52:53], v[60:61]\n"
"v_pk_mul_f32 v[54:55], v[54:55], v[62:63]\n"
"v_cvt_pk_bf16_f32 v184, v48, v49\n"
"v_cvt_pk_bf16_f32 v185, v50, v51\n"
"v_cvt_pk_bf16_f32 v186, v52, v53\n"
"v_cvt_pk_bf16_f32 v187, v54, v55\n"
"global_store_dwordx4 v31, v[184:187], %0 offset:1024 sc1\n"
"v_pk_mul_f32 v[96:97], v[216:217], v[216:217]\n"
"v_pk_mul_f32 v[98:99], v[218:219], v[218:219]\n"
"v_pk_mul_f32 v[100:101], v[220:221], v[220:221]\n"
"v_pk_mul_f32 v[102:103], v[222:223], v[222:223]\n"
"v_accvgpr_read_b32 v104, a88\n"
"v_accvgpr_read_b32 v105, a89\n"
"v_accvgpr_read_b32 v106, a90\n"
"v_accvgpr_read_b32 v107, a91\n"
"v_accvgpr_read_b32 v108, a92\n"
"v_accvgpr_read_b32 v109, a93\n"
"v_accvgpr_read_b32 v110, a94\n"
"v_accvgpr_read_b32 v111, a95\n"
"v_pk_fma_f32 v[96:97], v[96:97], v[34:35], v[32:33]\n"
"v_pk_fma_f32 v[98:99], v[98:99], v[34:35], v[32:33]\n"
"v_pk_fma_f32 v[100:101], v[100:101], v[34:35], v[32:33]\n"
"v_pk_fma_f32 v[102:103], v[102:103], v[34:35], v[32:33]\n"
"v_pk_mul_f32 v[96:97], v[96:97], v[216:217]\n"
"v_pk_mul_f32 v[98:99], v[98:99], v[218:219]\n"
"v_pk_mul_f32 v[100:101], v[100:101], v[220:221]\n"
"v_pk_mul_f32 v[102:103], v[102:103], v[222:223]\n"
"v_exp_f32 v96, v96\n"
"v_exp_f32 v97, v97\n"
"v_exp_f32 v98, v98\n"
"v_exp_f32 v99, v99\n"
"v_exp_f32 v100, v100\n"
"v_exp_f32 v101, v101\n"
"v_exp_f32 v102, v102\n"
"v_exp_f32 v103, v103\n"
"v_pk_mul_f32 v[104:105], v[104:105], v[216:217]\n"
"v_pk_mul_f32 v[106:107], v[106:107], v[218:219]\n"
"v_pk_mul_f32 v[108:109], v[108:109], v[220:221]\n"
"v_pk_mul_f32 v[110:111], v[110:111], v[222:223]\n"
"v_pk_add_f32 v[96:97], v[96:97], 1.0 op_sel_hi:[1,0]\n"
"v_pk_add_f32 v[98:99], v[98:99], 1.0 op_sel_hi:[1,0]\n"
"v_pk_add_f32 v[100:101], v[100:101], 1.0 op_sel_hi:[1,0]\n"
"v_pk_add_f32 v[102:103], v[102:103], 1.0 op_sel_hi:[1,0]\n"
"v_rcp_f32 v96, v96\n"
"v_rcp_f32 v97, v97\n"
"v_rcp_f32 v98, v98\n"
"v_rcp_f32 v99, v99\n"
"v_rcp_f32 v100, v100\n"
"v_rcp_f32 v101, v101\n"
"v_rcp_f32 v102, v102\n"
"v_rcp_f32 v103, v103\n"
"v_pk_mul_f32 v[96:97], v[96:97], v[104:105]\n"
"v_pk_mul_f32 v[98:99], v[98:99], v[106:107]\n"
"v_pk_mul_f32 v[100:101], v[100:101], v[108:109]\n"
"v_pk_mul_f32 v[102:103], v[102:103], v[110:111]\n"
"v_cvt_pk_bf16_f32 v216, v96, v97\n"
"v_cvt_pk_bf16_f32 v217, v98, v99\n"
"v_cvt_pk_bf16_f32 v218, v100, v101\n"
"v_cvt_pk_bf16_f32 v219, v102, v103\n"
"global_store_dwordx4 v31, v[216:219], %0 offset:2048 sc1\n"
"v_pk_mul_f32 v[48:49], v[248:249], v[248:249]\n"
"v_pk_mul_f32 v[50:51], v[250:251], v[250:251]\n"
"v_pk_mul_f32 v[52:53], v[252:253], v[252:253]\n"
"v_pk_mul_f32 v[54:55], v[254:255], v[254:255]\n"
"v_accvgpr_read_b32 v56, a120\n"
"v_accvgpr_read_b32 v57, a121\n"
"v_accvgpr_read_b32 v58, a122\n"
"v_accvgpr_read_b32 v59, a123\n"
"v_accvgpr_read_b32 v60, a124\n"
"v_accvgpr_read_b32 v61, a125\n"
"v_accvgpr_read_b32 v62, a126\n"
"v_accvgpr_read_b32 v63, a127\n"
"v_pk_fma_f32 v[48:49], v[48:49], v[34:35], v[32:33]\n"
"v_pk_fma_f32 v[50:51], v[50:51], v[34:35], v[32:33]\n"
"v_pk_fma_f32 v[52:53], v[52:53], v[34:35], v[32:33]\n"
"v_pk_fma_f32 v[54:55], v[54:55], v[34:35], v[32:33]\n"
"v_pk_mul_f32 v[48:49], v[48:49], v[248:249]\n"
"v_pk_mul_f32 v[50:51], v[50:51], v[250:251]\n"
"v_pk_mul_f32 v[52:53], v[52:53], v[252:253]\n"
"v_pk_mul_f32 v[54:55], v[54:55], v[254:255]\n"
"v_exp_f32 v48, v48\n"
"v_exp_f32 v49, v49\n"
"v_exp_f32 v50, v50\n"
"v_exp_f32 v51, v51\n"
"v_exp_f32 v52, v52\n"
"v_exp_f32 v53, v53\n"
"v_exp_f32 v54, v54\n"
"v_exp_f32 v55, v55\n"
"v_pk_mul_f32 v[56:57], v[56:57], v[248:249]\n"
"v_pk_mul_f32 v[58:59], v[58:59], v[250:251]\n"
"v_pk_mul_f32 v[60:61], v[60:61], v[252:253]\n"
"v_pk_mul_f32 v[62:63], v[62:63], v[254:255]\n"
"v_pk_add_f32 v[48:49], v[48:49], 1.0 op_sel_hi:[1,0]\n"
"v_pk_add_f32 v[50:51], v[50:51], 1.0 op_sel_hi:[1,0]\n"
"v_pk_add_f32 v[52:53], v[52:53], 1.0 op_sel_hi:[1,0]\n"
"v_pk_add_f32 v[54:55], v[54:55], 1.0 op_sel_hi:[1,0]\n"
"v_rcp_f32 v48, v48\n"
"v_rcp_f32 v49, v49\n"
"v_rcp_f32 v50, v50\n"
"v_rcp_f32 v51, v51\n"
"v_rcp_f32 v52, v52\n"
"v_rcp_f32 v53, v53\n"
"v_rcp_f32 v54, v54\n"
"v_rcp_f32 v55, v55\n"
"v_pk_mul_f32 v[48:49], v[48:49], v[56:57]\n"
"v_pk_mul_f32 v[50:51], v[50:51], v[58:59]\n"
"v_pk_mul_f32 v[52:53], v[52:53], v[60:61]\n"
"v_pk_mul_f32 v[54:55], v[54:55], v[62:63]\n"
"v_cvt_pk_bf16_f32 v248, v48, v49\n"
"v_cvt_pk_bf16_f32 v249, v50, v51\n"
"v_cvt_pk_bf16_f32 v250, v52, v53\n"
"v_cvt_pk_bf16_f32 v251, v54, v55\n"
"global_store_dwordx4 v31, v[248:251], %0 offset:3072 sc1\n"
"v_lshrrev_b32 v5, 2, v1\n"
"v_and_b32 v6, 7, v5\n"
"v_and_b32 v7, 3, v1\n"
"v_lshlrev_b32 v7, 3, v7\n"
"v_lshl_add_u32 v7, v5, 8, v7\n"
"v_add_u32 v7, s53, v7\n"
"v_xor_b32 v5, 0, v6\n"
"v_lshl_add_u32 v12, v5, 5, v7\n"
"v_xor_b32 v5, 1, v6\n"
"v_lshl_add_u32 v13, v5, 5, v7\n"
"v_xor_b32 v5, 2, v6\n"
"v_lshl_add_u32 v14, v5, 5, v7\n"
"v_xor_b32 v5, 3, v6\n"
"v_lshl_add_u32 v15, v5, 5, v7\n"
"v_xor_b32 v5, 4, v6\n"
"v_lshl_add_u32 v16, v5, 5, v7\n"
"v_xor_b32 v5, 5, v6\n"
"v_lshl_add_u32 v17, v5, 5, v7\n"
"v_xor_b32 v5, 6, v6\n"
"v_lshl_add_u32 v18, v5, 5, v7\n"
"v_xor_b32 v5, 7, v6\n"
"v_lshl_add_u32 v19, v5, 5, v7\n"
"v_add_u32 v20, s53, v2\n"
"v_xor_b32 v21, 0x10000, v20\n"
"v_xor_b32 v22, 0x14000, v20\n"
"s_lshl_b32 s61, s55, 13\n"
"v_add_u32 v23, s61, v2\n"
"s_lshl_b32 s61, s55, 16\n"
"v_lshlrev_b32 v28, 11, v4\n"
"v_lshl_add_u32 v30, v3, 4, v28\n"
"v_add_u32 v30, s61, v30\n"
"v_add_u32 v29, 0x8000, v30\n"
"s_lshr_b32 s60, s63, 1\n"
"s_sub_u32 s60, s60, 1\n"
"s_mov_b32 s69, 4096\n"
"s_waitcnt vmcnt(0)\n"
"s_barrier\n"
"s_cmp_lg_u32 s55, 0\n"
"s_cbranch_scc1 H_SIG\n"
"s_mov_b64 exec, 1\n"
"global_atomic_add v36, v37, %0 sc1\n"
"s_mov_b64 exec, -1\n"
"H_SIG:\n"
"G2_JOIN:\n"
"s_mov_b32 s54, s86\n"
"s_or_b32 s61, s54, s55\n"
"s_cmp_lg_u32 s61, 0\n"
"s_cbranch_scc1 FW_OK\n"
"FW_SPIN:\n"
"v_mov_b32 v5, 0\n"
"s_nop 1\n"
"global_load_dword v5, v5, s[48:49] sc1\n"
"s_waitcnt vmcnt(0)\n"
"v_readfirstlane_b32 s65, v5\n"
"s_add_u32 s61, s61, 1\n"
"s_cmp_ge_u32 s65, s51\n"
"s_cbranch_scc1 FW_OK\n"
"s_cmp_gt_u32 s61, 0x200000\n"
"s_cbranch_scc1 FW_OK\n"
"s_sleep 4\n"
"s_cmp_lg_u32 s65, 0\n"
"s_cbranch_scc1 FW_SPIN\n"
"s_sleep 40\n"
"s_branch FW_SPIN\n"
"FW_OK:\n"
"s_barrier\n"
"s_cmp_lg_u32 s54, 0\n"
"s_cbranch_scc1 FW_GO\n"
"s_mov_b32 s86, s50\n"
"s_waitcnt vmcnt(0)\n"
"buffer_load_dwordx4 a[128:131], v8, s[84:87], s34 offen offset:0 sc1\n"
"buffer_load_dwordx4 a[132:135], v8, s[84:87], s35 offen offset:0 sc1\n"
"buffer_load_dwordx4 a[136:139], v8, s[84:87], s34 offen offset:1024 sc1\n"
"buffer_load_dwordx4 a[140:143], v8, s[84:87], s35 offen offset:1024 sc1\n"
"buffer_load_dwordx4 a[144:147], v9, s[84:87], s34 offen offset:0 sc1\n"
"buffer_load_dwordx4 a[148:151], v9, s[84:87], s35 offen offset:0 sc1\n"
"buffer_load_dwordx4 a[152:155], v9, s[84:87], s34 offen offset:1024 sc1\n"
"buffer_load_dwordx4 a[156:159], v9, s[84:87], s35 offen offset:1024 sc1\n"
"buffer_load_dwordx4 a[160:163], v10, s[84:87], s34 offen offset:0 sc1\n"
"buffer_load_dwordx4 a[164:167], v10, s[84:87], s35 offen offset:0 sc1\n"
"buffer_load_dwordx4 a[168:171], v10, s[84:87], s34 offen offset:1024 sc1\n"
"buffer_load_dwordx4 a[172:175], v10, s[84:87], s35 offen offset:1024 sc1\n"
"buffer_load_dwordx4 a[176:179], v11, s[84:87], s34 offen offset:0 sc1\n"
"buffer_load_dwordx4 a[180:183], v11, s[84:87], s35 offen offset:0 sc1\n"
"buffer_load_dwordx4 a[184:187], v11, s[84:87], s34 offen offset:1024 sc1\n"
"buffer_load_dwordx4 a[188:191], v11, s[84:87], s35 offen offset:1024 sc1\n"
"FW_GO:\n"
"s_add_u32 s34, s34, 16384\n"
"s_add_u32 s35, s35, 16384\n"
"ds_read_b64_tr_b16 v[64:65], v12 offset:0\n"
"ds_read_b64_tr_b16 v[66:67], v12 offset:4096\n"
"ds_read_b64_tr_b16 v[68:69], v12 offset:8192\n"
"ds_read_b64_tr_b16 v[70:71], v12 offset:12288\n"
"ds_read_b64_tr_b16 v[72:73], v13 offset:0\n"
"ds_read_b64_tr_b16 v[74:75], v13 offset:4096\n"
"ds_read_b64_tr_b16 v[76:77], v13 offset:8192\n"
"ds_read_b64_tr_b16 v[78:79], v13 offset:12288\n"
"ds_read_b64_tr_b16 v[80:81], v14 offset:0\n"
"ds_read_b64_tr_b16 v[82:83], v14 offset:4096\n"
"ds_read_b64_tr_b16 v[84:85], v14 offset:8192\n"
"ds_read_b64_tr_b16 v[86:87], v14 offset:12288\n"
"ds_read_b64_tr_b16 v[88:89], v15 offset:0\n"
"ds_read_b64_tr_b16 v[90:91], v15 offset:4096\n"
"ds_read_b64_tr_b16 v[92:93], v15 offset:8192\n"
"ds_read_b64_tr_b16 v[94:95], v15 offset:12288\n"
"ds_read_b64_tr_b16 v[96:97], v16 offset:0\n"
"ds_read_b64_tr_b16 v[98:99], v16 offset:4096\n"
"ds_read_b64_tr_b16 v[100:101], v16 offset:8192\n"
"ds_read_b64_tr_b16 v[102:103], v16 offset:12288\n"
"ds_read_b64_tr_b16 v[104:105], v17 offset:0\n"
"ds_read_b64_tr_b16 v[106:107], v17 offset:4096\n"
"ds_read_b64_tr_b16 v[108:109], v17 offset:8192\n"
"ds_read_b64_tr_b16 v[110:111], v17 offset:12288\n"
"ds_read_b64_tr_b16 v[112:113], v18 offset:0\n"
"ds_read_b64_tr_b16 v[114:115], v18 offset:4096\n"
"ds_read_b64_tr_b16 v[116:117], v18 offset:8192\n"
"ds_read_b64_tr_b16 v[118:119], v18 offset:12288\n"
"ds_read_b64_tr_b16 v[120:121], v19 offset:0\n"
"ds_read_b64_tr_b16 v[122:123], v19 offset:4096\n"
"ds_read_b64_tr_b16 v[124:125], v19 offset:8192\n"
"ds_read_b64_tr_b16 v[126:127], v19 offset:12288\n"
"s_waitcnt vmcnt(15) lgkmcnt(15)\n"
"v_mfma_f32_16x16x32_bf16 v[128:131], v[64:67], a[128:131], 0\n"
"s_waitcnt vmcnt(14)\n"
"v_mfma_f32_16x16x32_bf16 v[128:131], v[68:71], a[132:135], v[128:131]\n"
"buffer_load_dwordx4 a[192:195], v8, s[84:87], s34 offen offset:0 sc1\n"
"v_mfma_f32_16x16x32_bf16 v[132:135], v[72:75], a[128:131], 0\n"
"v_mfma_f32_16x16x32_bf16 v[132:135], v[76:79], a[132:135], v[132:135]\n"
"buffer_load_dwordx4 a[196:199], v8, s[84:87], s35 offen offset:0 sc1\n"
"v_mfma_f32_16x16x32_bf16 v[136:139], v[80:83], a[128:131], 0\n"
"v_mfma_f32_16x16x32_bf16 v[136:139], v[84:87], a[132:135], v[136:139]\n"
"v_mfma_f32_16x16x32_bf16 v[140:143], v[88:91], a[128:131], 0\n"
"buffer_load_dwordx4 a[200:203], v8, s[84:87], s34 offen offset:1024 sc1\n"
"v_mfma_f32_16x16x32_bf16 v[140:143], v[92:95], a[132:135], v[140:143]\n"
"s_waitcnt lgkmcnt(14)\n"
"v_mfma_f32_16x16x32_bf16 v[144:147], v[96:99], a[128:131], 0\n"
"s_waitcnt lgkmcnt(12)\n"
"v_mfma_f32_16x16x32_bf16 v[144:147], v[100:103], a[132:135], v[144:147]\n"
"buffer_load_dwordx4 a[204:207], v8, s[84:87], s35 offen offset:1024 sc1\n"
"s_waitcnt lgkmcnt(10)\n"
"v_mfma_f32_16x16x32_bf16 v[148:151], v[104:107], a[128:131], 0\n"
"s_waitcnt lgkmcnt(8)\n"
"v_mfma_f32_16x16x32_bf16 v[148:151], v[108:111], a[132:135], v[148:151]\n"
"buffer_load_dwordx4 a[208:211], v9, s[84:87], s34 offen offset:0 sc1\n"
"s_waitcnt lgkmcnt(6)\n"
"v_mfma_f32_16x16x32_bf16 v[152:155], v[112:115], a[128:131], 0\n"
"s_waitcnt lgkmcnt(4)\n"
"v_mfma_f32_16x16x32_bf16 v[152:155], v[116:119], a[132:135], v[152:155]\n"
"s_waitcnt lgkmcnt(2)\n"
"v_mfma_f32_16x16x32_bf16 v[156:159], v[120:123], a[128:131], 0\n"
"buffer_load_dwordx4 a[212:215], v9, s[84:87], s35 offen offset:0 sc1\n"
"s_waitcnt lgkmcnt(0)\n"
"v_mfma_f32_16x16x32_bf16 v[156:159], v[124:127], a[132:135], v[156:159]\n"
"s_waitcnt vmcnt(19)\n"
"v_mfma_f32_16x16x32_bf16 v[160:163], v[64:67], a[136:139], 0\n"
"s_waitcnt vmcnt(18)\n"
"v_mfma_f32_16x16x32_bf16 v[160:163], v[68:71], a[140:143], v[160:163]\n"
"buffer_load_dwordx4 a[216:219], v9, s[84:87], s34 offen offset:1024 sc1\n"
"v_mfma_f32_16x16x32_bf16 v[164:167], v[72:75], a[136:139], 0\n"
"v_mfma_f32_16x16x32_bf16 v[164:167], v[76:79], a[140:143], v[164:167]\n"
"buffer_load_dwordx4 a[220:223], v9, s[84:87], s35 offen offset:1024 sc1\n"
"v_mfma_f32_16x16x32_bf16 v[168:171], v[80:83], a[136:139], 0\n"
"v_mfma_f32_16x16x32_bf16 v[168:171], v[84:87], a[140:143], v[168:171]\n"
"v_mfma_f32_16x16x32_bf16 v[172:175], v[88:91], a[136:139], 0\n"
"buffer_load_dwordx4 a[224:227], v10, s[84:87], s34 offen offset:0 sc1\n"
"v_mfma_f32_16x16x32_bf16 v[172:175], v[92:95], a[140:143], v[172:175]\n"
"v_mfma_f32_16x16x32_bf16 v[176:179], v[96:99], a[136:139], 0\n"
"v_mfma_f32_16x16x32_bf16 v[176:179], v[100:103], a[140:143], v[176:179]\n"
"buffer_load_dwordx4 a[228:231], v10, s[84:87], s35 offen offset:0 sc1\n"
"v_mfma_f32_16x16x32_bf16 v[180:183], v[104:107], a[136:139], 0\n"
"v_mfma_f32_16x16x32_bf16 v[180:183], v[108:111], a[140:143], v[180:183]\n"
"buffer_load_dwordx4 a[232:235], v10, s[84:87], s34 offen offset:1024 sc1\n"
"v_mfma_f32_16x16x32_bf16 v[184:187], v[112:115], a[136:139], 0\n"
"v_mfma_f32_16x16x32_bf16 v[184:187], v[116:119], a[140:143], v[184:187]\n"
"v_mfma_f32_16x16x32_bf16 v[188:191], v[120:123], a[136:139], 0\n"
"buffer_load_dwordx4 a[236:239], v10, s[84:87], s35 offen offset:1024 sc1\n"
"v_mfma_f32_16x16x32_bf16 v[188:191], v[124:127], a[140:143], v[188:191]\n"
"s_waitcnt vmcnt(23)\n"
"v_mfma_f32_16x16x32_bf16 v[192:195], v[64:67], a[144:147], 0\n"
"s_waitcnt vmcnt(22)\n"
"v_mfma_f32_16x16x32_bf16 v[192:195], v[68:71], a[148:151], v[192:195]\n"
"buffer_load_dwordx4 a[240:243], v11, s[84:87], s34 offen offset:0 sc1\n"
"v_mfma_f32_16x16x32_bf16 v[196:199], v[72:75], a[144:147], 0\n"
"v_mfma_f32_16x16x32_bf16 v[196:199], v[76:79], a[148:151], v[196:199]\n"
"buffer_load_dwordx4 a[244:247], v11, s[84:87], s35 offen offset:0 sc1\n"
"v_mfma_f32_16x16x32_bf16 v[200:203], v[80:83], a[144:147], 0\n"
"v_mfma_f32_16x16x32_bf16 v[200:203], v[84:87], a[148:151], v[200:203]\n"
"v_mfma_f32_16x16x32_bf16 v[204:207], v[88:91], a[144:147], 0\n"
"buffer_load_dwordx4 a[248:251], v11, s[84:87], s34 offen offset:1024 sc1\n"
"v_mfma_f32_16x16x32_bf16 v[204:207], v[92:95], a[148:151], v[204:207]\n"
"v_mfma_f32_16x16x32_bf16 v[208:211], v[96:99], a[144:147], 0\n"
"v_mfma_f32_16x16x32_bf16 v[208:211], v[100:103], a[148:151], v[208:211]\n"
"buffer_load_dwordx4 a[252:255], v11, s[84:87], s35 offen offset:1024 sc1\n"
"s_add_u32 s34, s34, 16384\n"
"s_add_u32 s35, s35, 16384\n"
"v_mfma_f32_16x16x32_bf16 v[212:215], v[104:107], a[144:147], 0\n"
"s_add_u32 m0, s53, 0\n"
"v_mfma_f32_16x16x32_bf16 v[212:215], v[108:111], a[148:151], v[212:215]\n"
"buffer_load_dwordx4 v24, s[88:91], s52 offen offset:0 lds\n"
"v_mfma_f32_16x16x32_bf16 v[216:219], v[112:115], a[144:147], 0\n"
"v_mfma_f32_16x16x32_bf16 v[216:219], v[116:119], a[148:151], v[216:219]\n"
"v_mfma_f32_16x16x32_bf16 v[220:223], v[120:123], a[144:147], 0\n"
"buffer_load_dwordx4 v25, s[88:91], s52 offen offset:1024 lds\n"
"v_mfma_f32_16x16x32_bf16 v[220:223], v[124:127], a[148:151], v[220:223]\n"
"s_waitcnt vmcnt(27)\n"
"v_mfma_f32_16x16x32_bf16 v[224:227], v[64:67], a[152:155], 0\n"
"s_waitcnt vmcnt(26)\n"
"v_mfma_f32_16x16x32_bf16 v[224:227], v[68:71], a[156:159], v[224:227]\n"
"buffer_load_dwordx4 v26, s[88:91], s52 offen offset:2048 lds\n"
"v_mfma_f32_16x16x32_bf16 v[228:231], v[72:75], a[152:155], 0\n"
"v_mfma_f32_16x16x32_bf16 v[228:231], v[76:79], a[156:159], v[228:231]\n"
"buffer_load_dwordx4 v27, s[88:91], s52 offen offset:3072 lds\n"
"s_add_u32 s52, s52, 32768\n"
"v_mfma_f32_16x16x32_bf16 v[232:235], v[80:83], a[152:155], 0\n"
"v_mfma_f32_16x16x32_bf16 v[232:235], v[84:87], a[156:159], v[232:235]\n"
"s_add_u32 m0, s53, 4096\n"
"v_mfma_f32_16x16x32_bf16 v[236:239], v[88:91], a[152:155], 0\n"
"buffer_load_dwordx4 v24, s[88:91], s52 offen offset:0 lds\n"
"v_mfma_f32_16x16x32_bf16 v[236:239], v[92:95], a[156:159], v[236:239]\n"
"v_mfma_f32_16x16x32_bf16 v[240:243], v[96:99], a[152:155], 0\n"
"v_mfma_f32_16x16x32_bf16 v[240:243], v[100:103], a[156:159], v[240:243]\n"
"buffer_load_dwordx4 v25, s[88:91], s52 offen offset:1024 lds\n"
"v_mfma_f32_16x16x32_bf16 v[244:247], v[104:107], a[152:155], 0\n"
"v_mfma_f32_16x16x32_bf16 v[244:247], v[108:111], a[156:159], v[244:247]\n"
"buffer_load_dwordx4 v26, s[88:91], s52 offen offset:2048 lds\n"
"v_mfma_f32_16x16x32_bf16 v[248:251], v[112:115], a[152:155], 0\n"
"v_mfma_f32_16x16x32_bf16 v[248:251], v[116:119], a[156:159], v[248:251]\n"
"v_mfma_f32_16x16x32_bf16 v[252:255], v[120:123], a[152:155], 0\n"
"buffer_load_dwordx4 v27, s[88:91], s52 offen offset:3072 lds\n"
"s_add_u32 s52, s52, 32768\n"
"v_mfma_f32_16x16x32_bf16 v[252:255], v[124:127], a[156:159], v[252:255]\n"
"s_waitcnt vmcnt(31)\n"
"v_mfma_f32_16x16x32_bf16 a[0:3], v[64:67], a[160:163], 0\n"
"s_add_u32 m0, s53, 8192\n"
"s_waitcnt vmcnt(30)\n"
"v_mfma_f32_16x16x32_bf16 a[0:3], v[68:71], a[164:167], a[0:3]\n"
"buffer_load_dwordx4 v24, s[88:91], s52 offen offset:0 lds\n"
"v_mfma_f32_16x16x32_bf16 a[4:7], v[72:75], a[160:163], 0\n"
"v_mfma_f32_16x16x32_bf16 a[4:7], v[76:79], a[164:167], a[4:7]\n"
"buffer_load_dwordx4 v25, s[88:91], s52 offen offset:1024 lds\n"
"v_mfma_f32_16x16x32_bf16 a[8:11], v[80:83], a[160:163], 0\n"
"v_mfma_f32_16x16x32_bf16 a[8:11], v[84:87], a[164:167], a[8:11]\n"
"v_mfma_f32_16x16x32_bf16 a[12:15], v[88:91], a[160:163], 0\n"
"buffer_load_dwordx4 v26, s[88:91], s52 offen offset:2048 lds\n"
"v_mfma_f32_16x16x32_bf16 a[12:15], v[92:95], a[164:167], a[12:15]\n"
"v_mfma_f32_16x16x32_bf16 a[16:19], v[96:99], a[160:163], 0\n"
"v_mfma_f32_16x16x32_bf16 a[16:19], v[100:103], a[164:167], a[16:19]\n"
"buffer_load_dwordx4 v27, s[88:91], s52 offen offset:3072 lds\n"
"s_add_u32 s52, s52, 32768\n"
"v_mfma_f32_16x16x32_bf16 a[20:23], v[104:107], a[160:163], 0\n"
"s_add_u32 m0, s53, 12288\n"
"v_mfma_f32_16x16x32_bf16 a[20:23], v[108:111], a[164:167], a[20:23]\n"
"buffer_load_dwordx4 v24, s[88:91], s52 offen offset:0 lds\n"
"v_mfma_f32_16x16x32_bf16 a[24:27], v[112:115], a[160:163], 0\n"
"v_mfma_f32_16x16x32_bf16 a[24:27], v[116:119], a[164:167], a[24:27]\n"
"v_mfma_f32_16x16x32_bf16 a[28:31], v[120:123], a[160:163], 0\n"
"buffer_load_dwordx4 v25, s[88:91], s52 offen offset:1024 lds\n"
"v_mfma_f32_16x16x32_bf16 a[28:31], v[124:127], a[164:167], a[28:31]\n"
"s_waitcnt vmcnt(35)\n"
"v_mfma_f32_16x16x32_bf16 a[32:35], v[64:67], a[168:171], 0\n"
"s_waitcnt vmcnt(34)\n"
"v_mfma_f32_16x16x32_bf16 a[32:35], v[68:71], a[172:175], a[32:35]\n"
"buffer_load_dwordx4 v26, s[88:91], s52 offen offset:2048 lds\n"
"v_mfma_f32_16x16x32_bf16 a[36:39], v[72:75], a[168:171], 0\n"
"v_mfma_f32_16x16x32_bf16 a[36:39], v[76:79], a[172:175], a[36:39]\n"
"buffer_load_dwordx4 v27, s[88:91], s52 offen offset:3072 lds\n"
"s_add_u32 s52, s52, 32768\n"
"v_mfma_f32_16x16x32_bf16 a[40:43], v[80:83], a[168:171], 0\n"
"v_mfma_f32_16x16x32_bf16 a[40:43], v[84:87], a[172:175], a[40:43]\n"
"v_mfma_f32_16x16x32_bf16 a[44:47], v[88:91], a[168:171], 0\n"
"buffer_load_dwordx4 a[128:131], v8, s[84:87], s34 offen offset:0 sc1\n"
"v_mfma_f32_16x16x32_bf16 a[44:47], v[92:95], a[172:175], a[44:47]\n"
"v_mfma_f32_16x16x32_bf16 a[48:51], v[96:99], a[168:171], 0\n"
"v_mfma_f32_16x16x32_bf16 a[48:51], v[100:103], a[172:175], a[48:51]\n"
"buffer_load_dwordx4 a[132:135], v8, s[84:87], s35 offen offset:0 sc1\n"
"v_mfma_f32_16x16x32_bf16 a[52:55], v[104:107], a[168:171], 0\n"
"v_mfma_f32_16x16x32_bf16 a[52:55], v[108:111], a[172:175], a[52:55]\n"
"buffer_load_dwordx4 a[136:139], v8, s[84:87], s34 offen offset:1024 sc1\n"
"v_mfma_f32_16x16x32_bf16 a[56:59], v[112:115], a[168:171], 0\n"
"v_mfma_f32_16x16x32_bf16 a[56:59], v[116:119], a[172:175], a[56:59]\n"
"v_mfma_f32_16x16x32_bf16 a[60:63], v[120:123], a[168:171], 0\n"
"buffer_load_dwordx4 a[140:143], v8, s[84:87], s35 offen offset:1024 sc1\n"
"v_mfma_f32_16x16x32_bf16 a[60:63], v[124:127], a[172:175], a[60:63]\n"
"s_waitcnt vmcnt(39)\n"
"v_mfma_f32_16x16x32_bf16 a[64:67], v[64:67], a[176:179], 0\n"
"s_waitcnt vmcnt(38)\n"
"v_mfma_f32_16x16x32_bf16 a[64:67], v[68:71], a[180:183], a[64:67]\n"
"buffer_load_dwordx4 a[144:147], v9, s[84:87], s34 offen offset:0 sc1\n"
"v_mfma_f32_16x16x32_bf16 a[68:71], v[72:75], a[176:179], 0\n"
"v_mfma_f32_16x16x32_bf16 a[68:71], v[76:79], a[180:183], a[68:71]\n"
"buffer_load_dwordx4 a[148:151], v9, s[84:87], s35 offen offset:0 sc1\n"
"v_mfma_f32_16x16x32_bf16 a[72:75], v[80:83], a[176:179], 0\n"
"v_mfma_f32_16x16x32_bf16 a[72:75], v[84:87], a[180:183], a[72:75]\n"
"v_mfma_f32_16x16x32_bf16 a[76:79], v[88:91], a[176:179], 0\n"
"buffer_load_dwordx4 a[152:155], v9, s[84:87], s34 offen offset:1024 sc1\n"
"v_mfma_f32_16x16x32_bf16 a[76:79], v[92:95], a[180:183], a[76:79]\n"
"v_mfma_f32_16x16x32_bf16 a[80:83], v[96:99], a[176:179], 0\n"
"v_mfma_f32_16x16x32_bf16 a[80:83], v[100:103], a[180:183], a[80:83]\n"
"buffer_load_dwordx4 a[156:159], v9, s[84:87], s35 offen offset:1024 sc1\n"
"v_mfma_f32_16x16x32_bf16 a[84:87], v[104:107], a[176:179], 0\n"
"v_mfma_f32_16x16x32_bf16 a[84:87], v[108:111], a[180:183], a[84:87]\n"
"buffer_load_dwordx4 a[160:163], v10, s[84:87], s34 offen offset:0 sc1\n"
"v_mfma_f32_16x16x32_bf16 a[88:91], v[112:115], a[176:179], 0\n"
"v_mfma_f32_16x16x32_bf16 a[88:91], v[116:119], a[180:183], a[88:91]\n"
"v_mfma_f32_16x16x32_bf16 a[92:95], v[120:123], a[176:179], 0\n"
"buffer_load_dwordx4 a[164:167], v10, s[84:87], s35 offen offset:0 sc1\n"
"v_mfma_f32_16x16x32_bf16 a[92:95], v[124:127], a[180:183], a[92:95]\n"
"s_waitcnt vmcnt(43)\n"
"v_mfma_f32_16x16x32_bf16 a[96:99], v[64:67], a[184:187], 0\n"
"ds_read_b64_tr_b16 v[64:65], v12 offset:32768\n"
"ds_read_b64_tr_b16 v[66:67], v12 offset:36864\n"
"s_waitcnt vmcnt(42)\n"
"v_mfma_f32_16x16x32_bf16 a[96:99], v[68:71], a[188:191], a[96:99]\n"
"ds_read_b64_tr_b16 v[68:69], v12 offset:40960\n"
"ds_read_b64_tr_b16 v[70:71], v12 offset:45056\n"
"buffer_load_dwordx4 a[168:171], v10, s[84:87], s34 offen offset:1024 sc1\n"
"v_mfma_f32_16x16x32_bf16 a[100:103], v[72:75], a[184:187], 0\n"
"ds_read_b64_tr_b16 v[72:73], v13 offset:32768\n"
"ds_read_b64_tr_b16 v[74:75], v13 offset:36864\n"
"v_mfma_f32_16x16x32_bf16 a[100:103], v[76:79], a[188:191], a[100:103]\n"
"ds_read_b64_tr_b16 v[76:77], v13 offset:40960\n"
"ds_read_b64_tr_b16 v[78:79], v13 offset:45056\n"
"v_mfma_f32_16x16x32_bf16 a[104:107], v[80:83], a[184:187], 0\n"
"ds_read_b64_tr_b16 v[80:81], v14 offset:32768\n"
"ds_read_b64_tr_b16 v[82:83], v14 offset:36864\n"
"v_mfma_f32_16x16x32_bf16 a[104:107], v[84:87], a[188:191], a[104:107]\n"
"ds_read_b64_tr_b16 v[84:85], v14 offset:40960\n"
"ds_read_b64_tr_b16 v[86:87], v14 offset:45056\n"
"buffer_load_dwordx4 a[172:175], v10, s[84:87], s35 offen offset:1024 sc1\n"
"v_mfma_f32_16x16x32_bf16 a[108:111], v[88:91], a[184:187], 0\n"
"ds_read_b64_tr_b16 v[88:89], v15 offset:32768\n"
"ds_read_b64_tr_b16 v[90:91], v15 offset:36864\n"
"v_mfma_f32_16x16x32_bf16 a[108:111], v[92:95], a[188:191], a[108:111]\n"
"ds_read_b64_tr_b16 v[92:93], v15 offset:40960\n"
"ds_read_b64_tr_b16 v[94:95], v15 offset:45056\n"
"v_mfma_f32_16x16x32_bf16 a[112:115], v[96:99], a[184:187], 0\n"
"ds_read_b64_tr_b16 v[96:97], v16 offset:32768\n"
"ds_read_b64_tr_b16 v[98:99], v16 offset:36864\n"
"v_mfma_f32_16x16x32_bf16 a[112:115], v[100:103], a[188:191], a[112:115]\n"
"ds_read_b64_tr_b16 v[100:101], v16 offset:40960\n"
"ds_read_b64_tr_b16 v[102:103], v16 offset:45056\n"
"buffer_load_dwordx4 a[176:179], v11, s[84:87], s34 offen offset:0 sc1\n"
"v_mfma_f32_16x16x32_bf16 a[116:119], v[104:107], a[184:187], 0\n"
"ds_read_b64_tr_b16 v[104:105], v17 offset:32768\n"
"ds_read_b64_tr_b16 v[106:107], v17 offset:36864\n"
"v_mfma_f32_16x16x32_bf16 a[116:119], v[108:111], a[188:191], a[116:119]\n"
"ds_read_b64_tr_b16 v[108:109], v17 offset:40960\n"
"ds_read_b64_tr_b16 v[110:111], v17 offset:45056\n"
"v_mfma_f32_16x16x32_bf16 a[120:123], v[112:115], a[184:187], 0\n"
"ds_read_b64_tr_b16 v[112:113], v18 offset:32768\n"
"ds_read_b64_tr_b16 v[114:115], v18 offset:36864\n"
"v_mfma_f32_16x16x32_bf16 a[120:123], v[116:119], a[188:191], a[120:123]\n"
"ds_read_b64_tr_b16 v[116:117], v18 offset:40960\n"
"ds_read_b64_tr_b16 v[118:119], v18 offset:45056\n"
"buffer_load_dwordx4 a[180:183], v11, s[84:87], s35 offen offset:0 sc1\n"
"v_mfma_f32_16x16x32_bf16 a[124:127], v[120:123], a[184:187], 0\n"
"ds_read_b64_tr_b16 v[120:121], v19 offset:32768\n"
"ds_read_b64_tr_b16 v[122:123], v19 offset:36864\n"
"v_mfma_f32_16x16x32_bf16 a[124:127], v[124:127], a[188:191], a[124:127]\n"
"ds_read_b64_tr_b16 v[124:125], v19 offset:40960\n"
"ds_read_b64_tr_b16 v[126:127], v19 offset:45056\n"
"G2_LOOP:\n"
"s_waitcnt vmcnt(45) lgkmcnt(15)\n"
"v_mfma_f32_16x16x32_bf16 v[128:131], v[64:67], a[192:195], v[128:131]\n"
"s_waitcnt vmcnt(44)\n"
"v_mfma_f32_16x16x32_bf16 v[128:131], v[68:71], a[196:199], v[128:131]\n"
"buffer_load_dwordx4 a[184:187], v11, s[84:87], s34 offen offset:1024 sc1\n"
"v_mfma_f32_16x16x32_bf16 v[132:135], v[72:75], a[192:195], v[132:135]\n"
"v_mfma_f32_16x16x32_bf16 v[132:135], v[76:79], a[196:199], v[132:135]\n"
"v_mfma_f32_16x16x32_bf16 v[136:139], v[80:83], a[192:195], v[136:139]\n"
"v_mfma_f32_16x16x32_bf16 v[136:139], v[84:87], a[196:199], v[136:139]\n"
"v_mfma_f32_16x16x32_bf16 v[140:143], v[88:91], a[192:195], v[140:143]\n"
"v_mfma_f32_16x16x32_bf16 v[140:143], v[92:95], a[196:199], v[140:143]\n"
"s_waitcnt lgkmcnt(14)\n"
"v_mfma_f32_16x16x32_bf16 v[144:147], v[96:99], a[192:195], v[144:147]\n"
"s_waitcnt lgkmcnt(12)\n"
"v_mfma_f32_16x16x32_bf16 v[144:147], v[100:103], a[196:199], v[144:147]\n"
"buffer_load_dwordx4 a[188:191], v11, s[84:87], s35 offen offset:1024 sc1\n"
"s_waitcnt lgkmcnt(10)\n"
"v_mfma_f32_16x16x32_bf16 v[148:151], v[104:107], a[192:195], v[148:151]\n"
"s_add_u32 s34, s34, 16384\n"
"s_add_u32 s35, s35, 16384\n"
"s_waitcnt lgkmcnt(8)\n"
"v_mfma_f32_16x16x32_bf16 v[148:151], v[108:111], a[196:199], v[148:151]\n"
"s_waitcnt lgkmcnt(6)\n"
"v_mfma_f32_16x16x32_bf16 v[152:155], v[112:115], a[192:195], v[152:155]\n"
"s_waitcnt lgkmcnt(4)\n"
"v_mfma_f32_16x16x32_bf16 v[152:155], v[116:119], a[196:199], v[152:155]\n"
"s_waitcnt lgkmcnt(2)\n"
"v_mfma_f32_16x16x32_bf16 v[156:159], v[120:123], a[192:195], v[156:159]\n"
"s_waitcnt lgkmcnt(0)\n"
"v_mfma_f32_16x16x32_bf16 v[156:159], v[124:127], a[196:199], v[156:159]\n"
"s_waitcnt vmcnt(45)\n"
"v_mfma_f32_16x16x32_bf16 v[160:163], v[64:67], a[200:203], v[160:163]\n"
"s_waitcnt vmcnt(44)\n"
"v_mfma_f32_16x16x32_bf16 v[160:163], v[68:71], a[204:207], v[160:163]\n"
"buffer_load_dwordx4 a[192:195], v8, s[84:87], s34 offen offset:0 sc1\n"
"v_mfma_f32_16x16x32_bf16 v[164:167], v[72:75], a[200:203], v[164:167]\n"
"s_add_u32 m0, s53, 32768\n"
"v_mfma_f32_16x16x32_bf16 v[164:167], v[76:79], a[204:207], v[164:167]\n"
"buffer_load_dwordx4 v24, s[88:91], s52 offen offset:0 lds\n"
"v_mfma_f32_16x16x32_bf16 v[168:171], v[80:83], a[200:203], v[168:171]\n"
"v_mfma_f32_16x16x32_bf16 v[168:171], v[84:87], a[204:207], v[168:171]\n"
"v_mfma_f32_16x16x32_bf16 v[172:175], v[88:91], a[200:203], v[172:175]\n"
"v_mfma_f32_16x16x32_bf16 v[172:175], v[92:95], a[204:207], v[172:175]\n"
"buffer_load_dwordx4 v25, s[88:91], s52 offen offset:1024 lds\n"
"v_mfma_f32_16x16x32_bf16 v[176:179], v[96:99], a[200:203], v[176:179]\n"
"v_mfma_f32_16x16x32_bf16 v[176:179], v[100:103], a[204:207], v[176:179]\n"
"buffer_load_dwordx4 a[196:199], v8, s[84:87], s35 offen offset:0 sc1\n"
"v_mfma_f32_16x16x32_bf16 v[180:183], v[104:107], a[200:203], v[180:183]\n"
"v_mfma_f32_16x16x32_bf16 v[180:183], v[108:111], a[204:207], v[180:183]\n"
"buffer_load_dwordx4 v26, s[88:91], s52 offen offset:2048 lds\n"
"v_mfma_f32_16x16x32_bf16 v[184:187], v[112:115], a[200:203], v[184:187]\n"
"v_mfma_f32_16x16x32_bf16 v[184:187], v[116:119], a[204:207], v[184:187]\n"
"buffer_load_dwordx4 v27, s[88:91], s52 offen offset:3072 lds\n"
"v_mfma_f32_16x16x32_bf16 v[188:191], v[120:123], a[200:203], v[188:191]\n"
"s_add_u32 s52, s52, 32768\n"
"v_mfma_f32_16x16x32_bf16 v[188:191], v[124:127], a[204:207], v[188:191]\n"
"s_waitcnt vmcnt(49)\n"
"v_mfma_f32_16x16x32_bf16 v[192:195], v[64:67], a[208:211], v[192:195]\n"
"s_waitcnt vmcnt(48)\n"
"v_mfma_f32_16x16x32_bf16 v[192:195], v[68:71], a[212:215], v[192:195]\n"
"buffer_load_dwordx4 a[200:203], v8, s[84:87], s34 offen offset:1024 sc1\n"
"v_mfma_f32_16x16x32_bf16 v[196:199], v[72:75], a[208:211], v[196:199]\n"
"s_add_u32 m0, s53, 36864\n"
"v_mfma_f32_16x16x32_bf16 v[196:199], v[76:79], a[212:215], v[196:199]\n"
"buffer_load_dwordx4 v24, s[88:91], s52 offen offset:0 lds\n"
"v_mfma_f32_16x16x32_bf16 v[200:203], v[80:83], a[208:211], v[200:203]\n"
"v_mfma_f32_16x16x32_bf16 v[200:203], v[84:87], a[212:215], v[200:203]\n"
"v_mfma_f32_16x16x32_bf16 v[204:207], v[88:91], a[208:211], v[204:207]\n"
"v_mfma_f32_16x16x32_bf16 v[204:207], v[92:95], a[212:215], v[204:207]\n"
"buffer_load_dwordx4 v25, s[88:91], s52 offen offset:1024 lds\n"
"v_mfma_f32_16x16x32_bf16 v[208:211], v[96:99], a[208:211], v[208:211]\n"
"v_mfma_f32_16x16x32_bf16 v[208:211], v[100:103], a[212:215], v[208:211]\n"
"buffer_load_dwordx4 a[204:207], v8, s[84:87], s35 offen offset:1024 sc1\n"
"v_mfma_f32_16x16x32_bf16 v[212:215], v[104:107], a[208:211], v[212:215]\n"
"v_mfma_f32_16x16x32_bf16 v[212:215], v[108:111], a[212:215], v[212:215]\n"
"buffer_load_dwordx4 v26, s[88:91], s52 offen offset:2048 lds\n"
"v_mfma_f32_16x16x32_bf16 v[216:219], v[112:115], a[208:211], v[216:219]\n"
"v_mfma_f32_16x16x32_bf16 v[216:219], v[116:119], a[212:215], v[216:219]\n"
"buffer_load_dwordx4 v27, s[88:91], s52 offen offset:3072 lds\n"
"v_mfma_f32_16x16x32_bf16 v[220:223], v[120:123], a[208:211], v[220:223]\n"
"s_add_u32 s52, s52, 32768\n"
"v_mfma_f32_16x16x32_bf16 v[220:223], v[124:127], a[212:215], v[220:223]\n"
"s_waitcnt vmcnt(53)\n"
"v_mfma_f32_16x16x32_bf16 v[224:227], v[64:67], a[216:219], v[224:227]\n"
"s_waitcnt vmcnt(52)\n"
"v_mfma_f32_16x16x32_bf16 v[224:227], v[68:71], a[220:223], v[224:227]\n"
"buffer_load_dwordx4 a[208:211], v9, s[84:87], s34 offen offset:0 sc1\n"
"v_mfma_f32_16x16x32_bf16 v[228:231], v[72:75], a[216:219], v[228:231]\n"
"v_mfma_f32_16x16x32_bf16 v[228:231], v[76:79], a[220:223], v[228:231]\n"
"v_mfma_f32_16x16x32_bf16 v[232:235], v[80:83], a[216:219], v[232:235]\n"
"s_add_u32 m0, s53, 40960\n"
"v_mfma_f32_16x16x32_bf16 v[232:235], v[84:87], a[220:223], v[232:235]\n"
"buffer_load_dwordx4 v24, s[88:91], s52 offen offset:0 lds\n"
"v_mfma_f32_16x16x32_bf16 v[236:239], v[88:91], a[216:219], v[236:239]\n"
"v_mfma_f32_16x16x32_bf16 v[236:239], v[92:95], a[220:223], v[236:239]\n"
"v_mfma_f32_16x16x32_bf16 v[240:243], v[96:99], a[216:219], v[240:243]\n"
"v_mfma_f32_16x16x32_bf16 v[240:243], v[100:103], a[220:223], v[240:243]\n"
"buffer_load_dwordx4 a[212:215], v9, s[84:87], s35 offen offset:0 sc1\n"
"v_mfma_f32_16x16x32_bf16 v[244:247], v[104:107], a[216:219], v[244:247]\n"
"v_mfma_f32_16x16x32_bf16 v[244:247], v[108:111], a[220:223], v[244:247]\n"
"v_mfma_f32_16x16x32_bf16 v[248:251], v[112:115], a[216:219], v[248:251]\n"
"v_mfma_f32_16x16x32_bf16 v[248:251], v[116:119], a[220:223], v[248:251]\n"
"buffer_load_dwordx4 v25, s[88:91], s52 offen offset:1024 lds\n"
"v_mfma_f32_16x16x32_bf16 v[252:255], v[120:123], a[216:219], v[252:255]\n"
"v_mfma_f32_16x16x32_bf16 v[252:255], v[124:127], a[220:223], v[252:255]\n"
"s_waitcnt vmcnt(55)\n"
"v_mfma_f32_16x16x32_bf16 a[0:3], v[64:67], a[224:227], a[0:3]\n"
"s_waitcnt vmcnt(54)\n"
"v_mfma_f32_16x16x32_bf16 a[0:3], v[68:71], a[228:231], a[0:3]\n"
"buffer_load_dwordx4 a[216:219], v9, s[84:87], s34 offen offset:1024 sc1\n"
"v_mfma_f32_16x16x32_bf16 a[4:7], v[72:75], a[224:227], a[4:7]\n"
"v_mfma_f32_16x16x32_bf16 a[4:7], v[76:79], a[228:231], a[4:7]\n"
"v_mfma_f32_16x16x32_bf16 a[8:11], v[80:83], a[224:227], a[8:11]\n"
"v_mfma_f32_16x16x32_bf16 a[8:11], v[84:87], a[228:231], a[8:11]\n"
"buffer_load_dwordx4 v26, s[88:91], s52 offen offset:2048 lds\n"
"v_mfma_f32_16x16x32_bf16 a[12:15], v[88:91], a[224:227], a[12:15]\n"
"v_mfma_f32_16x16x32_bf16 a[12:15], v[92:95], a[228:231], a[12:15]\n"
"v_mfma_f32_16x16x32_bf16 a[16:19], v[96:99], a[224:227], a[16:19]\n"
"v_mfma_f32_16x16x32_bf16 a[16:19], v[100:103], a[228:231], a[16:19]\n"
"buffer_load_dwordx4 a[220:223], v9, s[84:87], s35 offen offset:1024 sc1\n"
"v_mfma_f32_16x16x32_bf16 a[20:23], v[104:107], a[224:227], a[20:23]\n"
"v_mfma_f32_16x16x32_bf16 a[20:23], v[108:111], a[228:231], a[20:23]\n"
"v_mfma_f32_16x16x32_bf16 a[24:27], v[112:115], a[224:227], a[24:27]\n"
"v_mfma_f32_16x16x32_bf16 a[24:27], v[116:119], a[228:231], a[24:27]\n"
"buffer_load_dwordx4 v27, s[88:91], s52 offen offset:3072 lds\n"
"v_mfma_f32_16x16x32_bf16 a[28:31], v[120:123], a[224:227], a[28:31]\n"
"s_add_u32 s52, s52, 32768\n"
"v_mfma_f32_16x16x32_bf16 a[28:31], v[124:127], a[228:231], a[28:31]\n"
"s_waitcnt vmcnt(57)\n"
"v_mfma_f32_16x16x32_bf16 a[32:35], v[64:67], a[232:235], a[32:35]\n"
"s_waitcnt vmcnt(56)\n"
"v_mfma_f32_16x16x32_bf16 a[32:35], v[68:71], a[236:239], a[32:35]\n"
"buffer_load_dwordx4 a[224:227], v10, s[84:87], s34 offen offset:0 sc1\n"
"v_mfma_f32_16x16x32_bf16 a[36:39], v[72:75], a[232:235], a[36:39]\n"
"v_mfma_f32_16x16x32_bf16 a[36:39], v[76:79], a[236:239], a[36:39]\n"
"v_mfma_f32_16x16x32_bf16 a[40:43], v[80:83], a[232:235], a[40:43]\n"
"s_add_u32 m0, s53, 45056\n"
"v_mfma_f32_16x16x32_bf16 a[40:43], v[84:87], a[236:239], a[40:43]\n"
"buffer_load_dwordx4 v24, s[88:91], s52 offen offset:0 lds\n"
"v_mfma_f32_16x16x32_bf16 a[44:47], v[88:91], a[232:235], a[44:47]\n"
"v_mfma_f32_16x16x32_bf16 a[44:47], v[92:95], a[236:239], a[44:47]\n"
"v_mfma_f32_16x16x32_bf16 a[48:51], v[96:99], a[232:235], a[48:51]\n"
"v_mfma_f32_16x16x32_bf16 a[48:51], v[100:103], a[236:239], a[48:51]\n"
"buffer_load_dwordx4 a[228:231], v10, s[84:87], s35 offen offset:0 sc1\n"
"v_mfma_f32_16x16x32_bf16 a[52:55], v[104:107], a[232:235], a[52:55]\n"
"v_mfma_f32_16x16x32_bf16 a[52:55], v[108:111], a[236:239], a[52:55]\n"
"v_mfma_f32_16x16x32_bf16 a[56:59], v[112:115], a[232:235], a[56:59]\n"
"v_mfma_f32_16x16x32_bf16 a[56:59], v[116:119], a[236:239], a[56:59]\n"
"buffer_load_dwordx4 v25, s[88:91], s52 offen offset:1024 lds\n"
"v_mfma_f32_16x16x32_bf16 a[60:63], v[120:123], a[232:235], a[60:63]\n"
"v_mfma_f32_16x16x32_bf16 a[60:63], v[124:127], a[236:239], a[60:63]\n"
"s_waitcnt vmcnt(59)\n"
"v_mfma_f32_16x16x32_bf16 a[64:67], v[64:67], a[240:243], a[64:67]\n"
"s_waitcnt vmcnt(58)\n"
"v_mfma_f32_16x16x32_bf16 a[64:67], v[68:71], a[244:247], a[64:67]\n"
"buffer_load_dwordx4 a[232:235], v10, s[84:87], s34 offen offset:1024 sc1\n"
"v_mfma_f32_16x16x32_bf16 a[68:71], v[72:75], a[240:243], a[68:71]\n"
"v_mfma_f32_16x16x32_bf16 a[68:71], v[76:79], a[244:247], a[68:71]\n"
"v_mfma_f32_16x16x32_bf16 a[72:75], v[80:83], a[240:243], a[72:75]\n"
"v_mfma_f32_16x16x32_bf16 a[72:75], v[84:87], a[244:247], a[72:75]\n"
"buffer_load_dwordx4 v26, s[88:91], s52 offen offset:2048 lds\n"
"v_mfma_f32_16x16x32_bf16 a[76:79], v[88:91], a[240:243], a[76:79]\n"
"v_mfma_f32_16x16x32_bf16 a[76:79], v[92:95], a[244:247], a[76:79]\n"
"v_mfma_f32_16x16x32_bf16 a[80:83], v[96:99], a[240:243], a[80:83]\n"
"v_mfma_f32_16x16x32_bf16 a[80:83], v[100:103], a[244:247], a[80:83]\n"
"buffer_load_dwordx4 a[236:239], v10, s[84:87], s35 offen offset:1024 sc1\n"
"v_mfma_f32_16x16x32_bf16 a[84:87], v[104:107], a[240:243], a[84:87]\n"
"v_mfma_f32_16x16x32_bf16 a[84:87], v[108:111], a[244:247], a[84:87]\n"
"v_mfma_f32_16x16x32_bf16 a[88:91], v[112:115], a[240:243], a[88:91]\n"
"v_mfma_f32_16x16x32_bf16 a[88:91], v[116:119], a[244:247], a[88:91]\n"
"buffer_load_dwordx4 v27, s[88:91], s52 offen offset:3072 lds\n"
"v_mfma_f32_16x16x32_bf16 a[92:95], v[120:123], a[240:243], a[92:95]\n"
"s_add_u32 s52, s52, 32768\n"
"v_mfma_f32_16x16x32_bf16 a[92:95], v[124:127], a[244:247], a[92:95]\n"
"s_waitcnt vmcnt(61)\n"
"v_mfma_f32_16x16x32_bf16 a[96:99], v[64:67], a[248:251], a[96:99]\n"
"s_waitcnt vmcnt(54)\n"
"ds_read_b64_tr_b16 v[64:65], v12 offset:0\n"
"s_waitcnt vmcnt(48)\n"
"ds_read_b64_tr_b16 v[66:67], v12 offset:4096\n"
"v_mfma_f32_16x16x32_bf16 a[96:99], v[68:71], a[252:255], a[96:99]\n"
"s_waitcnt vmcnt(40)\n"
"ds_read_b64_tr_b16 v[68:69], v12 offset:8192\n"
"s_waitcnt vmcnt(32)\n"
"ds_read_b64_tr_b16 v[70:71], v12 offset:12288\n"
"buffer_load_dwordx4 a[240:243], v11, s[84:87], s34 offen offset:0 sc1\n"
"v_mfma_f32_16x16x32_bf16 a[100:103], v[72:75], a[248:251], a[100:103]\n"
"ds_read_b64_tr_b16 v[72:73], v13 offset:0\n"
"ds_read_b64_tr_b16 v[74:75], v13 offset:4096\n"
"v_mfma_f32_16x16x32_bf16 a[100:103], v[76:79], a[252:255], a[100:103]\n"
"ds_read_b64_tr_b16 v[76:77], v13 offset:8192\n"
"ds_read_b64_tr_b16 v[78:79], v13 offset:12288\n"
"v_mfma_f32_16x16x32_bf16 a[104:107], v[80:83], a[248:251], a[104:107]\n"
"ds_read_b64_tr_b16 v[80:81], v14 offset:0\n"
"ds_read_b64_tr_b16 v[82:83], v14 offset:4096\n"
"v_mfma_f32_16x16x32_bf16 a[104:107], v[84:87], a[252:255], a[104:107]\n"
"ds_read_b64_tr_b16 v[84:85], v14 offset:8192\n"
"ds_read_b64_tr_b16 v[86:87], v14 offset:12288\n"
"v_mfma_f32_16x16x32_bf16 a[108:111], v[88:91], a[248:251], a[108:111]\n"
"ds_read_b64_tr_b16 v[88:89], v15 offset:0\n"
"ds_read_b64_tr_b16 v[90:91], v15 offset:4096\n"
"v_mfma_f32_16x16x32_bf16 a[108:111], v[92:95], a[252:255], a[108:111]\n"
"ds_read_b64_tr_b16 v[92:93], v15 offset:8192\n"
"ds_read_b64_tr_b16 v[94:95], v15 offset:12288\n"
"v_mfma_f32_16x16x32_bf16 a[112:115], v[96:99], a[248:251], a[112:115]\n"
"ds_read_b64_tr_b16 v[96:97], v16 offset:0\n"
"ds_read_b64_tr_b16 v[98:99], v16 offset:4096\n"
"v_mfma_f32_16x16x32_bf16 a[112:115], v[100:103], a[252:255], a[112:115]\n"
"ds_read_b64_tr_b16 v[100:101], v16 offset:8192\n"
"ds_read_b64_tr_b16 v[102:103], v16 offset:12288\n"
"buffer_load_dwordx4 a[244:247], v11, s[84:87], s35 offen offset:0 sc1\n"
"v_mfma_f32_16x16x32_bf16 a[116:119], v[104:107], a[248:251], a[116:119]\n"
"ds_read_b64_tr_b16 v[104:105], v17 offset:0\n"
"ds_read_b64_tr_b16 v[106:107], v17 offset:4096\n"
"v_mfma_f32_16x16x32_bf16 a[116:119], v[108:111], a[252:255], a[116:119]\n"
"ds_read_b64_tr_b16 v[108:109], v17 offset:8192\n"
"ds_read_b64_tr_b16 v[110:111], v17 offset:12288\n"
"v_mfma_f32_16x16x32_bf16 a[120:123], v[112:115], a[248:251], a[120:123]\n"
"ds_read_b64_tr_b16 v[112:113], v18 offset:0\n"
"ds_read_b64_tr_b16 v[114:115], v18 offset:4096\n"
"v_mfma_f32_16x16x32_bf16 a[120:123], v[116:119], a[252:255], a[120:123]\n"
"ds_read_b64_tr_b16 v[116:117], v18 offset:8192\n"
"ds_read_b64_tr_b16 v[118:119], v18 offset:12288\n"
"v_mfma_f32_16x16x32_bf16 a[124:127], v[120:123], a[248:251], a[124:127]\n"
"ds_read_b64_tr_b16 v[120:121], v19 offset:0\n"
"ds_read_b64_tr_b16 v[122:123], v19 offset:4096\n"
"v_mfma_f32_16x16x32_bf16 a[124:127], v[124:127], a[252:255], a[124:127]\n"
"ds_read_b64_tr_b16 v[124:125], v19 offset:8192\n"
"ds_read_b64_tr_b16 v[126:127], v19 offset:12288\n"
"s_waitcnt vmcnt(45) lgkmcnt(15)\n"
"v_mfma_f32_16x16x32_bf16 v[128:131], v[64:67], a[128:131], v[128:131]\n"
"s_waitcnt vmcnt(44)\n"
"v_mfma_f32_16x16x32_bf16 v[128:131], v[68:71], a[132:135], v[128:131]\n"
"buffer_load_dwordx4 a[248:251], v11, s[84:87], s34 offen offset:1024 sc1\n"
"v_mfma_f32_16x16x32_bf16 v[132:135], v[72:75], a[128:131], v[132:135]\n"
"v_mfma_f32_16x16x32_bf16 v[132:135], v[76:79], a[132:135], v[132:135]\n"
"v_mfma_f32_16x16x32_bf16 v[136:139], v[80:83], a[128:131], v[136:139]\n"
"v_mfma_f32_16x16x32_bf16 v[136:139], v[84:87], a[132:135], v[136:139]\n"
"v_mfma_f32_16x16x32_bf16 v[140:143], v[88:91], a[128:131], v[140:143]\n"
"v_mfma_f32_16x16x32_bf16 v[140:143], v[92:95], a[132:135], v[140:143]\n"
"s_waitcnt lgkmcnt(14)\n"
"v_mfma_f32_16x16x32_bf16 v[144:147], v[96:99], a[128:131], v[144:147]\n"
"s_waitcnt lgkmcnt(12)\n"
"v_mfma_f32_16x16x32_bf16 v[144:147], v[100:103], a[132:135], v[144:147]\n"
"buffer_load_dwordx4 a[252:255], v11, s[84:87], s35 offen offset:1024 sc1\n"
"s_waitcnt lgkmcnt(10)\n"
"v_mfma_f32_16x16x32_bf16 v[148:151], v[104:107], a[128:131], v[148:151]\n"
"s_add_u32 s34, s34, 16384\n"
"s_add_u32 s35, s35, 16384\n"
"s_waitcnt lgkmcnt(8)\n"
"v_mfma_f32_16x16x32_bf16 v[148:151], v[108:111], a[132:135], v[148:151]\n"
"s_waitcnt lgkmcnt(6)\n"
"v_mfma_f32_16x16x32_bf16 v[152:155], v[112:115], a[128:131], v[152:155]\n"
"s_waitcnt lgkmcnt(4)\n"
"v_mfma_f32_16x16x32_bf16 v[152:155], v[116:119], a[132:135], v[152:155]\n"
"s_waitcnt lgkmcnt(2)\n"
"v_mfma_f32_16x16x32_bf16 v[156:159], v[120:123], a[128:131], v[156:159]\n"
"s_waitcnt lgkmcnt(0)\n"
"v_mfma_f32_16x16x32_bf16 v[156:159], v[124:127], a[132:135], v[156:159]\n"
"s_waitcnt vmcnt(45)\n"
"v_mfma_f32_16x16x32_bf16 v[160:163], v[64:67], a[136:139], v[160:163]\n"
"s_waitcnt vmcnt(44)\n"
"v_mfma_f32_16x16x32_bf16 v[160:163], v[68:71], a[140:143], v[160:163]\n"
"buffer_load_dwordx4 a[128:131], v8, s[84:87], s34 offen offset:0 sc1\n"
"v_mfma_f32_16x16x32_bf16 v[164:167], v[72:75], a[136:139], v[164:167]\n"
"s_add_u32 m0, s53, 0\n"
"v_mfma_f32_16x16x32_bf16 v[164:167], v[76:79], a[140:143], v[164:167]\n"
"buffer_load_dwordx4 v24, s[88:91], s52 offen offset:0 lds\n"
"v_mfma_f32_16x16x32_bf16 v[168:171], v[80:83], a[136:139], v[168:171]\n"
"v_mfma_f32_16x16x32_bf16 v[168:171], v[84:87], a[140:143], v[168:171]\n"
"v_mfma_f32_16x16x32_bf16 v[172:175], v[88:91], a[136:139], v[172:175]\n"
"v_mfma_f32_16x16x32_bf16 v[172:175], v[92:95], a[140:143], v[172:175]\n"
"buffer_load_dwordx4 v25, s[88:91], s52 offen offset:1024 lds\n"
"v_mfma_f32_16x16x32_bf16 v[176:179], v[96:99], a[136:139], v[176:179]\n"
"v_mfma_f32_16x16x32_bf16 v[176:179], v[100:103], a[140:143], v[176:179]\n"
"buffer_load_dwordx4 a[132:135], v8, s[84:87], s35 offen offset:0 sc1\n"
"v_mfma_f32_16x16x32_bf16 v[180:183], v[104:107], a[136:139], v[180:183]\n"
"v_mfma_f32_16x16x32_bf16 v[180:183], v[108:111], a[140:143], v[180:183]\n"
"buffer_load_dwordx4 v26, s[88:91], s52 offen offset:2048 lds\n"
"v_mfma_f32_16x16x32_bf16 v[184:187], v[112:115], a[136:139], v[184:187]\n"
"v_mfma_f32_16x16x32_bf16 v[184:187], v[116:119], a[140:143], v[184:187]\n"
"buffer_load_dwordx4 v27, s[88:91], s52 offen offset:3072 lds\n"
"v_mfma_f32_16x16x32_bf16 v[188:191], v[120:123], a[136:139], v[188:191]\n"
"s_add_u32 s52, s52, 32768\n"
"v_mfma_f32_16x16x32_bf16 v[188:191], v[124:127], a[140:143], v[188:191]\n"
"s_waitcnt vmcnt(49)\n"
"v_mfma_f32_16x16x32_bf16 v[192:195], v[64:67], a[144:147], v[192:195]\n"
"s_waitcnt vmcnt(48)\n"
"v_mfma_f32_16x16x32_bf16 v[192:195], v[68:71], a[148:151], v[192:195]\n"
"buffer_load_dwordx4 a[136:139], v8, s[84:87], s34 offen offset:1024 sc1\n"
"v_mfma_f32_16x16x32_bf16 v[196:199], v[72:75], a[144:147], v[196:199]\n"
"s_add_u32 m0, s53, 4096\n"
"v_mfma_f32_16x16x32_bf16 v[196:199], v[76:79], a[148:151], v[196:199]\n"
"buffer_load_dwordx4 v24, s[88:91], s52 offen offset:0 lds\n"
"v_mfma_f32_16x16x32_bf16 v[200:203], v[80:83], a[144:147], v[200:203]\n"
"v_mfma_f32_16x16x32_bf16 v[200:203], v[84:87], a[148:151], v[200:203]\n"
"v_mfma_f32_16x16x32_bf16 v[204:207], v[88:91], a[144:147], v[204:207]\n"
"v_mfma_f32_16x16x32_bf16 v[204:207], v[92:95], a[148:151], v[204:207]\n"
"buffer_load_dwordx4 v25, s[88:91], s52 offen offset:1024 lds\n"
"v_mfma_f32_16x16x32_bf16 v[208:211], v[96:99], a[144:147], v[208:211]\n"
"v_mfma_f32_16x16x32_bf16 v[208:211], v[100:103], a[148:151], v[208:211]\n"
"buffer_load_dwordx4 a[140:143], v8, s[84:87], s35 offen offset:1024 sc1\n"
"v_mfma_f32_16x16x32_bf16 v[212:215], v[104:107], a[144:147], v[212:215]\n"
"v_mfma_f32_16x16x32_bf16 v[212:215], v[108:111], a[148:151], v[212:215]\n"
"buffer_load_dwordx4 v26, s[88:91], s52 offen offset:2048 lds\n"
"v_mfma_f32_16x16x32_bf16 v[216:219], v[112:115], a[144:147], v[216:219]\n"
"v_mfma_f32_16x16x32_bf16 v[216:219], v[116:119], a[148:151], v[216:219]\n"
"buffer_load_dwordx4 v27, s[88:91], s52 offen offset:3072 lds\n"
"v_mfma_f32_16x16x32_bf16 v[220:223], v[120:123], a[144:147], v[220:223]\n"
"s_add_u32 s52, s52, 32768\n"
"v_mfma_f32_16x16x32_bf16 v[220:223], v[124:127], a[148:151], v[220:223]\n"
"s_waitcnt vmcnt(53)\n"
"v_mfma_f32_16x16x32_bf16 v[224:227], v[64:67], a[152:155], v[224:227]\n"
"s_waitcnt vmcnt(52)\n"
"v_mfma_f32_16x16x32_bf16 v[224:227], v[68:71], a[156:159], v[224:227]\n"
"buffer_load_dwordx4 a[144:147], v9, s[84:87], s34 offen offset:0 sc1\n"
"v_mfma_f32_16x16x32_bf16 v[228:231], v[72:75], a[152:155], v[228:231]\n"
"v_mfma_f32_16x16x32_bf16 v[228:231], v[76:79], a[156:159], v[228:231]\n"
"v_mfma_f32_16x16x32_bf16 v[232:235], v[80:83], a[152:155], v[232:235]\n"
"s_add_u32 m0, s53, 8192\n"
"v_mfma_f32_16x16x32_bf16 v[232:235], v[84:87], a[156:159], v[232:235]\n"
"buffer_load_dwordx4 v24, s[88:91], s52 offen offset:0 lds\n"
"v_mfma_f32_16x16x32_bf16 v[236:239], v[88:91], a[152:155], v[236:239]\n"
"v_mfma_f32_16x16x32_bf16 v[236:239], v[92:95], a[156:159], v[236:239]\n"
"v_mfma_f32_16x16x32_bf16 v[240:243], v[96:99], a[152:155], v[240:243]\n"
"v_mfma_f32_16x16x32_bf16 v[240:243], v[100:103], a[156:159], v[240:243]\n"
"buffer_load_dwordx4 a[148:151], v9, s[84:87], s35 offen offset:0 sc1\n"
"v_mfma_f32_16x16x32_bf16 v[244:247], v[104:107], a[152:155], v[244:247]\n"
"v_mfma_f32_16x16x32_bf16 v[244:247], v[108:111], a[156:159], v[244:247]\n"
"v_mfma_f32_16x16x32_bf16 v[248:251], v[112:115], a[152:155], v[248:251]\n"
"v_mfma_f32_16x16x32_bf16 v[248:251], v[116:119], a[156:159], v[248:251]\n"
"buffer_load_dwordx4 v25, s[88:91], s52 offen offset:1024 lds\n"
"v_mfma_f32_16x16x32_bf16 v[252:255], v[120:123], a[152:155], v[252:255]\n"
"v_mfma_f32_16x16x32_bf16 v[252:255], v[124:127], a[156:159], v[252:255]\n"
"s_waitcnt vmcnt(55)\n"
"v_mfma_f32_16x16x32_bf16 a[0:3], v[64:67], a[160:163], a[0:3]\n"
"s_waitcnt vmcnt(54)\n"
"v_mfma_f32_16x16x32_bf16 a[0:3], v[68:71], a[164:167], a[0:3]\n"
"buffer_load_dwordx4 a[152:155], v9, s[84:87], s34 offen offset:1024 sc1\n"
"v_mfma_f32_16x16x32_bf16 a[4:7], v[72:75], a[160:163], a[4:7]\n"
"v_mfma_f32_16x16x32_bf16 a[4:7], v[76:79], a[164:167], a[4:7]\n"
"v_mfma_f32_16x16x32_bf16 a[8:11], v[80:83], a[160:163], a[8:11]\n"
"v_mfma_f32_16x16x32_bf16 a[8:11], v[84:87], a[164:167], a[8:11]\n"
"buffer_load_dwordx4 v26, s[88:91], s52 offen offset:2048 lds\n"
"v_mfma_f32_16x16x32_bf16 a[12:15], v[88:91], a[160:163], a[12:15]\n"
"v_mfma_f32_16x16x32_bf16 a[12:15], v[92:95], a[164:167], a[12:15]\n"
"v_mfma_f32_16x16x32_bf16 a[16:19], v[96:99], a[160:163], a[16:19]\n"
"v_mfma_f32_16x16x32_bf16 a[16:19], v[100:103], a[164:167], a[16:19]\n"
"buffer_load_dwordx4 a[156:159], v9, s[84:87], s35 offen offset:1024 sc1\n"
"v_mfma_f32_16x16x32_bf16 a[20:23], v[104:107], a[160:163], a[20:23]\n"
"v_mfma_f32_16x16x32_bf16 a[20:23], v[108:111], a[164:167], a[20:23]\n"
"v_mfma_f32_16x16x32_bf16 a[24:27], v[112:115], a[160:163], a[24:27]\n"
"v_mfma_f32_16x16x32_bf16 a[24:27], v[116:119], a[164:167], a[24:27]\n"
"buffer_load_dwordx4 v27, s[88:91], s52 offen offset:3072 lds\n"
"v_mfma_f32_16x16x32_bf16 a[28:31], v[120:123], a[160:163], a[28:31]\n"
"s_add_u32 s52, s52, 32768\n"
"v_mfma_f32_16x16x32_bf16 a[28:31], v[124:127], a[164:167], a[28:31]\n"
"s_waitcnt vmcnt(57)\n"
"v_mfma_f32_16x16x32_bf16 a[32:35], v[64:67], a[168:171], a[32:35]\n"
"s_waitcnt vmcnt(56)\n"
"v_mfma_f32_16x16x32_bf16 a[32:35], v[68:71], a[172:175], a[32:35]\n"
"buffer_load_dwordx4 a[160:163], v10, s[84:87], s34 offen offset:0 sc1\n"
"v_mfma_f32_16x16x32_bf16 a[36:39], v[72:75], a[168:171], a[36:39]\n"
"v_mfma_f32_16x16x32_bf16 a[36:39], v[76:79], a[172:175], a[36:39]\n"
"v_mfma_f32_16x16x32_bf16 a[40:43], v[80:83], a[168:171], a[40:43]\n"
"s_add_u32 m0, s53, 12288\n"
"v_mfma_f32_16x16x32_bf16 a[40:43], v[84:87], a[172:175], a[40:43]\n"
"buffer_load_dwordx4 v24, s[88:91], s52 offen offset:0 lds\n"
"v_mfma_f32_16x16x32_bf16 a[44:47], v[88:91], a[168:171], a[44:47]\n"
"v_mfma_f32_16x16x32_bf16 a[44:47], v[92:95], a[172:175], a[44:47]\n"
"v_mfma_f32_16x16x32_bf16 a[48:51], v[96:99], a[168:171], a[48:51]\n"
"v_mfma_f32_16x16x32_bf16 a[48:51], v[100:103], a[172:175], a[48:51]\n"
"buffer_load_dwordx4 a[164:167], v10, s[84:87], s35 offen offset:0 sc1\n"
"v_mfma_f32_16x16x32_bf16 a[52:55], v[104:107], a[168:171], a[52:55]\n"
"v_mfma_f32_16x16x32_bf16 a[52:55], v[108:111], a[172:175], a[52:55]\n"
"v_mfma_f32_16x16x32_bf16 a[56:59], v[112:115], a[168:171], a[56:59]\n"
"v_mfma_f32_16x16x32_bf16 a[56:59], v[116:119], a[172:175], a[56:59]\n"
"buffer_load_dwordx4 v25, s[88:91], s52 offen offset:1024 lds\n"
"v_mfma_f32_16x16x32_bf16 a[60:63], v[120:123], a[168:171], a[60:63]\n"
"v_mfma_f32_16x16x32_bf16 a[60:63], v[124:127], a[172:175], a[60:63]\n"
"s_waitcnt vmcnt(59)\n"
"v_mfma_f32_16x16x32_bf16 a[64:67], v[64:67], a[176:179], a[64:67]\n"
"s_waitcnt vmcnt(58)\n"
"v_mfma_f32_16x16x32_bf16 a[64:67], v[68:71], a[180:183], a[64:67]\n"
"buffer_load_dwordx4 a[168:171], v10, s[84:87], s34 offen offset:1024 sc1\n"
"v_mfma_f32_16x16x32_bf16 a[68:71], v[72:75], a[176:179], a[68:71]\n"
"v_mfma_f32_16x16x32_bf16 a[68:71], v[76:79], a[180:183], a[68:71]\n"
"v_mfma_f32_16x16x32_bf16 a[72:75], v[80:83], a[176:179], a[72:75]\n"
"v_mfma_f32_16x16x32_bf16 a[72:75], v[84:87], a[180:183], a[72:75]\n"
"buffer_load_dwordx4 v26, s[88:91], s52 offen offset:2048 lds\n"
"v_mfma_f32_16x16x32_bf16 a[76:79], v[88:91], a[176:179], a[76:79]\n"
"v_mfma_f32_16x16x32_bf16 a[76:79], v[92:95], a[180:183], a[76:79]\n"
"v_mfma_f32_16x16x32_bf16 a[80:83], v[96:99], a[176:179], a[80:83]\n"
"v_mfma_f32_16x16x32_bf16 a[80:83], v[100:103], a[180:183], a[80:83]\n"
"buffer_load_dwordx4 a[172:175], v10, s[84:87], s35 offen offset:1024 sc1\n"
"v_mfma_f32_16x16x32_bf16 a[84:87], v[104:107], a[176:179], a[84:87]\n"
"v_mfma_f32_16x16x32_bf16 a[84:87], v[108:111], a[180:183], a[84:87]\n"
"v_mfma_f32_16x16x32_bf16 a[88:91], v[112:115], a[176:179], a[88:91]\n"
"v_mfma_f32_16x16x32_bf16 a[88:91], v[116:119], a[180:183], a[88:91]\n"
"buffer_load_dwordx4 v27, s[88:91], s52 offen offset:3072 lds\n"
"v_mfma_f32_16x16x32_bf16 a[92:95], v[120:123], a[176:179], a[92:95]\n"
"s_add_u32 s52, s52, 32768\n"
"v_mfma_f32_16x16x32_bf16 a[92:95], v[124:127], a[180:183], a[92:95]\n"
"s_waitcnt vmcnt(61)\n"
"v_mfma_f32_16x16x32_bf16 a[96:99], v[64:67], a[184:187], a[96:99]\n"
"s_waitcnt vmcnt(54)\n"
"ds_read_b64_tr_b16 v[64:65], v12 offset:32768\n"
"s_waitcnt vmcnt(48)\n"
"ds_read_b64_tr_b16 v[66:67], v12 offset:36864\n"
"v_mfma_f32_16x16x32_bf16 a[96:99], v[68:71], a[188:191], a[96:99]\n"
"s_waitcnt vmcnt(40)\n"
"ds_read_b64_tr_b16 v[68:69], v12 offset:40960\n"
"s_waitcnt vmcnt(32)\n"
"ds_read_b64_tr_b16 v[70:71], v12 offset:45056\n"
"buffer_load_dwordx4 a[176:179], v11, s[84:87], s34 offen offset:0 sc1\n"
"v_mfma_f32_16x16x32_bf16 a[100:103], v[72:75], a[184:187], a[100:103]\n"
"ds_read_b64_tr_b16 v[72:73], v13 offset:32768\n"
"ds_read_b64_tr_b16 v[74:75], v13 offset:36864\n"
"v_mfma_f32_16x16x32_bf16 a[100:103], v[76:79], a[188:191], a[100:103]\n"
"ds_read_b64_tr_b16 v[76:77], v13 offset:40960\n"
"ds_read_b64_tr_b16 v[78:79], v13 offset:45056\n"
"v_mfma_f32_16x16x32_bf16 a[104:107], v[80:83], a[184:187], a[104:107]\n"
"ds_read_b64_tr_b16 v[80:81], v14 offset:32768\n"
"ds_read_b64_tr_b16 v[82:83], v14 offset:36864\n"
"v_mfma_f32_16x16x32_bf16 a[104:107], v[84:87], a[188:191], a[104:107]\n"
"ds_read_b64_tr_b16 v[84:85], v14 offset:40960\n"
"ds_read_b64_tr_b16 v[86:87], v14 offset:45056\n"
"v_mfma_f32_16x16x32_bf16 a[108:111], v[88:91], a[184:187], a[108:111]\n"
"ds_read_b64_tr_b16 v[88:89], v15 offset:32768\n"
"ds_read_b64_tr_b16 v[90:91], v15 offset:36864\n"
"v_mfma_f32_16x16x32_bf16 a[108:111], v[92:95], a[188:191], a[108:111]\n"
"ds_read_b64_tr_b16 v[92:93], v15 offset:40960\n"
"ds_read_b64_tr_b16 v[94:95], v15 offset:45056\n"
"v_mfma_f32_16x16x32_bf16 a[112:115], v[96:99], a[184:187], a[112:115]\n"
"ds_read_b64_tr_b16 v[96:97], v16 offset:32768\n"
"ds_read_b64_tr_b16 v[98:99], v16 offset:36864\n"
"v_mfma_f32_16x16x32_bf16 a[112:115], v[100:103], a[188:191], a[112:115]\n"
"ds_read_b64_tr_b16 v[100:101], v16 offset:40960\n"
"ds_read_b64_tr_b16 v[102:103], v16 offset:45056\n"
"buffer_load_dwordx4 a[180:183], v11, s[84:87], s35 offen offset:0 sc1\n"
"v_mfma_f32_16x16x32_bf16 a[116:119], v[104:107], a[184:187], a[116:119]\n"
"ds_read_b64_tr_b16 v[104:105], v17 offset:32768\n"
"ds_read_b64_tr_b16 v[106:107], v17 offset:36864\n"
"v_mfma_f32_16x16x32_bf16 a[116:119], v[108:111], a[188:191], a[116:119]\n"
"ds_read_b64_tr_b16 v[108:109], v17 offset:40960\n"
"ds_read_b64_tr_b16 v[110:111], v17 offset:45056\n"
"v_mfma_f32_16x16x32_bf16 a[120:123], v[112:115], a[184:187], a[120:123]\n"
"ds_read_b64_tr_b16 v[112:113], v18 offset:32768\n"
"ds_read_b64_tr_b16 v[114:115], v18 offset:36864\n"
"v_mfma_f32_16x16x32_bf16 a[120:123], v[116:119], a[188:191], a[120:123]\n"
"ds_read_b64_tr_b16 v[116:117], v18 offset:40960\n"
"ds_read_b64_tr_b16 v[118:119], v18 offset:45056\n"
"v_mfma_f32_16x16x32_bf16 a[124:127], v[120:123], a[184:187], a[124:127]\n"
"ds_read_b64_tr_b16 v[120:121], v19 offset:32768\n"
"ds_read_b64_tr_b16 v[122:123], v19 offset:36864\n"
"v_mfma_f32_16x16x32_bf16 a[124:127], v[124:127], a[188:191], a[124:127]\n"
"ds_read_b64_tr_b16 v[124:125], v19 offset:40960\n"
"ds_read_b64_tr_b16 v[126:127], v19 offset:45056\n"
"s_sub_u32 s60, s60, 1\n"
"s_cmp_lg_u32 s60, 0\n"
"s_cbranch_scc1 G2_LOOP\n"
"s_waitcnt lgkmcnt(15)\n"
"v_mfma_f32_16x16x32_bf16 a[0:3], v[64:67], a[224:227], a[0:3]\n"
"v_mov_b32 v31, 0\n"
"v_mov_b32 v5, 1\n"
"v_mfma_f32_16x16x32_bf16 a[0:3], v[68:71], a[228:231], a[0:3]\n"
"v_mfma_f32_16x16x32_bf16 a[4:7], v[72:75], a[224:227], a[4:7]\n"
"s_mov_b64 exec, 1\n"
"global_atomic_add v31, v31, v5, s[46:47] sc0 sc1\n"
"s_mov_b64 exec, -1\n"
"v_mfma_f32_16x16x32_bf16 a[4:7], v[76:79], a[228:231], a[4:7]\n"
"v_mfma_f32_16x16x32_bf16 a[8:11], v[80:83], a[224:227], a[8:11]\n"
"v_mfma_f32_16x16x32_bf16 a[8:11], v[84:87], a[228:231], a[8:11]\n"
"v_mfma_f32_16x16x32_bf16 a[12:15], v[88:91], a[224:227], a[12:15]\n"
"v_mfma_f32_16x16x32_bf16 a[12:15], v[92:95], a[228:231], a[12:15]\n"
"s_waitcnt lgkmcnt(14)\n"
"v_mfma_f32_16x16x32_bf16 a[16:19], v[96:99], a[224:227], a[16:19]\n"
"s_waitcnt lgkmcnt(12)\n"
"v_mfma_f32_16x16x32_bf16 a[16:19], v[100:103], a[228:231], a[16:19]\n"
"s_waitcnt lgkmcnt(10)\n"
"v_mfma_f32_16x16x32_bf16 a[20:23], v[104:107], a[224:227], a[20:23]\n"
"s_waitcnt lgkmcnt(8)\n"
"v_mfma_f32_16x16x32_bf16 a[20:23], v[108:111], a[228:231], a[20:23]\n"
"s_waitcnt lgkmcnt(6)\n"
"v_mfma_f32_16x16x32_bf16 a[24:27], v[112:115], a[224:227], a[24:27]\n"
"s_waitcnt lgkmcnt(4)\n"
"v_mfma_f32_16x16x32_bf16 a[24:27], v[116:119], a[228:231], a[24:27]\n"
"s_waitcnt lgkmcnt(2)\n"
"v_mfma_f32_16x16x32_bf16 a[28:31], v[120:123], a[224:227], a[28:31]\n"
"s_waitcnt lgkmcnt(0)\n"
"v_mfma_f32_16x16x32_bf16 a[28:31], v[124:127], a[228:231], a[28:31]\n"
"v_mfma_f32_16x16x32_bf16 a[32:35], v[64:67], a[232:235], a[32:35]\n"
"v_mfma_f32_16x16x32_bf16 a[32:35], v[68:71], a[236:239], a[32:35]\n"
"s_waitcnt vmcnt(3)\n"
"ds_write_b128 v20, a[0:3] offset:0\n"
"v_mfma_f32_16x16x32_bf16 a[36:39], v[72:75], a[232:235], a[36:39]\n"
"v_mfma_f32_16x16x32_bf16 a[36:39], v[76:79], a[236:239], a[36:39]\n"
"ds_write_b128 v20, a[4:7] offset:1024\n"
"v_mfma_f32_16x16x32_bf16 a[40:43], v[80:83], a[232:235], a[40:43]\n"
"v_mfma_f32_16x16x32_bf16 a[40:43], v[84:87], a[236:239], a[40:43]\n"
"ds_write_b128 v20, a[8:11] offset:2048\n"
"v_mfma_f32_16x16x32_bf16 a[44:47], v[88:91], a[232:235], a[44:47]\n"
"v_mfma_f32_16x16x32_bf16 a[44:47], v[92:95], a[236:239], a[44:47]\n"
"ds_write_b128 v20, a[12:15] offset:3072\n"
"v_mfma_f32_16x16x32_bf16 a[48:51], v[96:99], a[232:235], a[48:51]\n"
"v_mfma_f32_16x16x32_bf16 a[48:51], v[100:103], a[236:239], a[48:51]\n"
"ds_write_b128 v20, a[16:19] offset:4096\n"
"v_mfma_f32_16x16x32_bf16 a[52:55], v[104:107], a[232:235], a[52:55]\n"
"v_mfma_f32_16x16x32_bf16 a[52:55], v[108:111], a[236:239], a[52:55]\n"
"ds_write_b128 v20, a[20:23] offset:5120\n"
"v_mfma_f32_16x16x32_bf16 a[56:59], v[112:115], a[232:235], a[56:59]\n"
"v_mfma_f32_16x16x32_bf16 a[56:59], v[116:119], a[236:239], a[56:59]\n"
"ds_write_b128 v20, a[24:27] offset:6144\n"
"v_mfma_f32_16x16x32_bf16 a[60:63], v[120:123], a[232:235], a[60:63]\n"
"v_mfma_f32_16x16x32_bf16 a[60:63], v[124:127], a[236:239], a[60:63]\n"
"ds_write_b128 v20, a[28:31] offset:7168\n"
"v_mfma_f32_16x16x32_bf16 a[64:67], v[64:67], a[240:243], a[64:67]\n"
"v_mfma_f32_16x16x32_bf16 a[64:67], v[68:71], a[244:247], a[64:67]\n"
"ds_write_b128 v20, a[32:35] offset:8192\n"
"v_mfma_f32_16x16x32_bf16 a[68:71], v[72:75], a[240:243], a[68:71]\n"
"v_mfma_f32_16x16x32_bf16 a[68:71], v[76:79], a[244:247], a[68:71]\n"
"ds_write_b128 v20, a[36:39] offset:9216\n"
"v_mfma_f32_16x16x32_bf16 a[72:75], v[80:83], a[240:243], a[72:75]\n"
"v_mfma_f32_16x16x32_bf16 a[72:75], v[84:87], a[244:247], a[72:75]\n"
"ds_write_b128 v20, a[40:43] offset:10240\n"
"v_mfma_f32_16x16x32_bf16 a[76:79], v[88:91], a[240:243], a[76:79]\n"
"v_mfma_f32_16x16x32_bf16 a[76:79], v[92:95], a[244:247], a[76:79]\n"
"ds_write_b128 v20, a[44:47] offset:11264\n"
"v_mfma_f32_16x16x32_bf16 a[80:83], v[96:99], a[240:243], a[80:83]\n"
"v_mfma_f32_16x16x32_bf16 a[80:83], v[100:103], a[244:247], a[80:83]\n"
"ds_write_b128 v20, a[48:51] offset:12288\n"
"v_mfma_f32_16x16x32_bf16 a[84:87], v[104:107], a[240:243], a[84:87]\n"
"v_mfma_f32_16x16x32_bf16 a[84:87], v[108:111], a[244:247], a[84:87]\n"
"ds_write_b128 v20, a[52:55] offset:13312\n"
"v_mfma_f32_16x16x32_bf16 a[88:91], v[112:115], a[240:243], a[88:91]\n"
"v_mfma_f32_16x16x32_bf16 a[88:91], v[116:119], a[244:247], a[88:91]\n"
"ds_write_b128 v20, a[56:59] offset:14336\n"
"v_mfma_f32_16x16x32_bf16 a[92:95], v[120:123], a[240:243], a[92:95]\n"
"v_mfma_f32_16x16x32_bf16 a[92:95], v[124:127], a[244:247], a[92:95]\n"
"ds_write_b128 v20, a[60:63] offset:15360\n"
"v_mfma_f32_16x16x32_bf16 a[96:99], v[64:67], a[248:251], a[96:99]\n"
"v_mfma_f32_16x16x32_bf16 a[96:99], v[68:71], a[252:255], a[96:99]\n"
"ds_write_b128 v20, a[64:67] offset:32768\n"
"v_mfma_f32_16x16x32_bf16 a[100:103], v[72:75], a[248:251], a[100:103]\n"
"v_mfma_f32_16x16x32_bf16 a[100:103], v[76:79], a[252:255], a[100:103]\n"
"ds_write_b128 v20, a[68:71] offset:33792\n"
"v_mfma_f32_16x16x32_bf16 a[104:107], v[80:83], a[248:251], a[104:107]\n"
"v_mfma_f32_16x16x32_bf16 a[104:107], v[84:87], a[252:255], a[104:107]\n"
"ds_write_b128 v20, a[72:75] offset:34816\n"
"v_mfma_f32_16x16x32_bf16 a[108:111], v[88:91], a[248:251], a[108:111]\n"
"v_mfma_f32_16x16x32_bf16 a[108:111], v[92:95], a[252:255], a[108:111]\n"
"ds_write_b128 v20, a[76:79] offset:35840\n"
"v_mfma_f32_16x16x32_bf16 a[112:115], v[96:99], a[248:251], a[112:115]\n"
"v_mfma_f32_16x16x32_bf16 a[112:115], v[100:103], a[252:255], a[112:115]\n"
"ds_write_b128 v20, a[80:83] offset:36864\n"
"v_mfma_f32_16x16x32_bf16 a[116:119], v[104:107], a[248:251], a[116:119]\n"
"v_mfma_f32_16x16x32_bf16 a[116:119], v[108:111], a[252:255], a[116:119]\n"
"ds_write_b128 v20, a[84:87] offset:37888\n"
"v_mfma_f32_16x16x32_bf16 a[120:123], v[112:115], a[248:251], a[120:123]\n"
"v_mfma_f32_16x16x32_bf16 a[120:123], v[116:119], a[252:255], a[120:123]\n"
"ds_write_b128 v20, a[88:91] offset:38912\n"
"v_mfma_f32_16x16x32_bf16 a[124:127], v[120:123], a[248:251], a[124:127]\n"
"v_mfma_f32_16x16x32_bf16 a[124:127], v[124:127], a[252:255], a[124:127]\n"
"ds_write_b128 v20, a[92:95] offset:39936\n"
"v_mfma_f32_16x16x32_bf16 v[192:195], v[64:67], a[208:211], v[192:195]\n"
"v_mfma_f32_16x16x32_bf16 v[192:195], v[68:71], a[212:215], v[192:195]\n"
"ds_write_b128 v20, a[96:99] offset:40960\n"
"v_mfma_f32_16x16x32_bf16 v[196:199], v[72:75], a[208:211], v[196:199]\n"
"v_mfma_f32_16x16x32_bf16 v[196:199], v[76:79], a[212:215], v[196:199]\n"
"ds_write_b128 v20, a[100:103] offset:41984\n"
"v_mfma_f32_16x16x32_bf16 v[200:203], v[80:83], a[208:211], v[200:203]\n"
"v_mfma_f32_16x16x32_bf16 v[200:203], v[84:87], a[212:215], v[200:203]\n"
"ds_write_b128 v20, a[104:107] offset:43008\n"
"v_mfma_f32_16x16x32_bf16 v[204:207], v[88:91], a[208:211], v[204:207]\n"
"v_mfma_f32_16x16x32_bf16 v[204:207], v[92:95], a[212:215], v[204:207]\n"
"ds_write_b128 v20, a[108:111] offset:44032\n"
"v_mfma_f32_16x16x32_bf16 v[208:211], v[96:99], a[208:211], v[208:211]\n"
"v_mfma_f32_16x16x32_bf16 v[208:211], v[100:103], a[212:215], v[208:211]\n"
"ds_write_b128 v20, a[112:115] offset:45056\n"
"v_mfma_f32_16x16x32_bf16 v[212:215], v[104:107], a[208:211], v[212:215]\n"
"v_mfma_f32_16x16x32_bf16 v[212:215], v[108:111], a[212:215], v[212:215]\n"
"ds_write_b128 v20, a[116:119] offset:46080\n"
"v_mfma_f32_16x16x32_bf16 v[216:219], v[112:115], a[208:211], v[216:219]\n"
"v_mfma_f32_16x16x32_bf16 v[216:219], v[116:119], a[212:215], v[216:219]\n"
"ds_write_b128 v20, a[120:123] offset:47104\n"
"v_mfma_f32_16x16x32_bf16 v[220:223], v[120:123], a[208:211], v[220:223]\n"
"v_mfma_f32_16x16x32_bf16 v[220:223], v[124:127], a[212:215], v[220:223]\n"
"ds_write_b128 v20, a[124:127] offset:48128\n"
"v_mfma_f32_16x16x32_bf16 v[224:227], v[64:67], a[216:219], v[224:227]\n"
"v_mfma_f32_16x16x32_bf16 v[224:227], v[68:71], a[220:223], v[224:227]\n"
"s_waitcnt lgkmcnt(0)\n"
"s_barrier\n"
"v_mfma_f32_16x16x32_bf16 v[228:231], v[72:75], a[216:219], v[228:231]\n"
"ds_read_b128 v[32:35], v21 offset:32768\n"
"v_mfma_f32_16x16x32_bf16 v[228:231], v[76:79], a[220:223], v[228:231]\n"
"ds_read_b128 v[36:39], v21 offset:33792\n"
"v_mfma_f32_16x16x32_bf16 v[232:235], v[80:83], a[216:219], v[232:235]\n"
"ds_read_b128 v[40:43], v21 offset:34816\n"
"v_mfma_f32_16x16x32_bf16 v[232:235], v[84:87], a[220:223], v[232:235]\n"
"ds_read_b128 v[44:47], v21 offset:35840\n"
"v_mfma_f32_16x16x32_bf16 v[236:239], v[88:91], a[216:219], v[236:239]\n"
"ds_read_b128 v[48:51], v21 offset:36864\n"
"v_mfma_f32_16x16x32_bf16 v[236:239], v[92:95], a[220:223], v[236:239]\n"
"ds_read_b128 v[52:55], v21 offset:37888\n"
"v_mfma_f32_16x16x32_bf16 v[240:243], v[96:99], a[216:219], v[240:243]\n"
"ds_read_b128 v[56:59], v21 offset:38912\n"
"v_mfma_f32_16x16x32_bf16 v[240:243], v[100:103], a[220:223], v[240:243]\n"
"ds_read_b128 v[60:63], v21 offset:39936\n"
"v_mfma_f32_16x16x32_bf16 v[244:247], v[104:107], a[216:219], v[244:247]\n"
"v_mfma_f32_16x16x32_bf16 v[244:247], v[108:111], a[220:223], v[244:247]\n"
"v_mfma_f32_16x16x32_bf16 v[248:251], v[112:115], a[216:219], v[248:251]\n"
"v_mfma_f32_16x16x32_bf16 v[248:251], v[116:119], a[220:223], v[248:251]\n"
"v_mfma_f32_16x16x32_bf16 v[252:255], v[120:123], a[216:219], v[252:255]\n"
"v_mfma_f32_16x16x32_bf16 v[252:255], v[124:127], a[220:223], v[252:255]\n"
"v_mfma_f32_16x16x32_bf16 v[128:131], v[64:67], a[192:195], v[128:131]\n"
"s_waitcnt lgkmcnt(7)\n"
"v_add_f32 v192, v192, v32\n"
"v_add_f32 v193, v193, v33\n"
"v_add_f32 v194, v194, v34\n"
"v_mfma_f32_16x16x32_bf16 v[128:131], v[68:71], a[196:199], v[128:131]\n"
"v_add_f32 v195, v195, v35\n"
"s_waitcnt lgkmcnt(6)\n"
"v_add_f32 v196, v196, v36\n"
"v_add_f32 v197, v197, v37\n"
"v_add_f32 v198, v198, v38\n"
"v_mfma_f32_16x16x32_bf16 v[132:135], v[72:75], a[192:195], v[132:135]\n"
"v_add_f32 v199, v199, v39\n"
"v_mfma_f32_16x16x32_bf16 v[132:135], v[76:79], a[196:199], v[132:135]\n"
"ds_write_b128 v21, v[192:195] offset:32768\n"
"ds_read_b128 v[32:35], v21 offset:40960\n"
"s_waitcnt lgkmcnt(7)\n"
"v_add_f32 v200, v200, v40\n"
"v_add_f32 v201, v201, v41\n"
"v_add_f32 v202, v202, v42\n"
"v_mfma_f32_16x16x32_bf16 v[136:139], v[80:83], a[192:195], v[136:139]\n"
"ds_write_b128 v21, v[196:199] offset:33792\n"
"ds_read_b128 v[36:39], v21 offset:41984\n"
"v_add_f32 v203, v203, v43\n"
"s_waitcnt lgkmcnt(8)\n"
"v_add_f32 v204, v204, v44\n"
"v_add_f32 v205, v205, v45\n"
"v_add_f32 v206, v206, v46\n"
"v_mfma_f32_16x16x32_bf16 v[136:139], v[84:87], a[196:199], v[136:139]\n"
"v_add_f32 v207, v207, v47\n"
"v_mfma_f32_16x16x32_bf16 v[140:143], v[88:91], a[192:195], v[140:143]\n"
"ds_write_b128 v21, v[200:203] offset:34816\n"
"ds_read_b128 v[40:43], v21 offset:43008\n"
"s_waitcnt lgkmcnt(9)\n"
"v_add_f32 v208, v208, v48\n"
"v_add_f32 v209, v209, v49\n"
"v_add_f32 v210, v210, v50\n"
"v_mfma_f32_16x16x32_bf16 v[140:143], v[92:95], a[196:199], v[140:143]\n"
"ds_write_b128 v21, v[204:207] offset:35840\n"
"ds_read_b128 v[44:47], v21 offset:44032\n"
"v_add_f32 v211, v211, v51\n"
"s_waitcnt lgkmcnt(10)\n"
"v_add_f32 v212, v212, v52\n"
"v_add_f32 v213, v213, v53\n"
"v_add_f32 v214, v214, v54\n"
"v_mfma_f32_16x16x32_bf16 v[144:147], v[96:99], a[192:195], v[144:147]\n"
"v_add_f32 v215, v215, v55\n"
"v_mfma_f32_16x16x32_bf16 v[144:147], v[100:103], a[196:199], v[144:147]\n"
"ds_write_b128 v21, v[208:211] offset:36864\n"
"ds_read_b128 v[48:51], v21 offset:45056\n"
"s_waitcnt lgkmcnt(11)\n"
"v_add_f32 v216, v216, v56\n"
"v_add_f32 v217, v217, v57\n"
"v_add_f32 v218, v218, v58\n"
"v_mfma_f32_16x16x32_bf16 v[148:151], v[104:107], a[192:195], v[148:151]\n"
"ds_write_b128 v21, v[212:215] offset:37888\n"
"ds_read_b128 v[52:55], v21 offset:46080\n"
"v_add_f32 v219, v219, v59\n"
"s_waitcnt lgkmcnt(12)\n"
"v_add_f32 v220, v220, v60\n"
"v_add_f32 v221, v221, v61\n"
"v_add_f32 v222, v222, v62\n"
"v_mfma_f32_16x16x32_bf16 v[148:151], v[108:111], a[196:199], v[148:151]\n"
"v_add_f32 v223, v223, v63\n"
"v_mfma_f32_16x16x32_bf16 v[152:155], v[112:115], a[192:195], v[152:155]\n"
"ds_write_b128 v21, v[216:219] offset:38912\n"
"ds_read_b128 v[56:59], v21 offset:47104\n"
"v_mfma_f32_16x16x32_bf16 v[152:155], v[116:119], a[196:199], v[152:155]\n"
"ds_write_b128 v21, v[220:223] offset:39936\n"
"ds_read_b128 v[60:63], v21 offset:48128\n"
"v_mfma_f32_16x16x32_bf16 v[156:159], v[120:123], a[192:195], v[156:159]\n"
"v_mfma_f32_16x16x32_bf16 v[156:159], v[124:127], a[196:199], v[156:159]\n"
"v_mfma_f32_16x16x32_bf16 v[160:163], v[64:67], a[200:203], v[160:163]\n"
"s_waitcnt lgkmcnt(14)\n"
"v_add_f32 v224, v224, v32\n"
"v_add_f32 v225, v225, v33\n"
"v_add_f32 v226, v226, v34\n"
"v_mfma_f32_16x16x32_bf16 v[160:163], v[68:71], a[204:207], v[160:163]\n"
"v_add_f32 v227, v227, v35\n"
"s_waitcnt lgkmcnt(12)\n"
"v_add_f32 v228, v228, v36\n"
"v_add_f32 v229, v229, v37\n"
"v_add_f32 v230, v230, v38\n"
"v_mfma_f32_16x16x32_bf16 v[164:167], v[72:75], a[200:203], v[164:167]\n"
"v_add_f32 v231, v231, v39\n"
"v_mfma_f32_16x16x32_bf16 v[164:167], v[76:79], a[204:207], v[164:167]\n"
"ds_write_b128 v21, v[224:227] offset:40960\n"
"ds_read_b128 v[32:35], v21 offset:0\n"
"s_waitcnt lgkmcnt(12)\n"
"v_add_f32 v232, v232, v40\n"
"v_add_f32 v233, v233, v41\n"
"v_add_f32 v234, v234, v42\n"
"v_mfma_f32_16x16x32_bf16 v[168:171], v[80:83], a[200:203], v[168:171]\n"
"ds_write_b128 v21, v[228:231] offset:41984\n"
"ds_read_b128 v[36:39], v21 offset:1024\n"
"v_add_f32 v235, v235, v43\n"
"s_waitcnt lgkmcnt(12)\n"
"v_add_f32 v236, v236, v44\n"
"v_add_f32 v237, v237, v45\n"
"v_add_f32 v238, v238, v46\n"
"v_mfma_f32_16x16x32_bf16 v[168:171], v[84:87], a[204:207], v[168:171]\n"
"v_add_f32 v239, v239, v47\n"
"v_mfma_f32_16x16x32_bf16 v[172:175], v[88:91], a[200:203], v[172:175]\n"
"ds_write_b128 v21, v[232:235] offset:43008\n"
"ds_read_b128 v[40:43], v21 offset:2048\n"
"s_waitcnt lgkmcnt(12)\n"
"v_add_f32 v240, v240, v48\n"
"v_add_f32 v241, v241, v49\n"
"v_add_f32 v242, v242, v50\n"
"v_mfma_f32_16x16x32_bf16 v[172:175], v[92:95], a[204:207], v[172:175]\n"
"ds_write_b128 v21, v[236:239] offset:44032\n"
"ds_read_b128 v[44:47], v21 offset:3072\n"
"v_add_f32 v243, v243, v51\n"
"s_waitcnt lgkmcnt(12)\n"
"v_add_f32 v244, v244, v52\n"
"v_add_f32 v245, v245, v53\n"
"v_add_f32 v246, v246, v54\n"
"v_mfma_f32_16x16x32_bf16 v[176:179], v[96:99], a[200:203], v[176:179]\n"
"v_add_f32 v247, v247, v55\n"
"v_mfma_f32_16x16x32_bf16 v[176:179], v[100:103], a[204:207], v[176:179]\n"
"ds_write_b128 v21, v[240:243] offset:45056\n"
"ds_read_b128 v[48:51], v21 offset:4096\n"
"s_waitcnt lgkmcnt(12)\n"
"v_add_f32 v248, v248, v56\n"
"v_add_f32 v249, v249, v57\n"
"v_add_f32 v250, v250, v58\n"
"v_mfma_f32_16x16x32_bf16 v[180:183], v[104:107], a[200:203], v[180:183]\n"
"ds_write_b128 v21, v[244:247] offset:46080\n"
"ds_read_b128 v[52:55], v21 offset:5120\n"
"s_waitcnt vmcnt(0)\n"
"v_readfirstlane_b32 s51, v31\n"
"v_add_f32 v251, v251, v59\n"
"s_waitcnt lgkmcnt(12)\n"
"v_add_f32 v252, v252, v60\n"
"v_add_f32 v253, v253, v61\n"
"v_add_f32 v254, v254, v62\n"
"v_mfma_f32_16x16x32_bf16 v[180:183], v[108:111], a[204:207], v[180:183]\n"
"v_add_f32 v255, v255, v63\n"
"v_mfma_f32_16x16x32_bf16 v[184:187], v[112:115], a[200:203], v[184:187]\n"
"ds_write_b128 v21, v[248:251] offset:47104\n"
"ds_read_b128 v[56:59], v21 offset:6144\n"
"v_mov_b32 v31, 0\n"
"v_mfma_f32_16x16x32_bf16 v[184:187], v[116:119], a[204:207], v[184:187]\n"
"ds_write_b128 v21, v[252:255] offset:48128\n"
"ds_read_b128 v[60:63], v21 offset:7168\n"
"global_load_dword v31, v31, s[46:47] offset:256 sc1\n"
"v_mfma_f32_16x16x32_bf16 v[188:191], v[120:123], a[200:203], v[188:191]\n"
"v_mfma_f32_16x16x32_bf16 v[188:191], v[124:127], a[204:207], v[188:191]\n"
"ds_read_b128 v[64:67], v21 offset:8192\n"
"ds_read_b128 v[68:71], v21 offset:9216\n"
"ds_read_b128 v[72:75], v21 offset:10240\n"
"ds_read_b128 v[76:79], v21 offset:11264\n"
"ds_read_b128 v[80:83], v21 offset:12288\n"
"ds_read_b128 v[84:87], v21 offset:13312\n"
"ds_read_b128 v[88:91], v21 offset:14336\n"
"ds_read_b128 v[92:95], v21 offset:15360\n"
"s_lshl_b32 s42, s51, 18\n"
"buffer_load_dwordx4 v[192:195], v30, s[40:43], 0 offen offset:0\n"
"buffer_load_dwordx4 v[196:199], v30, s[40:43], 0 offen offset:64\n"
"buffer_load_dwordx4 v[200:203], v30, s[40:43], 0 offen offset:128\n"
"buffer_load_dwordx4 v[204:207], v30, s[40:43], 0 offen offset:192\n"
"buffer_load_dwordx4 v[208:211], v29, s[40:43], 0 offen offset:0\n"
"buffer_load_dwordx4 v[212:215], v29, s[40:43], 0 offen offset:64\n"
"buffer_load_dwordx4 v[216:219], v29, s[40:43], 0 offen offset:128\n"
"buffer_load_dwordx4 v[220:223], v29, s[40:43], 0 offen offset:192\n"
"s_waitcnt vmcnt(8)\n"
"v_readfirstlane_b32 s54, v31\n"
"s_waitcnt lgkmcnt(8)\n"
"s_barrier\n"
"s_lshl_b32 s94, s54, 15\n"
"buffer_load_dwordx4 v[224:227], v23, s[92:95], 0 offen offset:0 sc1\n"
"buffer_load_dwordx4 v[228:231], v23, s[92:95], 0 offen offset:1024 sc1\n"
"buffer_load_dwordx4 v[232:235], v23, s[92:95], 0 offen offset:2048 sc1\n"
"buffer_load_dwordx4 v[236:239], v23, s[92:95], 0 offen offset:3072 sc1\n"
"buffer_load_dwordx4 v[240:243], v23, s[92:95], s69 offen offset:0 sc1\n"
"buffer_load_dwordx4 v[244:247], v23, s[92:95], s69 offen offset:1024 sc1\n"
"buffer_load_dwordx4 v[248:251], v23, s[92:95], s69 offen offset:2048 sc1\n"
"buffer_load_dwordx4 v[252:255], v23, s[92:95], s69 offen offset:3072 sc1\n"
"ds_read_b128 v[96:99], v22 offset:32768\n"
"ds_read_b128 v[100:103], v22 offset:33792\n"
"ds_read_b128 v[104:107], v22 offset:34816\n"
"ds_read_b128 v[108:111], v22 offset:35840\n"
"ds_read_b128 v[112:115], v22 offset:36864\n"
"ds_read_b128 v[116:119], v22 offset:37888\n"
"ds_read_b128 v[120:123], v22 offset:38912\n"
"ds_read_b128 v[124:127], v22 offset:39936\n"
"v_pk_add_f32 v[128:129], v[128:129], v[32:33]\n"
"v_pk_add_f32 v[130:131], v[130:131], v[34:35]\n"
"v_pk_add_f32 v[132:133], v[132:133], v[36:37]\n"
"v_pk_add_f32 v[134:135], v[134:135], v[38:39]\n"
"v_pk_add_f32 v[136:137], v[136:137], v[40:41]\n"
"v_pk_add_f32 v[138:139], v[138:139], v[42:43]\n"
"v_pk_add_f32 v[140:141], v[140:141], v[44:45]\n"
"v_pk_add_f32 v[142:143], v[142:143], v[46:47]\n"
"v_pk_add_f32 v[144:145], v[144:145], v[48:49]\n"
"v_pk_add_f32 v[146:147], v[146:147], v[50:51]\n"
"v_pk_add_f32 v[148:149], v[148:149], v[52:53]\n"
"v_pk_add_f32 v[150:151], v[150:151], v[54:55]\n"
"v_pk_add_f32 v[152:153], v[152:153], v[56:57]\n"
"v_pk_add_f32 v[154:155], v[154:155], v[58:59]\n"
"v_pk_add_f32 v[156:157], v[156:157], v[60:61]\n"
"v_pk_add_f32 v[158:159], v[158:159], v[62:63]\n"
"ds_read_b128 v[32:35], v22 offset:40960\n"
"ds_read_b128 v[36:39], v22 offset:41984\n"
"ds_read_b128 v[40:43], v22 offset:43008\n"
"ds_read_b128 v[44:47], v22 offset:44032\n"
"ds_read_b128 v[48:51], v22 offset:45056\n"
"ds_read_b128 v[52:55], v22 offset:46080\n"
"ds_read_b128 v[56:59], v22 offset:47104\n"
"ds_read_b128 v[60:63], v22 offset:48128\n"
"s_waitcnt lgkmcnt(15)\n"
"v_pk_add_f32 v[160:161], v[160:161], v[64:65]\n"
"v_pk_add_f32 v[162:163], v[162:163], v[66:67]\n"
"v_pk_add_f32 v[164:165], v[164:165], v[68:69]\n"
"v_pk_add_f32 v[166:167], v[166:167], v[70:71]\n"
"v_pk_add_f32 v[168:169], v[168:169], v[72:73]\n"
"v_pk_add_f32 v[170:171], v[170:171], v[74:75]\n"
"v_pk_add_f32 v[172:173], v[172:173], v[76:77]\n"
"v_pk_add_f32 v[174:175], v[174:175], v[78:79]\n"
"v_pk_add_f32 v[176:177], v[176:177], v[80:81]\n"
"v_pk_add_f32 v[178:179], v[178:179], v[82:83]\n"
"v_pk_add_f32 v[180:181], v[180:181], v[84:85]\n"
"v_pk_add_f32 v[182:183], v[182:183], v[86:87]\n"
"v_pk_add_f32 v[184:185], v[184:185], v[88:89]\n"
"v_pk_add_f32 v[186:187], v[186:187], v[90:91]\n"
"v_pk_add_f32 v[188:189], v[188:189], v[92:93]\n"
"v_pk_add_f32 v[190:191], v[190:191], v[94:95]\n"
"v_pk_add_f32 v[128:129], v[128:129], v[96:97]\n"
"v_pk_add_f32 v[130:131], v[130:131], v[98:99]\n"
"s_waitcnt lgkmcnt(14)\n"
"v_pk_add_f32 v[132:133], v[132:133], v[100:101]\n"
"v_pk_add_f32 v[134:135], v[134:135], v[102:103]\n"
"s_waitcnt lgkmcnt(13)\n"
"v_pk_add_f32 v[136:137], v[136:137], v[104:105]\n"
"v_pk_add_f32 v[138:139], v[138:139], v[106:107]\n"
"s_waitcnt lgkmcnt(12)\n"
"v_pk_add_f32 v[140:141], v[140:141], v[108:109]\n"
"v_pk_add_f32 v[142:143], v[142:143], v[110:111]\n"
"s_waitcnt lgkmcnt(11)\n"
"v_pk_add_f32 v[144:145], v[144:145], v[112:113]\n"
"v_pk_add_f32 v[146:147], v[146:147], v[114:115]\n"
"s_waitcnt lgkmcnt(10)\n"
"v_pk_add_f32 v[148:149], v[148:149], v[116:117]\n"
"v_pk_add_f32 v[150:151], v[150:151], v[118:119]\n"
"s_waitcnt lgkmcnt(9)\n"
"v_pk_add_f32 v[152:153], v[152:153], v[120:121]\n"
"v_pk_add_f32 v[154:155], v[154:155], v[122:123]\n"
"s_waitcnt lgkmcnt(8)\n"
"v_pk_add_f32 v[156:157], v[156:157], v[124:125]\n"
"v_pk_add_f32 v[158:159], v[158:159], v[126:127]\n"
"s_waitcnt lgkmcnt(7)\n"
"v_pk_add_f32 v[160:161], v[160:161], v[32:33]\n"
"v_pk_add_f32 v[162:163], v[162:163], v[34:35]\n"
"s_waitcnt lgkmcnt(6)\n"
"v_pk_add_f32 v[164:165], v[164:165], v[36:37]\n"
"v_pk_add_f32 v[166:167], v[166:167], v[38:39]\n"
"s_waitcnt lgkmcnt(5)\n"
"v_pk_add_f32 v[168:169], v[168:169], v[40:41]\n"
"v_pk_add_f32 v[170:171], v[170:171], v[42:43]\n"
"s_waitcnt lgkmcnt(4)\n"
"v_pk_add_f32 v[172:173], v[172:173], v[44:45]\n"
"v_pk_add_f32 v[174:175], v[174:175], v[46:47]\n"
"s_waitcnt lgkmcnt(3)\n"
"v_pk_add_f32 v[176:177], v[176:177], v[48:49]\n"
"v_pk_add_f32 v[178:179], v[178:179], v[50:51]\n"
"s_waitcnt lgkmcnt(2)\n"
"v_pk_add_f32 v[180:181], v[180:181], v[52:53]\n"
"v_pk_add_f32 v[182:183], v[182:183], v[54:55]\n"
"s_waitcnt lgkmcnt(1)\n"
"v_pk_add_f32 v[184:185], v[184:185], v[56:57]\n"
"v_pk_add_f32 v[186:187], v[186:187], v[58:59]\n"
"s_waitcnt lgkmcnt(0)\n"
"v_pk_add_f32 v[188:189], v[188:189], v[60:61]\n"
"v_pk_add_f32 v[190:191], v[190:191], v[62:63]\n"
"s_cmp_lg_u32 s64, 0\n"
"s_cbranch_scc1 G2_NOSILU\n"
"v_mov_b32 v62, 0xbfb8aa3b\n"
"v_mov_b32 v63, 0xbfb8aa3b\n"
"v_pk_mul_f32 v[64:65], v[128:129], v[62:63]\n"
"v_pk_mul_f32 v[66:67], v[130:131], v[62:63]\n"
"v_pk_mul_f32 v[68:69], v[132:133], v[62:63]\n"
"v_pk_mul_f32 v[70:71], v[134:135], v[62:63]\n"
"v_pk_mul_f32 v[72:73], v[136:137], v[62:63]\n"
"v_pk_mul_f32 v[74:75], v[138:139], v[62:63]\n"
"v_pk_mul_f32 v[76:77], v[140:141], v[62:63]\n"
"v_pk_mul_f32 v[78:79], v[142:143], v[62:63]\n"
"v_pk_mul_f32 v[80:81], v[144:145], v[62:63]\n"
"v_pk_mul_f32 v[82:83], v[146:147], v[62:63]\n"
"v_pk_mul_f32 v[84:85], v[148:149], v[62:63]\n"
"v_pk_mul_f32 v[86:87], v[150:151], v[62:63]\n"
"v_pk_mul_f32 v[88:89], v[152:153], v[62:63]\n"
"v_pk_mul_f32 v[90:91], v[154:155], v[62:63]\n"
"v_pk_mul_f32 v[92:93], v[156:157], v[62:63]\n"
"v_pk_mul_f32 v[94:95], v[158:159], v[62:63]\n"
"v_pk_mul_f32 v[96:97], v[160:161], v[62:63]\n"
"v_pk_mul_f32 v[98:99], v[162:163], v[62:63]\n"
"v_pk_mul_f32 v[100:101], v[164:165], v[62:63]\n"
"v_pk_mul_f32 v[102:103], v[166:167], v[62:63]\n"
"v_pk_mul_f32 v[104:105], v[168:169], v[62:63]\n"
"v_pk_mul_f32 v[106:107], v[170:171], v[62:63]\n"
"v_pk_mul_f32 v[108:109], v[172:173], v[62:63]\n"
"v_pk_mul_f32 v[110:111], v[174:175], v[62:63]\n"
"v_pk_mul_f32 v[112:113], v[176:177], v[62:63]\n"
"v_pk_mul_f32 v[114:115], v[178:179], v[62:63]\n"
"v_pk_mul_f32 v[116:117], v[180:181], v[62:63]\n"
"v_pk_mul_f32 v[118:119], v[182:183], v[62:63]\n"
"v_pk_mul_f32 v[120:121], v[184:185], v[62:63]\n"
"v_pk_mul_f32 v[122:123], v[186:187], v[62:63]\n"
"v_pk_mul_f32 v[124:125], v[188:189], v[62:63]\n"
"v_pk_mul_f32 v[126:127], v[190:191], v[62:63]\n"
"v_exp_f32 v64, v64\n"
"v_exp_f32 v65, v65\n"
"v_exp_f32 v66, v66\n"
"v_exp_f32 v67, v67\n"
"v_exp_f32 v68, v68\n"
"v_exp_f32 v69, v69\n"
"v_pk_add_f32 v[64:65], v[64:65], 1.0 op_sel_hi:[1,0]\n"
"v_exp_f32 v70, v70\n"
"v_exp_f32 v71, v71\n"
"v_pk_add_f32 v[66:67], v[66:67], 1.0 op_sel_hi:[1,0]\n"
"v_exp_f32 v72, v72\n"
"v_exp_f32 v73, v73\n"
"v_pk_add_f32 v[68:69], v[68:69], 1.0 op_sel_hi:[1,0]\n"
"v_exp_f32 v74, v74\n"
"v_exp_f32 v75, v75\n"
"v_pk_add_f32 v[70:71], v[70:71], 1.0 op_sel_hi:[1,0]\n"
"v_exp_f32 v76, v76\n"
"v_exp_f32 v77, v77\n"
"v_pk_add_f32 v[72:73], v[72:73], 1.0 op_sel_hi:[1,0]\n"
"v_exp_f32 v78, v78\n"
"v_exp_f32 v79, v79\n"
"v_pk_add_f32 v[74:75], v[74:75], 1.0 op_sel_hi:[1,0]\n"
"v_exp_f32 v80, v80\n"
"v_exp_f32 v81, v81\n"
"v_pk_add_f32 v[76:77], v[76:77], 1.0 op_sel_hi:[1,0]\n"
"v_exp_f32 v82, v82\n"
"v_exp_f32 v83, v83\n"
"v_pk_add_f32 v[78:79], v[78:79], 1.0 op_sel_hi:[1,0]\n"
"v_exp_f32 v84, v84\n"
"v_exp_f32 v85, v85\n"
"v_pk_add_f32 v[80:81], v[80:81], 1.0 op_sel_hi:[1,0]\n"
"v_exp_f32 v86, v86\n"
"v_exp_f32 v87, v87\n"
"v_pk_add_f32 v[82:83], v[82:83], 1.0 op_sel_hi:[1,0]\n"
"v_exp_f32 v88, v88\n"
"v_exp_f32 v89, v89\n"
"v_pk_add_f32 v[84:85], v[84:85], 1.0 op_sel_hi:[1,0]\n"
"v_exp_f32 v90, v90\n"
"v_exp_f32 v91, v91\n"
"v_pk_add_f32 v[86:87], v[86:87], 1.0 op_sel_hi:[1,0]\n"
"v_exp_f32 v92, v92\n"
"v_exp_f32 v93, v93\n"
"v_pk_add_f32 v[88:89], v[88:89], 1.0 op_sel_hi:[1,0]\n"
"v_exp_f32 v94, v94\n"
"v_exp_f32 v95, v95\n"
"v_pk_add_f32 v[90:91], v[90:91], 1.0 op_sel_hi:[1,0]\n"
"v_exp_f32 v96, v96\n"
"v_exp_f32 v97, v97\n"
"v_pk_add_f32 v[92:93], v[92:93], 1.0 op_sel_hi:[1,0]\n"
"v_exp_f32 v98, v98\n"
"v_exp_f32 v99, v99\n"
"v_pk_add_f32 v[94:95], v[94:95], 1.0 op_sel_hi:[1,0]\n"
"v_exp_f32 v100, v100\n"
"v_exp_f32 v101, v101\n"
"v_pk_add_f32 v[96:97], v[96:97], 1.0 op_sel_hi:[1,0]\n"
"v_exp_f32 v102, v102\n"
"v_exp_f32 v103, v103\n"
"v_pk_add_f32 v[98:99], v[98:99], 1.0 op_sel_hi:[1,0]\n"
"v_exp_f32 v104, v104\n"
"v_exp_f32 v105, v105\n"
"v_pk_add_f32 v[100:101], v[100:101], 1.0 op_sel_hi:[1,0]\n"
"v_exp_f32 v106, v106\n"
"v_exp_f32 v107, v107\n"
"v_pk_add_f32 v[102:103], v[102:103], 1.0 op_sel_hi:[1,0]\n"
"v_exp_f32 v108, v108\n"
"v_exp_f32 v109, v109\n"
"v_pk_add_f32 v[104:105], v[104:105], 1.0 op_sel_hi:[1,0]\n"
"v_exp_f32 v110, v110\n"
"v_exp_f32 v111, v111\n"
"v_pk_add_f32 v[106:107], v[106:107], 1.0 op_sel_hi:[1,0]\n"
"v_exp_f32 v112, v112\n"
"v_exp_f32 v113, v113\n"
"v_pk_add_f32 v[108:109], v[108:109], 1.0 op_sel_hi:[1,0]\n"
"v_exp_f32 v114, v114\n"
"v_exp_f32 v115, v115\n"
"v_pk_add_f32 v[110:111], v[110:111], 1.0 op_sel_hi:[1,0]\n"
"v_exp_f32 v116, v116\n"
"v_exp_f32 v117, v117\n"
"v_pk_add_f32 v[112:113], v[112:113], 1.0 op_sel_hi:[1,0]\n"
"v_exp_f32 v118, v118\n"
"v_exp_f32 v119, v119\n"
"v_pk_add_f32 v[114:115], v[114:115], 1.0 op_sel_hi:[1,0]\n"
"v_exp_f32 v120, v120\n"
"v_exp_f32 v121, v121\n"
"v_pk_add_f32 v[116:117], v[116:117], 1.0 op_sel_hi:[1,0]\n"
"v_exp_f32 v122, v122\n"
"v_exp_f32 v123, v123\n"
"v_pk_add_f32 v[118:119], v[118:119], 1.0 op_sel_hi:[1,0]\n"
"v_exp_f32 v124, v124\n"
"v_exp_f32 v125, v125\n"
"v_pk_add_f32 v[120:121], v[120:121], 1.0 op_sel_hi:[1,0]\n"
"v_exp_f32 v126, v126\n"
"v_exp_f32 v127, v127\n"
"v_pk_add_f32 v[122:123], v[122:123], 1.0 op_sel_hi:[1,0]\n"
"v_pk_add_f32 v[124:125], v[124:125], 1.0 op_sel_hi:[1,0]\n"
"v_pk_add_f32 v[126:127], v[126:127], 1.0 op_sel_hi:[1,0]\n"
"v_rcp_f32 v64, v64\n"
"v_rcp_f32 v65, v65\n"
"v_rcp_f32 v66, v66\n"
"v_rcp_f32 v67, v67\n"
"v_rcp_f32 v68, v68\n"
"v_rcp_f32 v69, v69\n"
"v_pk_mul_f32 v[128:129], v[128:129], v[64:65]\n"
"v_rcp_f32 v70, v70\n"
"v_rcp_f32 v71, v71\n"
"v_pk_mul_f32 v[130:131], v[130:131], v[66:67]\n"
"v_rcp_f32 v72, v72\n"
"v_rcp_f32 v73, v73\n"
"v_pk_mul_f32 v[132:133], v[132:133], v[68:69]\n"
"v_rcp_f32 v74, v74\n"
"v_rcp_f32 v75, v75\n"
"v_pk_mul_f32 v[134:135], v[134:135], v[70:71]\n"
"v_rcp_f32 v76, v76\n"
"v_rcp_f32 v77, v77\n"
"v_pk_mul_f32 v[136:137], v[136:137], v[72:73]\n"
"v_rcp_f32 v78, v78\n"
"v_rcp_f32 v79, v79\n"
"v_pk_mul_f32 v[138:139], v[138:139], v[74:75]\n"
"v_rcp_f32 v80, v80\n"
"v_rcp_f32 v81, v81\n"
"v_pk_mul_f32 v[140:141], v[140:141], v[76:77]\n"
"v_rcp_f32 v82, v82\n"
"v_rcp_f32 v83, v83\n"
"v_pk_mul_f32 v[142:143], v[142:143], v[78:79]\n"
"v_rcp_f32 v84, v84\n"
"v_rcp_f32 v85, v85\n"
"v_pk_mul_f32 v[144:145], v[144:145], v[80:81]\n"
"v_rcp_f32 v86, v86\n"
"v_rcp_f32 v87, v87\n"
"v_pk_mul_f32 v[146:147], v[146:147], v[82:83]\n"
"v_rcp_f32 v88, v88\n"
"v_rcp_f32 v89, v89\n"
"v_pk_mul_f32 v[148:149], v[148:149], v[84:85]\n"
"v_rcp_f32 v90, v90\n"
"v_rcp_f32 v91, v91\n"
"v_pk_mul_f32 v[150:151], v[150:151], v[86:87]\n"
"v_rcp_f32 v92, v92\n"
"v_rcp_f32 v93, v93\n"
"v_pk_mul_f32 v[152:153], v[152:153], v[88:89]\n"
"v_rcp_f32 v94, v94\n"
"v_rcp_f32 v95, v95\n"
"v_pk_mul_f32 v[154:155], v[154:155], v[90:91]\n"
"v_rcp_f32 v96, v96\n"
"v_rcp_f32 v97, v97\n"
"v_pk_mul_f32 v[156:157], v[156:157], v[92:93]\n"
"v_rcp_f32 v98, v98\n"
"v_rcp_f32 v99, v99\n"
"v_pk_mul_f32 v[158:159], v[158:159], v[94:95]\n"
"v_rcp_f32 v100, v100\n"
"v_rcp_f32 v101, v101\n"
"v_pk_mul_f32 v[160:161], v[160:161], v[96:97]\n"
"v_rcp_f32 v102, v102\n"
"v_rcp_f32 v103, v103\n"
"v_pk_mul_f32 v[162:163], v[162:163], v[98:99]\n"
"v_rcp_f32 v104, v104\n"
"v_rcp_f32 v105, v105\n"
"v_pk_mul_f32 v[164:165], v[164:165], v[100:101]\n"
"v_rcp_f32 v106, v106\n"
"v_rcp_f32 v107, v107\n"
"v_pk_mul_f32 v[166:167], v[166:167], v[102:103]\n"
"v_rcp_f32 v108, v108\n"
"v_rcp_f32 v109, v109\n"
"v_pk_mul_f32 v[168:169], v[168:169], v[104:105]\n"
"v_rcp_f32 v110, v110\n"
"v_rcp_f32 v111, v111\n"
"v_pk_mul_f32 v[170:171], v[170:171], v[106:107]\n"
"v_rcp_f32 v112, v112\n"
"v_rcp_f32 v113, v113\n"
"v_pk_mul_f32 v[172:173], v[172:173], v[108:109]\n"
"v_rcp_f32 v114, v114\n"
"v_rcp_f32 v115, v115\n"
"v_pk_mul_f32 v[174:175], v[174:175], v[110:111]\n"
"v_rcp_f32 v116, v116\n"
"v_rcp_f32 v117, v117\n"
"v_pk_mul_f32 v[176:177], v[176:177], v[112:113]\n"
"v_rcp_f32 v118, v118\n"
"v_rcp_f32 v119, v119\n"
"v_pk_mul_f32 v[178:179], v[178:179], v[114:115]\n"
"v_rcp_f32 v120, v120\n"
"v_rcp_f32 v121, v121\n"
"v_pk_mul_f32 v[180:181], v[180:181], v[116:117]\n"
"v_rcp_f32 v122, v122\n"
"v_rcp_f32 v123, v123\n"
"v_pk_mul_f32 v[182:183], v[182:183], v[118:119]\n"
"v_rcp_f32 v124, v124\n"
"v_rcp_f32 v125, v125\n"
"v_pk_mul_f32 v[184:185], v[184:185], v[120:121]\n"
"v_rcp_f32 v126, v126\n"
"v_rcp_f32 v127, v127\n"
"v_pk_mul_f32 v[186:187], v[186:187], v[122:123]\n"
"v_pk_mul_f32 v[188:189], v[188:189], v[124:125]\n"
"v_pk_mul_f32 v[190:191], v[190:191], v[126:127]\n"
"G2_NOSILU:\n"
"v_cvt_pk_bf16_f32 v64, v128, v129\n"
"v_cvt_pk_bf16_f32 v65, v130, v131\n"
"v_cvt_pk_bf16_f32 v66, v132, v133\n"
"v_cvt_pk_bf16_f32 v67, v134, v135\n"
"v_cvt_pk_bf16_f32 v68, v136, v137\n"
"v_cvt_pk_bf16_f32 v69, v138, v139\n"
"v_cvt_pk_bf16_f32 v70, v140, v141\n"
"v_cvt_pk_bf16_f32 v71, v142, v143\n"
"v_cvt_pk_bf16_f32 v72, v144, v145\n"
"v_cvt_pk_bf16_f32 v73, v146, v147\n"
"v_cvt_pk_bf16_f32 v74, v148, v149\n"
"v_cvt_pk_bf16_f32 v75, v150, v151\n"
"v_cvt_pk_bf16_f32 v76, v152, v153\n"
"v_cvt_pk_bf16_f32 v77, v154, v155\n"
"v_cvt_pk_bf16_f32 v78, v156, v157\n"
"v_cvt_pk_bf16_f32 v79, v158, v159\n"
"v_cvt_pk_bf16_f32 v80, v160, v161\n"
"v_cvt_pk_bf16_f32 v81, v162, v163\n"
"v_cvt_pk_bf16_f32 v82, v164, v165\n"
"v_cvt_pk_bf16_f32 v83, v166, v167\n"
"v_cvt_pk_bf16_f32 v84, v168, v169\n"
"v_cvt_pk_bf16_f32 v85, v170, v171\n"
"v_cvt_pk_bf16_f32 v86, v172, v173\n"
"v_cvt_pk_bf16_f32 v87, v174, v175\n"
"v_cvt_pk_bf16_f32 v88, v176, v177\n"
"v_cvt_pk_bf16_f32 v89, v178, v179\n"
"v_cvt_pk_bf16_f32 v90, v180, v181\n"
"v_cvt_pk_bf16_f32 v91, v182, v183\n"
"v_cvt_pk_bf16_f32 v92, v184, v185\n"
"v_cvt_pk_bf16_f32 v93, v186, v187\n"
"v_cvt_pk_bf16_f32 v94, v188, v189\n"
"v_cvt_pk_bf16_f32 v95, v190, v191\n"
"s_cmp_lg_u32 s51, 0\n"
"s_cbranch_scc1 G2_COMB\n"
"s_mov_b32 s94, 32768\n"
"buffer_store_dwordx4 v[64:67], v23, s[92:95], 0 offen offset:0 sc1\n"
"buffer_store_dwordx4 v[68:71], v23, s[92:95], 0 offen offset:1024 sc1\n"
"buffer_store_dwordx4 v[72:75], v23, s[92:95], 0 offen offset:2048 sc1\n"
"buffer_store_dwordx4 v[76:79], v23, s[92:95], 0 offen offset:3072 sc1\n"
"buffer_store_dwordx4 v[80:83], v23, s[92:95], s69 offen offset:0 sc1\n"
"buffer_store_dwordx4 v[84:87], v23, s[92:95], s69 offen offset:1024 sc1\n"
"buffer_store_dwordx4 v[88:91], v23, s[92:95], s69 offen offset:2048 sc1\n"
"buffer_store_dwordx4 v[92:95], v23, s[92:95], s69 offen offset:3072 sc1\n"
"v_mov_b32 v8, 0\n"
"v_mov_b32 v9, 1\n"
"s_waitcnt vmcnt(0)\n"
"s_mov_b64 exec, 1\n"
"global_atomic_add v8, v9, s[46:47] offset:256 sc1\n"
"s_mov_b64 exec, -1\n"
"s_barrier\n"
"s_branch G2_END\n"
"G2_SPIN:\n"
"v_mov_b32 v31, 0\n"
"s_nop 1\n"
"global_load_dword v31, v31, s[46:47] offset:256 sc1\n"
"s_waitcnt vmcnt(0)\n"
"v_readfirstlane_b32 s65, v31\n"
"s_add_u32 s61, s61, 1\n"
"s_cmp_lg_u32 s65, 0\n"
"s_cbranch_scc1 G2_SPUN\n"
"s_cmp_gt_u32 s61, 0x200000\n"
"s_cbranch_scc1 G2_SPUN\n"
"s_sleep 2\n"
"s_branch G2_SPIN\n"
"G2_LATE:\n"
"s_mov_b32 s94, 32768\n"
"buffer_load_dwordx4 v[224:227], v23, s[92:95], 0 offen offset:0 sc1\n"
"buffer_load_dwordx4 v[228:231], v23, s[92:95], 0 offen offset:1024 sc1\n"
"buffer_load_dwordx4 v[232:235], v23, s[92:95], 0 offen offset:2048 sc1\n"
"buffer_load_dwordx4 v[236:239], v23, s[92:95], 0 offen offset:3072 sc1\n"
"buffer_load_dwordx4 v[240:243], v23, s[92:95], s69 offen offset:0 sc1\n"
"buffer_load_dwordx4 v[244:247], v23, s[92:95], s69 offen offset:1024 sc1\n"
"buffer_load_dwordx4 v[248:251], v23, s[92:95], s69 offen offset:2048 sc1\n"
"buffer_load_dwordx4 v[252:255], v23, s[92:95], s69 offen offset:3072 sc1\n"
"s_waitcnt vmcnt(0)\n"
"s_branch G2_GOT\n"
"G2_COMB:\n"
"s_mov_b32 s61, 0\n"
"s_cmp_eq_u32 s54, 0\n"
"s_cbranch_scc1 G2_SPIN\n"
"G2_SPUN:\n"
"s_barrier\n"
"s_cmp_eq_u32 s54, 0\n"
"s_cbranch_scc1 G2_LATE\n"
"G2_GOT:\n"
"s_mov_b32 s65, 0xffff0000\n"
"s_waitcnt vmcnt(7)\n"
"v_lshlrev_b32 v128, 16, v224\n"
"v_and_b32 v129, s65, v224\n"
"v_lshlrev_b32 v130, 16, v64\n"
"v_and_b32 v131, s65, v64\n"
"v_lshlrev_b32 v132, 16, v225\n"
"v_and_b32 v133, s65, v225\n"
"v_lshlrev_b32 v134, 16, v65\n"
"v_and_b32 v135, s65, v65\n"
"v_lshlrev_b32 v136, 16, v226\n"
"v_and_b32 v137, s65, v226\n"
"v_lshlrev_b32 v138, 16, v66\n"
"v_and_b32 v139, s65, v66\n"
"v_lshlrev_b32 v140, 16, v227\n"
"v_and_b32 v141, s65, v227\n"
"v_lshlrev_b32 v142, 16, v67\n"
"v_and_b32 v143, s65, v67\n"
"v_pk_mul_f32 v[128:129], v[128:129], v[130:131]\n"
"v_pk_mul_f32 v[132:133], v[132:133], v[134:135]\n"
"v_pk_mul_f32 v[136:137], v[136:137], v[138:139]\n"
"v_pk_mul_f32 v[140:141], v[140:141], v[142:143]\n"
"v_cvt_pk_bf16_f32 v96, v128, v129\n"
"v_cvt_pk_bf16_f32 v97, v132, v133\n"
"v_cvt_pk_bf16_f32 v98, v136, v137\n"
"v_cvt_pk_bf16_f32 v99, v140, v141\n"
"v_lshlrev_b32 v144, 16, v192\n"
"v_and_b32 v145, s65, v192\n"
"v_lshlrev_b32 v146, 16, v193\n"
"v_and_b32 v147, s65, v193\n"
"v_lshlrev_b32 v148, 16, v194\n"
"v_and_b32 v149, s65, v194\n"
"v_lshlrev_b32 v150, 16, v195\n"
"v_and_b32 v151, s65, v195\n"
"v_permlane32_swap_b32 v96, v98\n"
"v_permlane32_swap_b32 v97, v99\n"
"s_nop 0\n"
"v_permlane16_swap_b32 v96, v98\n"
"v_permlane16_swap_b32 v97, v99\n"
"v_lshlrev_b32 v152, 16, v96\n"
"v_and_b32 v153, s65, v96\n"
"v_lshlrev_b32 v154, 16, v97\n"
"v_and_b32 v155, s65, v97\n"
"v_lshlrev_b32 v156, 16, v98\n"
"v_and_b32 v157, s65, v98\n"
"v_lshlrev_b32 v158, 16, v99\n"
"v_and_b32 v159, s65, v99\n"
"v_pk_add_f32 v[152:153], v[152:153], v[144:145]\n"
"v_pk_add_f32 v[154:155], v[154:155], v[146:147]\n"
"v_pk_add_f32 v[156:157], v[156:157], v[148:149]\n"
"v_pk_add_f32 v[158:159], v[158:159], v[150:151]\n"
"v_cvt_pk_bf16_f32 v96, v152, v153\n"
"v_cvt_pk_bf16_f32 v97, v154, v155\n"
"v_cvt_pk_bf16_f32 v98, v156, v157\n"
"v_cvt_pk_bf16_f32 v99, v158, v159\n"
"global_store_dwordx4 v30, v[96:99], s[44:45] offset:0 sc0 sc1\n"
"s_waitcnt vmcnt(7)\n"
"v_lshlrev_b32 v128, 16, v228\n"
"v_and_b32 v129, s65, v228\n"
"v_lshlrev_b32 v130, 16, v68\n"
"v_and_b32 v131, s65, v68\n"
"v_lshlrev_b32 v132, 16, v229\n"
"v_and_b32 v133, s65, v229\n"
"v_lshlrev_b32 v134, 16, v69\n"
"v_and_b32 v135, s65, v69\n"
"v_lshlrev_b32 v136, 16, v230\n"
"v_and_b32 v137, s65, v230\n"
"v_lshlrev_b32 v138, 16, v70\n"
"v_and_b32 v139, s65, v70\n"
"v_lshlrev_b32 v140, 16, v231\n"
"v_and_b32 v141, s65, v231\n"
"v_lshlrev_b32 v142, 16, v71\n"
"v_and_b32 v143, s65, v71\n"
"v_pk_mul_f32 v[128:129], v[128:129], v[130:131]\n"
"v_pk_mul_f32 v[132:133], v[132:133], v[134:135]\n"
"v_pk_mul_f32 v[136:137], v[136:137], v[138:139]\n"
"v_pk_mul_f32 v[140:141], v[140:141], v[142:143]\n"
"v_cvt_pk_bf16_f32 v100, v128, v129\n"
"v_cvt_pk_bf16_f32 v101, v132, v133\n"
"v_cvt_pk_bf16_f32 v102, v136, v137\n"
"v_cvt_pk_bf16_f32 v103, v140, v141\n"
"v_lshlrev_b32 v144, 16, v196\n"
"v_and_b32 v145, s65, v196\n"
"v_lshlrev_b32 v146, 16, v197\n"
"v_and_b32 v147, s65, v197\n"
"v_lshlrev_b32 v148, 16, v198\n"
"v_and_b32 v149, s65, v198\n"
"v_lshlrev_b32 v150, 16, v199\n"
"v_and_b32 v151, s65, v199\n"
"v_permlane32_swap_b32 v100, v102\n"
"v_permlane32_swap_b32 v101, v103\n"
"s_nop 0\n"
"v_permlane16_swap_b32 v100, v102\n"
"v_permlane16_swap_b32 v101, v103\n"
"v_lshlrev_b32 v152, 16, v100\n"
"v_and_b32 v153, s65, v100\n"
"v_lshlrev_b32 v154, 16, v101\n"
"v_and_b32 v155, s65, v101\n"
"v_lshlrev_b32 v156, 16, v102\n"
"v_and_b32 v157, s65, v102\n"
"v_lshlrev_b32 v158, 16, v103\n"
"v_and_b32 v159, s65, v103\n"
"v_pk_add_f32 v[152:153], v[152:153], v[144:145]\n"
"v_pk_add_f32 v[154:155], v[154:155], v[146:147]\n"
"v_pk_add_f32 v[156:157], v[156:157], v[148:149]\n"
"v_pk_add_f32 v[158:159], v[158:159], v[150:151]\n"
"v_cvt_pk_bf16_f32 v100, v152, v153\n"
"v_cvt_pk_bf16_f32 v101, v154, v155\n"
"v_cvt_pk_bf16_f32 v102, v156, v157\n"
"v_cvt_pk_bf16_f32 v103, v158, v159\n"
"global_store_dwordx4 v30, v[100:103], s[44:45] offset:64 sc0 sc1\n"
"s_waitcnt vmcnt(7)\n"
"v_lshlrev_b32 v128, 16, v232\n"
"v_and_b32 v129, s65, v232\n"
"v_lshlrev_b32 v130, 16, v72\n"
"v_and_b32 v131, s65, v72\n"
"v_lshlrev_b32 v132, 16, v233\n"
"v_and_b32 v133, s65, v233\n"
"v_lshlrev_b32 v134, 16, v73\n"
"v_and_b32 v135, s65, v73\n"
"v_lshlrev_b32 v136, 16, v234\n"
"v_and_b32 v137, s65, v234\n"
"v_lshlrev_b32 v138, 16, v74\n"
"v_and_b32 v139, s65, v74\n"
"v_lshlrev_b32 v140, 16, v235\n"
"v_and_b32 v141, s65, v235\n"
"v_lshlrev_b32 v142, 16, v75\n"
"v_and_b32 v143, s65, v75\n"
"v_pk_mul_f32 v[128:129], v[128:129], v[130:131]\n"
"v_pk_mul_f32 v[132:133], v[132:133], v[134:135]\n"
"v_pk_mul_f32 v[136:137], v[136:137], v[138:139]\n"
"v_pk_mul_f32 v[140:141], v[140:141], v[142:143]\n"
"v_cvt_pk_bf16_f32 v104, v128, v129\n"
"v_cvt_pk_bf16_f32 v105, v132, v133\n"
"v_cvt_pk_bf16_f32 v106, v136, v137\n"
"v_cvt_pk_bf16_f32 v107, v140, v141\n"
"v_lshlrev_b32 v144, 16, v200\n"
"v_and_b32 v145, s65, v200\n"
"v_lshlrev_b32 v146, 16, v201\n"
"v_and_b32 v147, s65, v201\n"
"v_lshlrev_b32 v148, 16, v202\n"
"v_and_b32 v149, s65, v202\n"
"v_lshlrev_b32 v150, 16, v203\n"
"v_and_b32 v151, s65, v203\n"
"v_permlane32_swap_b32 v104, v106\n"
"v_permlane32_swap_b32 v105, v107\n"
"s_nop 0\n"
"v_permlane16_swap_b32 v104, v106\n"
"v_permlane16_swap_b32 v105, v107\n"
"v_lshlrev_b32 v152, 16, v104\n"
"v_and_b32 v153, s65, v104\n"
"v_lshlrev_b32 v154, 16, v105\n"
"v_and_b32 v155, s65, v105\n"
"v_lshlrev_b32 v156, 16, v106\n"
"v_and_b32 v157, s65, v106\n"
"v_lshlrev_b32 v158, 16, v107\n"
"v_and_b32 v159, s65, v107\n"
"v_pk_add_f32 v[152:153], v[152:153], v[144:145]\n"
"v_pk_add_f32 v[154:155], v[154:155], v[146:147]\n"
"v_pk_add_f32 v[156:157], v[156:157], v[148:149]\n"
"v_pk_add_f32 v[158:159], v[158:159], v[150:151]\n"
"v_cvt_pk_bf16_f32 v104, v152, v153\n"
"v_cvt_pk_bf16_f32 v105, v154, v155\n"
"v_cvt_pk_bf16_f32 v106, v156, v157\n"
"v_cvt_pk_bf16_f32 v107, v158, v159\n"
"global_store_dwordx4 v30, v[104:107], s[44:45] offset:128 sc0 sc1\n"
"s_waitcnt vmcnt(7)\n"
"v_lshlrev_b32 v128, 16, v236\n"
"v_and_b32 v129, s65, v236\n"
"v_lshlrev_b32 v130, 16, v76\n"
"v_and_b32 v131, s65, v76\n"
"v_lshlrev_b32 v132, 16, v237\n"
"v_and_b32 v133, s65, v237\n"
"v_lshlrev_b32 v134, 16, v77\n"
"v_and_b32 v135, s65, v77\n"
"v_lshlrev_b32 v136, 16, v238\n"
"v_and_b32 v137, s65, v238\n"
"v_lshlrev_b32 v138, 16, v78\n"
"v_and_b32 v139, s65, v78\n"
"v_lshlrev_b32 v140, 16, v239\n"
"v_and_b32 v141, s65, v239\n"
"v_lshlrev_b32 v142, 16, v79\n"
"v_and_b32 v143, s65, v79\n"
"v_pk_mul_f32 v[128:129], v[128:129], v[130:131]\n"
"v_pk_mul_f32 v[132:133], v[132:133], v[134:135]\n"
"v_pk_mul_f32 v[136:137], v[136:137], v[138:139]\n"
"v_pk_mul_f32 v[140:141], v[140:141], v[142:143]\n"
"v_cvt_pk_bf16_f32 v108, v128, v129\n"
"v_cvt_pk_bf16_f32 v109, v132, v133\n"
"v_cvt_pk_bf16_f32 v110, v136, v137\n"
"v_cvt_pk_bf16_f32 v111, v140, v141\n"
"v_lshlrev_b32 v144, 16, v204\n"
"v_and_b32 v145, s65, v204\n"
"v_lshlrev_b32 v146, 16, v205\n"
"v_and_b32 v147, s65, v205\n"
"v_lshlrev_b32 v148, 16, v206\n"
"v_and_b32 v149, s65, v206\n"
"v_lshlrev_b32 v150, 16, v207\n"
"v_and_b32 v151, s65, v207\n"
"v_permlane32_swap_b32 v108, v110\n"
"v_permlane32_swap_b32 v109, v111\n"
"s_nop 0\n"
"v_permlane16_swap_b32 v108, v110\n"
"v_permlane16_swap_b32 v109, v111\n"
"v_lshlrev_b32 v152, 16, v108\n"
"v_and_b32 v153, s65, v108\n"
"v_lshlrev_b32 v154, 16, v109\n"
"v_and_b32 v155, s65, v109\n"
"v_lshlrev_b32 v156, 16, v110\n"
"v_and_b32 v157, s65, v110\n"
"v_lshlrev_b32 v158, 16, v111\n"
"v_and_b32 v159, s65, v111\n"
"v_pk_add_f32 v[152:153], v[152:153], v[144:145]\n"
"v_pk_add_f32 v[154:155], v[154:155], v[146:147]\n"
"v_pk_add_f32 v[156:157], v[156:157], v[148:149]\n"
"v_pk_add_f32 v[158:159], v[158:159], v[150:151]\n"
"v_cvt_pk_bf16_f32 v108, v152, v153\n"
"v_cvt_pk_bf16_f32 v109, v154, v155\n"
"v_cvt_pk_bf16_f32 v110, v156, v157\n"
"v_cvt_pk_bf16_f32 v111, v158, v159\n"
"global_store_dwordx4 v30, v[108:111], s[44:45] offset:192 sc0 sc1\n"
"s_waitcnt vmcnt(7)\n"
"v_lshlrev_b32 v128, 16, v240\n"
"v_and_b32 v129, s65, v240\n"
"v_lshlrev_b32 v130, 16, v80\n"
"v_and_b32 v131, s65, v80\n"
"v_lshlrev_b32 v132, 16, v241\n"
"v_and_b32 v133, s65, v241\n"
"v_lshlrev_b32 v134, 16, v81\n"
"v_and_b32 v135, s65, v81\n"
"v_lshlrev_b32 v136, 16, v242\n"
"v_and_b32 v137, s65, v242\n"
"v_lshlrev_b32 v138, 16, v82\n"
"v_and_b32 v139, s65, v82\n"
"v_lshlrev_b32 v140, 16, v243\n"
"v_and_b32 v141, s65, v243\n"
"v_lshlrev_b32 v142, 16, v83\n"
"v_and_b32 v143, s65, v83\n"
"v_pk_mul_f32 v[128:129], v[128:129], v[130:131]\n"
"v_pk_mul_f32 v[132:133], v[132:133], v[134:135]\n"
"v_pk_mul_f32 v[136:137], v[136:137], v[138:139]\n"
"v_pk_mul_f32 v[140:141], v[140:141], v[142:143]\n"
"v_cvt_pk_bf16_f32 v112, v128, v129\n"
"v_cvt_pk_bf16_f32 v113, v132, v133\n"
"v_cvt_pk_bf16_f32 v114, v136, v137\n"
"v_cvt_pk_bf16_f32 v115, v140, v141\n"
"v_lshlrev_b32 v144, 16, v208\n"
"v_and_b32 v145, s65, v208\n"
"v_lshlrev_b32 v146, 16, v209\n"
"v_and_b32 v147, s65, v209\n"
"v_lshlrev_b32 v148, 16, v210\n"
"v_and_b32 v149, s65, v210\n"
"v_lshlrev_b32 v150, 16, v211\n"
"v_and_b32 v151, s65, v211\n"
"v_permlane32_swap_b32 v112, v114\n"
"v_permlane32_swap_b32 v113, v115\n"
"s_nop 0\n"
"v_permlane16_swap_b32 v112, v114\n"
"v_permlane16_swap_b32 v113, v115\n"
"v_lshlrev_b32 v152, 16, v112\n"
"v_and_b32 v153, s65, v112\n"
"v_lshlrev_b32 v154, 16, v113\n"
"v_and_b32 v155, s65, v113\n"
"v_lshlrev_b32 v156, 16, v114\n"
"v_and_b32 v157, s65, v114\n"
"v_lshlrev_b32 v158, 16, v115\n"
"v_and_b32 v159, s65, v115\n"
"v_pk_add_f32 v[152:153], v[152:153], v[144:145]\n"
"v_pk_add_f32 v[154:155], v[154:155], v[146:147]\n"
"v_pk_add_f32 v[156:157], v[156:157], v[148:149]\n"
"v_pk_add_f32 v[158:159], v[158:159], v[150:151]\n"
"v_cvt_pk_bf16_f32 v112, v152, v153\n"
"v_cvt_pk_bf16_f32 v113, v154, v155\n"
"v_cvt_pk_bf16_f32 v114, v156, v157\n"
"v_cvt_pk_bf16_f32 v115, v158, v159\n"
"global_store_dwordx4 v29, v[112:115], s[44:45] offset:0 sc0 sc1\n"
"s_waitcnt vmcnt(7)\n"
"v_lshlrev_b32 v128, 16, v244\n"
"v_and_b32 v129, s65, v244\n"
"v_lshlrev_b32 v130, 16, v84\n"
"v_and_b32 v131, s65, v84\n"
"v_lshlrev_b32 v132, 16, v245\n"
"v_and_b32 v133, s65, v245\n"
"v_lshlrev_b32 v134, 16, v85\n"
"v_and_b32 v135, s65, v85\n"
"v_lshlrev_b32 v136, 16, v246\n"
"v_and_b32 v137, s65, v246\n"
"v_lshlrev_b32 v138, 16, v86\n"
"v_and_b32 v139, s65, v86\n"
"v_lshlrev_b32 v140, 16, v247\n"
"v_and_b32 v141, s65, v247\n"
"v_lshlrev_b32 v142, 16, v87\n"
"v_and_b32 v143, s65, v87\n"
"v_pk_mul_f32 v[128:129], v[128:129], v[130:131]\n"
"v_pk_mul_f32 v[132:133], v[132:133], v[134:135]\n"
"v_pk_mul_f32 v[136:137], v[136:137], v[138:139]\n"
"v_pk_mul_f32 v[140:141], v[140:141], v[142:143]\n"
"v_cvt_pk_bf16_f32 v116, v128, v129\n"
"v_cvt_pk_bf16_f32 v117, v132, v133\n"
"v_cvt_pk_bf16_f32 v118, v136, v137\n"
"v_cvt_pk_bf16_f32 v119, v140, v141\n"
"v_lshlrev_b32 v144, 16, v212\n"
"v_and_b32 v145, s65, v212\n"
"v_lshlrev_b32 v146, 16, v213\n"
"v_and_b32 v147, s65, v213\n"
"v_lshlrev_b32 v148, 16, v214\n"
"v_and_b32 v149, s65, v214\n"
"v_lshlrev_b32 v150, 16, v215\n"
"v_and_b32 v151, s65, v215\n"
"v_permlane32_swap_b32 v116, v118\n"
"v_permlane32_swap_b32 v117, v119\n"
"s_nop 0\n"
"v_permlane16_swap_b32 v116, v118\n"
"v_permlane16_swap_b32 v117, v119\n"
"v_lshlrev_b32 v152, 16, v116\n"
"v_and_b32 v153, s65, v116\n"
"v_lshlrev_b32 v154, 16, v117\n"
"v_and_b32 v155, s65, v117\n"
"v_lshlrev_b32 v156, 16, v118\n"
"v_and_b32 v157, s65, v118\n"
"v_lshlrev_b32 v158, 16, v119\n"
"v_and_b32 v159, s65, v119\n"
"v_pk_add_f32 v[152:153], v[152:153], v[144:145]\n"
"v_pk_add_f32 v[154:155], v[154:155], v[146:147]\n"
"v_pk_add_f32 v[156:157], v[156:157], v[148:149]\n"
"v_pk_add_f32 v[158:159], v[158:159], v[150:151]\n"
"v_cvt_pk_bf16_f32 v116, v152, v153\n"
"v_cvt_pk_bf16_f32 v117, v154, v155\n"
"v_cvt_pk_bf16_f32 v118, v156, v157\n"
"v_cvt_pk_bf16_f32 v119, v158, v159\n"
"global_store_dwordx4 v29, v[116:119], s[44:45] offset:64 sc0 sc1\n"
"s_waitcnt vmcnt(7)\n"
"v_lshlrev_b32 v128, 16, v248\n"
"v_and_b32 v129, s65, v248\n"
"v_lshlrev_b32 v130, 16, v88\n"
"v_and_b32 v131, s65, v88\n"
"v_lshlrev_b32 v132, 16, v249\n"
"v_and_b32 v133, s65, v249\n"
"v_lshlrev_b32 v134, 16, v89\n"
"v_and_b32 v135, s65, v89\n"
"v_lshlrev_b32 v136, 16, v250\n"
"v_and_b32 v137, s65, v250\n"
"v_lshlrev_b32 v138, 16, v90\n"
"v_and_b32 v139, s65, v90\n"
"v_lshlrev_b32 v140, 16, v251\n"
"v_and_b32 v141, s65, v251\n"
"v_lshlrev_b32 v142, 16, v91\n"
"v_and_b32 v143, s65, v91\n"
"v_pk_mul_f32 v[128:129], v[128:129], v[130:131]\n"
"v_pk_mul_f32 v[132:133], v[132:133], v[134:135]\n"
"v_pk_mul_f32 v[136:137], v[136:137], v[138:139]\n"
"v_pk_mul_f32 v[140:141], v[140:141], v[142:143]\n"
"v_cvt_pk_bf16_f32 v120, v128, v129\n"
"v_cvt_pk_bf16_f32 v121, v132, v133\n"
"v_cvt_pk_bf16_f32 v122, v136, v137\n"
"v_cvt_pk_bf16_f32 v123, v140, v141\n"
"v_lshlrev_b32 v144, 16, v216\n"
"v_and_b32 v145, s65, v216\n"
"v_lshlrev_b32 v146, 16, v217\n"
"v_and_b32 v147, s65, v217\n"
"v_lshlrev_b32 v148, 16, v218\n"
"v_and_b32 v149, s65, v218\n"
"v_lshlrev_b32 v150, 16, v219\n"
"v_and_b32 v151, s65, v219\n"
"v_permlane32_swap_b32 v120, v122\n"
"v_permlane32_swap_b32 v121, v123\n"
"s_nop 0\n"
"v_permlane16_swap_b32 v120, v122\n"
"v_permlane16_swap_b32 v121, v123\n"
"v_lshlrev_b32 v152, 16, v120\n"
"v_and_b32 v153, s65, v120\n"
"v_lshlrev_b32 v154, 16, v121\n"
"v_and_b32 v155, s65, v121\n"
"v_lshlrev_b32 v156, 16, v122\n"
"v_and_b32 v157, s65, v122\n"
"v_lshlrev_b32 v158, 16, v123\n"
"v_and_b32 v159, s65, v123\n"
"v_pk_add_f32 v[152:153], v[152:153], v[144:145]\n"
"v_pk_add_f32 v[154:155], v[154:155], v[146:147]\n"
"v_pk_add_f32 v[156:157], v[156:157], v[148:149]\n"
"v_pk_add_f32 v[158:159], v[158:159], v[150:151]\n"
"v_cvt_pk_bf16_f32 v120, v152, v153\n"
"v_cvt_pk_bf16_f32 v121, v154, v155\n"
"v_cvt_pk_bf16_f32 v122, v156, v157\n"
"v_cvt_pk_bf16_f32 v123, v158, v159\n"
"global_store_dwordx4 v29, v[120:123], s[44:45] offset:128 sc0 sc1\n"
"s_waitcnt vmcnt(7)\n"
"v_lshlrev_b32 v128, 16, v252\n"
"v_and_b32 v129, s65, v252\n"
"v_lshlrev_b32 v130, 16, v92\n"
"v_and_b32 v131, s65, v92\n"
"v_lshlrev_b32 v132, 16, v253\n"
"v_and_b32 v133, s65, v253\n"
"v_lshlrev_b32 v134, 16, v93\n"
"v_and_b32 v135, s65, v93\n"
"v_lshlrev_b32 v136, 16, v254\n"
"v_and_b32 v137, s65, v254\n"
"v_lshlrev_b32 v138, 16, v94\n"
"v_and_b32 v139, s65, v94\n"
"v_lshlrev_b32 v140, 16, v255\n"
"v_and_b32 v141, s65, v255\n"
"v_lshlrev_b32 v142, 16, v95\n"
"v_and_b32 v143, s65, v95\n"
"v_pk_mul_f32 v[128:129], v[128:129], v[130:131]\n"
"v_pk_mul_f32 v[132:133], v[132:133], v[134:135]\n"
"v_pk_mul_f32 v[136:137], v[136:137], v[138:139]\n"
"v_pk_mul_f32 v[140:141], v[140:141], v[142:143]\n"
"v_cvt_pk_bf16_f32 v124, v128, v129\n"
"v_cvt_pk_bf16_f32 v125, v132, v133\n"
"v_cvt_pk_bf16_f32 v126, v136, v137\n"
"v_cvt_pk_bf16_f32 v127, v140, v141\n"
"v_lshlrev_b32 v144, 16, v220\n"
"v_and_b32 v145, s65, v220\n"
"v_lshlrev_b32 v146, 16, v221\n"
"v_and_b32 v147, s65, v221\n"
"v_lshlrev_b32 v148, 16, v222\n"
"v_and_b32 v149, s65, v222\n"
"v_lshlrev_b32 v150, 16, v223\n"
"v_and_b32 v151, s65, v223\n"
"v_permlane32_swap_b32 v124, v126\n"
"v_permlane32_swap_b32 v125, v127\n"
"s_nop 0\n"
"v_permlane16_swap_b32 v124, v126\n"
"v_permlane16_swap_b32 v125, v127\n"
"v_lshlrev_b32 v152, 16, v124\n"
"v_and_b32 v153, s65, v124\n"
"v_lshlrev_b32 v154, 16, v125\n"
"v_and_b32 v155, s65, v125\n"
"v_lshlrev_b32 v156, 16, v126\n"
"v_and_b32 v157, s65, v126\n"
"v_lshlrev_b32 v158, 16, v127\n"
"v_and_b32 v159, s65, v127\n"
"v_pk_add_f32 v[152:153], v[152:153], v[144:145]\n"
"v_pk_add_f32 v[154:155], v[154:155], v[146:147]\n"
"v_pk_add_f32 v[156:157], v[156:157], v[148:149]\n"
"v_pk_add_f32 v[158:159], v[158:159], v[150:151]\n"
"v_cvt_pk_bf16_f32 v124, v152, v153\n"
"v_cvt_pk_bf16_f32 v125, v154, v155\n"
"v_cvt_pk_bf16_f32 v126, v156, v157\n"
"v_cvt_pk_bf16_f32 v127, v158, v159\n"
"global_store_dwordx4 v29, v[124:127], s[44:45] offset:192 sc0 sc1\n"
"G2_END:\n"
"s_branch MID_END\n"
"AT_SWAP:\n"
"s_mov_b64 s[44:45], %4\n"
"s_mov_b32 s46, %15\n"
"s_mov_b32 s47, 0x20000\n"
"s_mov_b64 s[48:49], %5\n"
"s_mov_b32 s50, %15\n"
"s_mov_b32 s51, 0x20000\n"
"s_mov_b32 s62, 0\n"
"s_mov_b32 s54, 458752\n"
"s_mul_i32 s61, s55, 114688\n"
"s_and_b32 s58, %17, 1\n"
"s_xor_b32 s58, s58, 0\n"
"s_lshl_b32 s98, s58, 2\n"
"s_sub_u32 s98, 16, s98\n"
"s_mov_b32 s99, 7168\n"
"v_mul_lo_u32 v6, s99, v3\n"
"v_mov_b32 v7, 0x7fff0000\n"
"v_add_u32 v6, s61, v6\n"
"v_add_u32 v5, 0, v3\n"
"v_lshlrev_b32 v5, 1, v5\n"
"v_xor_b32 v5, v5, v4\n"
"v_cmp_gt_u32 vcc, s98, v5\n"
"v_lshl_add_u32 v5, v5, 4, v6\n"
"v_add_u32 v20, 0, v5\n"
"v_cndmask_b32 v20, v7, v20, vcc\n"
"v_add_u32 v5, 4, v3\n"
"v_lshlrev_b32 v5, 1, v5\n"
"v_xor_b32 v5, v5, v4\n"
"v_cmp_gt_u32 vcc, s98, v5\n"
"v_lshl_add_u32 v5, v5, 4, v6\n"
"v_add_u32 v21, 27648, v5\n"
"v_cndmask_b32 v21, v7, v21, vcc\n"
"v_add_u32 v5, 0, v3\n"
"v_lshlrev_b32 v5, 1, v5\n"
"v_xor_b32 v5, v5, v4\n"
"v_cmp_gt_u32 vcc, s98, v5\n"
"v_lshl_add_u32 v5, v5, 4, v6\n"
"v_add_u32 v22, 55296, v5\n"
"v_cndmask_b32 v22, v7, v22, vcc\n"
"v_add_u32 v5, 4, v3\n"
"v_lshlrev_b32 v5, 1, v5\n"
"v_xor_b32 v5, v5, v4\n"
"v_cmp_gt_u32 vcc, s98, v5\n"
"v_lshl_add_u32 v5, v5, 4, v6\n"
"v_add_u32 v23, 82944, v5\n"
"v_cndmask_b32 v23, v7, v23, vcc\n"
"v_mov_b32 v24, v20\n"
"v_mov_b32 v25, v21\n"
"v_mov_b32 v26, v22\n"
"v_mov_b32 v27, v23\n"
"s_lshr_b32 s98, %17, 3\n"
"s_lshl_b32 s98, s98, 12\n"
"s_lshl_b32 s99, s55, 10\n"
"s_add_u32 s98, s98, s99\n"
"v_add_u32 v5, s98, v2\n"
"v_min_u32 v5, MID_END-MID_BEGIN-16, v5\n"
"v_and_b32 v5, -16, v5\n"
"s_getpc_b64 s[66:67]\n"
"CW_PC0:\n"
"s_sub_u32 s66, s66, CW_PC0-MID_BEGIN\n"
"s_subb_u32 s67, s67, 0\n"
"s_and_b32 s66, s66, -16\n"
"s_add_u32 m0, s59, 131072\n"
"s_nop 0\n"
"global_load_lds_dwordx4 v5, s[66:67]\n"
"s_branch AT_LOOP\n"
"AL_SPIN:\n"
"s_sleep 8\n"
"global_load_dwordx4 v[28:31], v37, %0 offset:0 sc1\n"
"global_load_dwordx4 v[32:35], v37, %0 offset:1024 sc1\n"
"s_waitcnt vmcnt(0)\n"
"s_add_u32 s63, s63, 1\n"
"s_cmp_gt_u32 s63, 0x200000\n"
"s_cbranch_scc1 AL_OK\n"
"s_branch AL_CHK\n"
"AL_EPOCH:\n"
"v_mov_b32 v94, 42500096\n"
"s_mov_b64 exec, 1\n"
"global_store_dwordx2 v94, v[92:93], %0 sc1\n"
"s_mov_b64 exec, -1\n"
"s_nop 1\n"
"s_branch AL_OK\n"
"AT_R1:\n"
"s_mov_b32 s63, 1\n"
"s_branch AT_RARE0\n"
"AT_R5:\n"
"s_mov_b32 s63, 5\n"
"s_branch AT_RARE0\n"
"AT_R9:\n"
"s_mov_b32 s63, 9\n"
"s_branch AT_RARE0\n"
"AT_R13:\n"
"s_mov_b32 s63, 13\n"
"s_branch AT_RARE0\n"
"AT_RARE0:\n"
"s_nop 7\n"
"v_max3_f32 v48, v96, v97, v98\n"
"v_max3_f32 v48, v48, v99, v100\n"
"v_max3_f32 v48, v48, v101, v102\n"
"v_max3_f32 v48, v48, v103, v104\n"
"v_max3_f32 v48, v48, v105, v106\n"
"v_max3_f32 v48, v48, v107, v108\n"
"v_max3_f32 v48, v48, v109, v110\n"
"v_max_f32 v48, v48, v111\n"
"v_mov_b32 v49, v48\n"
"s_nop 1\n"
"v_permlane32_swap_b32 v48, v49\n"
"v_max_f32 v48, v48, v49\n"
"v_mov_b32 v49, v48\n"
"s_nop 1\n"
"v_permlane16_swap_b32 v48, v49\n"
"v_max_f32 v48, v48, v49\n"
"v_max_f32 v48, 0, v48\n"
"v_exp_f32 v49, -v48\n"
"v_sub_f32 v64, v64, v48\n"
"v_sub_f32 v65, v65, v48\n"
"v_sub_f32 v66, v66, v48\n"
"v_sub_f32 v67, v67, v48\n"
"v_sub_f32 v50, v96, v48\n"
"v_sub_f32 v51, v97, v48\n"
"v_exp_f32 v50, v50\n"
"v_exp_f32 v51, v51\n"
"s_nop 0\n"
"v_cvt_pk_bf16_f32 v80, v50, v51\n"
"v_sub_f32 v50, v98, v48\n"
"v_sub_f32 v51, v99, v48\n"
"v_exp_f32 v50, v50\n"
"v_exp_f32 v51, v51\n"
"s_nop 0\n"
"v_cvt_pk_bf16_f32 v81, v50, v51\n"
"v_sub_f32 v50, v100, v48\n"
"v_sub_f32 v51, v101, v48\n"
"v_exp_f32 v50, v50\n"
"v_exp_f32 v51, v51\n"
"s_nop 0\n"
"v_cvt_pk_bf16_f32 v82, v50, v51\n"
"v_sub_f32 v50, v102, v48\n"
"v_sub_f32 v51, v103, v48\n"
"v_exp_f32 v50, v50\n"
"v_exp_f32 v51, v51\n"
"s_nop 0\n"
"v_cvt_pk_bf16_f32 v83, v50, v51\n"
"v_sub_f32 v50, v104, v48\n"
"v_sub_f32 v51, v105, v48\n"
"v_exp_f32 v50, v50\n"
"v_exp_f32 v51, v51\n"
"s_nop 0\n"
"v_cvt_pk_bf16_f32 v84, v50, v51\n"
"v_sub_f32 v50, v106, v48\n"
"v_sub_f32 v51, v107, v48\n"
"v_exp_f32 v50, v50\n"
"v_exp_f32 v51, v51\n"
"s_nop 0\n"
"v_cvt_pk_bf16_f32 v85, v50, v51\n"
"v_sub_f32 v50, v108, v48\n"
"v_sub_f32 v51, v109, v48\n"
"v_exp_f32 v50, v50\n"
"v_exp_f32 v51, v51\n"
"s_nop 0\n"
"v_cvt_pk_bf16_f32 v86, v50, v51\n"
"v_sub_f32 v50, v110, v48\n"
"v_sub_f32 v51, v111, v48\n"
"v_exp_f32 v50, v50\n"
"v_exp_f32 v51, v51\n"
"s_nop 0\n"
"v_cvt_pk_bf16_f32 v87, v50, v51\n"
"v_mfma_f32_16x16x32_bf16 v[40:43], v[56:59], v[80:83], 0\n"
"s_nop 0\n"
"v_mfma_f32_16x16x32_bf16 v[40:43], v[56:59], v[84:87], v[40:43]\n"
"v_mul_f32 v28, v28, v49\n"
"v_accvgpr_read_b32 v50, a0\n"
"v_mul_f32 v50, v50, v49\n"
"v_accvgpr_write_b32 a0, v50\n"
"v_accvgpr_read_b32 v51, a1\n"
"v_mul_f32 v51, v51, v49\n"
"v_accvgpr_write_b32 a1, v51\n"
"v_accvgpr_read_b32 v52, a2\n"
"v_mul_f32 v52, v52, v49\n"
"v_accvgpr_write_b32 a2, v52\n"
"v_accvgpr_read_b32 v53, a3\n"
"v_mul_f32 v53, v53, v49\n"
"v_accvgpr_write_b32 a3, v53\n"
"v_accvgpr_read_b32 v50, a4\n"
"v_mul_f32 v50, v50, v49\n"
"v_accvgpr_write_b32 a4, v50\n"
"v_accvgpr_read_b32 v51, a5\n"
"v_mul_f32 v51, v51, v49\n"
"v_accvgpr_write_b32 a5, v51\n"
"v_accvgpr_read_b32 v52, a6\n"
"v_mul_f32 v52, v52, v49\n"
"v_accvgpr_write_b32 a6, v52\n"
"v_accvgpr_read_b32 v53, a7\n"
"v_mul_f32 v53, v53, v49\n"
"v_accvgpr_write_b32 a7, v53\n"
"v_accvgpr_read_b32 v50, a8\n"
"v_mul_f32 v50, v50, v49\n"
"v_accvgpr_write_b32 a8, v50\n"
"v_accvgpr_read_b32 v51, a9\n"
"v_mul_f32 v51, v51, v49\n"
"v_accvgpr_write_b32 a9, v51\n"
"v_accvgpr_read_b32 v52, a10\n"
"v_mul_f32 v52, v52, v49\n"
"v_accvgpr_write_b32 a10, v52\n"
"v_accvgpr_read_b32 v53, a11\n"
"v_mul_f32 v53, v53, v49\n"
"v_accvgpr_write_b32 a11, v53\n"
"v_accvgpr_read_b32 v50, a12\n"
"v_mul_f32 v50, v50, v49\n"
"v_accvgpr_write_b32 a12, v50\n"
"v_accvgpr_read_b32 v51, a13\n"
"v_mul_f32 v51, v51, v49\n"
"v_accvgpr_write_b32 a13, v51\n"
"v_accvgpr_read_b32 v52, a14\n"
"v_mul_f32 v52, v52, v49\n"
"v_accvgpr_write_b32 a14, v52\n"
"v_accvgpr_read_b32 v53, a15\n"
"v_mul_f32 v53, v53, v49\n"
"v_accvgpr_write_b32 a15, v53\n"
"v_accvgpr_read_b32 v50, a16\n"
"v_mul_f32 v50, v50, v49\n"
"v_accvgpr_write_b32 a16, v50\n"
"v_accvgpr_read_b32 v51, a17\n"
"v_mul_f32 v51, v51, v49\n"
"v_accvgpr_write_b32 a17, v51\n"
"v_accvgpr_read_b32 v52, a18\n"
"v_mul_f32 v52, v52, v49\n"
"v_accvgpr_write_b32 a18, v52\n"
"v_accvgpr_read_b32 v53, a19\n"
"v_mul_f32 v53, v53, v49\n"
"v_accvgpr_write_b32 a19, v53\n"
"v_accvgpr_read_b32 v50, a20\n"
"v_mul_f32 v50, v50, v49\n"
"v_accvgpr_write_b32 a20, v50\n"
"v_accvgpr_read_b32 v51, a21\n"
"v_mul_f32 v51, v51, v49\n"
"v_accvgpr_write_b32 a21, v51\n"
"v_accvgpr_read_b32 v52, a22\n"
"v_mul_f32 v52, v52, v49\n"
"v_accvgpr_write_b32 a22, v52\n"
"v_accvgpr_read_b32 v53, a23\n"
"v_mul_f32 v53, v53, v49\n"
"v_accvgpr_write_b32 a23, v53\n"
"v_accvgpr_read_b32 v50, a24\n"
"v_mul_f32 v50, v50, v49\n"
"v_accvgpr_write_b32 a24, v50\n"
"v_accvgpr_read_b32 v51, a25\n"
"v_mul_f32 v51, v51, v49\n"
"v_accvgpr_write_b32 a25, v51\n"
"v_accvgpr_read_b32 v52, a26\n"
"v_mul_f32 v52, v52, v49\n"
"v_accvgpr_write_b32 a26, v52\n"
"v_accvgpr_read_b32 v53, a27\n"
"v_mul_f32 v53, v53, v49\n"
"v_accvgpr_write_b32 a27, v53\n"
"v_accvgpr_read_b32 v50, a28\n"
"v_mul_f32 v50, v50, v49\n"
"v_accvgpr_write_b32 a28, v50\n"
"v_accvgpr_read_b32 v51, a29\n"
"v_mul_f32 v51, v51, v49\n"
"v_accvgpr_write_b32 a29, v51\n"
"v_accvgpr_read_b32 v52, a30\n"
"v_mul_f32 v52, v52, v49\n"
"v_accvgpr_write_b32 a30, v52\n"
"v_accvgpr_read_b32 v53, a31\n"
"v_mul_f32 v53, v53, v49\n"
"v_accvgpr_write_b32 a31, v53\n"
"s_nop 7\n"
"s_cmp_eq_u32 s63, 1\n"
"s_cbranch_scc1 AT_B1\n"
"s_cmp_eq_u32 s63, 5\n"
"s_cbranch_scc1 AT_B5\n"
"s_cmp_eq_u32 s63, 9\n"
"s_cbranch_scc1 AT_B9\n"
"s_branch AT_B13\n"
"AT_R2:\n"
"s_mov_b32 s63, 2\n"
"s_branch AT_RARE1\n"
"AT_R6:\n"
"s_mov_b32 s63, 6\n"
"s_branch AT_RARE1\n"
"AT_R10:\n"
"s_mov_b32 s63, 10\n"
"s_branch AT_RARE1\n"
"AT_R14:\n"
"s_mov_b32 s63, 14\n"
"s_branch AT_RARE1\n"
"AT_RARE1:\n"
"s_nop 7\n"
"v_max3_f32 v48, v112, v113, v114\n"
"v_max3_f32 v48, v48, v115, v116\n"
"v_max3_f32 v48, v48, v117, v118\n"
"v_max3_f32 v48, v48, v119, v120\n"
"v_max3_f32 v48, v48, v121, v122\n"
"v_max3_f32 v48, v48, v123, v124\n"
"v_max3_f32 v48, v48, v125, v126\n"
"v_max_f32 v48, v48, v127\n"
"v_mov_b32 v49, v48\n"
"s_nop 1\n"
"v_permlane32_swap_b32 v48, v49\n"
"v_max_f32 v48, v48, v49\n"
"v_mov_b32 v49, v48\n"
"s_nop 1\n"
"v_permlane16_swap_b32 v48, v49\n"
"v_max_f32 v48, v48, v49\n"
"v_max_f32 v48, 0, v48\n"
"v_exp_f32 v49, -v48\n"
"v_sub_f32 v68, v68, v48\n"
"v_sub_f32 v69, v69, v48\n"
"v_sub_f32 v70, v70, v48\n"
"v_sub_f32 v71, v71, v48\n"
"v_sub_f32 v50, v112, v48\n"
"v_sub_f32 v51, v113, v48\n"
"v_exp_f32 v50, v50\n"
"v_exp_f32 v51, v51\n"
"s_nop 0\n"
"v_cvt_pk_bf16_f32 v88, v50, v51\n"
"v_sub_f32 v50, v114, v48\n"
"v_sub_f32 v51, v115, v48\n"
"v_exp_f32 v50, v50\n"
"v_exp_f32 v51, v51\n"
"s_nop 0\n"
"v_cvt_pk_bf16_f32 v89, v50, v51\n"
"v_sub_f32 v50, v116, v48\n"
"v_sub_f32 v51, v117, v48\n"
"v_exp_f32 v50, v50\n"
"v_exp_f32 v51, v51\n"
"s_nop 0\n"
"v_cvt_pk_bf16_f32 v90, v50, v51\n"
"v_sub_f32 v50, v118, v48\n"
"v_sub_f32 v51, v119, v48\n"
"v_exp_f32 v50, v50\n"
"v_exp_f32 v51, v51\n"
"s_nop 0\n"
"v_cvt_pk_bf16_f32 v91, v50, v51\n"
"v_sub_f32 v50, v120, v48\n"
"v_sub_f32 v51, v121, v48\n"
"v_exp_f32 v50, v50\n"
"v_exp_f32 v51, v51\n"
"s_nop 0\n"
"v_cvt_pk_bf16_f32 v92, v50, v51\n"
"v_sub_f32 v50, v122, v48\n"
"v_sub_f32 v51, v123, v48\n"
"v_exp_f32 v50, v50\n"
"v_exp_f32 v51, v51\n"
"s_nop 0\n"
"v_cvt_pk_bf16_f32 v93, v50, v51\n"
"v_sub_f32 v50, v124, v48\n"
"v_sub_f32 v51, v125, v48\n"
"v_exp_f32 v50, v50\n"
"v_exp_f32 v51, v51\n"
"s_nop 0\n"
"v_cvt_pk_bf16_f32 v94, v50, v51\n"
"v_sub_f32 v50, v126, v48\n"
"v_sub_f32 v51, v127, v48\n"
"v_exp_f32 v50, v50\n"
"v_exp_f32 v51, v51\n"
"s_nop 0\n"
"v_cvt_pk_bf16_f32 v95, v50, v51\n"
"v_mfma_f32_16x16x32_bf16 v[44:47], v[56:59], v[88:91], 0\n"
"s_nop 0\n"
"v_mfma_f32_16x16x32_bf16 v[44:47], v[56:59], v[92:95], v[44:47]\n"
"v_mul_f32 v29, v29, v49\n"
"v_accvgpr_read_b32 v50, a32\n"
"v_mul_f32 v50, v50, v49\n"
"v_accvgpr_write_b32 a32, v50\n"
"v_accvgpr_read_b32 v51, a33\n"
"v_mul_f32 v51, v51, v49\n"
"v_accvgpr_write_b32 a33, v51\n"
"v_accvgpr_read_b32 v52, a34\n"
"v_mul_f32 v52, v52, v49\n"
"v_accvgpr_write_b32 a34, v52\n"
"v_accvgpr_read_b32 v53, a35\n"
"v_mul_f32 v53, v53, v49\n"
"v_accvgpr_write_b32 a35, v53\n"
"v_accvgpr_read_b32 v50, a36\n"
"v_mul_f32 v50, v50, v49\n"
"v_accvgpr_write_b32 a36, v50\n"
"v_accvgpr_read_b32 v51, a37\n"
"v_mul_f32 v51, v51, v49\n"
"v_accvgpr_write_b32 a37, v51\n"
"v_accvgpr_read_b32 v52, a38\n"
"v_mul_f32 v52, v52, v49\n"
"v_accvgpr_write_b32 a38, v52\n"
"v_accvgpr_read_b32 v53, a39\n"
"v_mul_f32 v53, v53, v49\n"
"v_accvgpr_write_b32 a39, v53\n"
"v_accvgpr_read_b32 v50, a40\n"
"v_mul_f32 v50, v50, v49\n"
"v_accvgpr_write_b32 a40, v50\n"
"v_accvgpr_read_b32 v51, a41\n"
"v_mul_f32 v51, v51, v49\n"
"v_accvgpr_write_b32 a41, v51\n"
"v_accvgpr_read_b32 v52, a42\n"
"v_mul_f32 v52, v52, v49\n"
"v_accvgpr_write_b32 a42, v52\n"
"v_accvgpr_read_b32 v53, a43\n"
"v_mul_f32 v53, v53, v49\n"
"v_accvgpr_write_b32 a43, v53\n"
"v_accvgpr_read_b32 v50, a44\n"
"v_mul_f32 v50, v50, v49\n"
"v_accvgpr_write_b32 a44, v50\n"
"v_accvgpr_read_b32 v51, a45\n"
"v_mul_f32 v51, v51, v49\n"
"v_accvgpr_write_b32 a45, v51\n"
"v_accvgpr_read_b32 v52, a46\n"
"v_mul_f32 v52, v52, v49\n"
"v_accvgpr_write_b32 a46, v52\n"
"v_accvgpr_read_b32 v53, a47\n"
"v_mul_f32 v53, v53, v49\n"
"v_accvgpr_write_b32 a47, v53\n"
"v_accvgpr_read_b32 v50, a48\n"
"v_mul_f32 v50, v50, v49\n"
"v_accvgpr_write_b32 a48, v50\n"
"v_accvgpr_read_b32 v51, a49\n"
"v_mul_f32 v51, v51, v49\n"
"v_accvgpr_write_b32 a49, v51\n"
"v_accvgpr_read_b32 v52, a50\n"
"v_mul_f32 v52, v52, v49\n"
"v_accvgpr_write_b32 a50, v52\n"
"v_accvgpr_read_b32 v53, a51\n"
"v_mul_f32 v53, v53, v49\n"
"v_accvgpr_write_b32 a51, v53\n"
"v_accvgpr_read_b32 v50, a52\n"
"v_mul_f32 v50, v50, v49\n"
"v_accvgpr_write_b32 a52, v50\n"
"v_accvgpr_read_b32 v51, a53\n"
"v_mul_f32 v51, v51, v49\n"
"v_accvgpr_write_b32 a53, v51\n"
"v_accvgpr_read_b32 v52, a54\n"
"v_mul_f32 v52, v52, v49\n"
"v_accvgpr_write_b32 a54, v52\n"
"v_accvgpr_read_b32 v53, a55\n"
"v_mul_f32 v53, v53, v49\n"
"v_accvgpr_write_b32 a55, v53\n"
"v_accvgpr_read_b32 v50, a56\n"
"v_mul_f32 v50, v50, v49\n"
"v_accvgpr_write_b32 a56, v50\n"
"v_accvgpr_read_b32 v51, a57\n"
"v_mul_f32 v51, v51, v49\n"
"v_accvgpr_write_b32 a57, v51\n"
"v_accvgpr_read_b32 v52, a58\n"
"v_mul_f32 v52, v52, v49\n"
"v_accvgpr_write_b32 a58, v52\n"
"v_accvgpr_read_b32 v53, a59\n"
"v_mul_f32 v53, v53, v49\n"
"v_accvgpr_write_b32 a59, v53\n"
"v_accvgpr_read_b32 v50, a60\n"
"v_mul_f32 v50, v50, v49\n"
"v_accvgpr_write_b32 a60, v50\n"
"v_accvgpr_read_b32 v51, a61\n"
"v_mul_f32 v51, v51, v49\n"
"v_accvgpr_write_b32 a61, v51\n"
"v_accvgpr_read_b32 v52, a62\n"
"v_mul_f32 v52, v52, v49\n"
"v_accvgpr_write_b32 a62, v52\n"
"v_accvgpr_read_b32 v53, a63\n"
"v_mul_f32 v53, v53, v49\n"
"v_accvgpr_write_b32 a63, v53\n"
"s_nop 7\n"
"s_cmp_eq_u32 s63, 2\n"
"s_cbranch_scc1 AT_B2\n"
"s_cmp_eq_u32 s63, 6\n"
"s_cbranch_scc1 AT_B6\n"
"s_cmp_eq_u32 s63, 10\n"
"s_cbranch_scc1 AT_B10\n"
"s_branch AT_B14\n"
"AT_R3:\n"
"s_mov_b32 s63, 3\n"
"s_branch AT_RARE2\n"
"AT_R7:\n"
"s_mov_b32 s63, 7\n"
"s_branch AT_RARE2\n"
"AT_R11:\n"
"s_mov_b32 s63, 11\n"
"s_branch AT_RARE2\n"
"AT_R15:\n"
"s_mov_b32 s63, 15\n"
"s_branch AT_RARE2\n"
"AT_RARE2:\n"
"s_nop 7\n"
"v_max3_f32 v48, v96, v97, v98\n"
"v_max3_f32 v48, v48, v99, v100\n"
"v_max3_f32 v48, v48, v101, v102\n"
"v_max3_f32 v48, v48, v103, v104\n"
"v_max3_f32 v48, v48, v105, v106\n"
"v_max3_f32 v48, v48, v107, v108\n"
"v_max3_f32 v48, v48, v109, v110\n"
"v_max_f32 v48, v48, v111\n"
"v_mov_b32 v49, v48\n"
"s_nop 1\n"
"v_permlane32_swap_b32 v48, v49\n"
"v_max_f32 v48, v48, v49\n"
"v_mov_b32 v49, v48\n"
"s_nop 1\n"
"v_permlane16_swap_b32 v48, v49\n"
"v_max_f32 v48, v48, v49\n"
"v_max_f32 v48, 0, v48\n"
"v_exp_f32 v49, -v48\n"
"v_sub_f32 v72, v72, v48\n"
"v_sub_f32 v73, v73, v48\n"
"v_sub_f32 v74, v74, v48\n"
"v_sub_f32 v75, v75, v48\n"
"v_sub_f32 v50, v96, v48\n"
"v_sub_f32 v51, v97, v48\n"
"v_exp_f32 v50, v50\n"
"v_exp_f32 v51, v51\n"
"s_nop 0\n"
"v_cvt_pk_bf16_f32 v80, v50, v51\n"
"v_sub_f32 v50, v98, v48\n"
"v_sub_f32 v51, v99, v48\n"
"v_exp_f32 v50, v50\n"
"v_exp_f32 v51, v51\n"
"s_nop 0\n"
"v_cvt_pk_bf16_f32 v81, v50, v51\n"
"v_sub_f32 v50, v100, v48\n"
"v_sub_f32 v51, v101, v48\n"
"v_exp_f32 v50, v50\n"
"v_exp_f32 v51, v51\n"
"s_nop 0\n"
"v_cvt_pk_bf16_f32 v82, v50, v51\n"
"v_sub_f32 v50, v102, v48\n"
"v_sub_f32 v51, v103, v48\n"
"v_exp_f32 v50, v50\n"
"v_exp_f32 v51, v51\n"
"s_nop 0\n"
"v_cvt_pk_bf16_f32 v83, v50, v51\n"
"v_sub_f32 v50, v104, v48\n"
"v_sub_f32 v51, v105, v48\n"
"v_exp_f32 v50, v50\n"
"v_exp_f32 v51, v51\n"
"s_nop 0\n"
"v_cvt_pk_bf16_f32 v84, v50, v51\n"
"v_sub_f32 v50, v106, v48\n"
"v_sub_f32 v51, v107, v48\n"
"v_exp_f32 v50, v50\n"
"v_exp_f32 v51, v51\n"
"s_nop 0\n"
"v_cvt_pk_bf16_f32 v85, v50, v51\n"
"v_sub_f32 v50, v108, v48\n"
"v_sub_f32 v51, v109, v48\n"
"v_exp_f32 v50, v50\n"
"v_exp_f32 v51, v51\n"
"s_nop 0\n"
"v_cvt_pk_bf16_f32 v86, v50, v51\n"
"v_sub_f32 v50, v110, v48\n"
"v_sub_f32 v51, v111, v48\n"
"v_exp_f32 v50, v50\n"
"v_exp_f32 v51, v51\n"
"s_nop 0\n"
"v_cvt_pk_bf16_f32 v87, v50, v51\n"
"v_mfma_f32_16x16x32_bf16 v[40:43], v[56:59], v[80:83], 0\n"
"s_nop 0\n"
"v_mfma_f32_16x16x32_bf16 v[40:43], v[56:59], v[84:87], v[40:43]\n"
"v_mul_f32 v30, v30, v49\n"
"v_accvgpr_read_b32 v50, a64\n"
"v_mul_f32 v50, v50, v49\n"
"v_accvgpr_write_b32 a64, v50\n"
"v_accvgpr_read_b32 v51, a65\n"
"v_mul_f32 v51, v51, v49\n"
"v_accvgpr_write_b32 a65, v51\n"
"v_accvgpr_read_b32 v52, a66\n"
"v_mul_f32 v52, v52, v49\n"
"v_accvgpr_write_b32 a66, v52\n"
"v_accvgpr_read_b32 v53, a67\n"
"v_mul_f32 v53, v53, v49\n"
"v_accvgpr_write_b32 a67, v53\n"
"v_accvgpr_read_b32 v50, a68\n"
"v_mul_f32 v50, v50, v49\n"
"v_accvgpr_write_b32 a68, v50\n"
"v_accvgpr_read_b32 v51, a69\n"
"v_mul_f32 v51, v51, v49\n"
"v_accvgpr_write_b32 a69, v51\n"
"v_accvgpr_read_b32 v52, a70\n"
"v_mul_f32 v52, v52, v49\n"
"v_accvgpr_write_b32 a70, v52\n"
"v_accvgpr_read_b32 v53, a71\n"
"v_mul_f32 v53, v53, v49\n"
"v_accvgpr_write_b32 a71, v53\n"
"v_accvgpr_read_b32 v50, a72\n"
"v_mul_f32 v50, v50, v49\n"
"v_accvgpr_write_b32 a72, v50\n"
"v_accvgpr_read_b32 v51, a73\n"
"v_mul_f32 v51, v51, v49\n"
"v_accvgpr_write_b32 a73, v51\n"
"v_accvgpr_read_b32 v52, a74\n"
"v_mul_f32 v52, v52, v49\n"
"v_accvgpr_write_b32 a74, v52\n"
"v_accvgpr_read_b32 v53, a75\n"
"v_mul_f32 v53, v53, v49\n"
"v_accvgpr_write_b32 a75, v53\n"
"v_accvgpr_read_b32 v50, a76\n"
"v_mul_f32 v50, v50, v49\n"
"v_accvgpr_write_b32 a76, v50\n"
"v_accvgpr_read_b32 v51, a77\n"
"v_mul_f32 v51, v51, v49\n"
"v_accvgpr_write_b32 a77, v51\n"
"v_accvgpr_read_b32 v52, a78\n"
"v_mul_f32 v52, v52, v49\n"
"v_accvgpr_write_b32 a78, v52\n"
"v_accvgpr_read_b32 v53, a79\n"
"v_mul_f32 v53, v53, v49\n"
"v_accvgpr_write_b32 a79, v53\n"
"v_accvgpr_read_b32 v50, a80\n"
"v_mul_f32 v50, v50, v49\n"
"v_accvgpr_write_b32 a80, v50\n"
"v_accvgpr_read_b32 v51, a81\n"
"v_mul_f32 v51, v51, v49\n"
"v_accvgpr_write_b32 a81, v51\n"
"v_accvgpr_read_b32 v52, a82\n"
"v_mul_f32 v52, v52, v49\n"
"v_accvgpr_write_b32 a82, v52\n"
"v_accvgpr_read_b32 v53, a83\n"
"v_mul_f32 v53, v53, v49\n"
"v_accvgpr_write_b32 a83, v53\n"
"v_accvgpr_read_b32 v50, a84\n"
"v_mul_f32 v50, v50, v49\n"
"v_accvgpr_write_b32 a84, v50\n"
"v_accvgpr_read_b32 v51, a85\n"
"v_mul_f32 v51, v51, v49\n"
"v_accvgpr_write_b32 a85, v51\n"
"v_accvgpr_read_b32 v52, a86\n"
"v_mul_f32 v52, v52, v49\n"
"v_accvgpr_write_b32 a86, v52\n"
"v_accvgpr_read_b32 v53, a87\n"
"v_mul_f32 v53, v53, v49\n"
"v_accvgpr_write_b32 a87, v53\n"
"v_accvgpr_read_b32 v50, a88\n"
"v_mul_f32 v50, v50, v49\n"
"v_accvgpr_write_b32 a88, v50\n"
"v_accvgpr_read_b32 v51, a89\n"
"v_mul_f32 v51, v51, v49\n"
"v_accvgpr_write_b32 a89, v51\n"
"v_accvgpr_read_b32 v52, a90\n"
"v_mul_f32 v52, v52, v49\n"
"v_accvgpr_write_b32 a90, v52\n"
"v_accvgpr_read_b32 v53, a91\n"
"v_mul_f32 v53, v53, v49\n"
"v_accvgpr_write_b32 a91, v53\n"
"v_accvgpr_read_b32 v50, a92\n"
"v_mul_f32 v50, v50, v49\n"
"v_accvgpr_write_b32 a92, v50\n"
"v_accvgpr_read_b32 v51, a93\n"
"v_mul_f32 v51, v51, v49\n"
"v_accvgpr_write_b32 a93, v51\n"
"v_accvgpr_read_b32 v52, a94\n"
"v_mul_f32 v52, v52, v49\n"
"v_accvgpr_write_b32 a94, v52\n"
"v_accvgpr_read_b32 v53, a95\n"
"v_mul_f32 v53, v53, v49\n"
"v_accvgpr_write_b32 a95, v53\n"
"s_nop 7\n"
"s_cmp_eq_u32 s63, 3\n"
"s_cbranch_scc1 AT_B3\n"
"s_cmp_eq_u32 s63, 7\n"
"s_cbranch_scc1 AT_B7\n"
"s_cmp_eq_u32 s63, 11\n"
"s_cbranch_scc1 AT_B11\n"
"s_branch AT_B15\n"
"AT_R0:\n"
"s_mov_b32 s63, 0\n"
"s_branch AT_RARE3\n"
"AT_R4:\n"
"s_mov_b32 s63, 4\n"
"s_branch AT_RARE3\n"
"AT_R8:\n"
"s_mov_b32 s63, 8\n"
"s_branch AT_RARE3\n"
"AT_R12:\n"
"s_mov_b32 s63, 12\n"
"s_branch AT_RARE3\n"
"AT_R16:\n"
"s_mov_b32 s63, 16\n"
"s_branch AT_RARE3\n"
"AT_RARE3:\n"
"s_nop 7\n"
"v_max3_f32 v48, v112, v113, v114\n"
"v_max3_f32 v48, v48, v115, v116\n"
"v_max3_f32 v48, v48, v117, v118\n"
"v_max3_f32 v48, v48, v119, v120\n"
"v_max3_f32 v48, v48, v121, v122\n"
"v_max3_f32 v48, v48, v123, v124\n"
"v_max3_f32 v48, v48, v125, v126\n"
"v_max_f32 v48, v48, v127\n"
"v_mov_b32 v49, v48\n"
"s_nop 1\n"
"v_permlane32_swap_b32 v48, v49\n"
"v_max_f32 v48, v48, v49\n"
"v_mov_b32 v49, v48\n"
"s_nop 1\n"
"v_permlane16_swap_b32 v48, v49\n"
"v_max_f32 v48, v48, v49\n"
"v_max_f32 v48, 0, v48\n"
"v_exp_f32 v49, -v48\n"
"v_sub_f32 v76, v76, v48\n"
"v_sub_f32 v77, v77, v48\n"
"v_sub_f32 v78, v78, v48\n"
"v_sub_f32 v79, v79, v48\n"
"v_sub_f32 v50, v112, v48\n"
"v_sub_f32 v51, v113, v48\n"
"v_exp_f32 v50, v50\n"
"v_exp_f32 v51, v51\n"
"s_nop 0\n"
"v_cvt_pk_bf16_f32 v88, v50, v51\n"
"v_sub_f32 v50, v114, v48\n"
"v_sub_f32 v51, v115, v48\n"
"v_exp_f32 v50, v50\n"
"v_exp_f32 v51, v51\n"
"s_nop 0\n"
"v_cvt_pk_bf16_f32 v89, v50, v51\n"
"v_sub_f32 v50, v116, v48\n"
"v_sub_f32 v51, v117, v48\n"
"v_exp_f32 v50, v50\n"
"v_exp_f32 v51, v51\n"
"s_nop 0\n"
"v_cvt_pk_bf16_f32 v90, v50, v51\n"
"v_sub_f32 v50, v118, v48\n"
"v_sub_f32 v51, v119, v48\n"
"v_exp_f32 v50, v50\n"
"v_exp_f32 v51, v51\n"
"s_nop 0\n"
"v_cvt_pk_bf16_f32 v91, v50, v51\n"
"v_sub_f32 v50, v120, v48\n"
"v_sub_f32 v51, v121, v48\n"
"v_exp_f32 v50, v50\n"
"v_exp_f32 v51, v51\n"
"s_nop 0\n"
"v_cvt_pk_bf16_f32 v92, v50, v51\n"
"v_sub_f32 v50, v122, v48\n"
"v_sub_f32 v51, v123, v48\n"
"v_exp_f32 v50, v50\n"
"v_exp_f32 v51, v51\n"
"s_nop 0\n"
"v_cvt_pk_bf16_f32 v93, v50, v51\n"
"v_sub_f32 v50, v124, v48\n"
"v_sub_f32 v51, v125, v48\n"
"v_exp_f32 v50, v50\n"
"v_exp_f32 v51, v51\n"
"s_nop 0\n"
"v_cvt_pk_bf16_f32 v94, v50, v51\n"
"v_sub_f32 v50, v126, v48\n"
"v_sub_f32 v51, v127, v48\n"
"v_exp_f32 v50, v50\n"
"v_exp_f32 v51, v51\n"
"s_nop 0\n"
"v_cvt_pk_bf16_f32 v95, v50, v51\n"
"v_mfma_f32_16x16x32_bf16 v[44:47], v[56:59], v[88:91], 0\n"
"s_nop 0\n"
"v_mfma_f32_16x16x32_bf16 v[44:47], v[56:59], v[92:95], v[44:47]\n"
"v_mul_f32 v31, v31, v49\n"
"v_accvgpr_read_b32 v50, a96\n"
"v_mul_f32 v50, v50, v49\n"
"v_accvgpr_write_b32 a96, v50\n"
"v_accvgpr_read_b32 v51, a97\n"
"v_mul_f32 v51, v51, v49\n"
"v_accvgpr_write_b32 a97, v51\n"
"v_accvgpr_read_b32 v52, a98\n"
"v_mul_f32 v52, v52, v49\n"
"v_accvgpr_write_b32 a98, v52\n"
"v_accvgpr_read_b32 v53, a99\n"
"v_mul_f32 v53, v53, v49\n"
"v_accvgpr_write_b32 a99, v53\n"
"v_accvgpr_read_b32 v50, a100\n"
"v_mul_f32 v50, v50, v49\n"
"v_accvgpr_write_b32 a100, v50\n"
"v_accvgpr_read_b32 v51, a101\n"
"v_mul_f32 v51, v51, v49\n"
"v_accvgpr_write_b32 a101, v51\n"
"v_accvgpr_read_b32 v52, a102\n"
"v_mul_f32 v52, v52, v49\n"
"v_accvgpr_write_b32 a102, v52\n"
"v_accvgpr_read_b32 v53, a103\n"
"v_mul_f32 v53, v53, v49\n"
"v_accvgpr_write_b32 a103, v53\n"
"v_accvgpr_read_b32 v50, a104\n"
"v_mul_f32 v50, v50, v49\n"
"v_accvgpr_write_b32 a104, v50\n"
"v_accvgpr_read_b32 v51, a105\n"
"v_mul_f32 v51, v51, v49\n"
"v_accvgpr_write_b32 a105, v51\n"
"v_accvgpr_read_b32 v52, a106\n"
"v_mul_f32 v52, v52, v49\n"
"v_accvgpr_write_b32 a106, v52\n"
"v_accvgpr_read_b32 v53, a107\n"
"v_mul_f32 v53, v53, v49\n"
"v_accvgpr_write_b32 a107, v53\n"
"v_accvgpr_read_b32 v50, a108\n"
"v_mul_f32 v50, v50, v49\n"
"v_accvgpr_write_b32 a108, v50\n"
"v_accvgpr_read_b32 v51, a109\n"
"v_mul_f32 v51, v51, v49\n"
"v_accvgpr_write_b32 a109, v51\n"
"v_accvgpr_read_b32 v52, a110\n"
"v_mul_f32 v52, v52, v49\n"
"v_accvgpr_write_b32 a110, v52\n"
"v_accvgpr_read_b32 v53, a111\n"
"v_mul_f32 v53, v53, v49\n"
"v_accvgpr_write_b32 a111, v53\n"
"v_accvgpr_read_b32 v50, a112\n"
"v_mul_f32 v50, v50, v49\n"
"v_accvgpr_write_b32 a112, v50\n"
"v_accvgpr_read_b32 v51, a113\n"
"v_mul_f32 v51, v51, v49\n"
"v_accvgpr_write_b32 a113, v51\n"
"v_accvgpr_read_b32 v52, a114\n"
"v_mul_f32 v52, v52, v49\n"
"v_accvgpr_write_b32 a114, v52\n"
"v_accvgpr_read_b32 v53, a115\n"
"v_mul_f32 v53, v53, v49\n"
"v_accvgpr_write_b32 a115, v53\n"
"v_accvgpr_read_b32 v50, a116\n"
"v_mul_f32 v50, v50, v49\n"
"v_accvgpr_write_b32 a116, v50\n"
"v_accvgpr_read_b32 v51, a117\n"
"v_mul_f32 v51, v51, v49\n"
"v_accvgpr_write_b32 a117, v51\n"
"v_accvgpr_read_b32 v52, a118\n"
"v_mul_f32 v52, v52, v49\n"
"v_accvgpr_write_b32 a118, v52\n"
"v_accvgpr_read_b32 v53, a119\n"
"v_mul_f32 v53, v53, v49\n"
"v_accvgpr_write_b32 a119, v53\n"
"v_accvgpr_read_b32 v50, a120\n"
"v_mul_f32 v50, v50, v49\n"
"v_accvgpr_write_b32 a120, v50\n"
"v_accvgpr_read_b32 v51, a121\n"
"v_mul_f32 v51, v51, v49\n"
"v_accvgpr_write_b32 a121, v51\n"
"v_accvgpr_read_b32 v52, a122\n"
"v_mul_f32 v52, v52, v49\n"
"v_accvgpr_write_b32 a122, v52\n"
"v_accvgpr_read_b32 v53, a123\n"
"v_mul_f32 v53, v53, v49\n"
"v_accvgpr_write_b32 a123, v53\n"
"v_accvgpr_read_b32 v50, a124\n"
"v_mul_f32 v50, v50, v49\n"
"v_accvgpr_write_b32 a124, v50\n"
"v_accvgpr_read_b32 v51, a125\n"
"v_mul_f32 v51, v51, v49\n"
"v_accvgpr_write_b32 a125, v51\n"
"v_accvgpr_read_b32 v52, a126\n"
"v_mul_f32 v52, v52, v49\n"
"v_accvgpr_write_b32 a126, v52\n"
"v_accvgpr_read_b32 v53, a127\n"
"v_mul_f32 v53, v53, v49\n"
"v_accvgpr_write_b32 a127, v53\n"
"s_nop 7\n"
"s_cmp_eq_u32 s63, 0\n"
"s_cbranch_scc1 AT_B0\n"
"s_cmp_eq_u32 s63, 4\n"
"s_cbranch_scc1 AT_B4\n"
"s_cmp_eq_u32 s63, 8\n"
"s_cbranch_scc1 AT_B8\n"
"s_cmp_eq_u32 s63, 12\n"
"s_cbranch_scc1 AT_B12\n"
"s_branch AT_B16\n"
"MID_END:\n"
  :: "s"(ws), "s"(WQ), "s"(Kp), "s"(Vp), "s"(WG), "s"(WU), "s"(wo), "s"(wd), "s"(x), "s"(out),
     "s"(HoS), "s"(X1o), "s"(Oo), "s"(Ho), "s"(NWQ), "s"(NWG), "s"(wave), "s"(b), "v"(threadIdx.x)
  : "v1", "v2", "v3", "v4", "v5", "v6", "v7", "v8", "v9", "v10", "v11", "v12", "v13", "v14", "v15", "v16", "v17", "v18", "v19", "v20", "v21", "v22", "v23", "v24", "v25", "v26", "v27", "v28", "v29", "v30", "v31", "v32", "v33", "v34", "v35", "v36", "v37", "v38", "v39", "v40", "v41", "v42", "v43", "v44", "v45", "v46", "v47", "v48", "v49", "v50", "v51", "v52", "v53", "v54", "v55", "v56", "v57", "v58", "v59", "v60", "v61", "v62", "v63", "v64", "v65", "v66", "v67", "v68", "v69", "v70", "v71", "v72", "v73", "v74", "v75", "v76", "v77", "v78", "v79", "v80", "v81", "v82", "v83", "v84", "v85", "v86", "v87", "v88", "v89", "v90", "v91", "v92", "v93", "v94", "v95", "v96", "v97", "v98", "v99", "v100", "v101", "v102", "v103", "v104", "v105", "v106", "v107", "v108", "v109", "v110", "v111", "v112", "v113", "v114", "v115", "v116", "v117", "v118", "v119", "v120", "v121", "v122", "v123", "v124", "v125", "v126", "v127", "v128", "v129", "v130", "v131", "v132", "v133", "v134", "v135", "v136", "v137", "v138", "v139", "v140", "v141", "v142", "v143", "v144", "v145", "v146", "v147", "v148", "v149", "v150", "v151", "v152", "v153", "v154", "v155", "v156", "v157", "v158", "v159", "v160", "v161", "v162", "v163", "v164", "v165", "v166", "v167", "v168", "v169", "v170", "v171", "v172", "v173", "v174", "v175", "v176", "v177", "v178", "v179", "v180", "v181", "v182", "v183", "v184", "v185", "v186", "v187", "v188", "v189", "v190", "v191", "v192", "v193", "v194", "v195", "v196", "v197", "v198", "v199", "v200", "v201", "v202", "v203", "v204", "v205", "v206", "v207", "v208", "v209", "v210", "v211", "v212", "v213", "v214", "v215", "v216", "v217", "v218", "v219", "v220", "v221", "v222", "v223", "v224", "v225", "v226", "v227", "v228", "v229", "v230", "v231", "v232", "v233", "v234", "v235", "v236", "v237", "v238", "v239", "v240", "v241", "v242", "v243", "v244", "v245", "v246", "v247", "v248", "v249", "v250", "v251", "v252", "v253", "v254", "v255", "a0", "a1", "a2", "a3", "a4", "a5", "a6", "a7", "a8", "a9", "a10", "a11", "a12", "a13", "a14", "a15", "a16", "a17", "a18", "a19", "a20", "a21", "a22", "a23", "a24", "a25", "a26", "a27", "a28", "a29", "a30", "a31", "a32", "a33", "a34", "a35", "a36", "a37", "a38", "a39", "a40", "a41", "a42", "a43", "a44", "a45", "a46", "a47", "a48", "a49", "a50", "a51", "a52", "a53", "a54", "a55", "a56", "a57", "a58", "a59", "a60", "a61", "a62", "a63", "a64", "a65", "a66", "a67", "a68", "a69", "a70", "a71", "a72", "a73", "a74", "a75", "a76", "a77", "a78", "a79", "a80", "a81", "a82", "a83", "a84", "a85", "a86", "a87", "a88", "a89", "a90", "a91", "a92", "a93", "a94", "a95", "a96", "a97", "a98", "a99", "a100", "a101", "a102", "a103", "a104", "a105", "a106", "a107", "a108", "a109", "a110", "a111", "a112", "a113", "a114", "a115", "a116", "a117", "a118", "a119", "a120", "a121", "a122", "a123", "a124", "a125", "a126", "a127", "a128", "a129", "a130", "a131", "a132", "a133", "a134", "a135", "a136", "a137", "a138", "a139", "a140", "a141", "a142", "a143", "a144", "a145", "a146", "a147", "a148", "a149", "a150", "a151", "a152", "a153", "a154", "a155", "a156", "a157", "a158", "a159", "a160", "a161", "a162", "a163", "a164", "a165", "a166", "a167", "a168", "a169", "a170", "a171", "a172", "a173", "a174", "a175", "a176", "a177", "a178", "a179", "a180", "a181", "a182", "a183", "a184", "a185", "a186", "a187", "a188", "a189", "a190", "a191", "a192", "a193", "a194", "a195", "a196", "a197", "a198", "a199", "a200", "a201", "a202", "a203", "a204", "a205", "a206", "a207", "a208", "a209", "a210", "a211", "a212", "a213", "a214", "a215", "a216", "a217", "a218", "a219", "a220", "a221", "a222", "a223", "a224", "a225", "a226", "a227", "a228", "a229", "a230", "a231", "a232", "a233", "a234", "a235", "a236", "a237", "a238", "a239", "a240", "a241", "a242", "a243", "a244", "a245", "a246", "a247", "a248", "a249", "a250", "a251", "a252", "a253", "a254", "a255", "s34", "s35", "s36", "s37", "s38", "s39", "s40", "s41", "s42", "s43", "s44", "s45", "s46", "s47", "s48", "s49", "s50", "s51", "s52", "s53", "s54", "s55", "s56", "s57", "s58", "s59", "s60", "s61", "s62", "s63", "s64", "s65", "s66", "s67", "s68", "s69", "s70", "s71", "s72", "s73", "s74", "s75", "s76", "s77", "s78", "s79", "s80", "s81", "s82", "s83", "s84", "s85", "s86", "s87", "s88", "s89", "s90", "s91", "s92", "s93", "s94", "s95", "s96", "s97", "s98", "s99", "vcc", "scc", "memory");
}

extern "C" void kernel_launch(void* const* d_in, const int* in_sizes, int n_in, void* d_out, int out_size, void* d_ws, size_t ws_size, hipStream_t stream) {
  const u16* x  = (const u16*)d_in[0];
  const u16* kc = (const u16*)d_in[1];
  const u16* vc = (const u16*)d_in[2];
  const u16* wq = (const u16*)d_in[3];
  const u16* wo = (const u16*)d_in[4];
  const u16* wg = (const u16*)d_in[6];
  const u16* wu = (const u16*)d_in[7];
  const u16* wd = (const u16*)d_in[8];
  char* ws = (char*)d_ws;
  mid_kernel<<<256, 256, 0, stream>>>(kc, vc, wq, wg, wu, ws, wo, wd, x, (u16*)d_out);
}
```
